```python
import torch

def _softmax(x, axis=-1):
    return torch.softmax(x, dim=axis)
def _gelu(x):
    return torch.nn.functional.gelu(x, approximate="tanh")
def _silu(x):
    return torch.nn.functional.silu(x)
def _sigmoid(x):
    return torch.sigmoid(x)
def _layernorm(x, axis=-1, eps=1e-5):
    m = x.mean(dim=axis, keepdim=True)
    v = x.var(dim=axis, keepdim=True, unbiased=False)
    return (x - m) / torch.sqrt(v + eps)
def _scatter_add(tgt, idx, src, axis=0):
    return tgt.clone().index_add_(axis, idx, src)
def _segment_sum(src, seg, n):
    out = torch.zeros((n,) + src.shape[1:], dtype=src.dtype)
    return out.index_add_(0, seg, src)
def _pad(x, pw):
    flat = tuple(v for pair in reversed(pw) for v in pair)
    return torch.nn.functional.pad(x, flat)

kernel_name = 'recurrent_scan__mha_block_cached__glu_gated_swiglu__prompt'

def setup_inputs():

    _w = (lambda a: (lambda t: t.to(torch.bfloat16) if t.is_floating_point() else t)(torch.as_tensor(a)))
    def _A_setup():
        B, Sq, D = 1, 2048, 448
        H, Sk, Dh = 16, 2048, 256
        return {
            "x": _w((lambda *s: torch.randn(*s, dtype=torch.bfloat16) * 0.05)(B, Sq, D)),
            "k_cache": _w((lambda *s: torch.randn(*s, dtype=torch.bfloat16) * 1.6)(B, H, Sk, Dh)),
            "v_cache": _w((lambda *s: torch.randn(*s, dtype=torch.bfloat16) * 1.6)(B, H, Sk, Dh)),
            "wq": _w((lambda *s: torch.randn(*s, dtype=torch.bfloat16) * 1.6)(D, H * Dh) * (D ** -0.5)),
            "wo": _w((lambda *s: torch.randn(*s, dtype=torch.bfloat16) * 1.6)(H * Dh, D) * ((H * Dh) ** -0.5)),
        }
    _A_in = _A_setup()

    def _B_setup():
        B, S, D, F = 1, 2048, 448, 1792
        return {
            "x": _w((lambda *s: torch.randn(*s, dtype=torch.bfloat16) * 0.05)(B, S, D)),
            "w1": _w((lambda *s: torch.randn(*s, dtype=torch.bfloat16) * 0.2)(D, F) * (2.0 / D) ** 0.5),
            "w3": _w((lambda *s: torch.randn(*s, dtype=torch.bfloat16) * 0.2)(D, F) * (2.0 / D) ** 0.5),
            "w2": _w((lambda *s: torch.randn(*s, dtype=torch.bfloat16) * 0.2)(F, D) * (2.0 / F) ** 0.5),
        }
    _B_in = _B_setup()

    return {
        "A_x": _A_in["x"],
        "A_k_cache": _A_in["k_cache"],
        "A_v_cache": _A_in["v_cache"],
        "A_wq": _A_in["wq"],
        "A_wo": _A_in["wo"],
        "B_x": _B_in["x"],
        "B_w1": _B_in["w1"],
        "B_w3": _B_in["w3"],
        "B_w2": _B_in["w2"],
    }

def reference(A_x, A_k_cache, A_v_cache, A_wq, A_wo, B_x, B_w1, B_w3, B_w2):
    def _A_ref(x, k_cache, v_cache, wq, wo):
        B, Sq, D = x.shape
        H, Dh = 16, 256
        q = (lambda x, *d: x.permute(*d))((x @ wq).reshape(B, Sq, H, Dh), 0, 2, 1, 3)
        scores = torch.einsum("bhqd,bhkd->bhqk", q, k_cache) * (Dh ** -0.5)
        p = _softmax(scores, axis=-1)
        o = torch.einsum("bhqk,bhkd->bhqd", p, v_cache)
        return (lambda x, *d: x.permute(*d))(o, 0, 2, 1, 3).reshape(B, Sq, H * Dh) @ wo

    def _B_ref(x, w1, w3, w2):
        gate = torch.einsum("bsd,df->bsf", x, w1)
        up = torch.einsum("bsd,df->bsf", x, w3)
        h = _silu(gate) * up
        out = torch.einsum("bsf,fd->bsd", h, w2)
        return out

    x = A_x
    xn = _layernorm(x, axis=-1)
    s = _A_ref(xn, A_k_cache, A_v_cache, A_wq, A_wo)
    g = _silu(_B_ref(xn, B_w1, B_w3, B_w2))
    return x + s * g
```

```cpp
#include <hip/hip_runtime.h>
#include <hip/hip_bf16.h>
typedef unsigned short u16;
__device__ __forceinline__ float bf2f(u16 v){ return __uint_as_float(((unsigned)v)<<16); }
__device__ __forceinline__ u16 f2bf(float f){ __hip_bfloat16 b = __float2bfloat16(f); return *(u16*)&b; }
__device__ __forceinline__ float rb(float f){ return bf2f(f2bf(f)); }

__global__ void n_ln(const u16* x, u16* xn){
  int row = blockIdx.x; int t = threadIdx.x;
  float v[7]; float s=0;
  for(int i=0;i<7;i++){ v[i]=bf2f(x[row*448+t+64*i]); s+=v[i]; }
  for(int o=32;o>0;o>>=1) s+=__shfl_xor(s,o);
  float mean=s/448.f; float q=0;
  for(int i=0;i<7;i++){ float d=v[i]-mean; q+=d*d; }
  for(int o=32;o>0;o>>=1) q+=__shfl_xor(q,o);
  float var=rb(q/448.f); float m=rb(mean);
  float sd=rb(sqrtf(rb(var+1e-5f)));
  for(int i=0;i<7;i++) xn[row*448+t+64*i]=f2bf(rb(v[i]-m)/sd);
}
__global__ void n_gemm(const u16* A, const u16* B, u16* C, int M, int N, int K){
  __shared__ float As[16][17], Bs[16][17];
  int tx=threadIdx.x, ty=threadIdx.y; int row=blockIdx.y*16+ty, col=blockIdx.x*16+tx;
  float acc=0;
  for(int k0=0;k0<K;k0+=16){
    As[ty][tx]=bf2f(A[(size_t)row*K+k0+tx]); Bs[ty][tx]=bf2f(B[(size_t)(k0+ty)*N+col]);
    __syncthreads();
    for(int k=0;k<16;k++) acc+=As[ty][k]*Bs[k][tx];
    __syncthreads();
  }
  C[(size_t)row*N+col]=f2bf(acc);
}
__global__ void n_attn(const u16* q, const u16* K, const u16* V, u16* o){
  int row=blockIdx.x, h=blockIdx.y, t=threadIdx.x;
  __shared__ float qs[256]; __shared__ float p[2048]; __shared__ float red[256];
  qs[t]=bf2f(q[(size_t)row*4096+h*256+t]); __syncthreads();
  float mx=-1e30f;
  for(int k=t;k<2048;k+=256){ const u16* kr=K+((size_t)h*2048+k)*256; float s=0; for(int d=0;d<256;d++) s+=qs[d]*bf2f(kr[d]); s=rb(rb(s)*0.0625f); p[k]=s; mx=fmaxf(mx,s);}
  red[t]=mx; __syncthreads(); for(int o2=128;o2>0;o2>>=1){ if(t<o2) red[t]=fmaxf(red[t],red[t+o2]); __syncthreads(); } mx=red[0]; __syncthreads();
  float sum=0; for(int k=t;k<2048;k+=256){ float e=__expf(p[k]-mx); p[k]=e; sum+=e; }
  red[t]=sum; __syncthreads(); for(int o2=128;o2>0;o2>>=1){ if(t<o2) red[t]+=red[t+o2]; __syncthreads(); } sum=red[0]; __syncthreads();
  for(int k=t;k<2048;k+=256) p[k]=rb(p[k]/sum);
  __syncthreads();
  float acc=0; for(int k=0;k<2048;k++) acc+=p[k]*bf2f(V[((size_t)h*2048+k)*256+t]);
  o[(size_t)row*4096+h*256+t]=f2bf(acc);
}
__global__ void n_swiglu(const u16* g, const u16* u, u16* h, int n){
  int i=blockIdx.x*256+threadIdx.x; if(i<n){ float a=bf2f(g[i]); float s=rb(a/(1.f+__expf(-a))); h[i]=f2bf(s*bf2f(u[i])); }
}
__global__ void n_final(const u16* x, const u16* s, const u16* f, u16* out, int n){
  int i=blockIdx.x*256+threadIdx.x; if(i<n){ float a=bf2f(f[i]); float g=rb(a/(1.f+__expf(-a))); out[i]=f2bf(bf2f(x[i])+rb(bf2f(s[i])*g)); }
}
#define NAIVE_NAME kernel_launch
extern "C" void NAIVE_NAME(void* const* d_in, const int* in_sizes, int n_in, void* d_out, int out_size, void* d_ws, size_t ws_size, hipStream_t stream){
  const u16* x=(const u16*)d_in[0]; const u16* kc=(const u16*)d_in[1]; const u16* vc=(const u16*)d_in[2];
  const u16* wq=(const u16*)d_in[3]; const u16* wo=(const u16*)d_in[4]; const u16* w1=(const u16*)d_in[6]; const u16* w3=(const u16*)d_in[7]; const u16* w2=(const u16*)d_in[8];
  u16* ws=(u16*)d_ws;
  u16* xn=ws; u16* q=xn+2048*448; u16* o=q+2048*4096; u16* s=o+2048*4096; u16* g=s+2048*448; u16* u=g+2048*1792; u16* h=u+2048*1792; u16* f=h+2048*1792;
  n_ln<<<2048,64,0,stream>>>(x,xn);
  n_gemm<<<dim3(4096/16,128),dim3(16,16),0,stream>>>(xn,wq,q,2048,4096,448);
  n_attn<<<dim3(2048,16),256,0,stream>>>(q,kc,vc,o);
  n_gemm<<<dim3(448/16,128),dim3(16,16),0,stream>>>(o,wo,s,2048,448,4096);
  n_gemm<<<dim3(1792/16,128),dim3(16,16),0,stream>>>(xn,w1,g,2048,1792,448);
  n_gemm<<<dim3(1792/16,128),dim3(16,16),0,stream>>>(xn,w3,u,2048,1792,448);
  n_swiglu<<<2048*1792/256,256,0,stream>>>(g,u,h,2048*1792);
  n_gemm<<<dim3(448/16,128),dim3(16,16),0,stream>>>(h,w2,f,2048,448,1792);
  n_final<<<2048*448/256,256,0,stream>>>(x,s,f,(u16*)d_out,2048*448);
}
```

```cpp
#include <hip/hip_runtime.h>
#include <hip/hip_bf16.h>
#define KA_ASM \
  ".p2align 3\n" \
  "L_begin_%=:\n" \
  "s_getpc_b64 s[34:35]\n" \
  "s_bitcmp1_b32 %3, 1\n" \
  "s_cbranch_scc0 L_big_%=\n" \
  "s_load_dwordx16 s[12:27], %0, 0x0\n" \
  "s_load_dwordx4 s[28:31], %0, 0x40\n" \
  "v_mbcnt_lo_u32_b32 v253, -1, 0\n" \
  "v_mbcnt_hi_u32_b32 v253, -1, v253\n" \
  "s_mov_b32 s94, 0\n" \
  "s_lshr_b32 s95, %4, 0\n" \
  "s_and_b32 s95, s95, 7\n" \
  "s_mul_i32 s95, s95, 0x4000\n" \
  "s_add_u32 s94, s94, s95\n" \
  "s_mul_i32 s95, %1, 0x1000\n" \
  "s_add_u32 s94, s94, s95\n" \
  "v_writelane_b32 v255, s94, 36\n" \
  "v_lshlrev_b32 v252, 6, v253\n" \
  "v_add_u32 v252, s94, v252\n" \
  "v_min_u32 v252, L_rare0_%=-L_begin_%=, v252\n" \
  "global_load_dword a0, v252, s[34:35]\n" \
  "v_lshrrev_b32 v252, 3, v253\n" \
  "v_xor_b32 v251, v252, v253\n" \
  "v_and_b32_e64 v251, 7, v251\n" \
  "v_mul_u32_u24 v198, 0x380, v252\n" \
  "v_lshl_add_u32 v198, v251, 4, v198\n" \
  "s_mul_i32 s85, %1, 28672\n" \
  "s_mov_b32 s84, %2\n" \
  "s_lshl_b32 s88, %1, 14\n" \
  "s_add_u32 s88, s88, 0x1c000\n" \
  "s_cmp_lt_u32 %1, 3\n" \
  "s_cselect_b32 s88, s88, 0xc000\n" \
  "s_cmp_gt_u32 %1, 0\n" \
  "s_cselect_b32 s88, s88, 0x1c000\n" \
  "s_add_u32 s88, s88, s84\n" \
  "s_waitcnt lgkmcnt(0)\n" \
  "s_mov_b64 s[56:57], s[12:13]\n" \
  "s_mov_b32 s58, 0\n" \
  "s_mul_i32 s59, %4, 0x1c000\n" \
  "s_add_u32 s58, s58, s59\n" \
  "s_add_u32 s56, s56, s58\n" \
  "s_addc_u32 s57, s57, 0\n" \
  "s_and_b32 s57, s57, 0xffff\n" \
  "s_mov_b32 s58, 0x1c000\n" \
  "s_mov_b32 s59, 0x20000\n" \
  "s_add_u32 s90, s85, 0\n" \
  "s_add_u32 m0, s88, 0\n" \
  "s_nop 0\n" \
  "buffer_load_dwordx4 v198, s[56:59], s90 offen lds\n" \
  "s_add_u32 s91, s85, 0x1c00\n" \
  "s_add_u32 m0, s88, 0x1c00\n" \
  "s_nop 0\n" \
  "buffer_load_dwordx4 v198, s[56:59], s91 offen lds\n" \
  "s_add_u32 m0, s88, 0x380\n" \
  "s_nop 0\n" \
  "buffer_load_dwordx4 v198, s[56:59], s90 offen offset:128 lds\n" \
  "s_add_u32 m0, s88, 0x1f80\n" \
  "s_nop 0\n" \
  "buffer_load_dwordx4 v198, s[56:59], s91 offen offset:128 lds\n" \
  "s_cmp_lt_u32 %1, 2\n" \
  "s_cselect_b64 s[22:23], s[22:23], s[24:25]\n" \
  "s_mov_b64 s[48:49], s[22:23]\n" \
  "s_mov_b32 s50, 0\n" \
  "s_lshr_b32 s51, %3, 2\n" \
  "s_mul_i32 s51, s51, 0x380\n" \
  "s_add_u32 s50, s50, s51\n" \
  "s_add_u32 s48, s48, s50\n" \
  "s_addc_u32 s49, s49, 0\n" \
  "s_and_b32 s49, s49, 0xffff\n" \
  "s_mov_b32 s50, 0x188000\n" \
  "s_lshr_b32 s51, %3, 2\n" \
  "s_mul_i32 s51, s51, 0xfffffc80\n" \
  "s_add_u32 s50, s50, s51\n" \
  "s_mov_b32 s51, 0x20000\n" \
  "s_mov_b32 s72, 0x200\n" \
  "s_lshr_b32 s95, %3, 0\n" \
  "s_and_b32 s95, s95, 1\n" \
  "s_mul_i32 s95, s95, 0x80\n" \
  "s_add_u32 s72, s72, s95\n" \
  "s_mov_b32 s73, 0x300\n" \
  "s_lshr_b32 s95, %3, 0\n" \
  "s_and_b32 s95, s95, 1\n" \
  "s_mul_i32 s95, s95, 64\n" \
  "s_add_u32 s73, s73, s95\n" \
  "s_mov_b32 s94, 0\n" \
  "s_mul_i32 s95, %4, 0x80\n" \
  "s_add_u32 s94, s94, s95\n" \
  "s_lshr_b32 s95, %1, 0\n" \
  "s_and_b32 s95, s95, 1\n" \
  "s_mul_i32 s95, s95, 64\n" \
  "s_add_u32 s94, s94, s95\n" \
  "v_add_u32 v251, s94, v253\n" \
  "v_lshrrev_b32 v183, 2, v251\n" \
  "v_and_b32_e64 v249, 3, v251\n" \
  "v_mul_u32_u24 v183, 0xe00, v183\n" \
  "v_and_b32_e64 v249, 1, v249\n" \
  "v_lshl_add_u32 v183, v249, 6, v183\n" \
  "v_add_u32_e64 v183, s72, v183\n" \
  "s_add_u32 m0, s88, 0x700\n" \
  "s_nop 0\n" \
  "buffer_load_dwordx4 v198, s[56:59], s90 offen offset:256 lds\n" \
  "s_add_u32 m0, s88, 0x2300\n" \
  "s_nop 0\n" \
  "buffer_load_dwordx4 v198, s[56:59], s91 offen offset:256 lds\n" \
  "s_add_u32 m0, s88, 0xa80\n" \
  "s_nop 0\n" \
  "buffer_load_dwordx4 v198, s[56:59], s90 offen offset:384 lds\n" \
  "s_add_u32 m0, s88, 0x2680\n" \
  "s_nop 0\n" \
  "buffer_load_dwordx4 v198, s[56:59], s91 offen offset:384 lds\n" \
  "s_add_u32 m0, s88, 0xe00\n" \
  "s_nop 0\n" \
  "buffer_load_dwordx4 v198, s[56:59], s90 offen offset:512 lds\n" \
  "s_add_u32 m0, s88, 0x2a00\n" \
  "s_nop 0\n" \
  "buffer_load_dwordx4 v198, s[56:59], s91 offen offset:512 lds\n" \
  "s_add_u32 m0, s88, 0x1180\n" \
  "s_nop 0\n" \
  "buffer_load_dwordx4 v198, s[56:59], s90 offen offset:640 lds\n" \
  "s_add_u32 m0, s88, 0x2d80\n" \
  "s_nop 0\n" \
  "buffer_load_dwordx4 v198, s[56:59], s91 offen offset:640 lds\n" \
  "s_add_u32 m0, s88, 0x1500\n" \
  "s_nop 0\n" \
  "buffer_load_dwordx4 v198, s[56:59], s90 offen offset:768 lds\n" \
  "s_add_u32 m0, s88, 0x3100\n" \
  "s_nop 0\n" \
  "buffer_load_dwordx4 v198, s[56:59], s91 offen offset:768 lds\n" \
  "s_lshl_b32 s83, %1, 12\n" \
  "s_add_u32 s75, s83, s84\n" \
  "s_add_u32 s74, s75, 0x10000\n" \
  "s_and_b32 s92, %1, 1\n" \
  "s_lshl_b32 s92, s92, 5\n" \
  "s_lshr_b32 s93, %1, 1\n" \
  "s_lshl_b32 s93, s93, 7\n" \
  "v_and_b32 v249, 2, v252\n" \
  "v_add_u32 v252, s92, v252\n" \
  "v_xor_b32 v249, v249, v253\n" \
  "v_and_b32 v249, 7, v249\n" \
  "v_mul_u32_u24 v251, 0xe00, v252\n" \
  "v_xor_b32_e64 v179, 0, v249\n" \
  "v_lshl_add_u32 v179, v179, 4, v251\n" \
  "v_add_u32 v240, 0, v179\n" \
  "v_xor_b32 v179, 4, v249\n" \
  "v_lshl_add_u32 v179, v179, 4, v251\n" \
  "v_add_u32 v241, 0x6c00, v179\n" \
  "v_xor_b32_e64 v179, 0, v249\n" \
  "v_lshl_add_u32 v179, v179, 4, v251\n" \
  "v_add_u32 v242, 0xd800, v179\n" \
  "v_xor_b32_e64 v179, 4, v249\n" \
  "v_lshl_add_u32 v179, v179, 4, v251\n" \
  "v_add_u32 v243, 0x14400, v179\n" \
  "v_xor_b32 v179, 0, v249\n" \
  "v_and_b32 v179, 4, v179\n" \
  "v_lshl_or_b32 v240, v179, 28, v240\n" \
  "v_xor_b32 v179, 4, v249\n" \
  "v_and_b32 v179, 4, v179\n" \
  "v_lshl_or_b32 v241, v179, 28, v241\n" \
  "v_xor_b32 v179, 0, v249\n" \
  "v_and_b32 v179, 4, v179\n" \
  "v_lshl_or_b32 v242, v179, 28, v242\n" \
  "v_xor_b32 v179, 4, v249\n" \
  "v_and_b32 v179, 4, v179\n" \
  "v_lshl_or_b32 v243, v179, 28, v243\n" \
  "v_mul_u32_u24 v251, 0x2000, v252\n" \
  "v_add_u32 v251, s93, v251\n" \
  "v_xor_b32 v179, 0, v249\n" \
  "v_lshl_add_u32 v179, v179, 4, v251\n" \
  "v_add_u32 v236, 0, v179\n" \
  "v_xor_b32 v179, 4, v249\n" \
  "v_lshl_add_u32 v179, v179, 4, v251\n" \
  "v_add_u32 v237, 0xfc00, v179\n" \
  "v_xor_b32_e64 v179, 0, v249\n" \
  "v_lshl_add_u32 v179, v179, 4, v251\n" \
  "v_add_u32 v238, 0x1f800, v179\n" \
  "v_xor_b32_e64 v179, 4, v249\n" \
  "v_lshl_add_u32 v179, v179, 4, v251\n" \
  "v_add_u32 v239, 0x2f400, v179\n" \
  "s_mov_b64 s[44:45], s[18:19]\n" \
  "s_mov_b32 s46, 0\n" \
  "s_mul_i32 s47, %3, 0x200\n" \
  "s_add_u32 s46, s46, s47\n" \
  "s_add_u32 s44, s44, s46\n" \
  "s_addc_u32 s45, s45, 0\n" \
  "s_and_b32 s45, s45, 0xffff\n" \
  "s_mov_b32 s46, 0x380000\n" \
  "s_mul_i32 s47, %3, 0xfffffe00\n" \
  "s_add_u32 s46, s46, s47\n" \
  "s_mov_b32 s47, 0x20000\n" \
  "v_and_b32 v176, 15, v253\n" \
  "v_lshrrev_b32 v177, 4, v253\n" \
  "v_lshrrev_b32_e64 v251, 3, v176\n" \
  "v_mul_u32_u24 v251, 0x1c00, v251\n" \
  "v_and_b32_e64 v252, 7, v176\n" \
  "v_lshl_add_u32 v251, v252, 7, v251\n" \
  "v_add_u32 v251, s88, v251\n" \
  "v_add_u32 v249, 0, v177\n" \
  "v_xor_b32_e64 v249, v249, v252\n" \
  "v_lshl_add_u32 v204, v249, 4, v251\n" \
  "v_add_u32 v249, 4, v177\n" \
  "v_xor_b32 v249, v249, v252\n" \
  "v_lshl_add_u32 v205, v249, 4, v251\n" \
  "v_mov_b32 v200, 0x3f803f80\n" \
  "v_mov_b32 v201, 0x3f803f80\n" \
  "v_mov_b32 v202, 0x3f803f80\n" \
  "v_mov_b32 v203, 0x3f803f80\n" \
  "v_mov_b32 v192, 0\n" \
  "v_mov_b32 v193, 0\n" \
  "v_mov_b32 v196, 0\n" \
  "v_mov_b32 v197, 0\n" \
  "v_and_b32 v251, 1, v253\n" \
  "v_lshlrev_b32 v251, 4, v251\n" \
  "v_mov_b32 v252, 0x3f80\n" \
  "v_lshlrev_b32_e64 v251, v251, v252\n" \
  "s_mov_b32 s92, 0x300003\n" \
  "s_mov_b32 s93, 0x30000300\n" \
  "v_cndmask_b32 v194, 0, v251, s[92:93]\n" \
  "s_mov_b32 s94, 0xc0000c\n" \
  "s_mov_b32 s95, 0xc0000c00\n" \
  "v_cndmask_b32 v195, 0, v251, s[94:95]\n" \
  "s_mov_b64 s[36:37], s[14:15]\n" \
  "s_mov_b32 s38, 0\n" \
  "s_mul_i32 s39, %3, 0x100000\n" \
  "s_add_u32 s38, s38, s39\n" \
  "s_add_u32 s36, s36, s38\n" \
  "s_addc_u32 s37, s37, 0\n" \
  "s_and_b32 s37, s37, 0xffff\n" \
  "s_mov_b32 s38, 0x100000\n" \
  "s_mov_b32 s39, 0x20000\n" \
  "s_mov_b64 s[40:41], s[16:17]\n" \
  "s_mov_b32 s42, 0\n" \
  "s_mul_i32 s43, %3, 0x100000\n" \
  "s_add_u32 s42, s42, s43\n" \
  "s_add_u32 s40, s40, s42\n" \
  "s_addc_u32 s41, s41, 0\n" \
  "s_and_b32 s41, s41, 0xffff\n" \
  "s_mov_b32 s42, 0x100000\n" \
  "s_mov_b32 s43, 0x20000\n" \
  "s_mov_b64 s[52:53], s[20:21]\n" \
  "s_mov_b32 s54, 0\n" \
  "s_mul_i32 s55, %3, 0x38000\n" \
  "s_add_u32 s54, s54, s55\n" \
  "s_add_u32 s52, s52, s54\n" \
  "s_addc_u32 s53, s53, 0\n" \
  "s_and_b32 s53, s53, 0xffff\n" \
  "s_mov_b32 s54, 0x38000\n" \
  "s_mov_b32 s55, 0x20000\n" \
  "s_mov_b64 s[60:61], s[28:29]\n" \
  "s_mov_b32 s62, 0x1c00000\n" \
  "s_mul_i32 s63, %4, 0x70000\n" \
  "s_add_u32 s62, s62, s63\n" \
  "s_mul_i32 s63, %1, 0x1c000\n" \
  "s_add_u32 s62, s62, s63\n" \
  "s_add_u32 s60, s60, s62\n" \
  "s_addc_u32 s61, s61, 0\n" \
  "s_and_b32 s61, s61, 0xffff\n" \
  "s_mov_b32 s62, 0x1c000\n" \
  "s_mov_b32 s63, 0x20000\n" \
  "s_mov_b64 s[64:65], s[28:29]\n" \
  "s_mov_b32 s66, 0\n" \
  "s_mul_i32 s67, %3, 0x3800\n" \
  "s_add_u32 s66, s66, s67\n" \
  "s_mul_i32 s67, %4, 0x1c0000\n" \
  "s_add_u32 s66, s66, s67\n" \
  "s_mul_i32 s67, %1, 0x70000\n" \
  "s_add_u32 s66, s66, s67\n" \
  "s_add_u32 s64, s64, s66\n" \
  "s_addc_u32 s65, s65, 0\n" \
  "s_and_b32 s65, s65, 0xffff\n" \
  "s_mov_b32 s66, 0x3b800\n" \
  "s_mov_b32 s67, 0x20000\n" \
  "s_mov_b64 s[68:69], s[28:29]\n" \
  "s_mov_b32 s70, 0x2300000\n" \
  "s_add_u32 s68, s68, s70\n" \
  "s_addc_u32 s69, s69, 0\n" \
  "s_and_b32 s69, s69, 0xffff\n" \
  "s_mov_b32 s70, 0x20080\n" \
  "s_mov_b32 s71, 0x20000\n" \
  "s_mov_b32 s78, 0x80\n" \
  "s_mul_i32 s94, %4, 0x2000\n" \
  "s_add_u32 s78, s78, s94\n" \
  "s_mul_i32 s94, %1, 0x800\n" \
  "s_add_u32 s78, s78, s94\n" \
  "s_mul_i32 s94, %3, 0x80\n" \
  "s_add_u32 s78, s78, s94\n" \
  "s_mov_b64 s[92:93], s[26:27]\n" \
  "s_mov_b32 s94, 0\n" \
  "s_lshr_b32 s95, %6, 0\n" \
  "s_and_b32 s95, s95, 7\n" \
  "s_mul_i32 s95, s95, 0x80\n" \
  "s_add_u32 s94, s94, s95\n" \
  "s_add_u32 s92, s92, s94\n" \
  "s_addc_u32 s93, s93, 0\n" \
  "s_and_b32 s93, s93, 0xffff\n" \
  "s_mov_b32 s94, 0x188000\n" \
  "s_lshr_b32 s95, %6, 0\n" \
  "s_and_b32 s95, s95, 7\n" \
  "s_mul_i32 s95, s95, 0xffffff80\n" \
  "s_add_u32 s94, s94, s95\n" \
  "s_mov_b32 s95, 0x20000\n" \
  "s_cmp_lt_u32 %6, 8\n" \
  "s_cselect_b32 s94, s94, 0\n" \
  "v_writelane_b32 v255, s92, 37\n" \
  "v_writelane_b32 v255, s93, 38\n" \
  "v_writelane_b32 v255, s94, 39\n" \
  "s_mov_b64 s[92:93], s[28:29]\n" \
  "s_mov_b32 s94, 0x1c00000\n" \
  "s_mul_i32 s95, %5, 0x38000\n" \
  "s_add_u32 s94, s94, s95\n" \
  "s_lshr_b32 s95, %1, 1\n" \
  "s_mul_i32 s95, s95, 0x1c000\n" \
  "s_add_u32 s94, s94, s95\n" \
  "s_lshr_b32 s95, %1, 0\n" \
  "s_and_b32 s95, s95, 1\n" \
  "s_mul_i32 s95, s95, 0x800\n" \
  "s_add_u32 s94, s94, s95\n" \
  "s_add_u32 s92, s92, s94\n" \
  "s_addc_u32 s93, s93, 0\n" \
  "s_and_b32 s93, s93, 0xffff\n" \
  "s_mov_b32 s94, 0x1c000\n" \
  "s_mov_b32 s95, 0x20000\n" \
  "s_cmp_lt_u32 %6, 8\n" \
  "s_cselect_b32 s94, s94, 0\n" \
  "v_writelane_b32 v255, s92, 40\n" \
  "v_writelane_b32 v255, s93, 41\n" \
  "v_writelane_b32 v255, s94, 42\n" \
  "s_mov_b64 s[92:93], s[28:29]\n" \
  "s_mov_b32 s94, 0\n" \
  "s_mul_i32 s95, %5, 0xe0000\n" \
  "s_add_u32 s94, s94, s95\n" \
  "s_mul_i32 s95, %1, 0x38000\n" \
  "s_add_u32 s94, s94, s95\n" \
  "s_lshr_b32 s95, %6, 0\n" \
  "s_and_b32 s95, s95, 7\n" \
  "s_mul_i32 s95, s95, 0x800\n" \
  "s_add_u32 s94, s94, s95\n" \
  "s_add_u32 s92, s92, s94\n" \
  "s_addc_u32 s93, s93, 0\n" \
  "s_and_b32 s93, s93, 0xffff\n" \
  "s_mov_b32 s94, 0x35000\n" \
  "s_mov_b32 s95, 0x20000\n" \
  "s_cmp_lt_u32 %6, 8\n" \
  "s_cselect_b32 s94, s94, 0\n" \
  "v_writelane_b32 v255, s92, 43\n" \
  "v_writelane_b32 v255, s93, 44\n" \
  "v_writelane_b32 v255, s94, 45\n" \
  "s_mov_b64 s[92:93], s[12:13]\n" \
  "s_mov_b32 s94, 0\n" \
  "s_mul_i32 s95, %5, 0xe000\n" \
  "s_add_u32 s94, s94, s95\n" \
  "s_mul_i32 s95, %1, 0x3800\n" \
  "s_add_u32 s94, s94, s95\n" \
  "s_lshr_b32 s95, %6, 0\n" \
  "s_and_b32 s95, s95, 7\n" \
  "s_mul_i32 s95, s95, 0x80\n" \
  "s_add_u32 s94, s94, s95\n" \
  "s_add_u32 s92, s92, s94\n" \
  "s_addc_u32 s93, s93, 0\n" \
  "s_and_b32 s93, s93, 0xffff\n" \
  "s_mov_b32 s94, 0x3500\n" \
  "s_mov_b32 s95, 0x20000\n" \
  "s_cmp_lt_u32 %6, 8\n" \
  "s_cselect_b32 s94, s94, 0\n" \
  "v_writelane_b32 v255, s92, 46\n" \
  "v_writelane_b32 v255, s93, 47\n" \
  "v_writelane_b32 v255, s94, 48\n" \
  "s_mov_b64 s[92:93], s[30:31]\n" \
  "s_mov_b32 s94, 0\n" \
  "s_mul_i32 s95, %5, 0xe000\n" \
  "s_add_u32 s94, s94, s95\n" \
  "s_mul_i32 s95, %1, 0x3800\n" \
  "s_add_u32 s94, s94, s95\n" \
  "s_lshr_b32 s95, %6, 0\n" \
  "s_and_b32 s95, s95, 7\n" \
  "s_mul_i32 s95, s95, 0x80\n" \
  "s_add_u32 s94, s94, s95\n" \
  "s_add_u32 s92, s92, s94\n" \
  "s_addc_u32 s93, s93, 0\n" \
  "s_and_b32 s93, s93, 0xffff\n" \
  "s_mov_b32 s94, 0x3500\n" \
  "s_mov_b32 s95, 0x20000\n" \
  "s_cmp_lt_u32 %6, 8\n" \
  "s_cselect_b32 s94, s94, 0\n" \
  "v_writelane_b32 v255, s92, 49\n" \
  "v_writelane_b32 v255, s93, 50\n" \
  "v_writelane_b32 v255, s94, 51\n" \
  "s_mov_b32 s94, 0x80\n" \
  "s_mul_i32 s95, %5, 0x1000\n" \
  "s_add_u32 s94, s94, s95\n" \
  "s_lshr_b32 s95, %1, 1\n" \
  "s_mul_i32 s95, s95, 0x800\n" \
  "s_add_u32 s94, s94, s95\n" \
  "v_writelane_b32 v255, s94, 52\n" \
  "s_mov_b32 s94, 0\n" \
  "s_mul_i32 s95, %3, 0x19000\n" \
  "s_add_u32 s94, s94, s95\n" \
  "s_mul_i32 s95, %4, 0x1900\n" \
  "s_add_u32 s94, s94, s95\n" \
  "s_mul_i32 s95, %1, 0x640\n" \
  "s_add_u32 s94, s94, s95\n" \
  "v_writelane_b32 v255, s94, 53\n" \
  "s_mul_i32 s33, %3, 0\n" \
  "s_mov_b32 s76, 0x40004000\n" \
  "s_lshl_b32 s77, %1, 3\n" \
  "v_lshlrev_b32 v250, 4, v253\n" \
  "v_lshlrev_b32 v254, 24, v253\n" \
  "v_bfe_u32 v251, v253, 2, 2\n" \
  "v_lshl_add_u32 v251, v177, 3, v251\n" \
  "v_lshlrev_b32 v251, 7, v251\n" \
  "v_and_b32 v252, 3, v253\n" \
  "v_lshl_add_u32 v251, v252, 3, v251\n" \
  "v_add_u32_e64 v251, s84, v251\n" \
  "v_bfe_u32 v252, v253, 3, 2\n" \
  "v_xor_b32_e64 v249, 0, v252\n" \
  "v_lshl_add_u32 v232, v249, 5, v251\n" \
  "v_add_u32 v228, 0x10000, v232\n" \
  "v_xor_b32_e64 v249, 1, v252\n" \
  "v_lshl_add_u32 v233, v249, 5, v251\n" \
  "v_add_u32 v229, 0x10000, v233\n" \
  "v_xor_b32_e64 v249, 2, v252\n" \
  "v_lshl_add_u32 v234, v249, 5, v251\n" \
  "v_add_u32 v230, 0x10000, v234\n" \
  "v_xor_b32_e64 v249, 3, v252\n" \
  "v_lshl_add_u32 v235, v249, 5, v251\n" \
  "v_add_u32 v231, 0x10000, v235\n" \
  "v_lshrrev_b32 v178, 5, v253\n" \
  "v_lshlrev_b32 v252, 9, v178\n" \
  "v_add_u32 v252, s83, v252\n" \
  "v_add_u32 v249, 0, v178\n" \
  "v_add_u32 v249, s77, v249\n" \
  "v_and_b32 v249, 15, v249\n" \
  "v_xor_b32 v249, v249, v253\n" \
  "v_and_b32 v249, 31, v249\n" \
  "v_lshl_add_u32 v220, v249, 4, v252\n" \
  "v_add_u32 v249, 0, v178\n" \
  "v_lshlrev_b32 v249, 1, v249\n" \
  "v_xor_b32 v249, v249, v253\n" \
  "v_and_b32 v249, 31, v249\n" \
  "v_lshl_add_u32 v224, v249, 4, v252\n" \
  "v_add_u32 v249, 2, v178\n" \
  "v_add_u32 v249, s77, v249\n" \
  "v_and_b32 v249, 15, v249\n" \
  "v_xor_b32 v249, v249, v253\n" \
  "v_and_b32_e64 v249, 31, v249\n" \
  "v_lshl_add_u32 v221, v249, 4, v252\n" \
  "v_add_u32 v249, 2, v178\n" \
  "v_lshlrev_b32 v249, 1, v249\n" \
  "v_xor_b32 v249, v249, v253\n" \
  "v_and_b32 v249, 31, v249\n" \
  "v_lshl_add_u32 v225, v249, 4, v252\n" \
  "v_add_u32 v249, 4, v178\n" \
  "v_add_u32 v249, s77, v249\n" \
  "v_and_b32 v249, 15, v249\n" \
  "v_xor_b32 v249, v249, v253\n" \
  "v_and_b32_e64 v249, 31, v249\n" \
  "v_lshl_add_u32 v222, v249, 4, v252\n" \
  "v_add_u32 v249, 4, v178\n" \
  "v_lshlrev_b32 v249, 1, v249\n" \
  "v_xor_b32 v249, v249, v253\n" \
  "v_and_b32 v249, 31, v249\n" \
  "v_lshl_add_u32 v226, v249, 4, v252\n" \
  "v_add_u32 v249, 6, v178\n" \
  "v_add_u32 v249, s77, v249\n" \
  "v_and_b32 v249, 15, v249\n" \
  "s_add_u32 s86, s73, 0\n" \
  "s_add_u32 m0, s74, 0\n" \
  "s_nop 0\n" \
  "buffer_load_dwordx4 v240, s[48:51], s86 offen lds\n" \
  "v_xor_b32 v249, v249, v253\n" \
  "v_and_b32 v249, 31, v249\n" \
  "v_lshl_add_u32 v223, v249, 4, v252\n" \
  "v_add_u32 v249, 6, v178\n" \
  "v_lshlrev_b32 v249, 1, v249\n" \
  "v_xor_b32 v249, v249, v253\n" \
  "v_and_b32 v249, 31, v249\n" \
  "v_lshl_add_u32 v227, v249, 4, v252\n" \
  "v_lshlrev_b32 v252, 9, v176\n" \
  "v_add_u32 v252, s84, v252\n" \
  "v_add_u32 v252, 0x10000, v252\n" \
  "v_add_u32 v249, 0, v177\n" \
  "v_xor_b32 v249, v249, v176\n" \
  "v_lshl_add_u32 v208, v249, 4, v252\n" \
  "v_add_u32 v249, 4, v177\n" \
  "v_xor_b32 v249, v249, v176\n" \
  "v_lshl_add_u32 v209, v249, 4, v252\n" \
  "v_add_u32 v249, 8, v177\n" \
  "v_xor_b32 v249, v249, v176\n" \
  "v_lshl_add_u32 v210, v249, 4, v252\n" \
  "v_add_u32 v249, 12, v177\n" \
  "v_xor_b32 v249, v249, v176\n" \
  "v_lshl_add_u32 v211, v249, 4, v252\n" \
  "v_lshrrev_b32 v251, 2, v253\n" \
  "v_lshlrev_b32 v252, 9, v251\n" \
  "v_and_b32 v251, 7, v251\n" \
  "v_and_b32 v249, 3, v253\n" \
  "v_lshl_add_u32 v252, v249, 3, v252\n" \
  "v_add_u32_e64 v252, s84, v252\n" \
  "v_add_u32 v252, 0x10000, v252\n" \
  "v_xor_b32_e64 v249, 0, v251\n" \
  "v_lshl_add_u32 v212, v249, 5, v252\n" \
  "v_xor_b32_e64 v249, 1, v251\n" \
  "v_lshl_add_u32 v213, v249, 5, v252\n" \
  "v_xor_b32_e64 v249, 2, v251\n" \
  "v_lshl_add_u32 v214, v249, 5, v252\n" \
  "v_xor_b32_e64 v249, 3, v251\n" \
  "v_lshl_add_u32 v215, v249, 5, v252\n" \
  "v_xor_b32_e64 v249, 4, v251\n" \
  "v_lshl_add_u32 v216, v249, 5, v252\n" \
  "buffer_load_dwordx4 v241, s[48:51], s86 offen offset:1024 lds\n" \
  "v_xor_b32_e64 v249, 5, v251\n" \
  "v_lshl_add_u32 v217, v249, 5, v252\n" \
  "v_xor_b32_e64 v249, 6, v251\n" \
  "v_lshl_add_u32 v218, v249, 5, v252\n" \
  "v_xor_b32_e64 v249, 7, v251\n" \
  "v_lshl_add_u32 v219, v249, 5, v252\n" \
  "s_mov_b32 s94, 0\n" \
  "s_mul_i32 s95, %4, 0x4000\n" \
  "s_add_u32 s94, s94, s95\n" \
  "s_mul_i32 s95, %1, 0x1000\n" \
  "s_add_u32 s94, s94, s95\n" \
  "v_writelane_b32 v255, s94, 54\n" \
  "s_lshr_b32 s94, s94, 6\n" \
  "v_add_u32 v251, s94, v253\n" \
  "v_lshrrev_b32 v252, 3, v251\n" \
  "v_and_b32 v249, 7, v251\n" \
  "v_lshlrev_b32 v252, 13, v252\n" \
  "v_lshl_add_u32 v199, v249, 6, v252\n" \
  "buffer_load_dwordx4 v242, s[48:51], s86 offen offset:2048 lds\n" \
  "buffer_load_dwordx4 v243, s[48:51], s86 offen offset:3072 lds\n" \
  "s_waitcnt vmcnt(16)\n" \
  "ds_read_b128 v[0:3], v204\n" \
  "ds_read_b128 v[4:7], v205\n" \
  "s_waitcnt lgkmcnt(0)\n" \
  "s_add_u32 s90, s85, 0x3800\n" \
  "s_add_u32 m0, s88, 0\n" \
  "s_nop 0\n" \
  "buffer_load_dwordx4 v198, s[56:59], s90 offen lds\n" \
  "s_add_u32 s91, s85, 0x5400\n" \
  "s_add_u32 m0, s88, 0x1c00\n" \
  "s_nop 0\n" \
  "buffer_load_dwordx4 v198, s[56:59], s91 offen lds\n" \
  "s_waitcnt vmcnt(16)\n" \
  "ds_read_b128 v[8:11], v204 offset:1024\n" \
  "ds_read_b128 v[12:15], v205 offset:1024\n" \
  "v_mfma_f32_16x16x32_bf16 v[56:59], v[200:203], v[0:3], 0\n" \
  "v_mfma_f32_16x16x32_bf16 v[60:63], v[0:3], v[0:3], 0\n" \
  "v_mfma_f32_16x16x32_bf16 v[56:59], v[200:203], v[4:7], v[56:59]\n" \
  "v_mfma_f32_16x16x32_bf16 v[60:63], v[4:7], v[4:7], v[60:63]\n" \
  "s_waitcnt lgkmcnt(0)\n" \
  "s_add_u32 m0, s88, 0x380\n" \
  "s_nop 0\n" \
  "buffer_load_dwordx4 v198, s[56:59], s90 offen offset:128 lds\n" \
  "s_add_u32 m0, s88, 0x1f80\n" \
  "s_nop 0\n" \
  "buffer_load_dwordx4 v198, s[56:59], s91 offen offset:128 lds\n" \
  "s_waitcnt vmcnt(16)\n" \
  "ds_read_b128 v[16:19], v204 offset:2048\n" \
  "ds_read_b128 v[20:23], v205 offset:2048\n" \
  "v_mfma_f32_16x16x32_bf16 v[56:59], v[200:203], v[8:11], v[56:59]\n" \
  "v_mfma_f32_16x16x32_bf16 v[60:63], v[8:11], v[8:11], v[60:63]\n" \
  "v_mfma_f32_16x16x32_bf16 v[56:59], v[200:203], v[12:15], v[56:59]\n" \
  "v_mfma_f32_16x16x32_bf16 v[60:63], v[12:15], v[12:15], v[60:63]\n" \
  "s_waitcnt lgkmcnt(0)\n" \
  "s_add_u32 m0, s88, 0x700\n" \
  "s_nop 0\n" \
  "buffer_load_dwordx4 v198, s[56:59], s90 offen offset:256 lds\n" \
  "s_add_u32 m0, s88, 0x2300\n" \
  "s_nop 0\n" \
  "buffer_load_dwordx4 v198, s[56:59], s91 offen offset:256 lds\n" \
  "s_waitcnt vmcnt(16)\n" \
  "ds_read_b128 v[24:27], v204 offset:3072\n" \
  "ds_read_b128 v[28:31], v205 offset:3072\n" \
  "v_mfma_f32_16x16x32_bf16 v[56:59], v[200:203], v[16:19], v[56:59]\n" \
  "v_mfma_f32_16x16x32_bf16 v[60:63], v[16:19], v[16:19], v[60:63]\n" \
  "v_mfma_f32_16x16x32_bf16 v[56:59], v[200:203], v[20:23], v[56:59]\n" \
  "v_mfma_f32_16x16x32_bf16 v[60:63], v[20:23], v[20:23], v[60:63]\n" \
  "s_waitcnt lgkmcnt(0)\n" \
  "s_add_u32 m0, s88, 0xa80\n" \
  "s_nop 0\n" \
  "buffer_load_dwordx4 v198, s[56:59], s90 offen offset:384 lds\n" \
  "s_add_u32 m0, s88, 0x2680\n" \
  "s_nop 0\n" \
  "buffer_load_dwordx4 v198, s[56:59], s91 offen offset:384 lds\n" \
  "s_waitcnt vmcnt(16)\n" \
  "ds_read_b128 v[32:35], v204 offset:4096\n" \
  "ds_read_b128 v[36:39], v205 offset:4096\n" \
  "v_mfma_f32_16x16x32_bf16 v[56:59], v[200:203], v[24:27], v[56:59]\n" \
  "v_mfma_f32_16x16x32_bf16 v[60:63], v[24:27], v[24:27], v[60:63]\n" \
  "v_mfma_f32_16x16x32_bf16 v[56:59], v[200:203], v[28:31], v[56:59]\n" \
  "v_mfma_f32_16x16x32_bf16 v[60:63], v[28:31], v[28:31], v[60:63]\n" \
  "s_waitcnt lgkmcnt(0)\n" \
  "s_add_u32 m0, s88, 0xe00\n" \
  "s_nop 0\n" \
  "buffer_load_dwordx4 v198, s[56:59], s90 offen offset:512 lds\n" \
  "s_add_u32 m0, s88, 0x2a00\n" \
  "s_nop 0\n" \
  "buffer_load_dwordx4 v198, s[56:59], s91 offen offset:512 lds\n" \
  "s_waitcnt vmcnt(16)\n" \
  "ds_read_b128 v[40:43], v204 offset:5120\n" \
  "ds_read_b128 v[44:47], v205 offset:5120\n" \
  "v_mfma_f32_16x16x32_bf16 v[56:59], v[200:203], v[32:35], v[56:59]\n" \
  "v_mfma_f32_16x16x32_bf16 v[60:63], v[32:35], v[32:35], v[60:63]\n" \
  "v_mfma_f32_16x16x32_bf16 v[56:59], v[200:203], v[36:39], v[56:59]\n" \
  "v_mfma_f32_16x16x32_bf16 v[60:63], v[36:39], v[36:39], v[60:63]\n" \
  "s_waitcnt lgkmcnt(0)\n" \
  "s_add_u32 m0, s88, 0x1180\n" \
  "s_nop 0\n" \
  "buffer_load_dwordx4 v198, s[56:59], s90 offen offset:640 lds\n" \
  "s_add_u32 m0, s88, 0x2d80\n" \
  "s_nop 0\n" \
  "buffer_load_dwordx4 v198, s[56:59], s91 offen offset:640 lds\n" \
  "s_waitcnt vmcnt(16)\n" \
  "ds_read_b128 v[48:51], v204 offset:6144\n" \
  "ds_read_b128 v[52:55], v205 offset:6144\n" \
  "v_mfma_f32_16x16x32_bf16 v[56:59], v[200:203], v[40:43], v[56:59]\n" \
  "v_mfma_f32_16x16x32_bf16 v[60:63], v[40:43], v[40:43], v[60:63]\n" \
  "v_mfma_f32_16x16x32_bf16 v[56:59], v[200:203], v[44:47], v[56:59]\n" \
  "v_mfma_f32_16x16x32_bf16 v[60:63], v[44:47], v[44:47], v[60:63]\n" \
  "s_waitcnt lgkmcnt(0)\n" \
  "s_add_u32 m0, s88, 0x1500\n" \
  "s_nop 0\n" \
  "buffer_load_dwordx4 v198, s[56:59], s90 offen offset:768 lds\n" \
  "s_add_u32 m0, s88, 0x3100\n" \
  "s_nop 0\n" \
  "buffer_load_dwordx4 v198, s[56:59], s91 offen offset:768 lds\n" \
  "v_mfma_f32_16x16x32_bf16 v[56:59], v[200:203], v[48:51], v[56:59]\n" \
  "v_mfma_f32_16x16x32_bf16 v[60:63], v[48:51], v[48:51], v[60:63]\n" \
  "v_mfma_f32_16x16x32_bf16 v[56:59], v[200:203], v[52:55], v[56:59]\n" \
  "v_mfma_f32_16x16x32_bf16 v[60:63], v[52:55], v[52:55], v[60:63]\n" \
  "buffer_load_dword a2, v183, s[48:51], 0 offen\n" \
  "s_mov_b32 s88, 0x3000\n" \
  "s_lshr_b32 s95, %3, 2\n" \
  "s_mul_i32 s95, s95, 0x3800\n" \
  "s_add_u32 s88, s88, s95\n" \
  "s_lshr_b32 s95, %3, 0\n" \
  "s_and_b32 s95, s95, 1\n" \
  "s_mul_i32 s95, s95, 0x400\n" \
  "s_add_u32 s88, s88, s95\n" \
  "s_add_u32 s90, s88, 0xe000\n" \
  "s_mov_b32 s81, 0x2000\n" \
  "s_lshr_b32 s95, %3, 2\n" \
  "s_mul_i32 s95, s95, 0x3800\n" \
  "s_add_u32 s81, s81, s95\n" \
  "s_lshr_b32 s95, %3, 0\n" \
  "s_and_b32 s95, s95, 1\n" \
  "s_mul_i32 s95, s95, 0x800\n" \
  "s_add_u32 s81, s81, s95\n" \
  "s_add_u32 s82, s81, 0xe000\n" \
  "v_mul_f32 v56, 0xbb124925, v56\n" \
  "v_mul_f32 v57, 0xbb124925, v57\n" \
  "v_mul_f32 v58, 0xbb124925, v58\n" \
  "v_mul_f32 v59, 0xbb124925, v59\n" \
  "s_nop 1\n" \
  "v_mfma_f32_16x16x32_bf16 v[176:179], v[194:197], v[0:3], v[56:59]\n" \
  "v_mfma_f32_16x16x32_bf16 v[180:183], v[192:195], v[0:3], v[56:59]\n" \
  "s_mov_b32 s92, 0x100001\n" \
  "s_mov_b32 s93, 0x10000100\n" \
  "v_cndmask_b32 v251, 0, v60, s[92:93]\n" \
  "s_lshl_b64 s[92:93], s[92:93], 1\n" \
  "v_cndmask_b32 v251, v251, v61, s[92:93]\n" \
  "s_lshl_b64 s[92:93], s[92:93], 1\n" \
  "v_cndmask_b32 v251, v251, v62, s[92:93]\n" \
  "s_lshl_b64 s[92:93], s[92:93], 1\n" \
  "v_cndmask_b32 v251, v251, v63, s[92:93]\n" \
  "v_mov_b32 v252, v251\n" \
  "s_nop 1\n" \
  "v_permlane32_swap_b32 v251, v252\n" \
  "v_add_f32 v251, v251, v252\n" \
  "v_mov_b32 v252, v251\n" \
  "s_nop 1\n" \
  "v_permlane16_swap_b32 v251, v252\n" \
  "v_add_f32_e64 v251, v251, v252\n" \
  "v_mul_f32 v251, 0x3b124925, v251\n" \
  "v_fma_f32 v251, -v56, v56, v251\n" \
  "v_max_f32_e64 v251, 0, v251\n" \
  "v_add_f32 v246, 0x3727c5ac, v251\n" \
  "v_rsq_f32 v251, v246\n" \
  "s_nop 0\n" \
  "v_mul_f32 v244, 0xbfb8aa3b, v251\n" \
  "v_mul_f32 v248, 0x3db8aa3b, v251\n" \
  "v_mfma_f32_16x16x32_bf16 v[184:187], v[194:197], v[4:7], v[56:59]\n" \
  "v_mfma_f32_16x16x32_bf16 v[188:191], v[192:195], v[4:7], v[56:59]\n" \
  "s_add_u32 s87, s73, 0x38000\n" \
  "s_add_u32 m0, s75, 0\n" \
  "s_nop 0\n" \
  "buffer_load_dwordx4 v240, s[48:51], s87 offen lds\n" \
  "v_cvt_pk_bf16_f32 v128, v176, v177\n" \
  "v_cvt_pk_bf16_f32 v129, v178, v179\n" \
  "v_cvt_pk_bf16_f32 v130, v180, v181\n" \
  "v_cvt_pk_bf16_f32 v131, v182, v183\n" \
  "v_mfma_f32_16x16x32_bf16 v[176:179], v[194:197], v[8:11], v[56:59]\n" \
  "v_mfma_f32_16x16x32_bf16 v[180:183], v[192:195], v[8:11], v[56:59]\n" \
  "buffer_load_dwordx4 v241, s[48:51], s87 offen offset:1024 lds\n" \
  "v_cvt_pk_bf16_f32 v136, v184, v185\n" \
  "v_cvt_pk_bf16_f32 v137, v186, v187\n" \
  "v_cvt_pk_bf16_f32 v138, v188, v189\n" \
  "v_cvt_pk_bf16_f32 v139, v190, v191\n" \
  "v_mfma_f32_16x16x32_bf16 v[184:187], v[194:197], v[12:15], v[56:59]\n" \
  "v_mfma_f32_16x16x32_bf16 v[188:191], v[192:195], v[12:15], v[56:59]\n" \
  "buffer_load_dwordx4 v242, s[48:51], s87 offen offset:2048 lds\n" \
  "v_cvt_pk_bf16_f32 v144, v176, v177\n" \
  "v_cvt_pk_bf16_f32 v145, v178, v179\n" \
  "v_cvt_pk_bf16_f32 v146, v180, v181\n" \
  "v_cvt_pk_bf16_f32 v147, v182, v183\n" \
  "v_mfma_f32_16x16x32_bf16 v[176:179], v[194:197], v[16:19], v[56:59]\n" \
  "v_mfma_f32_16x16x32_bf16 v[180:183], v[192:195], v[16:19], v[56:59]\n" \
  "buffer_load_dwordx4 v243, s[48:51], s87 offen offset:3072 lds\n" \
  "v_cvt_pk_bf16_f32 v152, v184, v185\n" \
  "v_cvt_pk_bf16_f32 v153, v186, v187\n" \
  "v_cvt_pk_bf16_f32 v154, v188, v189\n" \
  "v_cvt_pk_bf16_f32 v155, v190, v191\n" \
  "v_mfma_f32_16x16x32_bf16 v[184:187], v[194:197], v[20:23], v[56:59]\n" \
  "v_mfma_f32_16x16x32_bf16 v[188:191], v[192:195], v[20:23], v[56:59]\n" \
  "v_mov_b32 v206, 0x2300000\n" \
  "global_load_dwordx2 v[206:207], v206, s[28:29] sc1\n" \
  "v_cvt_pk_bf16_f32 v160, v176, v177\n" \
  "v_cvt_pk_bf16_f32 v161, v178, v179\n" \
  "v_cvt_pk_bf16_f32 v162, v180, v181\n" \
  "v_cvt_pk_bf16_f32 v163, v182, v183\n" \
  "v_mfma_f32_16x16x32_bf16 v[176:179], v[194:197], v[24:27], v[56:59]\n" \
  "v_mfma_f32_16x16x32_bf16 v[180:183], v[192:195], v[24:27], v[56:59]\n" \
  "s_add_u32 s86, s73, 0x70000\n" \
  "s_add_u32 m0, s74, 0x4000\n" \
  "s_nop 0\n" \
  "buffer_load_dwordx4 v240, s[48:51], s86 offen lds\n" \
  "v_cvt_pk_bf16_f32 v168, v184, v185\n" \
  "v_cvt_pk_bf16_f32 v169, v186, v187\n" \
  "v_cvt_pk_bf16_f32 v170, v188, v189\n" \
  "v_cvt_pk_bf16_f32 v171, v190, v191\n" \
  "v_mfma_f32_16x16x32_bf16 v[184:187], v[194:197], v[28:31], v[56:59]\n" \
  "v_mfma_f32_16x16x32_bf16 v[188:191], v[192:195], v[28:31], v[56:59]\n" \
  "buffer_load_dwordx4 v241, s[48:51], s86 offen offset:1024 lds\n" \
  "v_cvt_pk_bf16_f32 v176, v176, v177\n" \
  "v_cvt_pk_bf16_f32 v178, v178, v179\n" \
  "v_cvt_pk_bf16_f32 v180, v180, v181\n" \
  "v_cvt_pk_bf16_f32 v182, v182, v183\n" \
  "v_accvgpr_write_b32 a192, v176\n" \
  "v_accvgpr_write_b32 a193, v178\n" \
  "v_accvgpr_write_b32 a194, v180\n" \
  "v_accvgpr_write_b32 a195, v182\n" \
  "v_mfma_f32_16x16x32_bf16 v[176:179], v[194:197], v[32:35], v[56:59]\n" \
  "v_mfma_f32_16x16x32_bf16 v[180:183], v[192:195], v[32:35], v[56:59]\n" \
  "buffer_load_dwordx4 v242, s[48:51], s86 offen offset:2048 lds\n" \
  "v_cvt_pk_bf16_f32 v184, v184, v185\n" \
  "v_cvt_pk_bf16_f32 v186, v186, v187\n" \
  "v_cvt_pk_bf16_f32 v188, v188, v189\n" \
  "v_cvt_pk_bf16_f32 v190, v190, v191\n" \
  "v_accvgpr_write_b32 a200, v184\n" \
  "v_accvgpr_write_b32 a201, v186\n" \
  "v_accvgpr_write_b32 a202, v188\n" \
  "v_accvgpr_write_b32 a203, v190\n" \
  "v_mfma_f32_16x16x32_bf16 v[184:187], v[194:197], v[36:39], v[56:59]\n" \
  "v_mfma_f32_16x16x32_bf16 v[188:191], v[192:195], v[36:39], v[56:59]\n" \
  "buffer_load_dwordx4 v243, s[48:51], s86 offen offset:3072 lds\n" \
  "v_cvt_pk_bf16_f32 v176, v176, v177\n" \
  "v_cvt_pk_bf16_f32 v178, v178, v179\n" \
  "v_cvt_pk_bf16_f32 v180, v180, v181\n" \
  "v_cvt_pk_bf16_f32 v182, v182, v183\n" \
  "v_accvgpr_write_b32 a208, v176\n" \
  "v_accvgpr_write_b32 a209, v178\n" \
  "v_accvgpr_write_b32 a210, v180\n" \
  "v_accvgpr_write_b32 a211, v182\n" \
  "v_mfma_f32_16x16x32_bf16 v[176:179], v[194:197], v[40:43], v[56:59]\n" \
  "v_mfma_f32_16x16x32_bf16 v[180:183], v[192:195], v[40:43], v[56:59]\n" \
  "s_add_u32 s87, s73, 0xa8000\n" \
  "s_add_u32 m0, s75, 0x4000\n" \
  "s_nop 0\n" \
  "buffer_load_dwordx4 v240, s[48:51], s87 offen lds\n" \
  "v_cvt_pk_bf16_f32 v184, v184, v185\n" \
  "v_cvt_pk_bf16_f32 v186, v186, v187\n" \
  "v_cvt_pk_bf16_f32 v188, v188, v189\n" \
  "v_cvt_pk_bf16_f32 v190, v190, v191\n" \
  "v_accvgpr_write_b32 a216, v184\n" \
  "v_accvgpr_write_b32 a217, v186\n" \
  "v_accvgpr_write_b32 a218, v188\n" \
  "v_accvgpr_write_b32 a219, v190\n" \
  "v_mfma_f32_16x16x32_bf16 v[184:187], v[194:197], v[44:47], v[56:59]\n" \
  "v_mfma_f32_16x16x32_bf16 v[188:191], v[192:195], v[44:47], v[56:59]\n" \
  "buffer_load_dwordx4 v241, s[48:51], s87 offen offset:1024 lds\n" \
  "v_cvt_pk_bf16_f32 v176, v176, v177\n" \
  "v_cvt_pk_bf16_f32 v178, v178, v179\n" \
  "v_cvt_pk_bf16_f32 v180, v180, v181\n" \
  "v_cvt_pk_bf16_f32 v182, v182, v183\n" \
  "v_accvgpr_write_b32 a224, v176\n" \
  "v_accvgpr_write_b32 a225, v178\n" \
  "v_accvgpr_write_b32 a226, v180\n" \
  "v_accvgpr_write_b32 a227, v182\n" \
  "v_mfma_f32_16x16x32_bf16 v[176:179], v[194:197], v[48:51], v[56:59]\n" \
  "v_mfma_f32_16x16x32_bf16 v[180:183], v[192:195], v[48:51], v[56:59]\n" \
  "buffer_load_dwordx4 v242, s[48:51], s87 offen offset:2048 lds\n" \
  "v_cvt_pk_bf16_f32 v184, v184, v185\n" \
  "v_cvt_pk_bf16_f32 v186, v186, v187\n" \
  "v_cvt_pk_bf16_f32 v188, v188, v189\n" \
  "v_cvt_pk_bf16_f32 v190, v190, v191\n" \
  "v_accvgpr_write_b32 a232, v184\n" \
  "v_accvgpr_write_b32 a233, v186\n" \
  "v_accvgpr_write_b32 a234, v188\n" \
  "v_accvgpr_write_b32 a235, v190\n" \
  "v_mfma_f32_16x16x32_bf16 v[184:187], v[194:197], v[52:55], v[56:59]\n" \
  "v_mfma_f32_16x16x32_bf16 v[188:191], v[192:195], v[52:55], v[56:59]\n" \
  "buffer_load_dwordx4 v243, s[48:51], s87 offen offset:3072 lds\n" \
  "v_cvt_pk_bf16_f32 v176, v176, v177\n" \
  "v_cvt_pk_bf16_f32 v178, v178, v179\n" \
  "v_cvt_pk_bf16_f32 v180, v180, v181\n" \
  "v_cvt_pk_bf16_f32 v182, v182, v183\n" \
  "v_accvgpr_write_b32 a240, v176\n" \
  "v_accvgpr_write_b32 a241, v178\n" \
  "v_accvgpr_write_b32 a242, v180\n" \
  "v_accvgpr_write_b32 a243, v182\n" \
  "s_add_u32 s86, s73, 0xe0000\n" \
  "s_add_u32 m0, s74, 0x8000\n" \
  "s_nop 0\n" \
  "buffer_load_dwordx4 v240, s[48:51], s86 offen lds\n" \
  "v_cvt_pk_bf16_f32 v184, v184, v185\n" \
  "v_cvt_pk_bf16_f32 v186, v186, v187\n" \
  "v_cvt_pk_bf16_f32 v188, v188, v189\n" \
  "v_cvt_pk_bf16_f32 v190, v190, v191\n" \
  "v_accvgpr_write_b32 a248, v184\n" \
  "v_accvgpr_write_b32 a249, v186\n" \
  "v_accvgpr_write_b32 a250, v188\n" \
  "v_accvgpr_write_b32 a251, v190\n" \
  "s_waitcnt vmcnt(27)\n" \
  "ds_read_b128 v[64:67], v204\n" \
  "ds_read_b128 v[68:71], v205\n" \
  "buffer_load_dwordx4 v241, s[48:51], s86 offen offset:1024 lds\n" \
  "s_waitcnt vmcnt(26)\n" \
  "ds_read_b128 v[72:75], v204 offset:1024\n" \
  "ds_read_b128 v[76:79], v205 offset:1024\n" \
  "buffer_load_dwordx4 v242, s[48:51], s86 offen offset:2048 lds\n" \
  "s_waitcnt lgkmcnt(2)\n" \
  "v_mfma_f32_16x16x32_bf16 v[120:123], v[200:203], v[64:67], 0\n" \
  "v_mfma_f32_16x16x32_bf16 v[124:127], v[64:67], v[64:67], 0\n" \
  "v_mfma_f32_16x16x32_bf16 v[120:123], v[200:203], v[68:71], v[120:123]\n" \
  "v_mfma_f32_16x16x32_bf16 v[124:127], v[68:71], v[68:71], v[124:127]\n" \
  "s_waitcnt vmcnt(25)\n" \
  "ds_read_b128 v[80:83], v204 offset:2048\n" \
  "ds_read_b128 v[84:87], v205 offset:2048\n" \
  "buffer_load_dwordx4 v243, s[48:51], s86 offen offset:3072 lds\n" \
  "s_waitcnt lgkmcnt(2)\n" \
  "v_mfma_f32_16x16x32_bf16 v[120:123], v[200:203], v[72:75], v[120:123]\n" \
  "v_mfma_f32_16x16x32_bf16 v[124:127], v[72:75], v[72:75], v[124:127]\n" \
  "v_mfma_f32_16x16x32_bf16 v[120:123], v[200:203], v[76:79], v[120:123]\n" \
  "v_mfma_f32_16x16x32_bf16 v[124:127], v[76:79], v[76:79], v[124:127]\n" \
  "s_waitcnt vmcnt(24)\n" \
  "ds_read_b128 v[88:91], v204 offset:3072\n" \
  "ds_read_b128 v[92:95], v205 offset:3072\n" \
  "s_add_u32 s87, s73, 0x118000\n" \
  "s_add_u32 m0, s75, 0x8000\n" \
  "s_nop 0\n" \
  "buffer_load_dwordx4 v240, s[48:51], s87 offen lds\n" \
  "s_waitcnt lgkmcnt(2)\n" \
  "v_mfma_f32_16x16x32_bf16 v[120:123], v[200:203], v[80:83], v[120:123]\n" \
  "v_mfma_f32_16x16x32_bf16 v[124:127], v[80:83], v[80:83], v[124:127]\n" \
  "v_mfma_f32_16x16x32_bf16 v[120:123], v[200:203], v[84:87], v[120:123]\n" \
  "v_mfma_f32_16x16x32_bf16 v[124:127], v[84:87], v[84:87], v[124:127]\n" \
  "s_waitcnt vmcnt(23)\n" \
  "ds_read_b128 v[96:99], v204 offset:4096\n" \
  "ds_read_b128 v[100:103], v205 offset:4096\n" \
  "buffer_load_dwordx4 v241, s[48:51], s87 offen offset:1024 lds\n" \
  "s_waitcnt lgkmcnt(2)\n" \
  "v_mfma_f32_16x16x32_bf16 v[120:123], v[200:203], v[88:91], v[120:123]\n" \
  "v_mfma_f32_16x16x32_bf16 v[124:127], v[88:91], v[88:91], v[124:127]\n" \
  "v_mfma_f32_16x16x32_bf16 v[120:123], v[200:203], v[92:95], v[120:123]\n" \
  "v_mfma_f32_16x16x32_bf16 v[124:127], v[92:95], v[92:95], v[124:127]\n" \
  "s_waitcnt vmcnt(22)\n" \
  "ds_read_b128 v[104:107], v204 offset:5120\n" \
  "ds_read_b128 v[108:111], v205 offset:5120\n" \
  "buffer_load_dwordx4 v242, s[48:51], s87 offen offset:2048 lds\n" \
  "s_waitcnt lgkmcnt(2)\n" \
  "v_mfma_f32_16x16x32_bf16 v[120:123], v[200:203], v[96:99], v[120:123]\n" \
  "v_mfma_f32_16x16x32_bf16 v[124:127], v[96:99], v[96:99], v[124:127]\n" \
  "v_mfma_f32_16x16x32_bf16 v[120:123], v[200:203], v[100:103], v[120:123]\n" \
  "v_mfma_f32_16x16x32_bf16 v[124:127], v[100:103], v[100:103], v[124:127]\n" \
  "s_waitcnt vmcnt(21)\n" \
  "ds_read_b128 v[112:115], v204 offset:6144\n" \
  "ds_read_b128 v[116:119], v205 offset:6144\n" \
  "buffer_load_dwordx4 v243, s[48:51], s87 offen offset:3072 lds\n" \
  "s_waitcnt lgkmcnt(2)\n" \
  "v_mfma_f32_16x16x32_bf16 v[120:123], v[200:203], v[104:107], v[120:123]\n" \
  "v_mfma_f32_16x16x32_bf16 v[124:127], v[104:107], v[104:107], v[124:127]\n" \
  "v_mfma_f32_16x16x32_bf16 v[120:123], v[200:203], v[108:111], v[120:123]\n" \
  "v_mfma_f32_16x16x32_bf16 v[124:127], v[108:111], v[108:111], v[124:127]\n" \
  "s_waitcnt vmcnt(16) lgkmcnt(0)\n" \
  "s_barrier\n" \
  "v_mfma_f32_16x16x32_bf16 v[120:123], v[200:203], v[112:115], v[120:123]\n" \
  "v_mfma_f32_16x16x32_bf16 v[124:127], v[112:115], v[112:115], v[124:127]\n" \
  "v_mfma_f32_16x16x32_bf16 v[120:123], v[200:203], v[116:119], v[120:123]\n" \
  "v_mfma_f32_16x16x32_bf16 v[124:127], v[116:119], v[116:119], v[124:127]\n" \
  "ds_read_b64_tr_b16 a[128:129], v228\n" \
  "ds_read_b64_tr_b16 a[130:131], v228 offset:512\n" \
  "ds_read_b64_tr_b16 a[132:133], v229\n" \
  "ds_read_b64_tr_b16 a[134:135], v229 offset:512\n" \
  "ds_read_b64_tr_b16 a[144:145], v228 offset:8192\n" \
  "ds_read_b64_tr_b16 a[146:147], v228 offset:8704\n" \
  "ds_read_b64_tr_b16 a[148:149], v229 offset:8192\n" \
  "ds_read_b64_tr_b16 a[150:151], v229 offset:8704\n" \
  "ds_read_b64_tr_b16 a[160:161], v228 offset:4096\n" \
  "ds_read_b64_tr_b16 a[162:163], v228 offset:4608\n" \
  "ds_read_b64_tr_b16 a[164:165], v229 offset:4096\n" \
  "ds_read_b64_tr_b16 a[166:167], v229 offset:4608\n" \
  "ds_read_b64_tr_b16 a[176:177], v228 offset:12288\n" \
  "ds_read_b64_tr_b16 a[178:179], v228 offset:12800\n" \
  "ds_read_b64_tr_b16 a[180:181], v229 offset:12288\n" \
  "ds_read_b64_tr_b16 a[182:183], v229 offset:12800\n" \
  "v_mul_f32 v120, 0xbb124925, v120\n" \
  "v_mul_f32 v121, 0xbb124925, v121\n" \
  "v_mul_f32 v122, 0xbb124925, v122\n" \
  "v_mul_f32 v123, 0xbb124925, v123\n" \
  "s_nop 1\n" \
  "v_mfma_f32_16x16x32_bf16 v[176:179], v[194:197], v[64:67], v[120:123]\n" \
  "v_mfma_f32_16x16x32_bf16 v[180:183], v[192:195], v[64:67], v[120:123]\n" \
  "v_mfma_f32_16x16x32_bf16 v[184:187], v[194:197], v[68:71], v[120:123]\n" \
  "v_mfma_f32_16x16x32_bf16 v[188:191], v[192:195], v[68:71], v[120:123]\n" \
  "s_mov_b32 s92, 0x100001\n" \
  "s_mov_b32 s93, 0x10000100\n" \
  "v_cndmask_b32 v251, 0, v124, s[92:93]\n" \
  "s_lshl_b64 s[92:93], s[92:93], 1\n" \
  "v_cndmask_b32 v251, v251, v125, s[92:93]\n" \
  "s_lshl_b64 s[92:93], s[92:93], 1\n" \
  "v_cndmask_b32 v251, v251, v126, s[92:93]\n" \
  "s_lshl_b64 s[92:93], s[92:93], 1\n" \
  "v_cndmask_b32 v251, v251, v127, s[92:93]\n" \
  "v_mov_b32 v252, v251\n" \
  "s_nop 1\n" \
  "v_permlane32_swap_b32 v251, v252\n" \
  "v_add_f32 v251, v251, v252\n" \
  "v_mov_b32 v252, v251\n" \
  "s_nop 1\n" \
  "v_permlane16_swap_b32 v251, v252\n" \
  "v_add_f32_e64 v251, v251, v252\n" \
  "v_mul_f32 v251, 0x3b124925, v251\n" \
  "v_fma_f32 v251, -v120, v120, v251\n" \
  "v_max_f32_e64 v251, 0, v251\n" \
  "v_add_f32 v247, 0x3727c5ac, v251\n" \
  "v_rsq_f32 v251, v247\n" \
  "s_nop 0\n" \
  "v_mul_f32 v245, 0xbfb8aa3b, v251\n" \
  "v_mul_f32 v249, 0x3db8aa3b, v251\n" \
  "v_cvt_pk_bf16_f32 v132, v176, v177\n" \
  "v_cvt_pk_bf16_f32 v133, v178, v179\n" \
  "v_cvt_pk_bf16_f32 v134, v180, v181\n" \
  "v_cvt_pk_bf16_f32 v135, v182, v183\n" \
  "v_mfma_f32_16x16x32_bf16 v[176:179], v[194:197], v[72:75], v[120:123]\n" \
  "v_mfma_f32_16x16x32_bf16 v[180:183], v[192:195], v[72:75], v[120:123]\n" \
  "v_cvt_pk_bf16_f32 v140, v184, v185\n" \
  "v_cvt_pk_bf16_f32 v141, v186, v187\n" \
  "v_cvt_pk_bf16_f32 v142, v188, v189\n" \
  "v_cvt_pk_bf16_f32 v143, v190, v191\n" \
  "v_mfma_f32_16x16x32_bf16 v[184:187], v[194:197], v[76:79], v[120:123]\n" \
  "v_mfma_f32_16x16x32_bf16 v[188:191], v[192:195], v[76:79], v[120:123]\n" \
  "v_readfirstlane_b32 s92, v206\n" \
  "v_readfirstlane_b32 s93, v207\n" \
  "s_nop 3\n" \
  "s_mul_i32 s79, s92, 0x4c957f2d\n" \
  "s_mul_hi_u32 s80, s92, 0x4c957f2d\n" \
  "s_mul_i32 s94, s92, 0x5851f42d\n" \
  "s_add_u32 s80, s80, s94\n" \
  "s_mul_i32 s94, s93, 0x4c957f2d\n" \
  "s_add_u32 s80, s80, s94\n" \
  "s_add_u32 s79, s79, 0xf767814f\n" \
  "s_addc_u32 s80, s80, 0x14057b7e\n" \
  "s_waitcnt lgkmcnt(0)\n" \
  "; HP [0] ('F', 2, 0)\n" \
  "v_mfma_f32_16x16x32_bf16 v[0:3], a[128:131], v[128:131], 0\n" \
  "ds_read_b64_tr_b16 a[64:65], v232\n" \
  "ds_read_b64_tr_b16 a[66:67], v232 offset:512\n" \
  "v_mfma_f32_16x16x32_bf16 v[4:7], a[128:131], v[132:135], 0\n" \
  "ds_read_b64_tr_b16 a[68:69], v233\n" \
  "s_add_u32 s86, s73, 0x150000\n" \
  "v_mfma_f32_16x16x32_bf16 v[8:11], a[132:135], v[128:131], 0\n" \
  "ds_read_b64_tr_b16 a[70:71], v233 offset:512\n" \
  "s_add_u32 m0, s74, 0xc000\n" \
  "v_mfma_f32_16x16x32_bf16 v[12:15], a[132:135], v[132:135], 0\n" \
  "buffer_load_dword a1, v199, s[44:47], 0 offen\n" \
  "v_mfma_f32_16x16x32_bf16 v[32:35], a[144:147], v[128:131], 0\n" \
  "ds_read_b64_tr_b16 a[80:81], v232 offset:8192\n" \
  "v_mfma_f32_16x16x32_bf16 v[36:39], a[144:147], v[132:135], 0\n" \
  "ds_read_b64_tr_b16 a[82:83], v232 offset:8704\n" \
  "ds_read_b64_tr_b16 a[84:85], v233 offset:8192\n" \
  "v_mfma_f32_16x16x32_bf16 v[40:43], a[148:151], v[128:131], 0\n" \
  "ds_read_b64_tr_b16 a[86:87], v233 offset:8704\n" \
  "v_cvt_pk_bf16_f32 v148, v176, v177\n" \
  "v_mfma_f32_16x16x32_bf16 v[44:47], a[148:151], v[132:135], 0\n" \
  "ds_read_b64_tr_b16 a[96:97], v232 offset:4096\n" \
  "v_cvt_pk_bf16_f32 v149, v178, v179\n" \
  "v_mfma_f32_16x16x32_bf16 v[0:3], a[160:163], v[136:139], v[0:3]\n" \
  "buffer_load_dwordx4 v240, s[48:51], s86 offen lds\n" \
  "v_cvt_pk_bf16_f32 v150, v180, v181\n" \
  "v_mfma_f32_16x16x32_bf16 v[4:7], a[160:163], v[140:143], v[4:7]\n" \
  "ds_read_b64_tr_b16 a[98:99], v232 offset:4608\n" \
  "v_cvt_pk_bf16_f32 v151, v182, v183\n" \
  "v_mfma_f32_16x16x32_bf16 v[8:11], a[164:167], v[136:139], v[8:11]\n" \
  "ds_read_b64_tr_b16 a[100:101], v233 offset:4096\n" \
  "ds_read_b64_tr_b16 a[102:103], v233 offset:4608\n" \
  "v_cvt_pk_bf16_f32 v156, v184, v185\n" \
  "v_mfma_f32_16x16x32_bf16 v[12:15], a[164:167], v[140:143], v[12:15]\n" \
  "ds_read_b64_tr_b16 a[112:113], v232 offset:12288\n" \
  "v_cvt_pk_bf16_f32 v157, v186, v187\n" \
  "v_mfma_f32_16x16x32_bf16 v[32:35], a[176:179], v[136:139], v[32:35]\n" \
  "ds_read_b64_tr_b16 a[114:115], v232 offset:12800\n" \
  "v_cvt_pk_bf16_f32 v158, v188, v189\n" \
  "v_mfma_f32_16x16x32_bf16 v[36:39], a[176:179], v[140:143], v[36:39]\n" \
  "buffer_load_dwordx4 v241, s[48:51], s86 offen offset:1024 lds\n" \
  "v_cvt_pk_bf16_f32 v159, v190, v191\n" \
  "v_mfma_f32_16x16x32_bf16 v[40:43], a[180:183], v[136:139], v[40:43]\n" \
  "ds_read_b64_tr_b16 a[116:117], v233 offset:12288\n" \
  "v_mfma_f32_16x16x32_bf16 v[44:47], a[180:183], v[140:143], v[44:47]\n" \
  "ds_read_b64_tr_b16 a[118:119], v233 offset:12800\n" \
  "v_mfma_f32_16x16x32_bf16 v[176:179], v[194:197], v[80:83], v[120:123]\n" \
  "v_mfma_f32_16x16x32_bf16 v[180:183], v[192:195], v[80:83], v[120:123]\n" \
  "v_mfma_f32_16x16x32_bf16 v[184:187], v[194:197], v[84:87], v[120:123]\n" \
  "buffer_load_dwordx4 v242, s[48:51], s86 offen offset:2048 lds\n" \
  "v_mfma_f32_16x16x32_bf16 v[188:191], v[192:195], v[84:87], v[120:123]\n" \
  "s_waitcnt vmcnt(12) lgkmcnt(0)\n" \
  "s_barrier\n" \
  "; HP [1, 2] ('F', 2, 1)\n" \
  "v_mfma_f32_16x16x32_bf16 v[0:3], a[64:67], v[144:147], v[0:3]\n" \
  "ds_read_b64_tr_b16 a[128:129], v228 offset:16384\n" \
  "ds_read_b64_tr_b16 a[130:131], v228 offset:16896\n" \
  "v_mfma_f32_16x16x32_bf16 v[4:7], a[64:67], v[148:151], v[4:7]\n" \
  "ds_read_b64_tr_b16 a[132:133], v229 offset:16384\n" \
  "s_add_u32 s87, s72, 0\n" \
  "v_mfma_f32_16x16x32_bf16 v[8:11], a[68:71], v[144:147], v[8:11]\n" \
  "buffer_load_dwordx4 v243, s[48:51], s86 offen offset:3072 lds\n" \
  "v_mfma_f32_16x16x32_bf16 v[12:15], a[68:71], v[148:151], v[12:15]\n" \
  "ds_read_b64_tr_b16 a[134:135], v229 offset:16896\n" \
  "s_add_u32 m0, s75, 0xc000\n" \
  "v_mfma_f32_16x16x32_bf16 v[32:35], a[80:83], v[144:147], v[32:35]\n" \
  "ds_read_b64_tr_b16 a[144:145], v228 offset:24576\n" \
  "ds_read_b64_tr_b16 a[146:147], v228 offset:25088\n" \
  "v_mfma_f32_16x16x32_bf16 v[36:39], a[80:83], v[148:151], v[36:39]\n" \
  "v_and_b32 v240, 0x3fffffff, v240\n" \
  "v_and_b32 v241, 0x3fffffff, v241\n" \
  "v_and_b32 v242, 0x3fffffff, v242\n" \
  "v_and_b32 v243, 0x3fffffff, v243\n" \
  "buffer_load_dwordx4 v240, s[48:51], s87 offen lds\n" \
  "v_mfma_f32_16x16x32_bf16 v[40:43], a[84:87], v[144:147], v[40:43]\n" \
  "ds_read_b64_tr_b16 a[148:149], v229 offset:24576\n" \
  "v_cvt_pk_bf16_f32 v164, v176, v177\n" \
  "v_mfma_f32_16x16x32_bf16 v[44:47], a[84:87], v[148:151], v[44:47]\n" \
  "ds_read_b64_tr_b16 a[150:151], v229 offset:25088\n" \
  "v_cvt_pk_bf16_f32 v165, v178, v179\n" \
  "v_mfma_f32_16x16x32_bf16 v[0:3], a[96:99], v[152:155], v[0:3]\n" \
  "buffer_load_dwordx4 v241, s[48:51], s87 offen offset:1024 lds\n" \
  "v_cvt_pk_bf16_f32 v166, v180, v181\n" \
  "v_mfma_f32_16x16x32_bf16 v[4:7], a[96:99], v[156:159], v[4:7]\n" \
  "ds_read_b64_tr_b16 a[160:161], v228 offset:20480\n" \
  "v_cvt_pk_bf16_f32 v167, v182, v183\n" \
  "v_mfma_f32_16x16x32_bf16 v[8:11], a[100:103], v[152:155], v[8:11]\n" \
  "ds_read_b64_tr_b16 a[162:163], v228 offset:20992\n" \
  "ds_read_b64_tr_b16 a[164:165], v229 offset:20480\n" \
  "v_cvt_pk_bf16_f32 v172, v184, v185\n" \
  "v_mfma_f32_16x16x32_bf16 v[12:15], a[100:103], v[156:159], v[12:15]\n" \
  "buffer_load_dwordx4 v242, s[48:51], s87 offen offset:2048 lds\n" \
  "v_cvt_pk_bf16_f32 v173, v186, v187\n" \
  "v_mfma_f32_16x16x32_bf16 v[32:35], a[112:115], v[152:155], v[32:35]\n" \
  "ds_read_b64_tr_b16 a[166:167], v229 offset:20992\n" \
  "v_cvt_pk_bf16_f32 v174, v188, v189\n" \
  "v_mfma_f32_16x16x32_bf16 v[36:39], a[112:115], v[156:159], v[36:39]\n" \
  "ds_read_b64_tr_b16 a[176:177], v228 offset:28672\n" \
  "v_cvt_pk_bf16_f32 v175, v190, v191\n" \
  "v_mfma_f32_16x16x32_bf16 v[40:43], a[116:119], v[152:155], v[40:43]\n" \
  "buffer_load_dwordx4 v243, s[48:51], s87 offen offset:3072 lds\n" \
  "v_mfma_f32_16x16x32_bf16 v[44:47], a[116:119], v[156:159], v[44:47]\n" \
  "ds_read_b64_tr_b16 a[178:179], v228 offset:29184\n" \
  "ds_read_b64_tr_b16 a[180:181], v229 offset:28672\n" \
  "v_mfma_f32_16x16x32_bf16 v[176:179], v[194:197], v[88:91], v[120:123]\n" \
  "ds_read_b64_tr_b16 a[182:183], v229 offset:29184\n" \
  "s_add_u32 s86, s72, 0x38000\n" \
  "s_add_u32 m0, s74, 0\n" \
  "v_mfma_f32_16x16x32_bf16 v[180:183], v[192:195], v[88:91], v[120:123]\n" \
  "buffer_load_dwordx4 v240, s[48:51], s86 offen lds\n" \
  "v_mfma_f32_16x16x32_bf16 v[184:187], v[194:197], v[92:95], v[120:123]\n" \
  "ds_read_b64_tr_b16 a[64:65], v232 offset:16384\n" \
  "s_add_u32 s87, s72, 0x70000\n" \
  "v_mfma_f32_16x16x32_bf16 v[188:191], v[192:195], v[92:95], v[120:123]\n" \
  "ds_read_b64_tr_b16 a[66:67], v232 offset:16896\n" \
  "s_waitcnt lgkmcnt(5)\n" \
  "v_mfma_f32_16x16x32_bf16 v[0:3], a[128:131], v[160:163], v[0:3]\n" \
  "buffer_load_dwordx4 v241, s[48:51], s86 offen offset:1024 lds\n" \
  "v_mfma_f32_16x16x32_bf16 v[4:7], a[128:131], v[164:167], v[4:7]\n" \
  "ds_read_b64_tr_b16 a[68:69], v233 offset:16384\n" \
  "ds_read_b64_tr_b16 a[70:71], v233 offset:16896\n" \
  "v_mfma_f32_16x16x32_bf16 v[8:11], a[132:135], v[160:163], v[8:11]\n" \
  "ds_read_b64_tr_b16 a[80:81], v232 offset:24576\n" \
  "v_mfma_f32_16x16x32_bf16 v[12:15], a[132:135], v[164:167], v[12:15]\n" \
  "buffer_load_dwordx4 v242, s[48:51], s86 offen offset:2048 lds\n" \
  "v_mfma_f32_16x16x32_bf16 v[32:35], a[144:147], v[160:163], v[32:35]\n" \
  "ds_read_b64_tr_b16 a[82:83], v232 offset:25088\n" \
  "s_waitcnt lgkmcnt(5)\n" \
  "v_mfma_f32_16x16x32_bf16 v[36:39], a[144:147], v[164:167], v[36:39]\n" \
  "ds_read_b64_tr_b16 a[84:85], v233 offset:24576\n" \
  "ds_read_b64_tr_b16 a[86:87], v233 offset:25088\n" \
  "v_cvt_pk_bf16_f32 v176, v176, v177\n" \
  "v_mfma_f32_16x16x32_bf16 v[40:43], a[148:151], v[160:163], v[40:43]\n" \
  "buffer_load_dwordx4 v243, s[48:51], s86 offen offset:3072 lds\n" \
  "v_cvt_pk_bf16_f32 v178, v178, v179\n" \
  "v_cvt_pk_bf16_f32 v180, v180, v181\n" \
  "v_mfma_f32_16x16x32_bf16 v[44:47], a[148:151], v[164:167], v[44:47]\n" \
  "ds_read_b64_tr_b16 a[96:97], v232 offset:20480\n" \
  "v_cvt_pk_bf16_f32 v182, v182, v183\n" \
  "v_accvgpr_write_b32 a196, v176\n" \
  "v_mfma_f32_16x16x32_bf16 v[0:3], a[160:163], v[168:171], v[0:3]\n" \
  "ds_read_b64_tr_b16 a[98:99], v232 offset:20992\n" \
  "s_add_u32 m0, s75, 0\n" \
  "v_accvgpr_write_b32 a197, v178\n" \
  "v_accvgpr_write_b32 a198, v180\n" \
  "v_mfma_f32_16x16x32_bf16 v[4:7], a[160:163], v[172:175], v[4:7]\n" \
  "buffer_load_dwordx4 v240, s[48:51], s87 offen lds\n" \
  "v_accvgpr_write_b32 a199, v182\n" \
  "v_mfma_f32_16x16x32_bf16 v[8:11], a[164:167], v[168:171], v[8:11]\n" \
  "ds_read_b64_tr_b16 a[100:101], v233 offset:20480\n" \
  "v_cvt_pk_bf16_f32 v184, v184, v185\n" \
  "v_cvt_pk_bf16_f32 v186, v186, v187\n" \
  "v_mfma_f32_16x16x32_bf16 v[12:15], a[164:167], v[172:175], v[12:15]\n" \
  "ds_read_b64_tr_b16 a[102:103], v233 offset:20992\n" \
  "ds_read_b64_tr_b16 a[112:113], v232 offset:28672\n" \
  "v_cvt_pk_bf16_f32 v188, v188, v189\n" \
  "v_cvt_pk_bf16_f32 v190, v190, v191\n" \
  "v_mfma_f32_16x16x32_bf16 v[32:35], a[176:179], v[168:171], v[32:35]\n" \
  "buffer_load_dwordx4 v241, s[48:51], s87 offen offset:1024 lds\n" \
  "v_accvgpr_write_b32 a204, v184\n" \
  "v_accvgpr_write_b32 a205, v186\n" \
  "v_mfma_f32_16x16x32_bf16 v[36:39], a[176:179], v[172:175], v[36:39]\n" \
  "ds_read_b64_tr_b16 a[114:115], v232 offset:29184\n" \
  "v_accvgpr_write_b32 a206, v188\n" \
  "v_accvgpr_write_b32 a207, v190\n" \
  "v_mfma_f32_16x16x32_bf16 v[40:43], a[180:183], v[168:171], v[40:43]\n" \
  "ds_read_b64_tr_b16 a[116:117], v233 offset:28672\n" \
  "v_mfma_f32_16x16x32_bf16 v[44:47], a[180:183], v[172:175], v[44:47]\n" \
  "ds_read_b64_tr_b16 a[118:119], v233 offset:29184\n" \
  "v_mfma_f32_16x16x32_bf16 v[176:179], v[194:197], v[96:99], v[120:123]\n" \
  "buffer_load_dwordx4 v242, s[48:51], s87 offen offset:2048 lds\n" \
  "v_mfma_f32_16x16x32_bf16 v[180:183], v[192:195], v[96:99], v[120:123]\n" \
  "v_mfma_f32_16x16x32_bf16 v[184:187], v[194:197], v[100:103], v[120:123]\n" \
  "v_mfma_f32_16x16x32_bf16 v[188:191], v[192:195], v[100:103], v[120:123]\n" \
  "buffer_load_dwordx4 v243, s[48:51], s87 offen offset:3072 lds\n" \
  "s_waitcnt vmcnt(17) lgkmcnt(0)\n" \
  "s_barrier\n" \
  "; HP [3, 4] ('F', 2, 3)\n" \
  "v_mfma_f32_16x16x32_bf16 v[0:3], a[64:67], a[192:195], v[0:3]\n" \
  "ds_read_b64_tr_b16 a[128:129], v228 offset:32768\n" \
  "ds_read_b64_tr_b16 a[130:131], v228 offset:33280\n" \
  "v_mfma_f32_16x16x32_bf16 v[4:7], a[64:67], a[196:199], v[4:7]\n" \
  "ds_read_b64_tr_b16 a[132:133], v229 offset:32768\n" \
  "s_add_u32 s86, s72, 0xa8000\n" \
  "v_mfma_f32_16x16x32_bf16 v[8:11], a[68:71], a[192:195], v[8:11]\n" \
  "ds_read_b64_tr_b16 a[134:135], v229 offset:33280\n" \
  "s_add_u32 m0, s74, 0x4000\n" \
  "v_mfma_f32_16x16x32_bf16 v[12:15], a[68:71], a[196:199], v[12:15]\n" \
  "buffer_load_dwordx4 v240, s[48:51], s86 offen lds\n" \
  "v_mfma_f32_16x16x32_bf16 v[32:35], a[80:83], a[192:195], v[32:35]\n" \
  "ds_read_b64_tr_b16 a[144:145], v228 offset:40960\n" \
  "s_add_u32 s87, s72, 0xe0000\n" \
  "v_mfma_f32_16x16x32_bf16 v[36:39], a[80:83], a[196:199], v[36:39]\n" \
  "ds_read_b64_tr_b16 a[146:147], v228 offset:41472\n" \
  "v_cvt_pk_bf16_f32 v176, v176, v177\n" \
  "v_mfma_f32_16x16x32_bf16 v[40:43], a[84:87], a[192:195], v[40:43]\n" \
  "ds_read_b64_tr_b16 a[148:149], v229 offset:40960\n" \
  "v_cvt_pk_bf16_f32 v178, v178, v179\n" \
  "v_cvt_pk_bf16_f32 v180, v180, v181\n" \
  "v_mfma_f32_16x16x32_bf16 v[44:47], a[84:87], a[196:199], v[44:47]\n" \
  "ds_read_b64_tr_b16 a[150:151], v229 offset:41472\n" \
  "v_cvt_pk_bf16_f32 v182, v182, v183\n" \
  "v_accvgpr_write_b32 a212, v176\n" \
  "v_mfma_f32_16x16x32_bf16 v[0:3], a[96:99], a[200:203], v[0:3]\n" \
  "buffer_load_dwordx4 v241, s[48:51], s86 offen offset:1024 lds\n" \
  "v_accvgpr_write_b32 a213, v178\n" \
  "v_accvgpr_write_b32 a214, v180\n" \
  "v_mfma_f32_16x16x32_bf16 v[4:7], a[96:99], a[204:207], v[4:7]\n" \
  "ds_read_b64_tr_b16 a[160:161], v228 offset:36864\n" \
  "v_accvgpr_write_b32 a215, v182\n" \
  "v_mfma_f32_16x16x32_bf16 v[8:11], a[100:103], a[200:203], v[8:11]\n" \
  "ds_read_b64_tr_b16 a[162:163], v228 offset:37376\n" \
  "v_cvt_pk_bf16_f32 v184, v184, v185\n" \
  "v_cvt_pk_bf16_f32 v186, v186, v187\n" \
  "v_mfma_f32_16x16x32_bf16 v[12:15], a[100:103], a[204:207], v[12:15]\n" \
  "ds_read_b64_tr_b16 a[164:165], v229 offset:36864\n" \
  "ds_read_b64_tr_b16 a[166:167], v229 offset:37376\n" \
  "v_cvt_pk_bf16_f32 v188, v188, v189\n" \
  "v_cvt_pk_bf16_f32 v190, v190, v191\n" \
  "v_mfma_f32_16x16x32_bf16 v[32:35], a[112:115], a[200:203], v[32:35]\n" \
  "ds_read_b64_tr_b16 a[176:177], v228 offset:45056\n" \
  "v_accvgpr_write_b32 a220, v184\n" \
  "v_accvgpr_write_b32 a221, v186\n" \
  "v_mfma_f32_16x16x32_bf16 v[36:39], a[112:115], a[204:207], v[36:39]\n" \
  "buffer_load_dwordx4 v242, s[48:51], s86 offen offset:2048 lds\n" \
  "v_accvgpr_write_b32 a222, v188\n" \
  "v_accvgpr_write_b32 a223, v190\n" \
  "v_mfma_f32_16x16x32_bf16 v[40:43], a[116:119], a[200:203], v[40:43]\n" \
  "ds_read_b64_tr_b16 a[178:179], v228 offset:45568\n" \
  "v_mfma_f32_16x16x32_bf16 v[44:47], a[116:119], a[204:207], v[44:47]\n" \
  "ds_read_b64_tr_b16 a[180:181], v229 offset:45056\n" \
  "v_mfma_f32_16x16x32_bf16 v[176:179], v[194:197], v[104:107], v[120:123]\n" \
  "ds_read_b64_tr_b16 a[182:183], v229 offset:45568\n" \
  "v_mfma_f32_16x16x32_bf16 v[180:183], v[192:195], v[104:107], v[120:123]\n" \
  "ds_read_b64_tr_b16 a[64:65], v232 offset:32768\n" \
  "s_waitcnt lgkmcnt(5)\n" \
  "v_mfma_f32_16x16x32_bf16 v[184:187], v[194:197], v[108:111], v[120:123]\n" \
  "buffer_load_dwordx4 v243, s[48:51], s86 offen offset:3072 lds\n" \
  "v_mfma_f32_16x16x32_bf16 v[188:191], v[192:195], v[108:111], v[120:123]\n" \
  "ds_read_b64_tr_b16 a[66:67], v232 offset:33280\n" \
  "s_add_u32 m0, s75, 0x4000\n" \
  "v_mfma_f32_16x16x32_bf16 v[0:3], a[128:131], a[208:211], v[0:3]\n" \
  "ds_read_b64_tr_b16 a[68:69], v233 offset:32768\n" \
  "v_mfma_f32_16x16x32_bf16 v[4:7], a[128:131], a[212:215], v[4:7]\n" \
  "ds_read_b64_tr_b16 a[70:71], v233 offset:33280\n" \
  "v_mfma_f32_16x16x32_bf16 v[8:11], a[132:135], a[208:211], v[8:11]\n" \
  "ds_read_b64_tr_b16 a[80:81], v232 offset:40960\n" \
  "s_waitcnt lgkmcnt(5)\n" \
  "v_mfma_f32_16x16x32_bf16 v[12:15], a[132:135], a[212:215], v[12:15]\n" \
  "buffer_load_dwordx4 v240, s[48:51], s87 offen lds\n" \
  "v_mfma_f32_16x16x32_bf16 v[32:35], a[144:147], a[208:211], v[32:35]\n" \
  "ds_read_b64_tr_b16 a[82:83], v232 offset:41472\n" \
  "ds_read_b64_tr_b16 a[84:85], v233 offset:40960\n" \
  "v_mfma_f32_16x16x32_bf16 v[36:39], a[144:147], a[212:215], v[36:39]\n" \
  "ds_read_b64_tr_b16 a[86:87], v233 offset:41472\n" \
  "v_cvt_pk_bf16_f32 v176, v176, v177\n" \
  "v_mfma_f32_16x16x32_bf16 v[40:43], a[148:151], a[208:211], v[40:43]\n" \
  "ds_read_b64_tr_b16 a[96:97], v232 offset:36864\n" \
  "v_cvt_pk_bf16_f32 v178, v178, v179\n" \
  "v_cvt_pk_bf16_f32 v180, v180, v181\n" \
  "v_mfma_f32_16x16x32_bf16 v[44:47], a[148:151], a[212:215], v[44:47]\n" \
  "ds_read_b64_tr_b16 a[98:99], v232 offset:37376\n" \
  "v_cvt_pk_bf16_f32 v182, v182, v183\n" \
  "v_accvgpr_write_b32 a228, v176\n" \
  "v_mfma_f32_16x16x32_bf16 v[0:3], a[160:163], a[216:219], v[0:3]\n" \
  "buffer_load_dwordx4 v241, s[48:51], s87 offen offset:1024 lds\n" \
  "v_accvgpr_write_b32 a229, v178\n" \
  "v_accvgpr_write_b32 a230, v180\n" \
  "v_mfma_f32_16x16x32_bf16 v[4:7], a[160:163], a[220:223], v[4:7]\n" \
  "ds_read_b64_tr_b16 a[100:101], v233 offset:36864\n" \
  "v_accvgpr_write_b32 a231, v182\n" \
  "v_mfma_f32_16x16x32_bf16 v[8:11], a[164:167], a[216:219], v[8:11]\n" \
  "ds_read_b64_tr_b16 a[102:103], v233 offset:37376\n" \
  "v_cvt_pk_bf16_f32 v184, v184, v185\n" \
  "v_cvt_pk_bf16_f32 v186, v186, v187\n" \
  "v_mfma_f32_16x16x32_bf16 v[12:15], a[164:167], a[220:223], v[12:15]\n" \
  "ds_read_b64_tr_b16 a[112:113], v232 offset:45056\n" \
  "v_cvt_pk_bf16_f32 v188, v188, v189\n" \
  "v_cvt_pk_bf16_f32 v190, v190, v191\n" \
  "v_mfma_f32_16x16x32_bf16 v[32:35], a[176:179], a[216:219], v[32:35]\n" \
  "ds_read_b64_tr_b16 a[114:115], v232 offset:45568\n" \
  "v_accvgpr_write_b32 a236, v184\n" \
  "v_accvgpr_write_b32 a237, v186\n" \
  "v_mfma_f32_16x16x32_bf16 v[36:39], a[176:179], a[220:223], v[36:39]\n" \
  "buffer_load_dwordx4 v242, s[48:51], s87 offen offset:2048 lds\n" \
  "v_accvgpr_write_b32 a238, v188\n" \
  "v_accvgpr_write_b32 a239, v190\n" \
  "v_mfma_f32_16x16x32_bf16 v[40:43], a[180:183], a[216:219], v[40:43]\n" \
  "ds_read_b64_tr_b16 a[116:117], v233 offset:45056\n" \
  "v_mfma_f32_16x16x32_bf16 v[44:47], a[180:183], a[220:223], v[44:47]\n" \
  "ds_read_b64_tr_b16 a[118:119], v233 offset:45568\n" \
  "v_mfma_f32_16x16x32_bf16 v[176:179], v[194:197], v[112:115], v[120:123]\n" \
  "v_mfma_f32_16x16x32_bf16 v[180:183], v[192:195], v[112:115], v[120:123]\n" \
  "v_mfma_f32_16x16x32_bf16 v[184:187], v[194:197], v[116:119], v[120:123]\n" \
  "buffer_load_dwordx4 v243, s[48:51], s87 offen offset:3072 lds\n" \
  "v_mfma_f32_16x16x32_bf16 v[188:191], v[192:195], v[116:119], v[120:123]\n" \
  "s_waitcnt vmcnt(16) lgkmcnt(0)\n" \
  "s_barrier\n" \
  "; HP [5, 6] ('F', 2, 5)\n" \
  "v_mfma_f32_16x16x32_bf16 v[0:3], a[64:67], a[224:227], v[0:3]\n" \
  "ds_read_b64_tr_b16 a[128:129], v228 offset:49152\n" \
  "ds_read_b64_tr_b16 a[130:131], v228 offset:49664\n" \
  "v_mfma_f32_16x16x32_bf16 v[4:7], a[64:67], a[228:231], v[4:7]\n" \
  "ds_read_b64_tr_b16 a[132:133], v229 offset:49152\n" \
  "ds_read_b64_tr_b16 a[134:135], v229 offset:49664\n" \
  "s_add_u32 s86, s72, 0x118000\n" \
  "s_add_u32 m0, s74, 0x8000\n" \
  "v_mfma_f32_16x16x32_bf16 v[8:11], a[68:71], a[224:227], v[8:11]\n" \
  "buffer_load_dwordx4 v240, s[48:51], s86 offen lds\n" \
  "v_mfma_f32_16x16x32_bf16 v[12:15], a[68:71], a[228:231], v[12:15]\n" \
  "ds_read_b64_tr_b16 a[144:145], v228 offset:57344\n" \
  "ds_read_b64_tr_b16 a[146:147], v228 offset:57856\n" \
  "v_mfma_f32_16x16x32_bf16 v[32:35], a[80:83], a[224:227], v[32:35]\n" \
  "ds_read_b64_tr_b16 a[148:149], v229 offset:57344\n" \
  "ds_read_b64_tr_b16 a[150:151], v229 offset:57856\n" \
  "v_mfma_f32_16x16x32_bf16 v[36:39], a[80:83], a[228:231], v[36:39]\n" \
  "ds_read_b64_tr_b16 a[160:161], v228 offset:53248\n" \
  "ds_read_b64_tr_b16 a[162:163], v228 offset:53760\n" \
  "v_cvt_pk_bf16_f32 v176, v176, v177\n" \
  "v_cvt_pk_bf16_f32 v178, v178, v179\n" \
  "v_cvt_pk_bf16_f32 v180, v180, v181\n" \
  "v_mfma_f32_16x16x32_bf16 v[40:43], a[84:87], a[224:227], v[40:43]\n" \
  "buffer_load_dwordx4 v241, s[48:51], s86 offen offset:1024 lds\n" \
  "v_cvt_pk_bf16_f32 v182, v182, v183\n" \
  "v_accvgpr_write_b32 a244, v176\n" \
  "v_accvgpr_write_b32 a245, v178\n" \
  "v_mfma_f32_16x16x32_bf16 v[44:47], a[84:87], a[228:231], v[44:47]\n" \
  "ds_read_b64_tr_b16 a[164:165], v229 offset:53248\n" \
  "ds_read_b64_tr_b16 a[166:167], v229 offset:53760\n" \
  "v_accvgpr_write_b32 a246, v180\n" \
  "v_accvgpr_write_b32 a247, v182\n" \
  "v_cvt_pk_bf16_f32 v184, v184, v185\n" \
  "v_mfma_f32_16x16x32_bf16 v[0:3], a[96:99], a[232:235], v[0:3]\n" \
  "ds_read_b64_tr_b16 a[176:177], v228 offset:61440\n" \
  "ds_read_b64_tr_b16 a[178:179], v228 offset:61952\n" \
  "v_cvt_pk_bf16_f32 v186, v186, v187\n" \
  "v_cvt_pk_bf16_f32 v188, v188, v189\n" \
  "v_cvt_pk_bf16_f32 v190, v190, v191\n" \
  "v_mfma_f32_16x16x32_bf16 v[4:7], a[96:99], a[236:239], v[4:7]\n" \
  "ds_read_b64_tr_b16 a[180:181], v229 offset:61440\n" \
  "ds_read_b64_tr_b16 a[182:183], v229 offset:61952\n" \
  "v_accvgpr_write_b32 a252, v184\n" \
  "v_accvgpr_write_b32 a253, v186\n" \
  "v_accvgpr_write_b32 a254, v188\n" \
  "v_accvgpr_write_b32 a255, v190\n" \
  "v_mfma_f32_16x16x32_bf16 v[8:11], a[100:103], a[232:235], v[8:11]\n" \
  "buffer_load_dwordx4 v242, s[48:51], s86 offen offset:2048 lds\n" \
  "v_mfma_f32_16x16x32_bf16 v[12:15], a[100:103], a[236:239], v[12:15]\n" \
  "ds_read_b64_tr_b16 a[64:65], v232 offset:49152\n" \
  "ds_read_b64_tr_b16 a[66:67], v232 offset:49664\n" \
  "v_mfma_f32_16x16x32_bf16 v[32:35], a[112:115], a[232:235], v[32:35]\n" \
  "ds_read_b64_tr_b16 a[68:69], v233 offset:49152\n" \
  "ds_read_b64_tr_b16 a[70:71], v233 offset:49664\n" \
  "v_mfma_f32_16x16x32_bf16 v[36:39], a[112:115], a[236:239], v[36:39]\n" \
  "ds_read_b64_tr_b16 a[72:73], v234 offset:49152\n" \
  "ds_read_b64_tr_b16 a[74:75], v234 offset:49664\n" \
  "v_mfma_f32_16x16x32_bf16 v[40:43], a[116:119], a[232:235], v[40:43]\n" \
  "buffer_load_dwordx4 v243, s[48:51], s86 offen offset:3072 lds\n" \
  "v_mfma_f32_16x16x32_bf16 v[44:47], a[116:119], a[236:239], v[44:47]\n" \
  "ds_read_b64_tr_b16 a[76:77], v235 offset:49152\n" \
  "ds_read_b64_tr_b16 a[78:79], v235 offset:49664\n" \
  "s_waitcnt lgkmcnt(8)\n" \
  "v_mfma_f32_16x16x32_bf16 v[0:3], a[128:131], a[240:243], v[0:3]\n" \
  "ds_read_b64_tr_b16 a[80:81], v232 offset:57344\n" \
  "ds_read_b64_tr_b16 a[82:83], v232 offset:57856\n" \
  "v_mfma_f32_16x16x32_bf16 v[4:7], a[128:131], a[244:247], v[4:7]\n" \
  "ds_read_b64_tr_b16 a[84:85], v233 offset:57344\n" \
  "ds_read_b64_tr_b16 a[86:87], v233 offset:57856\n" \
  "s_add_u32 s87, s72, 0x150000\n" \
  "s_add_u32 m0, s75, 0x8000\n" \
  "v_mfma_f32_16x16x32_bf16 v[8:11], a[132:135], a[240:243], v[8:11]\n" \
  "buffer_load_dwordx4 v240, s[48:51], s87 offen lds\n" \
  "v_mfma_f32_16x16x32_bf16 v[12:15], a[132:135], a[244:247], v[12:15]\n" \
  "ds_read_b64_tr_b16 a[88:89], v234 offset:57344\n" \
  "ds_read_b64_tr_b16 a[90:91], v234 offset:57856\n" \
  "v_mfma_f32_16x16x32_bf16 v[32:35], a[144:147], a[240:243], v[32:35]\n" \
  "ds_read_b64_tr_b16 a[92:93], v235 offset:57344\n" \
  "ds_read_b64_tr_b16 a[94:95], v235 offset:57856\n" \
  "v_mfma_f32_16x16x32_bf16 v[36:39], a[144:147], a[244:247], v[36:39]\n" \
  "ds_read_b64_tr_b16 a[96:97], v232 offset:53248\n" \
  "ds_read_b64_tr_b16 a[98:99], v232 offset:53760\n" \
  "v_mfma_f32_16x16x32_bf16 v[40:43], a[148:151], a[240:243], v[40:43]\n" \
  "buffer_load_dwordx4 v241, s[48:51], s87 offen offset:1024 lds\n" \
  "v_mfma_f32_16x16x32_bf16 v[44:47], a[148:151], a[244:247], v[44:47]\n" \
  "ds_read_b64_tr_b16 a[100:101], v233 offset:53248\n" \
  "ds_read_b64_tr_b16 a[102:103], v233 offset:53760\n" \
  "v_mfma_f32_16x16x32_bf16 v[0:3], a[160:163], a[248:251], v[0:3]\n" \
  "ds_read_b64_tr_b16 a[104:105], v234 offset:53248\n" \
  "ds_read_b64_tr_b16 a[106:107], v234 offset:53760\n" \
  "v_mfma_f32_16x16x32_bf16 v[4:7], a[160:163], a[252:255], v[4:7]\n" \
  "ds_read_b64_tr_b16 a[108:109], v235 offset:53248\n" \
  "ds_read_b64_tr_b16 a[110:111], v235 offset:53760\n" \
  "v_mfma_f32_16x16x32_bf16 v[8:11], a[164:167], a[248:251], v[8:11]\n" \
  "buffer_load_dwordx4 v242, s[48:51], s87 offen offset:2048 lds\n" \
  "v_mfma_f32_16x16x32_bf16 v[12:15], a[164:167], a[252:255], v[12:15]\n" \
  "ds_read_b64_tr_b16 a[112:113], v232 offset:61440\n" \
  "ds_read_b64_tr_b16 a[114:115], v232 offset:61952\n" \
  "v_mfma_f32_16x16x32_bf16 v[32:35], a[176:179], a[248:251], v[32:35]\n" \
  "v_mfma_f32_16x16x32_bf16 v[36:39], a[176:179], a[252:255], v[36:39]\n" \
  "v_mfma_f32_16x16x32_bf16 v[40:43], a[180:183], a[248:251], v[40:43]\n" \
  "buffer_load_dwordx4 v243, s[48:51], s87 offen offset:3072 lds\n" \
  "v_mfma_f32_16x16x32_bf16 v[44:47], a[180:183], a[252:255], v[44:47]\n" \
  "ds_read_b64_tr_b16 a[116:117], v233 offset:61440\n" \
  "ds_read_b64_tr_b16 a[118:119], v233 offset:61952\n" \
  "ds_read_b64_tr_b16 a[120:121], v234 offset:61440\n" \
  "ds_read_b64_tr_b16 a[122:123], v234 offset:61952\n" \
  "ds_read_b64_tr_b16 a[124:125], v235 offset:61440\n" \
  "ds_read_b64_tr_b16 a[126:127], v235 offset:61952\n" \
  "s_waitcnt vmcnt(16) lgkmcnt(0)\n" \
  "s_barrier\n" \
  "; HP [7, 8] ('F', 0, 0)\n" \
  "v_mfma_f32_16x16x32_bf16 v[64:67], a[64:67], v[128:131], 0\n" \
  "ds_read_b64_tr_b16 a[128:129], v228\n" \
  "ds_read_b64_tr_b16 a[130:131], v228 offset:512\n" \
  "v_mfma_f32_16x16x32_bf16 v[68:71], a[64:67], v[132:135], 0\n" \
  "ds_read_b64_tr_b16 a[132:133], v229\n" \
  "s_mov_b32 s86, 0\n" \
  "v_mfma_f32_16x16x32_bf16 v[72:75], a[68:71], v[128:131], 0\n" \
  "ds_read_b64_tr_b16 a[134:135], v229 offset:512\n" \
  "s_add_u32 m0, s74, 0xc000\n" \
  "v_mfma_f32_16x16x32_bf16 v[76:79], a[68:71], v[132:135], 0\n" \
  "ds_read_b64_tr_b16 a[136:137], v230\n" \
  "s_mov_b32 s87, 0x80000\n" \
  "v_mfma_f32_16x16x32_bf16 v[80:83], a[72:75], v[128:131], 0\n" \
  "ds_read_b64_tr_b16 a[138:139], v230 offset:512\n" \
  "ds_read_b64_tr_b16 a[140:141], v231\n" \
  "v_mfma_f32_16x16x32_bf16 v[84:87], a[72:75], v[132:135], 0\n" \
  "buffer_load_dwordx4 v236, s[44:47], s86 offen lds\n" \
  "v_mfma_f32_16x16x32_bf16 v[88:91], a[76:79], v[128:131], 0\n" \
  "ds_read_b64_tr_b16 a[142:143], v231 offset:512\n" \
  "v_mul_f32 v176, v0, v244\n" \
  "v_mfma_f32_16x16x32_bf16 v[92:95], a[76:79], v[132:135], 0\n" \
  "ds_read_b64_tr_b16 a[144:145], v228 offset:8192\n" \
  "v_mul_f32_e64 v177, v1, v244\n" \
  "v_mfma_f32_16x16x32_bf16 v[96:99], a[80:83], v[128:131], 0\n" \
  "ds_read_b64_tr_b16 a[146:147], v228 offset:8704\n" \
  "v_mul_f32_e64 v178, v2, v244\n" \
  "v_mfma_f32_16x16x32_bf16 v[100:103], a[80:83], v[132:135], 0\n" \
  "ds_read_b64_tr_b16 a[148:149], v229 offset:8192\n" \
  "v_mul_f32_e64 v179, v3, v244\n" \
  "v_mfma_f32_16x16x32_bf16 v[104:107], a[84:87], v[128:131], 0\n" \
  "ds_read_b64_tr_b16 a[150:151], v229 offset:8704\n" \
  "ds_read_b64_tr_b16 a[152:153], v230 offset:8192\n" \
  "v_mfma_f32_16x16x32_bf16 v[108:111], a[84:87], v[132:135], 0\n" \
  "ds_read_b64_tr_b16 a[154:155], v230 offset:8704\n" \
  "v_mul_f32_e64 v32, v0, v32\n" \
  "v_mfma_f32_16x16x32_bf16 v[112:115], a[88:91], v[128:131], 0\n" \
  "ds_read_b64_tr_b16 a[156:157], v231 offset:8192\n" \
  "v_mul_f32_e64 v33, v1, v33\n" \
  "v_mfma_f32_16x16x32_bf16 v[116:119], a[88:91], v[132:135], 0\n" \
  "buffer_load_dwordx4 v237, s[44:47], s86 offen offset:1024 lds\n" \
  "v_mfma_f32_16x16x32_bf16 v[120:123], a[92:95], v[128:131], 0\n" \
  "ds_read_b64_tr_b16 a[158:159], v231 offset:8704\n" \
  "v_mul_f32_e64 v34, v2, v34\n" \
  "v_mfma_f32_16x16x32_bf16 v[124:127], a[92:95], v[132:135], 0\n" \
  "ds_read_b64_tr_b16 a[160:161], v228 offset:4096\n" \
  "v_mul_f32_e64 v35, v3, v35\n" \
  "v_mfma_f32_16x16x32_bf16 v[64:67], a[96:99], v[136:139], v[64:67]\n" \
  "ds_read_b64_tr_b16 a[162:163], v228 offset:4608\n" \
  "ds_read_b64_tr_b16 a[164:165], v229 offset:4096\n" \
  "v_mfma_f32_16x16x32_bf16 v[68:71], a[96:99], v[140:143], v[68:71]\n" \
  "ds_read_b64_tr_b16 a[166:167], v229 offset:4608\n" \
  "v_mul_f32_e64 v180, v4, v245\n" \
  "v_mfma_f32_16x16x32_bf16 v[72:75], a[100:103], v[136:139], v[72:75]\n" \
  "ds_read_b64_tr_b16 a[168:169], v230 offset:4096\n" \
  "v_mul_f32_e64 v181, v5, v245\n" \
  "v_mfma_f32_16x16x32_bf16 v[76:79], a[100:103], v[140:143], v[76:79]\n" \
  "ds_read_b64_tr_b16 a[170:171], v230 offset:4608\n" \
  "v_mul_f32_e64 v182, v6, v245\n" \
  "v_mfma_f32_16x16x32_bf16 v[80:83], a[104:107], v[136:139], v[80:83]\n" \
  "ds_read_b64_tr_b16 a[172:173], v231 offset:4096\n" \
  "v_mul_f32_e64 v183, v7, v245\n" \
  "v_mfma_f32_16x16x32_bf16 v[84:87], a[104:107], v[140:143], v[84:87]\n" \
  "buffer_load_dwordx4 v238, s[44:47], s86 offen offset:2048 lds\n" \
  "v_mfma_f32_16x16x32_bf16 v[88:91], a[108:111], v[136:139], v[88:91]\n" \
  "ds_read_b64_tr_b16 a[174:175], v231 offset:4608\n" \
  "ds_read_b64_tr_b16 a[176:177], v228 offset:12288\n" \
  "v_mfma_f32_16x16x32_bf16 v[92:95], a[108:111], v[140:143], v[92:95]\n" \
  "ds_read_b64_tr_b16 a[178:179], v228 offset:12800\n" \
  "v_mul_f32_e64 v36, v4, v36\n" \
  "v_mfma_f32_16x16x32_bf16 v[96:99], a[112:115], v[136:139], v[96:99]\n" \
  "ds_read_b64_tr_b16 a[180:181], v229 offset:12288\n" \
  "v_mul_f32_e64 v37, v5, v37\n" \
  "v_mfma_f32_16x16x32_bf16 v[100:103], a[112:115], v[140:143], v[100:103]\n" \
  "ds_read_b64_tr_b16 a[182:183], v229 offset:12800\n" \
  "v_mul_f32_e64 v38, v6, v38\n" \
  "v_mfma_f32_16x16x32_bf16 v[104:107], a[116:119], v[136:139], v[104:107]\n" \
  "ds_read_b64_tr_b16 a[184:185], v230 offset:12288\n" \
  "v_mul_f32_e64 v39, v7, v39\n" \
  "v_mfma_f32_16x16x32_bf16 v[108:111], a[116:119], v[140:143], v[108:111]\n" \
  "ds_read_b64_tr_b16 a[186:187], v230 offset:12800\n" \
  "ds_read_b64_tr_b16 a[188:189], v231 offset:12288\n" \
  "v_mfma_f32_16x16x32_bf16 v[112:115], a[120:123], v[136:139], v[112:115]\n" \
  "ds_read_b64_tr_b16 a[190:191], v231 offset:12800\n" \
  "v_mfma_f32_16x16x32_bf16 v[116:119], a[120:123], v[140:143], v[116:119]\n" \
  "buffer_load_dwordx4 v239, s[44:47], s86 offen offset:3072 lds\n" \
  "v_mfma_f32_16x16x32_bf16 v[120:123], a[124:127], v[136:139], v[120:123]\n" \
  "ds_read_b64_tr_b16 a[64:65], v232\n" \
  "s_add_u32 m0, s75, 0xc000\n" \
  "v_mfma_f32_16x16x32_bf16 v[124:127], a[124:127], v[140:143], v[124:127]\n" \
  "ds_read_b64_tr_b16 a[66:67], v232 offset:512\n" \
  "s_waitcnt lgkmcnt(6)\n" \
  "v_mfma_f32_16x16x32_bf16 v[64:67], a[128:131], v[144:147], v[64:67]\n" \
  "ds_read_b64_tr_b16 a[68:69], v233\n" \
  "ds_read_b64_tr_b16 a[70:71], v233 offset:512\n" \
  "v_mfma_f32_16x16x32_bf16 v[68:71], a[128:131], v[148:151], v[68:71]\n" \
  "ds_read_b64_tr_b16 a[72:73], v234\n" \
  "v_mfma_f32_16x16x32_bf16 v[72:75], a[132:135], v[144:147], v[72:75]\n" \
  "ds_read_b64_tr_b16 a[74:75], v234 offset:512\n" \
  "v_exp_f32 v176, v176\n" \
  "v_mfma_f32_16x16x32_bf16 v[76:79], a[132:135], v[148:151], v[76:79]\n" \
  "ds_read_b64_tr_b16 a[76:77], v235\n" \
  "v_fma_f32 v176, v176, v246, v246\n" \
  "v_mfma_f32_16x16x32_bf16 v[80:83], a[136:139], v[144:147], v[80:83]\n" \
  "ds_read_b64_tr_b16 a[78:79], v235 offset:512\n" \
  "v_mfma_f32_16x16x32_bf16 v[84:87], a[136:139], v[148:151], v[84:87]\n" \
  "buffer_load_dwordx4 v236, s[44:47], s87 offen lds\n" \
  "v_mfma_f32_16x16x32_bf16 v[88:91], a[140:143], v[144:147], v[88:91]\n" \
  "ds_read_b64_tr_b16 a[80:81], v232 offset:8192\n" \
  "ds_read_b64_tr_b16 a[82:83], v232 offset:8704\n" \
  "v_exp_f32_e64 v177, v177\n" \
  "v_mfma_f32_16x16x32_bf16 v[92:95], a[140:143], v[148:151], v[92:95]\n" \
  "ds_read_b64_tr_b16 a[84:85], v233 offset:8192\n" \
  "v_fma_f32 v177, v177, v246, v246\n" \
  "v_mfma_f32_16x16x32_bf16 v[96:99], a[144:147], v[144:147], v[96:99]\n" \
  "ds_read_b64_tr_b16 a[86:87], v233 offset:8704\n" \
  "v_mfma_f32_16x16x32_bf16 v[100:103], a[144:147], v[148:151], v[100:103]\n" \
  "ds_read_b64_tr_b16 a[88:89], v234 offset:8192\n" \
  "v_exp_f32_e64 v178, v178\n" \
  "v_mfma_f32_16x16x32_bf16 v[104:107], a[148:151], v[144:147], v[104:107]\n" \
  "ds_read_b64_tr_b16 a[90:91], v234 offset:8704\n" \
  "v_fma_f32 v178, v178, v246, v246\n" \
  "v_mfma_f32_16x16x32_bf16 v[108:111], a[148:151], v[148:151], v[108:111]\n" \
  "ds_read_b64_tr_b16 a[92:93], v235 offset:8192\n" \
  "ds_read_b64_tr_b16 a[94:95], v235 offset:8704\n" \
  "v_mfma_f32_16x16x32_bf16 v[112:115], a[152:155], v[144:147], v[112:115]\n" \
  "ds_read_b64_tr_b16 a[96:97], v232 offset:4096\n" \
  "v_mfma_f32_16x16x32_bf16 v[116:119], a[152:155], v[148:151], v[116:119]\n" \
  "buffer_load_dwordx4 v237, s[44:47], s87 offen offset:1024 lds\n" \
  "v_exp_f32_e64 v179, v179\n" \
  "v_mfma_f32_16x16x32_bf16 v[120:123], a[156:159], v[144:147], v[120:123]\n" \
  "ds_read_b64_tr_b16 a[98:99], v232 offset:4608\n" \
  "v_fma_f32 v179, v179, v246, v246\n" \
  "v_mfma_f32_16x16x32_bf16 v[124:127], a[156:159], v[148:151], v[124:127]\n" \
  "ds_read_b64_tr_b16 a[100:101], v233 offset:4096\n" \
  "v_mfma_f32_16x16x32_bf16 v[64:67], a[160:163], v[152:155], v[64:67]\n" \
  "ds_read_b64_tr_b16 a[102:103], v233 offset:4608\n" \
  "v_rcp_f32_e64 v176, v176\n" \
  "v_mfma_f32_16x16x32_bf16 v[68:71], a[160:163], v[156:159], v[68:71]\n" \
  "ds_read_b64_tr_b16 a[104:105], v234 offset:4096\n" \
  "ds_read_b64_tr_b16 a[106:107], v234 offset:4608\n" \
  "v_mfma_f32_16x16x32_bf16 v[72:75], a[164:167], v[152:155], v[72:75]\n" \
  "ds_read_b64_tr_b16 a[108:109], v235 offset:4096\n" \
  "v_mul_f32_e64 v32, v32, v176\n" \
  "v_mfma_f32_16x16x32_bf16 v[76:79], a[164:167], v[156:159], v[76:79]\n" \
  "ds_read_b64_tr_b16 a[110:111], v235 offset:4608\n" \
  "s_waitcnt lgkmcnt(7)\n" \
  "v_mfma_f32_16x16x32_bf16 v[80:83], a[168:171], v[152:155], v[80:83]\n" \
  "ds_read_b64_tr_b16 a[112:113], v232 offset:12288\n" \
  "v_rcp_f32 v177, v177\n" \
  "v_mfma_f32_16x16x32_bf16 v[84:87], a[168:171], v[156:159], v[84:87]\n" \
  "buffer_load_dwordx4 v238, s[44:47], s87 offen offset:2048 lds\n" \
  "v_rcp_f32_e64 v178, v178\n" \
  "v_mfma_f32_16x16x32_bf16 v[88:91], a[172:175], v[152:155], v[88:91]\n" \
  "ds_read_b64_tr_b16 a[114:115], v232 offset:12800\n" \
  "v_mul_f32_e64 v33, v33, v177\n" \
  "v_mfma_f32_16x16x32_bf16 v[92:95], a[172:175], v[156:159], v[92:95]\n" \
  "ds_read_b64_tr_b16 a[116:117], v233 offset:12288\n" \
  "ds_read_b64_tr_b16 a[118:119], v233 offset:12800\n" \
  "v_mfma_f32_16x16x32_bf16 v[96:99], a[176:179], v[152:155], v[96:99]\n" \
  "ds_read_b64_tr_b16 a[120:121], v234 offset:12288\n" \
  "v_mul_f32_e64 v34, v34, v178\n" \
  "v_mfma_f32_16x16x32_bf16 v[100:103], a[176:179], v[156:159], v[100:103]\n" \
  "ds_read_b64_tr_b16 a[122:123], v234 offset:12800\n" \
  "v_cvt_pk_bf16_f32 v0, v32, v33\n" \
  "v_mfma_f32_16x16x32_bf16 v[104:107], a[180:183], v[152:155], v[104:107]\n" \
  "ds_read_b64_tr_b16 a[124:125], v235 offset:12288\n" \
  "v_mul_f32_e64 v184, v8, v244\n" \
  "v_mfma_f32_16x16x32_bf16 v[108:111], a[180:183], v[156:159], v[108:111]\n" \
  "ds_read_b64_tr_b16 a[126:127], v235 offset:12800\n" \
  "v_mul_f32_e64 v185, v9, v244\n" \
  "v_mfma_f32_16x16x32_bf16 v[112:115], a[184:187], v[152:155], v[112:115]\n" \
  "v_rcp_f32_e64 v179, v179\n" \
  "v_mfma_f32_16x16x32_bf16 v[116:119], a[184:187], v[156:159], v[116:119]\n" \
  "buffer_load_dwordx4 v239, s[44:47], s87 offen offset:3072 lds\n" \
  "v_mfma_f32_16x16x32_bf16 v[120:123], a[188:191], v[152:155], v[120:123]\n" \
  "v_mul_f32_e64 v35, v35, v179\n" \
  "v_cvt_pk_bf16_f32 v1, v34, v35\n" \
  "v_mfma_f32_16x16x32_bf16 v[124:127], a[188:191], v[156:159], v[124:127]\n" \
  "v_exp_f32_e64 v180, v180\n" \
  "s_waitcnt vmcnt(16) lgkmcnt(0)\n" \
  "s_barrier\n" \
  "; HP [9, 10] ('F', 0, 2)\n" \
  "v_mfma_f32_16x16x32_bf16 v[64:67], a[64:67], v[160:163], v[64:67]\n" \
  "ds_read_b64_tr_b16 a[128:129], v228 offset:16384\n" \
  "ds_read_b64_tr_b16 a[130:131], v228 offset:16896\n" \
  "v_mfma_f32_16x16x32_bf16 v[68:71], a[64:67], v[164:167], v[68:71]\n" \
  "ds_read_b64_tr_b16 a[132:133], v229 offset:16384\n" \
  "v_fma_f32 v180, v180, v247, v247\n" \
  "v_mfma_f32_16x16x32_bf16 v[72:75], a[68:71], v[160:163], v[72:75]\n" \
  "ds_read_b64_tr_b16 a[134:135], v229 offset:16896\n" \
  "v_mul_f32_e64 v186, v10, v244\n" \
  "v_mfma_f32_16x16x32_bf16 v[76:79], a[68:71], v[164:167], v[76:79]\n" \
  "ds_read_b64_tr_b16 a[136:137], v230 offset:16384\n" \
  "v_mul_f32_e64 v187, v11, v244\n" \
  "v_mfma_f32_16x16x32_bf16 v[80:83], a[72:75], v[160:163], v[80:83]\n" \
  "ds_read_b64_tr_b16 a[138:139], v230 offset:16896\n" \
  "ds_read_b64_tr_b16 a[140:141], v231 offset:16384\n" \
  "s_mov_b32 s86, 0x100000\n" \
  "s_add_u32 m0, s74, 0\n" \
  "v_mfma_f32_16x16x32_bf16 v[84:87], a[72:75], v[164:167], v[84:87]\n" \
  "buffer_load_dwordx4 v236, s[44:47], s86 offen lds\n" \
  "v_mfma_f32_16x16x32_bf16 v[88:91], a[76:79], v[160:163], v[88:91]\n" \
  "ds_read_b64_tr_b16 a[142:143], v231 offset:16896\n" \
  "v_mul_f32 v40, v8, v40\n" \
  "v_mfma_f32_16x16x32_bf16 v[92:95], a[76:79], v[164:167], v[92:95]\n" \
  "ds_read_b64_tr_b16 a[144:145], v228 offset:24576\n" \
  "v_mul_f32_e64 v41, v9, v41\n" \
  "v_mfma_f32_16x16x32_bf16 v[96:99], a[80:83], v[160:163], v[96:99]\n" \
  "ds_read_b64_tr_b16 a[146:147], v228 offset:25088\n" \
  "v_mul_f32_e64 v42, v10, v42\n" \
  "v_mfma_f32_16x16x32_bf16 v[100:103], a[80:83], v[164:167], v[100:103]\n" \
  "ds_read_b64_tr_b16 a[148:149], v229 offset:24576\n" \
  "v_mul_f32_e64 v43, v11, v43\n" \
  "v_mfma_f32_16x16x32_bf16 v[104:107], a[84:87], v[160:163], v[104:107]\n" \
  "ds_read_b64_tr_b16 a[150:151], v229 offset:25088\n" \
  "ds_read_b64_tr_b16 a[152:153], v230 offset:24576\n" \
  "v_mfma_f32_16x16x32_bf16 v[108:111], a[84:87], v[164:167], v[108:111]\n" \
  "ds_read_b64_tr_b16 a[154:155], v230 offset:25088\n" \
  "s_mov_b32 s87, 0x180000\n" \
  "v_mfma_f32_16x16x32_bf16 v[112:115], a[88:91], v[160:163], v[112:115]\n" \
  "ds_read_b64_tr_b16 a[156:157], v231 offset:24576\n" \
  "v_mfma_f32_16x16x32_bf16 v[116:119], a[88:91], v[164:167], v[116:119]\n" \
  "buffer_load_dwordx4 v237, s[44:47], s86 offen offset:1024 lds\n" \
  "v_mfma_f32_16x16x32_bf16 v[120:123], a[92:95], v[160:163], v[120:123]\n" \
  "ds_read_b64_tr_b16 a[158:159], v231 offset:25088\n" \
  "v_exp_f32_e64 v181, v181\n" \
  "v_mfma_f32_16x16x32_bf16 v[124:127], a[92:95], v[164:167], v[124:127]\n" \
  "ds_read_b64_tr_b16 a[160:161], v228 offset:20480\n" \
  "v_fma_f32 v181, v181, v247, v247\n" \
  "v_mfma_f32_16x16x32_bf16 v[64:67], a[96:99], v[168:171], v[64:67]\n" \
  "ds_read_b64_tr_b16 a[162:163], v228 offset:20992\n" \
  "ds_read_b64_tr_b16 a[164:165], v229 offset:20480\n" \
  "v_mfma_f32_16x16x32_bf16 v[68:71], a[96:99], v[172:175], v[68:71]\n" \
  "ds_read_b64_tr_b16 a[166:167], v229 offset:20992\n" \
  "v_mfma_f32_16x16x32_bf16 v[72:75], a[100:103], v[168:171], v[72:75]\n" \
  "ds_read_b64_tr_b16 a[168:169], v230 offset:20480\n" \
  "v_exp_f32_e64 v182, v182\n" \
  "v_mfma_f32_16x16x32_bf16 v[76:79], a[100:103], v[172:175], v[76:79]\n" \
  "ds_read_b64_tr_b16 a[170:171], v230 offset:20992\n" \
  "v_fma_f32 v182, v182, v247, v247\n" \
  "v_mfma_f32_16x16x32_bf16 v[80:83], a[104:107], v[168:171], v[80:83]\n" \
  "ds_read_b64_tr_b16 a[172:173], v231 offset:20480\n" \
  "v_mfma_f32_16x16x32_bf16 v[84:87], a[104:107], v[172:175], v[84:87]\n" \
  "buffer_load_dwordx4 v238, s[44:47], s86 offen offset:2048 lds\n" \
  "v_exp_f32_e64 v183, v183\n" \
  "v_mfma_f32_16x16x32_bf16 v[88:91], a[108:111], v[168:171], v[88:91]\n" \
  "ds_read_b64_tr_b16 a[174:175], v231 offset:20992\n" \
  "ds_read_b64_tr_b16 a[176:177], v228 offset:28672\n" \
  "v_mfma_f32_16x16x32_bf16 v[92:95], a[108:111], v[172:175], v[92:95]\n" \
  "ds_read_b64_tr_b16 a[178:179], v228 offset:29184\n" \
  "v_fma_f32 v183, v183, v247, v247\n" \
  "v_mfma_f32_16x16x32_bf16 v[96:99], a[112:115], v[168:171], v[96:99]\n" \
  "ds_read_b64_tr_b16 a[180:181], v229 offset:28672\n" \
  "v_mfma_f32_16x16x32_bf16 v[100:103], a[112:115], v[172:175], v[100:103]\n" \
  "ds_read_b64_tr_b16 a[182:183], v229 offset:29184\n" \
  "v_rcp_f32_e64 v180, v180\n" \
  "v_mfma_f32_16x16x32_bf16 v[104:107], a[116:119], v[168:171], v[104:107]\n" \
  "ds_read_b64_tr_b16 a[184:185], v230 offset:28672\n" \
  "v_mul_f32_e64 v36, v36, v180\n" \
  "v_mfma_f32_16x16x32_bf16 v[108:111], a[116:119], v[172:175], v[108:111]\n" \
  "ds_read_b64_tr_b16 a[186:187], v230 offset:29184\n" \
  "ds_read_b64_tr_b16 a[188:189], v231 offset:28672\n" \
  "v_mfma_f32_16x16x32_bf16 v[112:115], a[120:123], v[168:171], v[112:115]\n" \
  "ds_read_b64_tr_b16 a[190:191], v231 offset:29184\n" \
  "s_waitcnt lgkmcnt(7)\n" \
  "v_mfma_f32_16x16x32_bf16 v[116:119], a[120:123], v[172:175], v[116:119]\n" \
  "buffer_load_dwordx4 v239, s[44:47], s86 offen offset:3072 lds\n" \
  "v_rcp_f32 v181, v181\n" \
  "v_mfma_f32_16x16x32_bf16 v[120:123], a[124:127], v[168:171], v[120:123]\n" \
  "ds_read_b64_tr_b16 a[64:65], v232 offset:16384\n" \
  "v_mul_f32_e64 v37, v37, v181\n" \
  "v_mfma_f32_16x16x32_bf16 v[124:127], a[124:127], v[172:175], v[124:127]\n" \
  "ds_read_b64_tr_b16 a[66:67], v232 offset:16896\n" \
  "v_cvt_pk_bf16_f32 v4, v36, v37\n" \
  "v_mfma_f32_16x16x32_bf16 v[64:67], a[128:131], a[192:195], v[64:67]\n" \
  "ds_read_b64_tr_b16 a[68:69], v233 offset:16384\n" \
  "ds_read_b64_tr_b16 a[70:71], v233 offset:16896\n" \
  "v_mfma_f32_16x16x32_bf16 v[68:71], a[128:131], a[196:199], v[68:71]\n" \
  "ds_read_b64_tr_b16 a[72:73], v234 offset:16384\n" \
  "s_add_u32 m0, s75, 0\n" \
  "v_mfma_f32_16x16x32_bf16 v[72:75], a[132:135], a[192:195], v[72:75]\n" \
  "ds_read_b64_tr_b16 a[74:75], v234 offset:16896\n" \
  "v_rcp_f32 v182, v182\n" \
  "v_mfma_f32_16x16x32_bf16 v[76:79], a[132:135], a[196:199], v[76:79]\n" \
  "ds_read_b64_tr_b16 a[76:77], v235 offset:16384\n" \
  "v_mul_f32_e64 v38, v38, v182\n" \
  "v_mfma_f32_16x16x32_bf16 v[80:83], a[136:139], a[192:195], v[80:83]\n" \
  "ds_read_b64_tr_b16 a[78:79], v235 offset:16896\n" \
  "v_mul_f32_e64 v188, v12, v245\n" \
  "v_mfma_f32_16x16x32_bf16 v[84:87], a[136:139], a[196:199], v[84:87]\n" \
  "buffer_load_dwordx4 v236, s[44:47], s87 offen lds\n" \
  "v_mfma_f32_16x16x32_bf16 v[88:91], a[140:143], a[192:195], v[88:91]\n" \
  "ds_read_b64_tr_b16 a[80:81], v232 offset:24576\n" \
  "ds_read_b64_tr_b16 a[82:83], v232 offset:25088\n" \
  "v_mfma_f32_16x16x32_bf16 v[92:95], a[140:143], a[196:199], v[92:95]\n" \
  "ds_read_b64_tr_b16 a[84:85], v233 offset:24576\n" \
  "v_mul_f32_e64 v189, v13, v245\n" \
  "v_mfma_f32_16x16x32_bf16 v[96:99], a[144:147], a[192:195], v[96:99]\n" \
  "ds_read_b64_tr_b16 a[86:87], v233 offset:25088\n" \
  "v_mul_f32_e64 v190, v14, v245\n" \
  "v_mfma_f32_16x16x32_bf16 v[100:103], a[144:147], a[196:199], v[100:103]\n" \
  "ds_read_b64_tr_b16 a[88:89], v234 offset:24576\n" \
  "v_mul_f32_e64 v191, v15, v245\n" \
  "v_mfma_f32_16x16x32_bf16 v[104:107], a[148:151], a[192:195], v[104:107]\n" \
  "ds_read_b64_tr_b16 a[90:91], v234 offset:25088\n" \
  "v_mfma_f32_16x16x32_bf16 v[108:111], a[148:151], a[196:199], v[108:111]\n" \
  "ds_read_b64_tr_b16 a[92:93], v235 offset:24576\n" \
  "ds_read_b64_tr_b16 a[94:95], v235 offset:25088\n" \
  "v_mfma_f32_16x16x32_bf16 v[112:115], a[152:155], a[192:195], v[112:115]\n" \
  "ds_read_b64_tr_b16 a[96:97], v232 offset:20480\n" \
  "v_mfma_f32_16x16x32_bf16 v[116:119], a[152:155], a[196:199], v[116:119]\n" \
  "buffer_load_dwordx4 v237, s[44:47], s87 offen offset:1024 lds\n" \
  "v_rcp_f32_e64 v183, v183\n" \
  "v_mfma_f32_16x16x32_bf16 v[120:123], a[156:159], a[192:195], v[120:123]\n" \
  "ds_read_b64_tr_b16 a[98:99], v232 offset:20992\n" \
  "v_mul_f32_e64 v39, v39, v183\n" \
  "v_mfma_f32_16x16x32_bf16 v[124:127], a[156:159], a[196:199], v[124:127]\n" \
  "ds_read_b64_tr_b16 a[100:101], v233 offset:20480\n" \
  "v_cvt_pk_bf16_f32 v5, v38, v39\n" \
  "v_mfma_f32_16x16x32_bf16 v[64:67], a[160:163], a[200:203], v[64:67]\n" \
  "ds_read_b64_tr_b16 a[102:103], v233 offset:20992\n" \
  "v_mul_f32_e64 v44, v12, v44\n" \
  "v_mfma_f32_16x16x32_bf16 v[68:71], a[160:163], a[204:207], v[68:71]\n" \
  "ds_read_b64_tr_b16 a[104:105], v234 offset:20480\n" \
  "ds_read_b64_tr_b16 a[106:107], v234 offset:20992\n" \
  "v_mfma_f32_16x16x32_bf16 v[72:75], a[164:167], a[200:203], v[72:75]\n" \
  "ds_read_b64_tr_b16 a[108:109], v235 offset:20480\n" \
  "v_mul_f32_e64 v45, v13, v45\n" \
  "v_mfma_f32_16x16x32_bf16 v[76:79], a[164:167], a[204:207], v[76:79]\n" \
  "ds_read_b64_tr_b16 a[110:111], v235 offset:20992\n" \
  "v_mul_f32_e64 v46, v14, v46\n" \
  "v_mfma_f32_16x16x32_bf16 v[80:83], a[168:171], a[200:203], v[80:83]\n" \
  "ds_read_b64_tr_b16 a[112:113], v232 offset:28672\n" \
  "v_mul_f32_e64 v47, v15, v47\n" \
  "v_mfma_f32_16x16x32_bf16 v[84:87], a[168:171], a[204:207], v[84:87]\n" \
  "buffer_load_dwordx4 v238, s[44:47], s87 offen offset:2048 lds\n" \
  "v_mfma_f32_16x16x32_bf16 v[88:91], a[172:175], a[200:203], v[88:91]\n" \
  "ds_read_b64_tr_b16 a[114:115], v232 offset:29184\n" \
  "s_waitcnt lgkmcnt(6)\n" \
  "v_mfma_f32_16x16x32_bf16 v[92:95], a[172:175], a[204:207], v[92:95]\n" \
  "ds_read_b64_tr_b16 a[116:117], v233 offset:28672\n" \
  "ds_read_b64_tr_b16 a[118:119], v233 offset:29184\n" \
  "v_mfma_f32_16x16x32_bf16 v[96:99], a[176:179], a[200:203], v[96:99]\n" \
  "ds_read_b64_tr_b16 a[120:121], v234 offset:28672\n" \
  "v_mfma_f32_16x16x32_bf16 v[100:103], a[176:179], a[204:207], v[100:103]\n" \
  "ds_read_b64_tr_b16 a[122:123], v234 offset:29184\n" \
  "v_mfma_f32_16x16x32_bf16 v[104:107], a[180:183], a[200:203], v[104:107]\n" \
  "ds_read_b64_tr_b16 a[124:125], v235 offset:28672\n" \
  "v_exp_f32 v184, v184\n" \
  "v_mfma_f32_16x16x32_bf16 v[108:111], a[180:183], a[204:207], v[108:111]\n" \
  "ds_read_b64_tr_b16 a[126:127], v235 offset:29184\n" \
  "v_fma_f32 v184, v184, v246, v246\n" \
  "v_mfma_f32_16x16x32_bf16 v[112:115], a[184:187], a[200:203], v[112:115]\n" \
  "v_exp_f32_e64 v185, v185\n" \
  "v_mfma_f32_16x16x32_bf16 v[116:119], a[184:187], a[204:207], v[116:119]\n" \
  "buffer_load_dwordx4 v239, s[44:47], s87 offen offset:3072 lds\n" \
  "v_mfma_f32_16x16x32_bf16 v[120:123], a[188:191], a[200:203], v[120:123]\n" \
  "v_exp_f32_e64 v186, v186\n" \
  "v_mfma_f32_16x16x32_bf16 v[124:127], a[188:191], a[204:207], v[124:127]\n" \
  "v_exp_f32_e64 v187, v187\n" \
  "s_waitcnt vmcnt(16) lgkmcnt(0)\n" \
  "s_barrier\n" \
  "; HP [11, 12] ('F', 0, 4)\n" \
  "v_mfma_f32_16x16x32_bf16 v[64:67], a[64:67], a[208:211], v[64:67]\n" \
  "ds_read_b64_tr_b16 a[128:129], v228 offset:32768\n" \
  "ds_read_b64_tr_b16 a[130:131], v228 offset:33280\n" \
  "v_mfma_f32_16x16x32_bf16 v[68:71], a[64:67], a[212:215], v[68:71]\n" \
  "ds_read_b64_tr_b16 a[132:133], v229 offset:32768\n" \
  "v_fma_f32 v185, v185, v246, v246\n" \
  "v_mfma_f32_16x16x32_bf16 v[72:75], a[68:71], a[208:211], v[72:75]\n" \
  "ds_read_b64_tr_b16 a[134:135], v229 offset:33280\n" \
  "v_fma_f32 v186, v186, v246, v246\n" \
  "v_mfma_f32_16x16x32_bf16 v[76:79], a[68:71], a[212:215], v[76:79]\n" \
  "ds_read_b64_tr_b16 a[136:137], v230 offset:32768\n" \
  "v_fma_f32 v187, v187, v246, v246\n" \
  "v_mfma_f32_16x16x32_bf16 v[80:83], a[72:75], a[208:211], v[80:83]\n" \
  "ds_read_b64_tr_b16 a[138:139], v230 offset:33280\n" \
  "ds_read_b64_tr_b16 a[140:141], v231 offset:32768\n" \
  "s_mov_b32 s86, 0x200000\n" \
  "s_add_u32 m0, s74, 0x4000\n" \
  "v_mfma_f32_16x16x32_bf16 v[84:87], a[72:75], a[212:215], v[84:87]\n" \
  "buffer_load_dwordx4 v236, s[44:47], s86 offen lds\n" \
  "v_mfma_f32_16x16x32_bf16 v[88:91], a[76:79], a[208:211], v[88:91]\n" \
  "ds_read_b64_tr_b16 a[142:143], v231 offset:33280\n" \
  "s_mov_b32 s87, 0x280000\n" \
  "v_mfma_f32_16x16x32_bf16 v[92:95], a[76:79], a[212:215], v[92:95]\n" \
  "ds_read_b64_tr_b16 a[144:145], v228 offset:40960\n" \
  "v_rcp_f32_e64 v184, v184\n" \
  "v_mfma_f32_16x16x32_bf16 v[96:99], a[80:83], a[208:211], v[96:99]\n" \
  "ds_read_b64_tr_b16 a[146:147], v228 offset:41472\n" \
  "v_mul_f32_e64 v40, v40, v184\n" \
  "v_mfma_f32_16x16x32_bf16 v[100:103], a[80:83], a[212:215], v[100:103]\n" \
  "ds_read_b64_tr_b16 a[148:149], v229 offset:40960\n" \
  "v_mfma_f32_16x16x32_bf16 v[104:107], a[84:87], a[208:211], v[104:107]\n" \
  "ds_read_b64_tr_b16 a[150:151], v229 offset:41472\n" \
  "ds_read_b64_tr_b16 a[152:153], v230 offset:40960\n" \
  "v_mfma_f32_16x16x32_bf16 v[108:111], a[84:87], a[212:215], v[108:111]\n" \
  "ds_read_b64_tr_b16 a[154:155], v230 offset:41472\n" \
  "v_rcp_f32_e64 v185, v185\n" \
  "v_mfma_f32_16x16x32_bf16 v[112:115], a[88:91], a[208:211], v[112:115]\n" \
  "ds_read_b64_tr_b16 a[156:157], v231 offset:40960\n" \
  "v_mul_f32_e64 v41, v41, v185\n" \
  "v_mfma_f32_16x16x32_bf16 v[116:119], a[88:91], a[212:215], v[116:119]\n" \
  "buffer_load_dwordx4 v237, s[44:47], s86 offen offset:1024 lds\n" \
  "v_mfma_f32_16x16x32_bf16 v[120:123], a[92:95], a[208:211], v[120:123]\n" \
  "ds_read_b64_tr_b16 a[158:159], v231 offset:41472\n" \
  "v_cvt_pk_bf16_f32 v2, v40, v41\n" \
  "v_mfma_f32_16x16x32_bf16 v[124:127], a[92:95], a[212:215], v[124:127]\n" \
  "ds_read_b64_tr_b16 a[160:161], v228 offset:36864\n" \
  "v_mfma_f32_16x16x32_bf16 v[64:67], a[96:99], a[216:219], v[64:67]\n" \
  "ds_read_b64_tr_b16 a[162:163], v228 offset:37376\n" \
  "ds_read_b64_tr_b16 a[164:165], v229 offset:36864\n" \
  "v_rcp_f32_e64 v186, v186\n" \
  "v_mfma_f32_16x16x32_bf16 v[68:71], a[96:99], a[220:223], v[68:71]\n" \
  "ds_read_b64_tr_b16 a[166:167], v229 offset:37376\n" \
  "v_mul_f32_e64 v42, v42, v186\n" \
  "v_mfma_f32_16x16x32_bf16 v[72:75], a[100:103], a[216:219], v[72:75]\n" \
  "ds_read_b64_tr_b16 a[168:169], v230 offset:36864\n" \
  "v_mfma_f32_16x16x32_bf16 v[76:79], a[100:103], a[220:223], v[76:79]\n" \
  "ds_read_b64_tr_b16 a[170:171], v230 offset:37376\n" \
  "v_mfma_f32_16x16x32_bf16 v[80:83], a[104:107], a[216:219], v[80:83]\n" \
  "ds_read_b64_tr_b16 a[172:173], v231 offset:36864\n" \
  "v_rcp_f32_e64 v187, v187\n" \
  "v_mfma_f32_16x16x32_bf16 v[84:87], a[104:107], a[220:223], v[84:87]\n" \
  "buffer_load_dwordx4 v238, s[44:47], s86 offen offset:2048 lds\n" \
  "v_mfma_f32_16x16x32_bf16 v[88:91], a[108:111], a[216:219], v[88:91]\n" \
  "ds_read_b64_tr_b16 a[174:175], v231 offset:37376\n" \
  "ds_read_b64_tr_b16 a[176:177], v228 offset:45056\n" \
  "v_mul_f32_e64 v43, v43, v187\n" \
  "v_mfma_f32_16x16x32_bf16 v[92:95], a[108:111], a[220:223], v[92:95]\n" \
  "ds_read_b64_tr_b16 a[178:179], v228 offset:45568\n" \
  "v_cvt_pk_bf16_f32 v3, v42, v43\n" \
  "v_mfma_f32_16x16x32_bf16 v[96:99], a[112:115], a[216:219], v[96:99]\n" \
  "buffer_store_dwordx4 v[0:3], v250, s[60:63], s88 offen sc1\n" \
  "v_mfma_f32_16x16x32_bf16 v[100:103], a[112:115], a[220:223], v[100:103]\n" \
  "ds_read_b64_tr_b16 a[180:181], v229 offset:45056\n" \
  "ds_read_b64_tr_b16 a[182:183], v229 offset:45568\n" \
  "v_mfma_f32_16x16x32_bf16 v[104:107], a[116:119], a[216:219], v[104:107]\n" \
  "ds_read_b64_tr_b16 a[184:185], v230 offset:45056\n" \
  "s_waitcnt lgkmcnt(6)\n" \
  "v_mfma_f32_16x16x32_bf16 v[108:111], a[116:119], a[220:223], v[108:111]\n" \
  "ds_read_b64_tr_b16 a[186:187], v230 offset:45568\n" \
  "ds_read_b64_tr_b16 a[188:189], v231 offset:45056\n" \
  "v_mfma_f32_16x16x32_bf16 v[112:115], a[120:123], a[216:219], v[112:115]\n" \
  "ds_read_b64_tr_b16 a[190:191], v231 offset:45568\n" \
  "v_exp_f32 v188, v188\n" \
  "v_mfma_f32_16x16x32_bf16 v[116:119], a[120:123], a[220:223], v[116:119]\n" \
  "buffer_load_dwordx4 v239, s[44:47], s86 offen offset:3072 lds\n" \
  "v_mfma_f32_16x16x32_bf16 v[120:123], a[124:127], a[216:219], v[120:123]\n" \
  "ds_read_b64_tr_b16 a[64:65], v232 offset:32768\n" \
  "v_fma_f32 v188, v188, v247, v247\n" \
  "v_mfma_f32_16x16x32_bf16 v[124:127], a[124:127], a[220:223], v[124:127]\n" \
  "ds_read_b64_tr_b16 a[66:67], v232 offset:33280\n" \
  "s_add_u32 m0, s75, 0x4000\n" \
  "v_exp_f32_e64 v189, v189\n" \
  "v_mfma_f32_16x16x32_bf16 v[64:67], a[128:131], a[224:227], v[64:67]\n" \
  "ds_read_b64_tr_b16 a[68:69], v233 offset:32768\n" \
  "ds_read_b64_tr_b16 a[70:71], v233 offset:33280\n" \
  "v_mfma_f32_16x16x32_bf16 v[68:71], a[128:131], a[228:231], v[68:71]\n" \
  "ds_read_b64_tr_b16 a[72:73], v234 offset:32768\n" \
  "v_fma_f32 v189, v189, v247, v247\n" \
  "v_mfma_f32_16x16x32_bf16 v[72:75], a[132:135], a[224:227], v[72:75]\n" \
  "ds_read_b64_tr_b16 a[74:75], v234 offset:33280\n" \
  "v_mfma_f32_16x16x32_bf16 v[76:79], a[132:135], a[228:231], v[76:79]\n" \
  "ds_read_b64_tr_b16 a[76:77], v235 offset:32768\n" \
  "v_exp_f32_e64 v190, v190\n" \
  "v_mfma_f32_16x16x32_bf16 v[80:83], a[136:139], a[224:227], v[80:83]\n" \
  "ds_read_b64_tr_b16 a[78:79], v235 offset:33280\n" \
  "v_fma_f32 v190, v190, v247, v247\n" \
  "v_mfma_f32_16x16x32_bf16 v[84:87], a[136:139], a[228:231], v[84:87]\n" \
  "buffer_load_dwordx4 v236, s[44:47], s87 offen lds\n" \
  "v_mfma_f32_16x16x32_bf16 v[88:91], a[140:143], a[224:227], v[88:91]\n" \
  "ds_read_b64_tr_b16 a[80:81], v232 offset:40960\n" \
  "ds_read_b64_tr_b16 a[82:83], v232 offset:41472\n" \
  "v_mfma_f32_16x16x32_bf16 v[92:95], a[140:143], a[228:231], v[92:95]\n" \
  "ds_read_b64_tr_b16 a[84:85], v233 offset:40960\n" \
  "v_exp_f32_e64 v191, v191\n" \
  "v_mfma_f32_16x16x32_bf16 v[96:99], a[144:147], a[224:227], v[96:99]\n" \
  "ds_read_b64_tr_b16 a[86:87], v233 offset:41472\n" \
  "v_fma_f32 v191, v191, v247, v247\n" \
  "v_mfma_f32_16x16x32_bf16 v[100:103], a[144:147], a[228:231], v[100:103]\n" \
  "ds_read_b64_tr_b16 a[88:89], v234 offset:40960\n" \
  "v_mfma_f32_16x16x32_bf16 v[104:107], a[148:151], a[224:227], v[104:107]\n" \
  "ds_read_b64_tr_b16 a[90:91], v234 offset:41472\n" \
  "v_mfma_f32_16x16x32_bf16 v[108:111], a[148:151], a[228:231], v[108:111]\n" \
  "ds_read_b64_tr_b16 a[92:93], v235 offset:40960\n" \
  "ds_read_b64_tr_b16 a[94:95], v235 offset:41472\n" \
  "v_rcp_f32_e64 v188, v188\n" \
  "v_mfma_f32_16x16x32_bf16 v[112:115], a[152:155], a[224:227], v[112:115]\n" \
  "ds_read_b64_tr_b16 a[96:97], v232 offset:36864\n" \
  "v_mul_f32_e64 v44, v44, v188\n" \
  "v_mfma_f32_16x16x32_bf16 v[116:119], a[152:155], a[228:231], v[116:119]\n" \
  "buffer_load_dwordx4 v237, s[44:47], s87 offen offset:1024 lds\n" \
  "v_mfma_f32_16x16x32_bf16 v[120:123], a[156:159], a[224:227], v[120:123]\n" \
  "ds_read_b64_tr_b16 a[98:99], v232 offset:37376\n" \
  "v_rcp_f32_e64 v189, v189\n" \
  "v_mfma_f32_16x16x32_bf16 v[124:127], a[156:159], a[228:231], v[124:127]\n" \
  "ds_read_b64_tr_b16 a[100:101], v233 offset:36864\n" \
  "v_mul_f32_e64 v45, v45, v189\n" \
  "v_mfma_f32_16x16x32_bf16 v[64:67], a[160:163], a[232:235], v[64:67]\n" \
  "ds_read_b64_tr_b16 a[102:103], v233 offset:37376\n" \
  "v_cvt_pk_bf16_f32 v6, v44, v45\n" \
  "v_mfma_f32_16x16x32_bf16 v[68:71], a[160:163], a[236:239], v[68:71]\n" \
  "ds_read_b64_tr_b16 a[104:105], v234 offset:36864\n" \
  "ds_read_b64_tr_b16 a[106:107], v234 offset:37376\n" \
  "v_mfma_f32_16x16x32_bf16 v[72:75], a[164:167], a[232:235], v[72:75]\n" \
  "ds_read_b64_tr_b16 a[108:109], v235 offset:36864\n" \
  "v_mfma_f32_16x16x32_bf16 v[76:79], a[164:167], a[236:239], v[76:79]\n" \
  "ds_read_b64_tr_b16 a[110:111], v235 offset:37376\n" \
  "s_waitcnt lgkmcnt(7)\n" \
  "v_mfma_f32_16x16x32_bf16 v[80:83], a[168:171], a[232:235], v[80:83]\n" \
  "ds_read_b64_tr_b16 a[112:113], v232 offset:45056\n" \
  "v_rcp_f32 v190, v190\n" \
  "v_mfma_f32_16x16x32_bf16 v[84:87], a[168:171], a[236:239], v[84:87]\n" \
  "buffer_load_dwordx4 v238, s[44:47], s87 offen offset:2048 lds\n" \
  "v_mfma_f32_16x16x32_bf16 v[88:91], a[172:175], a[232:235], v[88:91]\n" \
  "ds_read_b64_tr_b16 a[114:115], v232 offset:45568\n" \
  "v_mul_f32_e64 v46, v46, v190\n" \
  "v_mfma_f32_16x16x32_bf16 v[92:95], a[172:175], a[236:239], v[92:95]\n" \
  "ds_read_b64_tr_b16 a[116:117], v233 offset:45056\n" \
  "ds_read_b64_tr_b16 a[118:119], v233 offset:45568\n" \
  "v_mfma_f32_16x16x32_bf16 v[96:99], a[176:179], a[232:235], v[96:99]\n" \
  "ds_read_b64_tr_b16 a[120:121], v234 offset:45056\n" \
  "v_rcp_f32_e64 v191, v191\n" \
  "v_mfma_f32_16x16x32_bf16 v[100:103], a[176:179], a[236:239], v[100:103]\n" \
  "ds_read_b64_tr_b16 a[122:123], v234 offset:45568\n" \
  "v_mul_f32_e64 v47, v47, v191\n" \
  "v_mfma_f32_16x16x32_bf16 v[104:107], a[180:183], a[232:235], v[104:107]\n" \
  "ds_read_b64_tr_b16 a[124:125], v235 offset:45056\n" \
  "v_cvt_pk_bf16_f32 v7, v46, v47\n" \
  "v_mfma_f32_16x16x32_bf16 v[108:111], a[180:183], a[236:239], v[108:111]\n" \
  "buffer_store_dwordx4 v[4:7], v250, s[60:63], s90 offen sc1\n" \
  "v_mfma_f32_16x16x32_bf16 v[112:115], a[184:187], a[232:235], v[112:115]\n" \
  "v_mfma_f32_16x16x32_bf16 v[116:119], a[184:187], a[236:239], v[116:119]\n" \
  "buffer_load_dwordx4 v239, s[44:47], s87 offen offset:3072 lds\n" \
  "v_mfma_f32_16x16x32_bf16 v[120:123], a[188:191], a[232:235], v[120:123]\n" \
  "v_mfma_f32_16x16x32_bf16 v[124:127], a[188:191], a[236:239], v[124:127]\n" \
  "ds_read_b64_tr_b16 a[126:127], v235 offset:45568\n" \
  "s_waitcnt vmcnt(18) lgkmcnt(0)\n" \
  "s_barrier\n" \
  "s_nop 7\n" \
  "s_branch L_join_%=\n" \
  "L_big_%=:\n" \
  "s_load_dwordx16 s[12:27], %0, 0x0\n" \
  "s_load_dwordx4 s[28:31], %0, 0x40\n" \
  "v_mbcnt_lo_u32_b32 v253, -1, 0\n" \
  "v_mbcnt_hi_u32_b32 v253, -1, v253\n" \
  "s_mov_b32 s94, 0\n" \
  "s_lshr_b32 s95, %4, 0\n" \
  "s_and_b32 s95, s95, 7\n" \
  "s_mul_i32 s95, s95, 0x4000\n" \
  "s_add_u32 s94, s94, s95\n" \
  "s_mul_i32 s95, %1, 0x1000\n" \
  "s_add_u32 s94, s94, s95\n" \
  "v_writelane_b32 v255, s94, 36\n" \
  "v_lshlrev_b32 v252, 6, v253\n" \
  "v_add_u32_e64 v252, s94, v252\n" \
  "v_min_u32 v252, L_rare0_%=-L_begin_%=, v252\n" \
  "global_load_dword a0, v252, s[34:35]\n" \
  "v_lshrrev_b32 v252, 3, v253\n" \
  "v_xor_b32 v251, v252, v253\n" \
  "v_and_b32_e64 v251, 7, v251\n" \
  "v_mul_u32_u24 v198, 0x380, v252\n" \
  "v_lshl_add_u32 v198, v251, 4, v198\n" \
  "s_mul_i32 s85, %1, 28672\n" \
  "s_mov_b32 s84, %2\n" \
  "s_lshl_b32 s88, %1, 14\n" \
  "s_add_u32 s88, s88, 0x1c000\n" \
  "s_cmp_lt_u32 %1, 3\n" \
  "s_cselect_b32 s88, s88, 0xc000\n" \
  "s_cmp_gt_u32 %1, 0\n" \
  "s_cselect_b32 s88, s88, 0x1c000\n" \
  "s_add_u32 s88, s88, s84\n" \
  "s_waitcnt lgkmcnt(0)\n" \
  "s_mov_b64 s[56:57], s[12:13]\n" \
  "s_mov_b32 s58, 0\n" \
  "s_mul_i32 s59, %4, 0x1c000\n" \
  "s_add_u32 s58, s58, s59\n" \
  "s_add_u32 s56, s56, s58\n" \
  "s_addc_u32 s57, s57, 0\n" \
  "s_and_b32 s57, s57, 0xffff\n" \
  "s_mov_b32 s58, 0x1c000\n" \
  "s_mov_b32 s59, 0x20000\n" \
  "s_add_u32 s90, s85, 0\n" \
  "s_add_u32 m0, s88, 0\n" \
  "s_nop 0\n" \
  "buffer_load_dwordx4 v198, s[56:59], s90 offen lds\n" \
  "s_add_u32 s91, s85, 0x1c00\n" \
  "s_add_u32 m0, s88, 0x1c00\n" \
  "s_nop 0\n" \
  "buffer_load_dwordx4 v198, s[56:59], s91 offen lds\n" \
  "s_add_u32 m0, s88, 0x380\n" \
  "s_nop 0\n" \
  "buffer_load_dwordx4 v198, s[56:59], s90 offen offset:128 lds\n" \
  "s_add_u32 m0, s88, 0x1f80\n" \
  "s_nop 0\n" \
  "buffer_load_dwordx4 v198, s[56:59], s91 offen offset:128 lds\n" \
  "s_cmp_lt_u32 %1, 2\n" \
  "s_cselect_b64 s[22:23], s[22:23], s[24:25]\n" \
  "s_mov_b64 s[48:49], s[22:23]\n" \
  "s_mov_b32 s50, 0\n" \
  "s_lshr_b32 s51, %3, 2\n" \
  "s_mul_i32 s51, s51, 0x380\n" \
  "s_add_u32 s50, s50, s51\n" \
  "s_add_u32 s48, s48, s50\n" \
  "s_addc_u32 s49, s49, 0\n" \
  "s_and_b32 s49, s49, 0xffff\n" \
  "s_mov_b32 s50, 0x188000\n" \
  "s_lshr_b32 s51, %3, 2\n" \
  "s_mul_i32 s51, s51, 0xfffffc80\n" \
  "s_add_u32 s50, s50, s51\n" \
  "s_mov_b32 s51, 0x20000\n" \
  "s_mov_b32 s72, 0\n" \
  "s_lshr_b32 s95, %3, 0\n" \
  "s_and_b32 s95, s95, 1\n" \
  "s_mul_i32 s95, s95, 0x100\n" \
  "s_add_u32 s72, s72, s95\n" \
  "s_mov_b32 s94, 0\n" \
  "s_mul_i32 s95, %4, 0x80\n" \
  "s_add_u32 s94, s94, s95\n" \
  "s_lshr_b32 s95, %1, 0\n" \
  "s_and_b32 s95, s95, 1\n" \
  "s_mul_i32 s95, s95, 64\n" \
  "s_add_u32 s94, s94, s95\n" \
  "v_add_u32 v251, s94, v253\n" \
  "v_lshrrev_b32 v183, 2, v251\n" \
  "v_and_b32 v249, 3, v251\n" \
  "v_mul_u32_u24 v183, 0xe00, v183\n" \
  "v_and_b32_e64 v249, 3, v249\n" \
  "v_lshl_add_u32 v183, v249, 6, v183\n" \
  "v_add_u32_e64 v183, s72, v183\n" \
  "s_add_u32 m0, s88, 0x700\n" \
  "s_nop 0\n" \
  "buffer_load_dwordx4 v198, s[56:59], s90 offen offset:256 lds\n" \
  "s_add_u32 m0, s88, 0x2300\n" \
  "s_nop 0\n" \
  "buffer_load_dwordx4 v198, s[56:59], s91 offen offset:256 lds\n" \
  "s_add_u32 m0, s88, 0xa80\n" \
  "s_nop 0\n" \
  "buffer_load_dwordx4 v198, s[56:59], s90 offen offset:384 lds\n" \
  "s_add_u32 m0, s88, 0x2680\n" \
  "s_nop 0\n" \
  "buffer_load_dwordx4 v198, s[56:59], s91 offen offset:384 lds\n" \
  "s_add_u32 m0, s88, 0xe00\n" \
  "s_nop 0\n" \
  "buffer_load_dwordx4 v198, s[56:59], s90 offen offset:512 lds\n" \
  "s_add_u32 m0, s88, 0x2a00\n" \
  "s_nop 0\n" \
  "buffer_load_dwordx4 v198, s[56:59], s91 offen offset:512 lds\n" \
  "s_add_u32 m0, s88, 0x1180\n" \
  "s_nop 0\n" \
  "buffer_load_dwordx4 v198, s[56:59], s90 offen offset:640 lds\n" \
  "s_add_u32 m0, s88, 0x2d80\n" \
  "s_nop 0\n" \
  "buffer_load_dwordx4 v198, s[56:59], s91 offen offset:640 lds\n" \
  "s_add_u32 m0, s88, 0x1500\n" \
  "s_nop 0\n" \
  "buffer_load_dwordx4 v198, s[56:59], s90 offen offset:768 lds\n" \
  "s_add_u32 m0, s88, 0x3100\n" \
  "s_nop 0\n" \
  "buffer_load_dwordx4 v198, s[56:59], s91 offen offset:768 lds\n" \
  "s_lshl_b32 s83, %1, 12\n" \
  "s_add_u32 s75, s83, s84\n" \
  "s_add_u32 s74, s75, 0x10000\n" \
  "s_and_b32 s92, %1, 1\n" \
  "s_lshl_b32 s92, s92, 5\n" \
  "s_lshr_b32 s93, %1, 1\n" \
  "s_lshl_b32 s93, s93, 7\n" \
  "v_and_b32 v249, 2, v252\n" \
  "v_add_u32 v252, s92, v252\n" \
  "v_xor_b32 v249, v249, v253\n" \
  "v_and_b32 v249, 7, v249\n" \
  "v_mul_u32_u24 v251, 0xe00, v252\n" \
  "v_xor_b32_e64 v179, 0, v249\n" \
  "v_lshl_add_u32 v179, v179, 4, v251\n" \
  "v_add_u32 v240, 0, v179\n" \
  "v_xor_b32 v179, 4, v249\n" \
  "v_lshl_add_u32 v179, v179, 4, v251\n" \
  "v_add_u32 v241, 0x6c00, v179\n" \
  "v_xor_b32_e64 v179, 0, v249\n" \
  "v_lshl_add_u32 v179, v179, 4, v251\n" \
  "v_add_u32 v242, 0xd800, v179\n" \
  "v_xor_b32_e64 v179, 4, v249\n" \
  "v_lshl_add_u32 v179, v179, 4, v251\n" \
  "v_add_u32 v243, 0x14400, v179\n" \
  "v_mul_u32_u24 v251, 0x2000, v252\n" \
  "v_add_u32 v251, s93, v251\n" \
  "v_xor_b32 v179, 0, v249\n" \
  "v_lshl_add_u32 v179, v179, 4, v251\n" \
  "v_add_u32 v236, 0, v179\n" \
  "v_xor_b32 v179, 4, v249\n" \
  "v_lshl_add_u32 v179, v179, 4, v251\n" \
  "v_add_u32 v237, 0xfc00, v179\n" \
  "v_xor_b32_e64 v179, 0, v249\n" \
  "v_lshl_add_u32 v179, v179, 4, v251\n" \
  "v_add_u32 v238, 0x1f800, v179\n" \
  "v_xor_b32_e64 v179, 4, v249\n" \
  "v_lshl_add_u32 v179, v179, 4, v251\n" \
  "v_add_u32 v239, 0x2f400, v179\n" \
  "s_mov_b64 s[44:45], s[18:19]\n" \
  "s_mov_b32 s46, 0\n" \
  "s_mul_i32 s47, %3, 0x200\n" \
  "s_add_u32 s46, s46, s47\n" \
  "s_add_u32 s44, s44, s46\n" \
  "s_addc_u32 s45, s45, 0\n" \
  "s_and_b32 s45, s45, 0xffff\n" \
  "s_mov_b32 s46, 0x380000\n" \
  "s_mul_i32 s47, %3, 0xfffffe00\n" \
  "s_add_u32 s46, s46, s47\n" \
  "s_mov_b32 s47, 0x20000\n" \
  "v_and_b32 v176, 15, v253\n" \
  "v_lshrrev_b32 v177, 4, v253\n" \
  "v_lshrrev_b32_e64 v251, 3, v176\n" \
  "v_mul_u32_u24 v251, 0x1c00, v251\n" \
  "v_and_b32_e64 v252, 7, v176\n" \
  "v_lshl_add_u32 v251, v252, 7, v251\n" \
  "v_add_u32 v251, s88, v251\n" \
  "v_add_u32 v249, 0, v177\n" \
  "v_xor_b32_e64 v249, v249, v252\n" \
  "v_lshl_add_u32 v204, v249, 4, v251\n" \
  "v_add_u32 v249, 4, v177\n" \
  "v_xor_b32 v249, v249, v252\n" \
  "v_lshl_add_u32 v205, v249, 4, v251\n" \
  "v_mov_b32 v200, 0x3f803f80\n" \
  "v_mov_b32 v201, 0x3f803f80\n" \
  "v_mov_b32 v202, 0x3f803f80\n" \
  "v_mov_b32 v203, 0x3f803f80\n" \
  "v_mov_b32 v192, 0\n" \
  "v_mov_b32 v193, 0\n" \
  "v_mov_b32 v196, 0\n" \
  "v_mov_b32 v197, 0\n" \
  "v_and_b32 v251, 1, v253\n" \
  "v_lshlrev_b32 v251, 4, v251\n" \
  "v_mov_b32 v252, 0x3f80\n" \
  "v_lshlrev_b32_e64 v251, v251, v252\n" \
  "s_mov_b32 s92, 0x300003\n" \
  "s_mov_b32 s93, 0x30000300\n" \
  "v_cndmask_b32 v194, 0, v251, s[92:93]\n" \
  "s_mov_b32 s94, 0xc0000c\n" \
  "s_mov_b32 s95, 0xc0000c00\n" \
  "v_cndmask_b32 v195, 0, v251, s[94:95]\n" \
  "s_mov_b64 s[36:37], s[14:15]\n" \
  "s_mov_b32 s38, 0\n" \
  "s_mul_i32 s39, %3, 0x100000\n" \
  "s_add_u32 s38, s38, s39\n" \
  "s_add_u32 s36, s36, s38\n" \
  "s_addc_u32 s37, s37, 0\n" \
  "s_and_b32 s37, s37, 0xffff\n" \
  "s_mov_b32 s38, 0x100000\n" \
  "s_mov_b32 s39, 0x20000\n" \
  "s_mov_b64 s[40:41], s[16:17]\n" \
  "s_mov_b32 s42, 0\n" \
  "s_mul_i32 s43, %3, 0x100000\n" \
  "s_add_u32 s42, s42, s43\n" \
  "s_add_u32 s40, s40, s42\n" \
  "s_addc_u32 s41, s41, 0\n" \
  "s_and_b32 s41, s41, 0xffff\n" \
  "s_mov_b32 s42, 0x100000\n" \
  "s_mov_b32 s43, 0x20000\n" \
  "s_mov_b64 s[52:53], s[20:21]\n" \
  "s_mov_b32 s54, 0\n" \
  "s_mul_i32 s55, %3, 0x38000\n" \
  "s_add_u32 s54, s54, s55\n" \
  "s_add_u32 s52, s52, s54\n" \
  "s_addc_u32 s53, s53, 0\n" \
  "s_and_b32 s53, s53, 0xffff\n" \
  "s_mov_b32 s54, 0x38000\n" \
  "s_mov_b32 s55, 0x20000\n" \
  "s_mov_b64 s[60:61], s[28:29]\n" \
  "s_mov_b32 s62, 0x1c00000\n" \
  "s_mul_i32 s63, %4, 0x70000\n" \
  "s_add_u32 s62, s62, s63\n" \
  "s_mul_i32 s63, %1, 0x1c000\n" \
  "s_add_u32 s62, s62, s63\n" \
  "s_add_u32 s60, s60, s62\n" \
  "s_addc_u32 s61, s61, 0\n" \
  "s_and_b32 s61, s61, 0xffff\n" \
  "s_mov_b32 s62, 0x1c000\n" \
  "s_mov_b32 s63, 0x20000\n" \
  "s_mov_b64 s[64:65], s[28:29]\n" \
  "s_mov_b32 s66, 0\n" \
  "s_mul_i32 s67, %3, 0x3800\n" \
  "s_add_u32 s66, s66, s67\n" \
  "s_mul_i32 s67, %4, 0x1c0000\n" \
  "s_add_u32 s66, s66, s67\n" \
  "s_mul_i32 s67, %1, 0x70000\n" \
  "s_add_u32 s66, s66, s67\n" \
  "s_add_u32 s64, s64, s66\n" \
  "s_addc_u32 s65, s65, 0\n" \
  "s_and_b32 s65, s65, 0xffff\n" \
  "s_mov_b32 s66, 0x3b800\n" \
  "s_mov_b32 s67, 0x20000\n" \
  "s_mov_b64 s[68:69], s[28:29]\n" \
  "s_mov_b32 s70, 0x2300000\n" \
  "s_add_u32 s68, s68, s70\n" \
  "s_addc_u32 s69, s69, 0\n" \
  "s_and_b32 s69, s69, 0xffff\n" \
  "s_mov_b32 s70, 0x20080\n" \
  "s_mov_b32 s71, 0x20000\n" \
  "s_mov_b32 s78, 0x80\n" \
  "s_mul_i32 s94, %4, 0x2000\n" \
  "s_add_u32 s78, s78, s94\n" \
  "s_mul_i32 s94, %1, 0x800\n" \
  "s_add_u32 s78, s78, s94\n" \
  "s_mul_i32 s94, %3, 0x80\n" \
  "s_add_u32 s78, s78, s94\n" \
  "s_mov_b64 s[92:93], s[26:27]\n" \
  "s_mov_b32 s94, 0\n" \
  "s_lshr_b32 s95, %6, 0\n" \
  "s_and_b32 s95, s95, 7\n" \
  "s_mul_i32 s95, s95, 0x80\n" \
  "s_add_u32 s94, s94, s95\n" \
  "s_add_u32 s92, s92, s94\n" \
  "s_addc_u32 s93, s93, 0\n" \
  "s_and_b32 s93, s93, 0xffff\n" \
  "s_mov_b32 s94, 0x188000\n" \
  "s_lshr_b32 s95, %6, 0\n" \
  "s_and_b32 s95, s95, 7\n" \
  "s_mul_i32 s95, s95, 0xffffff80\n" \
  "s_add_u32 s94, s94, s95\n" \
  "s_mov_b32 s95, 0x20000\n" \
  "s_cmp_lt_u32 %6, 8\n" \
  "s_cselect_b32 s94, s94, 0\n" \
  "v_writelane_b32 v255, s92, 37\n" \
  "v_writelane_b32 v255, s93, 38\n" \
  "v_writelane_b32 v255, s94, 39\n" \
  "s_mov_b64 s[92:93], s[28:29]\n" \
  "s_mov_b32 s94, 0x1c00000\n" \
  "s_mul_i32 s95, %5, 0x38000\n" \
  "s_add_u32 s94, s94, s95\n" \
  "s_lshr_b32 s95, %1, 1\n" \
  "s_mul_i32 s95, s95, 0x1c000\n" \
  "s_add_u32 s94, s94, s95\n" \
  "s_lshr_b32 s95, %1, 0\n" \
  "s_and_b32 s95, s95, 1\n" \
  "s_mul_i32 s95, s95, 0x800\n" \
  "s_add_u32 s94, s94, s95\n" \
  "s_add_u32 s92, s92, s94\n" \
  "s_addc_u32 s93, s93, 0\n" \
  "s_and_b32 s93, s93, 0xffff\n" \
  "s_mov_b32 s94, 0x1c000\n" \
  "s_mov_b32 s95, 0x20000\n" \
  "s_cmp_lt_u32 %6, 8\n" \
  "s_cselect_b32 s94, s94, 0\n" \
  "v_writelane_b32 v255, s92, 40\n" \
  "v_writelane_b32 v255, s93, 41\n" \
  "v_writelane_b32 v255, s94, 42\n" \
  "s_mov_b64 s[92:93], s[28:29]\n" \
  "s_mov_b32 s94, 0\n" \
  "s_mul_i32 s95, %5, 0xe0000\n" \
  "s_add_u32 s94, s94, s95\n" \
  "s_mul_i32 s95, %1, 0x38000\n" \
  "s_add_u32 s94, s94, s95\n" \
  "s_lshr_b32 s95, %6, 0\n" \
  "s_and_b32 s95, s95, 7\n" \
  "s_mul_i32 s95, s95, 0x800\n" \
  "s_add_u32 s94, s94, s95\n" \
  "s_add_u32 s92, s92, s94\n" \
  "s_addc_u32 s93, s93, 0\n" \
  "s_and_b32 s93, s93, 0xffff\n" \
  "s_mov_b32 s94, 0x35000\n" \
  "s_mov_b32 s95, 0x20000\n" \
  "s_cmp_lt_u32 %6, 8\n" \
  "s_cselect_b32 s94, s94, 0\n" \
  "v_writelane_b32 v255, s92, 43\n" \
  "v_writelane_b32 v255, s93, 44\n" \
  "v_writelane_b32 v255, s94, 45\n" \
  "s_mov_b64 s[92:93], s[12:13]\n" \
  "s_mov_b32 s94, 0\n" \
  "s_mul_i32 s95, %5, 0xe000\n" \
  "s_add_u32 s94, s94, s95\n" \
  "s_mul_i32 s95, %1, 0x3800\n" \
  "s_add_u32 s94, s94, s95\n" \
  "s_lshr_b32 s95, %6, 0\n" \
  "s_and_b32 s95, s95, 7\n" \
  "s_mul_i32 s95, s95, 0x80\n" \
  "s_add_u32 s94, s94, s95\n" \
  "s_add_u32 s92, s92, s94\n" \
  "s_addc_u32 s93, s93, 0\n" \
  "s_and_b32 s93, s93, 0xffff\n" \
  "s_mov_b32 s94, 0x3500\n" \
  "s_mov_b32 s95, 0x20000\n" \
  "s_cmp_lt_u32 %6, 8\n" \
  "s_cselect_b32 s94, s94, 0\n" \
  "v_writelane_b32 v255, s92, 46\n" \
  "v_writelane_b32 v255, s93, 47\n" \
  "v_writelane_b32 v255, s94, 48\n" \
  "s_mov_b64 s[92:93], s[30:31]\n" \
  "s_mov_b32 s94, 0\n" \
  "s_mul_i32 s95, %5, 0xe000\n" \
  "s_add_u32 s94, s94, s95\n" \
  "s_mul_i32 s95, %1, 0x3800\n" \
  "s_add_u32 s94, s94, s95\n" \
  "s_lshr_b32 s95, %6, 0\n" \
  "s_and_b32 s95, s95, 7\n" \
  "s_mul_i32 s95, s95, 0x80\n" \
  "s_add_u32 s94, s94, s95\n" \
  "s_add_u32 s92, s92, s94\n" \
  "s_addc_u32 s93, s93, 0\n" \
  "s_and_b32 s93, s93, 0xffff\n" \
  "s_mov_b32 s94, 0x3500\n" \
  "s_mov_b32 s95, 0x20000\n" \
  "s_cmp_lt_u32 %6, 8\n" \
  "s_cselect_b32 s94, s94, 0\n" \
  "v_writelane_b32 v255, s92, 49\n" \
  "v_writelane_b32 v255, s93, 50\n" \
  "v_writelane_b32 v255, s94, 51\n" \
  "s_mov_b32 s94, 0x80\n" \
  "s_mul_i32 s95, %5, 0x1000\n" \
  "s_add_u32 s94, s94, s95\n" \
  "s_lshr_b32 s95, %1, 1\n" \
  "s_mul_i32 s95, s95, 0x800\n" \
  "s_add_u32 s94, s94, s95\n" \
  "v_writelane_b32 v255, s94, 52\n" \
  "s_mov_b32 s94, 0\n" \
  "s_mul_i32 s95, %3, 0x19000\n" \
  "s_add_u32 s94, s94, s95\n" \
  "s_mul_i32 s95, %4, 0x1900\n" \
  "s_add_u32 s94, s94, s95\n" \
  "s_mul_i32 s95, %1, 0x640\n" \
  "s_add_u32 s94, s94, s95\n" \
  "v_writelane_b32 v255, s94, 53\n" \
  "s_mul_i32 s33, %3, 0\n" \
  "s_mov_b32 s76, 0x40004000\n" \
  "s_lshl_b32 s77, %1, 3\n" \
  "v_lshlrev_b32 v250, 4, v253\n" \
  "v_lshlrev_b32 v254, 24, v253\n" \
  "v_bfe_u32 v251, v253, 2, 2\n" \
  "v_lshl_add_u32 v251, v177, 3, v251\n" \
  "v_lshlrev_b32 v251, 7, v251\n" \
  "v_and_b32 v252, 3, v253\n" \
  "v_lshl_add_u32 v251, v252, 3, v251\n" \
  "v_add_u32_e64 v251, s84, v251\n" \
  "v_bfe_u32 v252, v253, 3, 2\n" \
  "v_xor_b32_e64 v249, 0, v252\n" \
  "v_lshl_add_u32 v232, v249, 5, v251\n" \
  "v_add_u32 v228, 0x10000, v232\n" \
  "v_xor_b32_e64 v249, 1, v252\n" \
  "v_lshl_add_u32 v233, v249, 5, v251\n" \
  "v_add_u32 v229, 0x10000, v233\n" \
  "v_xor_b32_e64 v249, 2, v252\n" \
  "v_lshl_add_u32 v234, v249, 5, v251\n" \
  "v_add_u32 v230, 0x10000, v234\n" \
  "v_xor_b32_e64 v249, 3, v252\n" \
  "v_lshl_add_u32 v235, v249, 5, v251\n" \
  "v_add_u32 v231, 0x10000, v235\n" \
  "v_lshrrev_b32 v178, 5, v253\n" \
  "v_lshlrev_b32 v252, 9, v178\n" \
  "v_add_u32 v252, s83, v252\n" \
  "v_add_u32 v249, 0, v178\n" \
  "v_add_u32 v249, s77, v249\n" \
  "v_and_b32 v249, 15, v249\n" \
  "v_xor_b32 v249, v249, v253\n" \
  "v_and_b32 v249, 31, v249\n" \
  "v_lshl_add_u32 v220, v249, 4, v252\n" \
  "v_add_u32 v249, 0, v178\n" \
  "v_lshlrev_b32 v249, 1, v249\n" \
  "v_xor_b32 v249, v249, v253\n" \
  "v_and_b32 v249, 31, v249\n" \
  "v_lshl_add_u32 v224, v249, 4, v252\n" \
  "v_add_u32 v249, 2, v178\n" \
  "v_add_u32 v249, s77, v249\n" \
  "v_and_b32 v249, 15, v249\n" \
  "v_xor_b32 v249, v249, v253\n" \
  "v_and_b32_e64 v249, 31, v249\n" \
  "v_lshl_add_u32 v221, v249, 4, v252\n" \
  "v_add_u32 v249, 2, v178\n" \
  "v_lshlrev_b32 v249, 1, v249\n" \
  "v_xor_b32 v249, v249, v253\n" \
  "v_and_b32 v249, 31, v249\n" \
  "v_lshl_add_u32 v225, v249, 4, v252\n" \
  "v_add_u32 v249, 4, v178\n" \
  "v_add_u32 v249, s77, v249\n" \
  "v_and_b32 v249, 15, v249\n" \
  "v_xor_b32 v249, v249, v253\n" \
  "v_and_b32_e64 v249, 31, v249\n" \
  "v_lshl_add_u32 v222, v249, 4, v252\n" \
  "v_add_u32 v249, 4, v178\n" \
  "v_lshlrev_b32 v249, 1, v249\n" \
  "v_xor_b32 v249, v249, v253\n" \
  "v_and_b32 v249, 31, v249\n" \
  "v_lshl_add_u32 v226, v249, 4, v252\n" \
  "v_add_u32 v249, 6, v178\n" \
  "v_add_u32 v249, s77, v249\n" \
  "v_and_b32_e64 v249, 15, v249\n" \
  "s_add_u32 s86, s72, 0x80\n" \
  "s_add_u32 m0, s74, 0\n" \
  "s_nop 0\n" \
  "buffer_load_dwordx4 v240, s[48:51], s86 offen lds\n" \
  "v_xor_b32 v249, v249, v253\n" \
  "v_and_b32 v249, 31, v249\n" \
  "v_lshl_add_u32 v223, v249, 4, v252\n" \
  "v_add_u32 v249, 6, v178\n" \
  "v_lshlrev_b32 v249, 1, v249\n" \
  "v_xor_b32 v249, v249, v253\n" \
  "v_and_b32 v249, 31, v249\n" \
  "v_lshl_add_u32 v227, v249, 4, v252\n" \
  "v_lshlrev_b32 v252, 9, v176\n" \
  "v_add_u32 v252, s84, v252\n" \
  "v_add_u32 v252, 0x10000, v252\n" \
  "v_add_u32 v249, 0, v177\n" \
  "v_xor_b32 v249, v249, v176\n" \
  "v_lshl_add_u32 v208, v249, 4, v252\n" \
  "v_add_u32 v249, 4, v177\n" \
  "v_xor_b32 v249, v249, v176\n" \
  "v_lshl_add_u32 v209, v249, 4, v252\n" \
  "v_add_u32 v249, 8, v177\n" \
  "v_xor_b32 v249, v249, v176\n" \
  "v_lshl_add_u32 v210, v249, 4, v252\n" \
  "v_add_u32 v249, 12, v177\n" \
  "v_xor_b32 v249, v249, v176\n" \
  "v_lshl_add_u32 v211, v249, 4, v252\n" \
  "v_lshrrev_b32 v251, 2, v253\n" \
  "v_lshlrev_b32 v252, 9, v251\n" \
  "v_and_b32 v251, 7, v251\n" \
  "v_and_b32 v249, 3, v253\n" \
  "v_lshl_add_u32 v252, v249, 3, v252\n" \
  "v_add_u32_e64 v252, s84, v252\n" \
  "v_add_u32 v252, 0x10000, v252\n" \
  "v_xor_b32_e64 v249, 0, v251\n" \
  "v_lshl_add_u32 v212, v249, 5, v252\n" \
  "v_xor_b32_e64 v249, 1, v251\n" \
  "v_lshl_add_u32 v213, v249, 5, v252\n" \
  "v_xor_b32_e64 v249, 2, v251\n" \
  "v_lshl_add_u32 v214, v249, 5, v252\n" \
  "v_xor_b32_e64 v249, 3, v251\n" \
  "v_lshl_add_u32 v215, v249, 5, v252\n" \
  "v_xor_b32_e64 v249, 4, v251\n" \
  "v_lshl_add_u32 v216, v249, 5, v252\n" \
  "buffer_load_dwordx4 v241, s[48:51], s86 offen offset:1024 lds\n" \
  "v_xor_b32_e64 v249, 5, v251\n" \
  "v_lshl_add_u32 v217, v249, 5, v252\n" \
  "v_xor_b32_e64 v249, 6, v251\n" \
  "v_lshl_add_u32 v218, v249, 5, v252\n" \
  "v_xor_b32_e64 v249, 7, v251\n" \
  "v_lshl_add_u32 v219, v249, 5, v252\n" \
  "s_mov_b32 s94, 0\n" \
  "s_mul_i32 s95, %4, 0x4000\n" \
  "s_add_u32 s94, s94, s95\n" \
  "s_mul_i32 s95, %1, 0x1000\n" \
  "s_add_u32 s94, s94, s95\n" \
  "v_writelane_b32 v255, s94, 54\n" \
  "s_lshr_b32 s94, s94, 6\n" \
  "v_add_u32 v251, s94, v253\n" \
  "v_lshrrev_b32 v252, 3, v251\n" \
  "v_and_b32 v249, 7, v251\n" \
  "v_lshlrev_b32 v252, 13, v252\n" \
  "v_lshl_add_u32 v199, v249, 6, v252\n" \
  "buffer_load_dwordx4 v242, s[48:51], s86 offen offset:2048 lds\n" \
  "buffer_load_dwordx4 v243, s[48:51], s86 offen offset:3072 lds\n" \
  "s_waitcnt vmcnt(16)\n" \
  "ds_read_b128 v[0:3], v204\n" \
  "ds_read_b128 v[4:7], v205\n" \
  "s_waitcnt lgkmcnt(0)\n" \
  "s_add_u32 s90, s85, 0x3800\n" \
  "s_add_u32 m0, s88, 0\n" \
  "s_nop 0\n" \
  "buffer_load_dwordx4 v198, s[56:59], s90 offen lds\n" \
  "s_add_u32 s91, s85, 0x5400\n" \
  "s_add_u32 m0, s88, 0x1c00\n" \
  "s_nop 0\n" \
  "buffer_load_dwordx4 v198, s[56:59], s91 offen lds\n" \
  "s_waitcnt vmcnt(16)\n" \
  "ds_read_b128 v[8:11], v204 offset:1024\n" \
  "ds_read_b128 v[12:15], v205 offset:1024\n" \
  "v_mfma_f32_16x16x32_bf16 v[56:59], v[200:203], v[0:3], 0\n" \
  "v_mfma_f32_16x16x32_bf16 v[60:63], v[0:3], v[0:3], 0\n" \
  "v_mfma_f32_16x16x32_bf16 v[56:59], v[200:203], v[4:7], v[56:59]\n" \
  "v_mfma_f32_16x16x32_bf16 v[60:63], v[4:7], v[4:7], v[60:63]\n" \
  "s_waitcnt lgkmcnt(0)\n" \
  "s_add_u32 m0, s88, 0x380\n" \
  "s_nop 0\n" \
  "buffer_load_dwordx4 v198, s[56:59], s90 offen offset:128 lds\n" \
  "s_add_u32 m0, s88, 0x1f80\n" \
  "s_nop 0\n" \
  "buffer_load_dwordx4 v198, s[56:59], s91 offen offset:128 lds\n" \
  "s_waitcnt vmcnt(16)\n" \
  "ds_read_b128 v[16:19], v204 offset:2048\n" \
  "ds_read_b128 v[20:23], v205 offset:2048\n" \
  "v_mfma_f32_16x16x32_bf16 v[56:59], v[200:203], v[8:11], v[56:59]\n" \
  "v_mfma_f32_16x16x32_bf16 v[60:63], v[8:11], v[8:11], v[60:63]\n" \
  "v_mfma_f32_16x16x32_bf16 v[56:59], v[200:203], v[12:15], v[56:59]\n" \
  "v_mfma_f32_16x16x32_bf16 v[60:63], v[12:15], v[12:15], v[60:63]\n" \
  "s_waitcnt lgkmcnt(0)\n" \
  "s_add_u32 m0, s88, 0x700\n" \
  "s_nop 0\n" \
  "buffer_load_dwordx4 v198, s[56:59], s90 offen offset:256 lds\n" \
  "s_add_u32 m0, s88, 0x2300\n" \
  "s_nop 0\n" \
  "buffer_load_dwordx4 v198, s[56:59], s91 offen offset:256 lds\n" \
  "s_waitcnt vmcnt(16)\n" \
  "ds_read_b128 v[24:27], v204 offset:3072\n" \
  "ds_read_b128 v[28:31], v205 offset:3072\n" \
  "v_mfma_f32_16x16x32_bf16 v[56:59], v[200:203], v[16:19], v[56:59]\n" \
  "v_mfma_f32_16x16x32_bf16 v[60:63], v[16:19], v[16:19], v[60:63]\n" \
  "v_mfma_f32_16x16x32_bf16 v[56:59], v[200:203], v[20:23], v[56:59]\n" \
  "v_mfma_f32_16x16x32_bf16 v[60:63], v[20:23], v[20:23], v[60:63]\n" \
  "s_waitcnt lgkmcnt(0)\n" \
  "s_add_u32 m0, s88, 0xa80\n" \
  "s_nop 0\n" \
  "buffer_load_dwordx4 v198, s[56:59], s90 offen offset:384 lds\n" \
  "s_add_u32 m0, s88, 0x2680\n" \
  "s_nop 0\n" \
  "buffer_load_dwordx4 v198, s[56:59], s91 offen offset:384 lds\n" \
  "s_waitcnt vmcnt(16)\n" \
  "ds_read_b128 v[32:35], v204 offset:4096\n" \
  "ds_read_b128 v[36:39], v205 offset:4096\n" \
  "v_mfma_f32_16x16x32_bf16 v[56:59], v[200:203], v[24:27], v[56:59]\n" \
  "v_mfma_f32_16x16x32_bf16 v[60:63], v[24:27], v[24:27], v[60:63]\n" \
  "v_mfma_f32_16x16x32_bf16 v[56:59], v[200:203], v[28:31], v[56:59]\n" \
  "v_mfma_f32_16x16x32_bf16 v[60:63], v[28:31], v[28:31], v[60:63]\n" \
  "s_waitcnt lgkmcnt(0)\n" \
  "s_add_u32 m0, s88, 0xe00\n" \
  "s_nop 0\n" \
  "buffer_load_dwordx4 v198, s[56:59], s90 offen offset:512 lds\n" \
  "s_add_u32 m0, s88, 0x2a00\n" \
  "s_nop 0\n" \
  "buffer_load_dwordx4 v198, s[56:59], s91 offen offset:512 lds\n" \
  "s_waitcnt vmcnt(16)\n" \
  "ds_read_b128 v[40:43], v204 offset:5120\n" \
  "ds_read_b128 v[44:47], v205 offset:5120\n" \
  "v_mfma_f32_16x16x32_bf16 v[56:59], v[200:203], v[32:35], v[56:59]\n" \
  "v_mfma_f32_16x16x32_bf16 v[60:63], v[32:35], v[32:35], v[60:63]\n" \
  "v_mfma_f32_16x16x32_bf16 v[56:59], v[200:203], v[36:39], v[56:59]\n" \
  "v_mfma_f32_16x16x32_bf16 v[60:63], v[36:39], v[36:39], v[60:63]\n" \
  "s_waitcnt lgkmcnt(0)\n" \
  "s_add_u32 m0, s88, 0x1180\n" \
  "s_nop 0\n" \
  "buffer_load_dwordx4 v198, s[56:59], s90 offen offset:640 lds\n" \
  "s_add_u32 m0, s88, 0x2d80\n" \
  "s_nop 0\n" \
  "buffer_load_dwordx4 v198, s[56:59], s91 offen offset:640 lds\n" \
  "s_waitcnt vmcnt(16)\n" \
  "ds_read_b128 v[48:51], v204 offset:6144\n" \
  "ds_read_b128 v[52:55], v205 offset:6144\n" \
  "v_mfma_f32_16x16x32_bf16 v[56:59], v[200:203], v[40:43], v[56:59]\n" \
  "v_mfma_f32_16x16x32_bf16 v[60:63], v[40:43], v[40:43], v[60:63]\n" \
  "v_mfma_f32_16x16x32_bf16 v[56:59], v[200:203], v[44:47], v[56:59]\n" \
  "v_mfma_f32_16x16x32_bf16 v[60:63], v[44:47], v[44:47], v[60:63]\n" \
  "s_waitcnt lgkmcnt(0)\n" \
  "s_add_u32 m0, s88, 0x1500\n" \
  "s_nop 0\n" \
  "buffer_load_dwordx4 v198, s[56:59], s90 offen offset:768 lds\n" \
  "s_add_u32 m0, s88, 0x3100\n" \
  "s_nop 0\n" \
  "buffer_load_dwordx4 v198, s[56:59], s91 offen offset:768 lds\n" \
  "v_mfma_f32_16x16x32_bf16 v[56:59], v[200:203], v[48:51], v[56:59]\n" \
  "v_mfma_f32_16x16x32_bf16 v[60:63], v[48:51], v[48:51], v[60:63]\n" \
  "v_mfma_f32_16x16x32_bf16 v[56:59], v[200:203], v[52:55], v[56:59]\n" \
  "v_mfma_f32_16x16x32_bf16 v[60:63], v[52:55], v[52:55], v[60:63]\n" \
  "buffer_load_dword a2, v183, s[48:51], 0 offen\n" \
  "s_mov_b32 s81, 0\n" \
  "s_lshr_b32 s95, %3, 2\n" \
  "s_mul_i32 s95, s95, 0x3800\n" \
  "s_add_u32 s81, s81, s95\n" \
  "s_lshr_b32 s95, %3, 0\n" \
  "s_and_b32 s95, s95, 1\n" \
  "s_mul_i32 s95, s95, 0x1000\n" \
  "s_add_u32 s81, s81, s95\n" \
  "s_add_u32 s82, s81, 0xe000\n" \
  "v_mul_f32 v56, 0xbb124925, v56\n" \
  "v_mul_f32 v57, 0xbb124925, v57\n" \
  "v_mul_f32 v58, 0xbb124925, v58\n" \
  "v_mul_f32 v59, 0xbb124925, v59\n" \
  "s_nop 1\n" \
  "v_mfma_f32_16x16x32_bf16 v[176:179], v[194:197], v[0:3], v[56:59]\n" \
  "v_mfma_f32_16x16x32_bf16 v[180:183], v[192:195], v[0:3], v[56:59]\n" \
  "s_mov_b32 s92, 0x100001\n" \
  "s_mov_b32 s93, 0x10000100\n" \
  "v_cndmask_b32 v251, 0, v60, s[92:93]\n" \
  "s_lshl_b64 s[92:93], s[92:93], 1\n" \
  "v_cndmask_b32 v251, v251, v61, s[92:93]\n" \
  "s_lshl_b64 s[92:93], s[92:93], 1\n" \
  "v_cndmask_b32 v251, v251, v62, s[92:93]\n" \
  "s_lshl_b64 s[92:93], s[92:93], 1\n" \
  "v_cndmask_b32 v251, v251, v63, s[92:93]\n" \
  "v_mov_b32 v252, v251\n" \
  "s_nop 1\n" \
  "v_permlane32_swap_b32 v251, v252\n" \
  "v_add_f32 v251, v251, v252\n" \
  "v_mov_b32 v252, v251\n" \
  "s_nop 1\n" \
  "v_permlane16_swap_b32 v251, v252\n" \
  "v_add_f32_e64 v251, v251, v252\n" \
  "v_mul_f32 v251, 0x3b124925, v251\n" \
  "v_fma_f32 v251, -v56, v56, v251\n" \
  "v_max_f32_e64 v251, 0, v251\n" \
  "v_add_f32 v246, 0x3727c5ac, v251\n" \
  "v_rsq_f32 v251, v246\n" \
  "s_nop 0\n" \
  "v_mul_f32 v244, 0xbfb8aa3b, v251\n" \
  "v_mul_f32 v248, 0x3db8aa3b, v251\n" \
  "v_mfma_f32_16x16x32_bf16 v[184:187], v[194:197], v[4:7], v[56:59]\n" \
  "v_mfma_f32_16x16x32_bf16 v[188:191], v[192:195], v[4:7], v[56:59]\n" \
  "s_add_u32 s87, s72, 0x38080\n" \
  "s_add_u32 m0, s75, 0\n" \
  "s_nop 0\n" \
  "buffer_load_dwordx4 v240, s[48:51], s87 offen lds\n" \
  "v_cvt_pk_bf16_f32 v128, v176, v177\n" \
  "v_cvt_pk_bf16_f32 v129, v178, v179\n" \
  "v_cvt_pk_bf16_f32 v130, v180, v181\n" \
  "v_cvt_pk_bf16_f32 v131, v182, v183\n" \
  "v_mfma_f32_16x16x32_bf16 v[176:179], v[194:197], v[8:11], v[56:59]\n" \
  "v_mfma_f32_16x16x32_bf16 v[180:183], v[192:195], v[8:11], v[56:59]\n" \
  "buffer_load_dwordx4 v241, s[48:51], s87 offen offset:1024 lds\n" \
  "v_cvt_pk_bf16_f32 v136, v184, v185\n" \
  "v_cvt_pk_bf16_f32 v137, v186, v187\n" \
  "v_cvt_pk_bf16_f32 v138, v188, v189\n" \
  "v_cvt_pk_bf16_f32 v139, v190, v191\n" \
  "v_mfma_f32_16x16x32_bf16 v[184:187], v[194:197], v[12:15], v[56:59]\n" \
  "v_mfma_f32_16x16x32_bf16 v[188:191], v[192:195], v[12:15], v[56:59]\n" \
  "buffer_load_dwordx4 v242, s[48:51], s87 offen offset:2048 lds\n" \
  "v_cvt_pk_bf16_f32 v144, v176, v177\n" \
  "v_cvt_pk_bf16_f32 v145, v178, v179\n" \
  "v_cvt_pk_bf16_f32 v146, v180, v181\n" \
  "v_cvt_pk_bf16_f32 v147, v182, v183\n" \
  "v_mfma_f32_16x16x32_bf16 v[176:179], v[194:197], v[16:19], v[56:59]\n" \
  "v_mfma_f32_16x16x32_bf16 v[180:183], v[192:195], v[16:19], v[56:59]\n" \
  "buffer_load_dwordx4 v243, s[48:51], s87 offen offset:3072 lds\n" \
  "v_cvt_pk_bf16_f32 v152, v184, v185\n" \
  "v_cvt_pk_bf16_f32 v153, v186, v187\n" \
  "v_cvt_pk_bf16_f32 v154, v188, v189\n" \
  "v_cvt_pk_bf16_f32 v155, v190, v191\n" \
  "v_mfma_f32_16x16x32_bf16 v[184:187], v[194:197], v[20:23], v[56:59]\n" \
  "v_mfma_f32_16x16x32_bf16 v[188:191], v[192:195], v[20:23], v[56:59]\n" \
  "v_mov_b32 v206, 0x2300000\n" \
  "global_load_dwordx2 v[206:207], v206, s[28:29] sc1\n" \
  "v_cvt_pk_bf16_f32 v160, v176, v177\n" \
  "v_cvt_pk_bf16_f32 v161, v178, v179\n" \
  "v_cvt_pk_bf16_f32 v162, v180, v181\n" \
  "v_cvt_pk_bf16_f32 v163, v182, v183\n" \
  "v_mfma_f32_16x16x32_bf16 v[176:179], v[194:197], v[24:27], v[56:59]\n" \
  "v_mfma_f32_16x16x32_bf16 v[180:183], v[192:195], v[24:27], v[56:59]\n" \
  "s_add_u32 s86, s72, 0x70080\n" \
  "s_add_u32 m0, s74, 0x4000\n" \
  "s_nop 0\n" \
  "buffer_load_dwordx4 v240, s[48:51], s86 offen lds\n" \
  "v_cvt_pk_bf16_f32 v168, v184, v185\n" \
  "v_cvt_pk_bf16_f32 v169, v186, v187\n" \
  "v_cvt_pk_bf16_f32 v170, v188, v189\n" \
  "v_cvt_pk_bf16_f32 v171, v190, v191\n" \
  "v_mfma_f32_16x16x32_bf16 v[184:187], v[194:197], v[28:31], v[56:59]\n" \
  "v_mfma_f32_16x16x32_bf16 v[188:191], v[192:195], v[28:31], v[56:59]\n" \
  "buffer_load_dwordx4 v241, s[48:51], s86 offen offset:1024 lds\n" \
  "v_cvt_pk_bf16_f32 v176, v176, v177\n" \
  "v_cvt_pk_bf16_f32 v178, v178, v179\n" \
  "v_cvt_pk_bf16_f32 v180, v180, v181\n" \
  "v_cvt_pk_bf16_f32 v182, v182, v183\n" \
  "v_accvgpr_write_b32 a192, v176\n" \
  "v_accvgpr_write_b32 a193, v178\n" \
  "v_accvgpr_write_b32 a194, v180\n" \
  "v_accvgpr_write_b32 a195, v182\n" \
  "v_mfma_f32_16x16x32_bf16 v[176:179], v[194:197], v[32:35], v[56:59]\n" \
  "v_mfma_f32_16x16x32_bf16 v[180:183], v[192:195], v[32:35], v[56:59]\n" \
  "buffer_load_dwordx4 v242, s[48:51], s86 offen offset:2048 lds\n" \
  "v_cvt_pk_bf16_f32 v184, v184, v185\n" \
  "v_cvt_pk_bf16_f32 v186, v186, v187\n" \
  "v_cvt_pk_bf16_f32 v188, v188, v189\n" \
  "v_cvt_pk_bf16_f32 v190, v190, v191\n" \
  "v_accvgpr_write_b32 a200, v184\n" \
  "v_accvgpr_write_b32 a201, v186\n" \
  "v_accvgpr_write_b32 a202, v188\n" \
  "v_accvgpr_write_b32 a203, v190\n" \
  "v_mfma_f32_16x16x32_bf16 v[184:187], v[194:197], v[36:39], v[56:59]\n" \
  "v_mfma_f32_16x16x32_bf16 v[188:191], v[192:195], v[36:39], v[56:59]\n" \
  "buffer_load_dwordx4 v243, s[48:51], s86 offen offset:3072 lds\n" \
  "v_cvt_pk_bf16_f32 v176, v176, v177\n" \
  "v_cvt_pk_bf16_f32 v178, v178, v179\n" \
  "v_cvt_pk_bf16_f32 v180, v180, v181\n" \
  "v_cvt_pk_bf16_f32 v182, v182, v183\n" \
  "v_accvgpr_write_b32 a208, v176\n" \
  "v_accvgpr_write_b32 a209, v178\n" \
  "v_accvgpr_write_b32 a210, v180\n" \
  "v_accvgpr_write_b32 a211, v182\n" \
  "v_mfma_f32_16x16x32_bf16 v[176:179], v[194:197], v[40:43], v[56:59]\n" \
  "v_mfma_f32_16x16x32_bf16 v[180:183], v[192:195], v[40:43], v[56:59]\n" \
  "s_add_u32 s87, s72, 0xa8080\n" \
  "s_add_u32 m0, s75, 0x4000\n" \
  "s_nop 0\n" \
  "buffer_load_dwordx4 v240, s[48:51], s87 offen lds\n" \
  "v_cvt_pk_bf16_f32 v184, v184, v185\n" \
  "v_cvt_pk_bf16_f32 v186, v186, v187\n" \
  "v_cvt_pk_bf16_f32 v188, v188, v189\n" \
  "v_cvt_pk_bf16_f32 v190, v190, v191\n" \
  "v_accvgpr_write_b32 a216, v184\n" \
  "v_accvgpr_write_b32 a217, v186\n" \
  "v_accvgpr_write_b32 a218, v188\n" \
  "v_accvgpr_write_b32 a219, v190\n" \
  "v_mfma_f32_16x16x32_bf16 v[184:187], v[194:197], v[44:47], v[56:59]\n" \
  "v_mfma_f32_16x16x32_bf16 v[188:191], v[192:195], v[44:47], v[56:59]\n" \
  "buffer_load_dwordx4 v241, s[48:51], s87 offen offset:1024 lds\n" \
  "v_cvt_pk_bf16_f32 v176, v176, v177\n" \
  "v_cvt_pk_bf16_f32 v178, v178, v179\n" \
  "v_cvt_pk_bf16_f32 v180, v180, v181\n" \
  "v_cvt_pk_bf16_f32 v182, v182, v183\n" \
  "v_accvgpr_write_b32 a224, v176\n" \
  "v_accvgpr_write_b32 a225, v178\n" \
  "v_accvgpr_write_b32 a226, v180\n" \
  "v_accvgpr_write_b32 a227, v182\n" \
  "v_mfma_f32_16x16x32_bf16 v[176:179], v[194:197], v[48:51], v[56:59]\n" \
  "v_mfma_f32_16x16x32_bf16 v[180:183], v[192:195], v[48:51], v[56:59]\n" \
  "buffer_load_dwordx4 v242, s[48:51], s87 offen offset:2048 lds\n" \
  "v_cvt_pk_bf16_f32 v184, v184, v185\n" \
  "v_cvt_pk_bf16_f32 v186, v186, v187\n" \
  "v_cvt_pk_bf16_f32 v188, v188, v189\n" \
  "v_cvt_pk_bf16_f32 v190, v190, v191\n" \
  "v_accvgpr_write_b32 a232, v184\n" \
  "v_accvgpr_write_b32 a233, v186\n" \
  "v_accvgpr_write_b32 a234, v188\n" \
  "v_accvgpr_write_b32 a235, v190\n" \
  "v_mfma_f32_16x16x32_bf16 v[184:187], v[194:197], v[52:55], v[56:59]\n" \
  "v_mfma_f32_16x16x32_bf16 v[188:191], v[192:195], v[52:55], v[56:59]\n" \
  "buffer_load_dwordx4 v243, s[48:51], s87 offen offset:3072 lds\n" \
  "v_cvt_pk_bf16_f32 v176, v176, v177\n" \
  "v_cvt_pk_bf16_f32 v178, v178, v179\n" \
  "v_cvt_pk_bf16_f32 v180, v180, v181\n" \
  "v_cvt_pk_bf16_f32 v182, v182, v183\n" \
  "v_accvgpr_write_b32 a240, v176\n" \
  "v_accvgpr_write_b32 a241, v178\n" \
  "v_accvgpr_write_b32 a242, v180\n" \
  "v_accvgpr_write_b32 a243, v182\n" \
  "s_add_u32 s86, s72, 0xe0080\n" \
  "s_add_u32 m0, s74, 0x8000\n" \
  "s_nop 0\n" \
  "buffer_load_dwordx4 v240, s[48:51], s86 offen lds\n" \
  "v_cvt_pk_bf16_f32 v184, v184, v185\n" \
  "v_cvt_pk_bf16_f32 v186, v186, v187\n" \
  "v_cvt_pk_bf16_f32 v188, v188, v189\n" \
  "v_cvt_pk_bf16_f32 v190, v190, v191\n" \
  "v_accvgpr_write_b32 a248, v184\n" \
  "v_accvgpr_write_b32 a249, v186\n" \
  "v_accvgpr_write_b32 a250, v188\n" \
  "v_accvgpr_write_b32 a251, v190\n" \
  "s_waitcnt vmcnt(27)\n" \
  "ds_read_b128 v[64:67], v204\n" \
  "ds_read_b128 v[68:71], v205\n" \
  "buffer_load_dwordx4 v241, s[48:51], s86 offen offset:1024 lds\n" \
  "s_waitcnt vmcnt(26)\n" \
  "ds_read_b128 v[72:75], v204 offset:1024\n" \
  "ds_read_b128 v[76:79], v205 offset:1024\n" \
  "buffer_load_dwordx4 v242, s[48:51], s86 offen offset:2048 lds\n" \
  "s_waitcnt lgkmcnt(2)\n" \
  "v_mfma_f32_16x16x32_bf16 v[120:123], v[200:203], v[64:67], 0\n" \
  "v_mfma_f32_16x16x32_bf16 v[124:127], v[64:67], v[64:67], 0\n" \
  "v_mfma_f32_16x16x32_bf16 v[120:123], v[200:203], v[68:71], v[120:123]\n" \
  "v_mfma_f32_16x16x32_bf16 v[124:127], v[68:71], v[68:71], v[124:127]\n" \
  "s_waitcnt vmcnt(25)\n" \
  "ds_read_b128 v[80:83], v204 offset:2048\n" \
  "ds_read_b128 v[84:87], v205 offset:2048\n" \
  "buffer_load_dwordx4 v243, s[48:51], s86 offen offset:3072 lds\n" \
  "s_waitcnt lgkmcnt(2)\n" \
  "v_mfma_f32_16x16x32_bf16 v[120:123], v[200:203], v[72:75], v[120:123]\n" \
  "v_mfma_f32_16x16x32_bf16 v[124:127], v[72:75], v[72:75], v[124:127]\n" \
  "v_mfma_f32_16x16x32_bf16 v[120:123], v[200:203], v[76:79], v[120:123]\n" \
  "v_mfma_f32_16x16x32_bf16 v[124:127], v[76:79], v[76:79], v[124:127]\n" \
  "s_waitcnt vmcnt(24)\n" \
  "ds_read_b128 v[88:91], v204 offset:3072\n" \
  "ds_read_b128 v[92:95], v205 offset:3072\n" \
  "s_add_u32 s87, s72, 0x118080\n" \
  "s_add_u32 m0, s75, 0x8000\n" \
  "s_nop 0\n" \
  "buffer_load_dwordx4 v240, s[48:51], s87 offen lds\n" \
  "s_waitcnt lgkmcnt(2)\n" \
  "v_mfma_f32_16x16x32_bf16 v[120:123], v[200:203], v[80:83], v[120:123]\n" \
  "v_mfma_f32_16x16x32_bf16 v[124:127], v[80:83], v[80:83], v[124:127]\n" \
  "v_mfma_f32_16x16x32_bf16 v[120:123], v[200:203], v[84:87], v[120:123]\n" \
  "v_mfma_f32_16x16x32_bf16 v[124:127], v[84:87], v[84:87], v[124:127]\n" \
  "s_waitcnt vmcnt(23)\n" \
  "ds_read_b128 v[96:99], v204 offset:4096\n" \
  "ds_read_b128 v[100:103], v205 offset:4096\n" \
  "buffer_load_dwordx4 v241, s[48:51], s87 offen offset:1024 lds\n" \
  "s_waitcnt lgkmcnt(2)\n" \
  "v_mfma_f32_16x16x32_bf16 v[120:123], v[200:203], v[88:91], v[120:123]\n" \
  "v_mfma_f32_16x16x32_bf16 v[124:127], v[88:91], v[88:91], v[124:127]\n" \
  "v_mfma_f32_16x16x32_bf16 v[120:123], v[200:203], v[92:95], v[120:123]\n" \
  "v_mfma_f32_16x16x32_bf16 v[124:127], v[92:95], v[92:95], v[124:127]\n" \
  "s_waitcnt vmcnt(22)\n" \
  "ds_read_b128 v[104:107], v204 offset:5120\n" \
  "ds_read_b128 v[108:111], v205 offset:5120\n" \
  "buffer_load_dwordx4 v242, s[48:51], s87 offen offset:2048 lds\n" \
  "s_waitcnt lgkmcnt(2)\n" \
  "v_mfma_f32_16x16x32_bf16 v[120:123], v[200:203], v[96:99], v[120:123]\n" \
  "v_mfma_f32_16x16x32_bf16 v[124:127], v[96:99], v[96:99], v[124:127]\n" \
  "v_mfma_f32_16x16x32_bf16 v[120:123], v[200:203], v[100:103], v[120:123]\n" \
  "v_mfma_f32_16x16x32_bf16 v[124:127], v[100:103], v[100:103], v[124:127]\n" \
  "s_waitcnt vmcnt(21)\n" \
  "ds_read_b128 v[112:115], v204 offset:6144\n" \
  "ds_read_b128 v[116:119], v205 offset:6144\n" \
  "buffer_load_dwordx4 v243, s[48:51], s87 offen offset:3072 lds\n" \
  "s_waitcnt lgkmcnt(2)\n" \
  "v_mfma_f32_16x16x32_bf16 v[120:123], v[200:203], v[104:107], v[120:123]\n" \
  "v_mfma_f32_16x16x32_bf16 v[124:127], v[104:107], v[104:107], v[124:127]\n" \
  "v_mfma_f32_16x16x32_bf16 v[120:123], v[200:203], v[108:111], v[120:123]\n" \
  "v_mfma_f32_16x16x32_bf16 v[124:127], v[108:111], v[108:111], v[124:127]\n" \
  "s_waitcnt vmcnt(16) lgkmcnt(0)\n" \
  "s_barrier\n" \
  "v_mfma_f32_16x16x32_bf16 v[120:123], v[200:203], v[112:115], v[120:123]\n" \
  "v_mfma_f32_16x16x32_bf16 v[124:127], v[112:115], v[112:115], v[124:127]\n" \
  "v_mfma_f32_16x16x32_bf16 v[120:123], v[200:203], v[116:119], v[120:123]\n" \
  "v_mfma_f32_16x16x32_bf16 v[124:127], v[116:119], v[116:119], v[124:127]\n" \
  "ds_read_b64_tr_b16 a[128:129], v228\n" \
  "ds_read_b64_tr_b16 a[130:131], v228 offset:512\n" \
  "ds_read_b64_tr_b16 a[132:133], v229\n" \
  "ds_read_b64_tr_b16 a[134:135], v229 offset:512\n" \
  "ds_read_b64_tr_b16 a[136:137], v230\n" \
  "ds_read_b64_tr_b16 a[138:139], v230 offset:512\n" \
  "ds_read_b64_tr_b16 a[140:141], v231\n" \
  "ds_read_b64_tr_b16 a[142:143], v231 offset:512\n" \
  "ds_read_b64_tr_b16 a[144:145], v228 offset:8192\n" \
  "ds_read_b64_tr_b16 a[146:147], v228 offset:8704\n" \
  "ds_read_b64_tr_b16 a[148:149], v229 offset:8192\n" \
  "ds_read_b64_tr_b16 a[150:151], v229 offset:8704\n" \
  "ds_read_b64_tr_b16 a[152:153], v230 offset:8192\n" \
  "ds_read_b64_tr_b16 a[154:155], v230 offset:8704\n" \
  "ds_read_b64_tr_b16 a[156:157], v231 offset:8192\n" \
  "ds_read_b64_tr_b16 a[158:159], v231 offset:8704\n" \
  "ds_read_b64_tr_b16 a[160:161], v228 offset:4096\n" \
  "ds_read_b64_tr_b16 a[162:163], v228 offset:4608\n" \
  "ds_read_b64_tr_b16 a[164:165], v229 offset:4096\n" \
  "ds_read_b64_tr_b16 a[166:167], v229 offset:4608\n" \
  "ds_read_b64_tr_b16 a[168:169], v230 offset:4096\n" \
  "ds_read_b64_tr_b16 a[170:171], v230 offset:4608\n" \
  "ds_read_b64_tr_b16 a[172:173], v231 offset:4096\n" \
  "ds_read_b64_tr_b16 a[174:175], v231 offset:4608\n" \
  "ds_read_b64_tr_b16 a[176:177], v228 offset:12288\n" \
  "ds_read_b64_tr_b16 a[178:179], v228 offset:12800\n" \
  "ds_read_b64_tr_b16 a[180:181], v229 offset:12288\n" \
  "ds_read_b64_tr_b16 a[182:183], v229 offset:12800\n" \
  "ds_read_b64_tr_b16 a[184:185], v230 offset:12288\n" \
  "ds_read_b64_tr_b16 a[186:187], v230 offset:12800\n" \
  "ds_read_b64_tr_b16 a[188:189], v231 offset:12288\n" \
  "ds_read_b64_tr_b16 a[190:191], v231 offset:12800\n" \
  "v_mul_f32 v120, 0xbb124925, v120\n" \
  "v_mul_f32 v121, 0xbb124925, v121\n" \
  "v_mul_f32 v122, 0xbb124925, v122\n" \
  "v_mul_f32 v123, 0xbb124925, v123\n" \
  "s_nop 1\n" \
  "v_mfma_f32_16x16x32_bf16 v[176:179], v[194:197], v[64:67], v[120:123]\n" \
  "v_mfma_f32_16x16x32_bf16 v[180:183], v[192:195], v[64:67], v[120:123]\n" \
  "v_mfma_f32_16x16x32_bf16 v[184:187], v[194:197], v[68:71], v[120:123]\n" \
  "v_mfma_f32_16x16x32_bf16 v[188:191], v[192:195], v[68:71], v[120:123]\n" \
  "s_mov_b32 s92, 0x100001\n" \
  "s_mov_b32 s93, 0x10000100\n" \
  "v_cndmask_b32 v251, 0, v124, s[92:93]\n" \
  "s_lshl_b64 s[92:93], s[92:93], 1\n" \
  "v_cndmask_b32 v251, v251, v125, s[92:93]\n" \
  "s_lshl_b64 s[92:93], s[92:93], 1\n" \
  "v_cndmask_b32 v251, v251, v126, s[92:93]\n" \
  "s_lshl_b64 s[92:93], s[92:93], 1\n" \
  "v_cndmask_b32 v251, v251, v127, s[92:93]\n" \
  "v_mov_b32 v252, v251\n" \
  "s_nop 1\n" \
  "v_permlane32_swap_b32 v251, v252\n" \
  "v_add_f32 v251, v251, v252\n" \
  "v_mov_b32 v252, v251\n" \
  "s_nop 1\n" \
  "v_permlane16_swap_b32 v251, v252\n" \
  "v_add_f32_e64 v251, v251, v252\n" \
  "v_mul_f32 v251, 0x3b124925, v251\n" \
  "v_fma_f32 v251, -v120, v120, v251\n" \
  "v_max_f32_e64 v251, 0, v251\n" \
  "v_add_f32 v247, 0x3727c5ac, v251\n" \
  "v_rsq_f32 v251, v247\n" \
  "s_nop 0\n" \
  "v_mul_f32 v245, 0xbfb8aa3b, v251\n" \
  "v_mul_f32 v249, 0x3db8aa3b, v251\n" \
  "v_cvt_pk_bf16_f32 v132, v176, v177\n" \
  "v_cvt_pk_bf16_f32 v133, v178, v179\n" \
  "v_cvt_pk_bf16_f32 v134, v180, v181\n" \
  "v_cvt_pk_bf16_f32 v135, v182, v183\n" \
  "v_mfma_f32_16x16x32_bf16 v[176:179], v[194:197], v[72:75], v[120:123]\n" \
  "v_mfma_f32_16x16x32_bf16 v[180:183], v[192:195], v[72:75], v[120:123]\n" \
  "v_cvt_pk_bf16_f32 v140, v184, v185\n" \
  "v_cvt_pk_bf16_f32 v141, v186, v187\n" \
  "v_cvt_pk_bf16_f32 v142, v188, v189\n" \
  "v_cvt_pk_bf16_f32 v143, v190, v191\n" \
  "v_mfma_f32_16x16x32_bf16 v[184:187], v[194:197], v[76:79], v[120:123]\n" \
  "v_mfma_f32_16x16x32_bf16 v[188:191], v[192:195], v[76:79], v[120:123]\n" \
  "v_readfirstlane_b32 s92, v206\n" \
  "v_readfirstlane_b32 s93, v207\n" \
  "s_nop 3\n" \
  "s_mul_i32 s79, s92, 0x4c957f2d\n" \
  "s_mul_hi_u32 s80, s92, 0x4c957f2d\n" \
  "s_mul_i32 s94, s92, 0x5851f42d\n" \
  "s_add_u32 s80, s80, s94\n" \
  "s_mul_i32 s94, s93, 0x4c957f2d\n" \
  "s_add_u32 s80, s80, s94\n" \
  "s_add_u32 s79, s79, 0xf767814f\n" \
  "s_addc_u32 s80, s80, 0x14057b7e\n" \
  "s_waitcnt lgkmcnt(0)\n" \
  "; HP [0] ('F', 1, 0)\n" \
  "v_mfma_f32_16x16x32_bf16 v[0:3], a[128:131], v[128:131], 0\n" \
  "ds_read_b64_tr_b16 a[64:65], v232\n" \
  "ds_read_b64_tr_b16 a[66:67], v232 offset:512\n" \
  "v_mfma_f32_16x16x32_bf16 v[4:7], a[128:131], v[132:135], 0\n" \
  "ds_read_b64_tr_b16 a[68:69], v233\n" \
  "s_add_u32 s86, s72, 0x150080\n" \
  "v_mfma_f32_16x16x32_bf16 v[8:11], a[132:135], v[128:131], 0\n" \
  "ds_read_b64_tr_b16 a[70:71], v233 offset:512\n" \
  "s_add_u32 m0, s74, 0xc000\n" \
  "v_mfma_f32_16x16x32_bf16 v[12:15], a[132:135], v[132:135], 0\n" \
  "ds_read_b64_tr_b16 a[72:73], v234\n" \
  "v_mfma_f32_16x16x32_bf16 v[16:19], a[136:139], v[128:131], 0\n" \
  "ds_read_b64_tr_b16 a[74:75], v234 offset:512\n" \
  "v_mfma_f32_16x16x32_bf16 v[20:23], a[136:139], v[132:135], 0\n" \
  "ds_read_b64_tr_b16 a[76:77], v235\n" \
  "v_cvt_pk_bf16_f32 v148, v176, v177\n" \
  "v_mfma_f32_16x16x32_bf16 v[24:27], a[140:143], v[128:131], 0\n" \
  "buffer_load_dword a1, v199, s[44:47], 0 offen\n" \
  "v_mfma_f32_16x16x32_bf16 v[28:31], a[140:143], v[132:135], 0\n" \
  "ds_read_b64_tr_b16 a[78:79], v235 offset:512\n" \
  "v_cvt_pk_bf16_f32 v149, v178, v179\n" \
  "v_mfma_f32_16x16x32_bf16 v[32:35], a[144:147], v[128:131], 0\n" \
  "ds_read_b64_tr_b16 a[80:81], v232 offset:8192\n" \
  "v_cvt_pk_bf16_f32 v150, v180, v181\n" \
  "v_mfma_f32_16x16x32_bf16 v[36:39], a[144:147], v[132:135], 0\n" \
  "ds_read_b64_tr_b16 a[82:83], v232 offset:8704\n" \
  "v_cvt_pk_bf16_f32 v151, v182, v183\n" \
  "v_mfma_f32_16x16x32_bf16 v[40:43], a[148:151], v[128:131], 0\n" \
  "ds_read_b64_tr_b16 a[84:85], v233 offset:8192\n" \
  "ds_read_b64_tr_b16 a[86:87], v233 offset:8704\n" \
  "v_mfma_f32_16x16x32_bf16 v[44:47], a[148:151], v[132:135], 0\n" \
  "ds_read_b64_tr_b16 a[88:89], v234 offset:8192\n" \
  "v_cvt_pk_bf16_f32 v156, v184, v185\n" \
  "v_mfma_f32_16x16x32_bf16 v[48:51], a[152:155], v[128:131], 0\n" \
  "ds_read_b64_tr_b16 a[90:91], v234 offset:8704\n" \
  "v_cvt_pk_bf16_f32 v157, v186, v187\n" \
  "v_mfma_f32_16x16x32_bf16 v[52:55], a[152:155], v[132:135], 0\n" \
  "ds_read_b64_tr_b16 a[92:93], v235 offset:8192\n" \
  "v_cvt_pk_bf16_f32 v158, v188, v189\n" \
  "v_mfma_f32_16x16x32_bf16 v[56:59], a[156:159], v[128:131], 0\n" \
  "ds_read_b64_tr_b16 a[94:95], v235 offset:8704\n" \
  "v_cvt_pk_bf16_f32 v159, v190, v191\n" \
  "v_mfma_f32_16x16x32_bf16 v[60:63], a[156:159], v[132:135], 0\n" \
  "buffer_load_dwordx4 v240, s[48:51], s86 offen lds\n" \
  "v_mfma_f32_16x16x32_bf16 v[0:3], a[160:163], v[136:139], v[0:3]\n" \
  "ds_read_b64_tr_b16 a[96:97], v232 offset:4096\n" \
  "v_mfma_f32_16x16x32_bf16 v[4:7], a[160:163], v[140:143], v[4:7]\n" \
  "ds_read_b64_tr_b16 a[98:99], v232 offset:4608\n" \
  "v_mfma_f32_16x16x32_bf16 v[8:11], a[164:167], v[136:139], v[8:11]\n" \
  "ds_read_b64_tr_b16 a[100:101], v233 offset:4096\n" \
  "v_mfma_f32_16x16x32_bf16 v[12:15], a[164:167], v[140:143], v[12:15]\n" \
  "ds_read_b64_tr_b16 a[102:103], v233 offset:4608\n" \
  "v_mfma_f32_16x16x32_bf16 v[16:19], a[168:171], v[136:139], v[16:19]\n" \
  "ds_read_b64_tr_b16 a[104:105], v234 offset:4096\n" \
  "v_mfma_f32_16x16x32_bf16 v[20:23], a[168:171], v[140:143], v[20:23]\n" \
  "ds_read_b64_tr_b16 a[106:107], v234 offset:4608\n" \
  "ds_read_b64_tr_b16 a[108:109], v235 offset:4096\n" \
  "v_mfma_f32_16x16x32_bf16 v[24:27], a[172:175], v[136:139], v[24:27]\n" \
  "ds_read_b64_tr_b16 a[110:111], v235 offset:4608\n" \
  "v_mfma_f32_16x16x32_bf16 v[28:31], a[172:175], v[140:143], v[28:31]\n" \
  "ds_read_b64_tr_b16 a[112:113], v232 offset:12288\n" \
  "v_mfma_f32_16x16x32_bf16 v[32:35], a[176:179], v[136:139], v[32:35]\n" \
  "buffer_load_dwordx4 v241, s[48:51], s86 offen offset:1024 lds\n" \
  "v_mfma_f32_16x16x32_bf16 v[36:39], a[176:179], v[140:143], v[36:39]\n" \
  "ds_read_b64_tr_b16 a[114:115], v232 offset:12800\n" \
  "v_mfma_f32_16x16x32_bf16 v[40:43], a[180:183], v[136:139], v[40:43]\n" \
  "ds_read_b64_tr_b16 a[116:117], v233 offset:12288\n" \
  "v_mfma_f32_16x16x32_bf16 v[44:47], a[180:183], v[140:143], v[44:47]\n" \
  "ds_read_b64_tr_b16 a[118:119], v233 offset:12800\n" \
  "v_mfma_f32_16x16x32_bf16 v[48:51], a[184:187], v[136:139], v[48:51]\n" \
  "ds_read_b64_tr_b16 a[120:121], v234 offset:12288\n" \
  "v_mfma_f32_16x16x32_bf16 v[52:55], a[184:187], v[140:143], v[52:55]\n" \
  "ds_read_b64_tr_b16 a[122:123], v234 offset:12800\n" \
  "v_mfma_f32_16x16x32_bf16 v[56:59], a[188:191], v[136:139], v[56:59]\n" \
  "ds_read_b64_tr_b16 a[124:125], v235 offset:12288\n" \
  "v_mfma_f32_16x16x32_bf16 v[60:63], a[188:191], v[140:143], v[60:63]\n" \
  "ds_read_b64_tr_b16 a[126:127], v235 offset:12800\n" \
  "v_mfma_f32_16x16x32_bf16 v[176:179], v[194:197], v[80:83], v[120:123]\n" \
  "v_mfma_f32_16x16x32_bf16 v[180:183], v[192:195], v[80:83], v[120:123]\n" \
  "buffer_load_dwordx4 v242, s[48:51], s86 offen offset:2048 lds\n" \
  "v_mfma_f32_16x16x32_bf16 v[184:187], v[194:197], v[84:87], v[120:123]\n" \
  "v_mfma_f32_16x16x32_bf16 v[188:191], v[192:195], v[84:87], v[120:123]\n" \
  "s_waitcnt vmcnt(12) lgkmcnt(0)\n" \
  "s_barrier\n" \
  "; HP [1, 2] ('F', 1, 1)\n" \
  "v_mfma_f32_16x16x32_bf16 v[0:3], a[64:67], v[144:147], v[0:3]\n" \
  "ds_read_b64_tr_b16 a[128:129], v228 offset:16384\n" \
  "ds_read_b64_tr_b16 a[130:131], v228 offset:16896\n" \
  "v_mfma_f32_16x16x32_bf16 v[4:7], a[64:67], v[148:151], v[4:7]\n" \
  "ds_read_b64_tr_b16 a[132:133], v229 offset:16384\n" \
  "s_add_u32 s87, s72, 0\n" \
  "v_mfma_f32_16x16x32_bf16 v[8:11], a[68:71], v[144:147], v[8:11]\n" \
  "ds_read_b64_tr_b16 a[134:135], v229 offset:16896\n" \
  "v_mfma_f32_16x16x32_bf16 v[12:15], a[68:71], v[148:151], v[12:15]\n" \
  "buffer_load_dwordx4 v243, s[48:51], s86 offen offset:3072 lds\n" \
  "v_mfma_f32_16x16x32_bf16 v[16:19], a[72:75], v[144:147], v[16:19]\n" \
  "ds_read_b64_tr_b16 a[136:137], v230 offset:16384\n" \
  "s_add_u32 m0, s75, 0xc000\n" \
  "v_mfma_f32_16x16x32_bf16 v[20:23], a[72:75], v[148:151], v[20:23]\n" \
  "ds_read_b64_tr_b16 a[138:139], v230 offset:16896\n" \
  "v_cvt_pk_bf16_f32 v164, v176, v177\n" \
  "v_mfma_f32_16x16x32_bf16 v[24:27], a[76:79], v[144:147], v[24:27]\n" \
  "ds_read_b64_tr_b16 a[140:141], v231 offset:16384\n" \
  "v_cvt_pk_bf16_f32 v165, v178, v179\n" \
  "v_mfma_f32_16x16x32_bf16 v[28:31], a[76:79], v[148:151], v[28:31]\n" \
  "ds_read_b64_tr_b16 a[142:143], v231 offset:16896\n" \
  "v_cvt_pk_bf16_f32 v166, v180, v181\n" \
  "v_mfma_f32_16x16x32_bf16 v[32:35], a[80:83], v[144:147], v[32:35]\n" \
  "ds_read_b64_tr_b16 a[144:145], v228 offset:24576\n" \
  "ds_read_b64_tr_b16 a[146:147], v228 offset:25088\n" \
  "v_mfma_f32_16x16x32_bf16 v[36:39], a[80:83], v[148:151], v[36:39]\n" \
  "buffer_load_dwordx4 v240, s[48:51], s87 offen lds\n" \
  "v_mfma_f32_16x16x32_bf16 v[40:43], a[84:87], v[144:147], v[40:43]\n" \
  "ds_read_b64_tr_b16 a[148:149], v229 offset:24576\n" \
  "v_cvt_pk_bf16_f32 v167, v182, v183\n" \
  "v_mfma_f32_16x16x32_bf16 v[44:47], a[84:87], v[148:151], v[44:47]\n" \
  "ds_read_b64_tr_b16 a[150:151], v229 offset:25088\n" \
  "v_cvt_pk_bf16_f32 v172, v184, v185\n" \
  "v_mfma_f32_16x16x32_bf16 v[48:51], a[88:91], v[144:147], v[48:51]\n" \
  "ds_read_b64_tr_b16 a[152:153], v230 offset:24576\n" \
  "v_cvt_pk_bf16_f32 v173, v186, v187\n" \
  "v_mfma_f32_16x16x32_bf16 v[52:55], a[88:91], v[148:151], v[52:55]\n" \
  "ds_read_b64_tr_b16 a[154:155], v230 offset:25088\n" \
  "v_cvt_pk_bf16_f32 v174, v188, v189\n" \
  "v_mfma_f32_16x16x32_bf16 v[56:59], a[92:95], v[144:147], v[56:59]\n" \
  "buffer_load_dwordx4 v241, s[48:51], s87 offen offset:1024 lds\n" \
  "v_mfma_f32_16x16x32_bf16 v[60:63], a[92:95], v[148:151], v[60:63]\n" \
  "ds_read_b64_tr_b16 a[156:157], v231 offset:24576\n" \
  "v_cvt_pk_bf16_f32 v175, v190, v191\n" \
  "v_mfma_f32_16x16x32_bf16 v[0:3], a[96:99], v[152:155], v[0:3]\n" \
  "ds_read_b64_tr_b16 a[158:159], v231 offset:25088\n" \
  "s_add_u32 s86, s72, 0x38000\n" \
  "v_mfma_f32_16x16x32_bf16 v[4:7], a[96:99], v[156:159], v[4:7]\n" \
  "ds_read_b64_tr_b16 a[160:161], v228 offset:20480\n" \
  "ds_read_b64_tr_b16 a[162:163], v228 offset:20992\n" \
  "v_mfma_f32_16x16x32_bf16 v[8:11], a[100:103], v[152:155], v[8:11]\n" \
  "ds_read_b64_tr_b16 a[164:165], v229 offset:20480\n" \
  "v_mfma_f32_16x16x32_bf16 v[12:15], a[100:103], v[156:159], v[12:15]\n" \
  "ds_read_b64_tr_b16 a[166:167], v229 offset:20992\n" \
  "v_mfma_f32_16x16x32_bf16 v[16:19], a[104:107], v[152:155], v[16:19]\n" \
  "buffer_load_dwordx4 v242, s[48:51], s87 offen offset:2048 lds\n" \
  "v_mfma_f32_16x16x32_bf16 v[20:23], a[104:107], v[156:159], v[20:23]\n" \
  "ds_read_b64_tr_b16 a[168:169], v230 offset:20480\n" \
  "v_mfma_f32_16x16x32_bf16 v[24:27], a[108:111], v[152:155], v[24:27]\n" \
  "ds_read_b64_tr_b16 a[170:171], v230 offset:20992\n" \
  "v_mfma_f32_16x16x32_bf16 v[28:31], a[108:111], v[156:159], v[28:31]\n" \
  "ds_read_b64_tr_b16 a[172:173], v231 offset:20480\n" \
  "v_mfma_f32_16x16x32_bf16 v[32:35], a[112:115], v[152:155], v[32:35]\n" \
  "ds_read_b64_tr_b16 a[174:175], v231 offset:20992\n" \
  "v_mfma_f32_16x16x32_bf16 v[36:39], a[112:115], v[156:159], v[36:39]\n" \
  "ds_read_b64_tr_b16 a[176:177], v228 offset:28672\n" \
  "ds_read_b64_tr_b16 a[178:179], v228 offset:29184\n" \
  "v_mfma_f32_16x16x32_bf16 v[40:43], a[116:119], v[152:155], v[40:43]\n" \
  "buffer_load_dwordx4 v243, s[48:51], s87 offen offset:3072 lds\n" \
  "v_mfma_f32_16x16x32_bf16 v[44:47], a[116:119], v[156:159], v[44:47]\n" \
  "ds_read_b64_tr_b16 a[180:181], v229 offset:28672\n" \
  "s_add_u32 m0, s74, 0\n" \
  "v_mfma_f32_16x16x32_bf16 v[48:51], a[120:123], v[152:155], v[48:51]\n" \
  "ds_read_b64_tr_b16 a[182:183], v229 offset:29184\n" \
  "s_add_u32 s87, s72, 0x70000\n" \
  "v_mfma_f32_16x16x32_bf16 v[52:55], a[120:123], v[156:159], v[52:55]\n" \
  "ds_read_b64_tr_b16 a[184:185], v230 offset:28672\n" \
  "v_mfma_f32_16x16x32_bf16 v[56:59], a[124:127], v[152:155], v[56:59]\n" \
  "ds_read_b64_tr_b16 a[186:187], v230 offset:29184\n" \
  "v_mfma_f32_16x16x32_bf16 v[60:63], a[124:127], v[156:159], v[60:63]\n" \
  "buffer_load_dwordx4 v240, s[48:51], s86 offen lds\n" \
  "v_mfma_f32_16x16x32_bf16 v[176:179], v[194:197], v[88:91], v[120:123]\n" \
  "ds_read_b64_tr_b16 a[188:189], v231 offset:28672\n" \
  "v_mfma_f32_16x16x32_bf16 v[180:183], v[192:195], v[88:91], v[120:123]\n" \
  "ds_read_b64_tr_b16 a[190:191], v231 offset:29184\n" \
  "v_mfma_f32_16x16x32_bf16 v[184:187], v[194:197], v[92:95], v[120:123]\n" \
  "ds_read_b64_tr_b16 a[64:65], v232 offset:16384\n" \
  "ds_read_b64_tr_b16 a[66:67], v232 offset:16896\n" \
  "v_mfma_f32_16x16x32_bf16 v[188:191], v[192:195], v[92:95], v[120:123]\n" \
  "ds_read_b64_tr_b16 a[68:69], v233 offset:16384\n" \
  "s_waitcnt lgkmcnt(6)\n" \
  "v_mfma_f32_16x16x32_bf16 v[0:3], a[128:131], v[160:163], v[0:3]\n" \
  "ds_read_b64_tr_b16 a[70:71], v233 offset:16896\n" \
  "v_mfma_f32_16x16x32_bf16 v[4:7], a[128:131], v[164:167], v[4:7]\n" \
  "buffer_load_dwordx4 v241, s[48:51], s86 offen offset:1024 lds\n" \
  "v_mfma_f32_16x16x32_bf16 v[8:11], a[132:135], v[160:163], v[8:11]\n" \
  "ds_read_b64_tr_b16 a[72:73], v234 offset:16384\n" \
  "v_mfma_f32_16x16x32_bf16 v[12:15], a[132:135], v[164:167], v[12:15]\n" \
  "ds_read_b64_tr_b16 a[74:75], v234 offset:16896\n" \
  "v_mfma_f32_16x16x32_bf16 v[16:19], a[136:139], v[160:163], v[16:19]\n" \
  "ds_read_b64_tr_b16 a[76:77], v235 offset:16384\n" \
  "s_waitcnt lgkmcnt(5)\n" \
  "v_mfma_f32_16x16x32_bf16 v[20:23], a[136:139], v[164:167], v[20:23]\n" \
  "ds_read_b64_tr_b16 a[78:79], v235 offset:16896\n" \
  "v_cvt_pk_bf16_f32 v176, v176, v177\n" \
  "v_mfma_f32_16x16x32_bf16 v[24:27], a[140:143], v[160:163], v[24:27]\n" \
  "buffer_load_dwordx4 v242, s[48:51], s86 offen offset:2048 lds\n" \
  "v_mfma_f32_16x16x32_bf16 v[28:31], a[140:143], v[164:167], v[28:31]\n" \
  "ds_read_b64_tr_b16 a[80:81], v232 offset:24576\n" \
  "ds_read_b64_tr_b16 a[82:83], v232 offset:25088\n" \
  "v_mfma_f32_16x16x32_bf16 v[32:35], a[144:147], v[160:163], v[32:35]\n" \
  "ds_read_b64_tr_b16 a[84:85], v233 offset:24576\n" \
  "v_cvt_pk_bf16_f32 v178, v178, v179\n" \
  "v_mfma_f32_16x16x32_bf16 v[36:39], a[144:147], v[164:167], v[36:39]\n" \
  "ds_read_b64_tr_b16 a[86:87], v233 offset:25088\n" \
  "v_cvt_pk_bf16_f32 v180, v180, v181\n" \
  "v_mfma_f32_16x16x32_bf16 v[40:43], a[148:151], v[160:163], v[40:43]\n" \
  "ds_read_b64_tr_b16 a[88:89], v234 offset:24576\n" \
  "v_cvt_pk_bf16_f32 v182, v182, v183\n" \
  "v_mfma_f32_16x16x32_bf16 v[44:47], a[148:151], v[164:167], v[44:47]\n" \
  "ds_read_b64_tr_b16 a[90:91], v234 offset:25088\n" \
  "v_accvgpr_write_b32 a196, v176\n" \
  "v_mfma_f32_16x16x32_bf16 v[48:51], a[152:155], v[160:163], v[48:51]\n" \
  "buffer_load_dwordx4 v243, s[48:51], s86 offen offset:3072 lds\n" \
  "v_mfma_f32_16x16x32_bf16 v[52:55], a[152:155], v[164:167], v[52:55]\n" \
  "ds_read_b64_tr_b16 a[92:93], v235 offset:24576\n" \
  "v_accvgpr_write_b32 a197, v178\n" \
  "v_mfma_f32_16x16x32_bf16 v[56:59], a[156:159], v[160:163], v[56:59]\n" \
  "ds_read_b64_tr_b16 a[94:95], v235 offset:25088\n" \
  "v_accvgpr_write_b32 a198, v180\n" \
  "v_mfma_f32_16x16x32_bf16 v[60:63], a[156:159], v[164:167], v[60:63]\n" \
  "ds_read_b64_tr_b16 a[96:97], v232 offset:20480\n" \
  "ds_read_b64_tr_b16 a[98:99], v232 offset:20992\n" \
  "v_mfma_f32_16x16x32_bf16 v[0:3], a[160:163], v[168:171], v[0:3]\n" \
  "ds_read_b64_tr_b16 a[100:101], v233 offset:20480\n" \
  "s_add_u32 m0, s75, 0\n" \
  "v_accvgpr_write_b32 a199, v182\n" \
  "v_mfma_f32_16x16x32_bf16 v[4:7], a[160:163], v[172:175], v[4:7]\n" \
  "buffer_load_dwordx4 v240, s[48:51], s87 offen lds\n" \
  "v_mfma_f32_16x16x32_bf16 v[8:11], a[164:167], v[168:171], v[8:11]\n" \
  "ds_read_b64_tr_b16 a[102:103], v233 offset:20992\n" \
  "v_cvt_pk_bf16_f32 v184, v184, v185\n" \
  "v_mfma_f32_16x16x32_bf16 v[12:15], a[164:167], v[172:175], v[12:15]\n" \
  "ds_read_b64_tr_b16 a[104:105], v234 offset:20480\n" \
  "v_cvt_pk_bf16_f32 v186, v186, v187\n" \
  "v_mfma_f32_16x16x32_bf16 v[16:19], a[168:171], v[168:171], v[16:19]\n" \
  "ds_read_b64_tr_b16 a[106:107], v234 offset:20992\n" \
  "v_cvt_pk_bf16_f32 v188, v188, v189\n" \
  "v_mfma_f32_16x16x32_bf16 v[20:23], a[168:171], v[172:175], v[20:23]\n" \
  "ds_read_b64_tr_b16 a[108:109], v235 offset:20480\n" \
  "v_cvt_pk_bf16_f32 v190, v190, v191\n" \
  "v_mfma_f32_16x16x32_bf16 v[24:27], a[172:175], v[168:171], v[24:27]\n" \
  "ds_read_b64_tr_b16 a[110:111], v235 offset:20992\n" \
  "v_accvgpr_write_b32 a204, v184\n" \
  "v_mfma_f32_16x16x32_bf16 v[28:31], a[172:175], v[172:175], v[28:31]\n" \
  "buffer_load_dwordx4 v241, s[48:51], s87 offen offset:1024 lds\n" \
  "v_mfma_f32_16x16x32_bf16 v[32:35], a[176:179], v[168:171], v[32:35]\n" \
  "ds_read_b64_tr_b16 a[112:113], v232 offset:28672\n" \
  "ds_read_b64_tr_b16 a[114:115], v232 offset:29184\n" \
  "v_mfma_f32_16x16x32_bf16 v[36:39], a[176:179], v[172:175], v[36:39]\n" \
  "ds_read_b64_tr_b16 a[116:117], v233 offset:28672\n" \
  "v_accvgpr_write_b32 a205, v186\n" \
  "v_mfma_f32_16x16x32_bf16 v[40:43], a[180:183], v[168:171], v[40:43]\n" \
  "ds_read_b64_tr_b16 a[118:119], v233 offset:29184\n" \
  "v_accvgpr_write_b32 a206, v188\n" \
  "v_mfma_f32_16x16x32_bf16 v[44:47], a[180:183], v[172:175], v[44:47]\n" \
  "ds_read_b64_tr_b16 a[120:121], v234 offset:28672\n" \
  "v_accvgpr_write_b32 a207, v190\n" \
  "v_mfma_f32_16x16x32_bf16 v[48:51], a[184:187], v[168:171], v[48:51]\n" \
  "buffer_load_dwordx4 v242, s[48:51], s87 offen offset:2048 lds\n" \
  "v_mfma_f32_16x16x32_bf16 v[52:55], a[184:187], v[172:175], v[52:55]\n" \
  "ds_read_b64_tr_b16 a[122:123], v234 offset:29184\n" \
  "v_mfma_f32_16x16x32_bf16 v[56:59], a[188:191], v[168:171], v[56:59]\n" \
  "ds_read_b64_tr_b16 a[124:125], v235 offset:28672\n" \
  "v_mfma_f32_16x16x32_bf16 v[60:63], a[188:191], v[172:175], v[60:63]\n" \
  "ds_read_b64_tr_b16 a[126:127], v235 offset:29184\n" \
  "v_mfma_f32_16x16x32_bf16 v[176:179], v[194:197], v[96:99], v[120:123]\n" \
  "v_mfma_f32_16x16x32_bf16 v[180:183], v[192:195], v[96:99], v[120:123]\n" \
  "v_mfma_f32_16x16x32_bf16 v[184:187], v[194:197], v[100:103], v[120:123]\n" \
  "buffer_load_dwordx4 v243, s[48:51], s87 offen offset:3072 lds\n" \
  "v_mfma_f32_16x16x32_bf16 v[188:191], v[192:195], v[100:103], v[120:123]\n" \
  "s_waitcnt vmcnt(17) lgkmcnt(0)\n" \
  "s_barrier\n" \
  "; HP [3, 4] ('F', 1, 3)\n" \
  "v_mfma_f32_16x16x32_bf16 v[0:3], a[64:67], a[192:195], v[0:3]\n" \
  "ds_read_b64_tr_b16 a[128:129], v228 offset:32768\n" \
  "ds_read_b64_tr_b16 a[130:131], v228 offset:33280\n" \
  "v_mfma_f32_16x16x32_bf16 v[4:7], a[64:67], a[196:199], v[4:7]\n" \
  "ds_read_b64_tr_b16 a[132:133], v229 offset:32768\n" \
  "s_add_u32 s86, s72, 0xa8000\n" \
  "v_mfma_f32_16x16x32_bf16 v[8:11], a[68:71], a[192:195], v[8:11]\n" \
  "ds_read_b64_tr_b16 a[134:135], v229 offset:33280\n" \
  "s_add_u32 m0, s74, 0x4000\n" \
  "v_mfma_f32_16x16x32_bf16 v[12:15], a[68:71], a[196:199], v[12:15]\n" \
  "ds_read_b64_tr_b16 a[136:137], v230 offset:32768\n" \
  "s_add_u32 s87, s72, 0xe0000\n" \
  "v_mfma_f32_16x16x32_bf16 v[16:19], a[72:75], a[192:195], v[16:19]\n" \
  "ds_read_b64_tr_b16 a[138:139], v230 offset:33280\n" \
  "v_mfma_f32_16x16x32_bf16 v[20:23], a[72:75], a[196:199], v[20:23]\n" \
  "ds_read_b64_tr_b16 a[140:141], v231 offset:32768\n" \
  "v_cvt_pk_bf16_f32 v176, v176, v177\n" \
  "v_mfma_f32_16x16x32_bf16 v[24:27], a[76:79], a[192:195], v[24:27]\n" \
  "buffer_load_dwordx4 v240, s[48:51], s86 offen lds\n" \
  "v_mfma_f32_16x16x32_bf16 v[28:31], a[76:79], a[196:199], v[28:31]\n" \
  "ds_read_b64_tr_b16 a[142:143], v231 offset:33280\n" \
  "v_cvt_pk_bf16_f32 v178, v178, v179\n" \
  "v_mfma_f32_16x16x32_bf16 v[32:35], a[80:83], a[192:195], v[32:35]\n" \
  "ds_read_b64_tr_b16 a[144:145], v228 offset:40960\n" \
  "v_cvt_pk_bf16_f32 v180, v180, v181\n" \
  "v_mfma_f32_16x16x32_bf16 v[36:39], a[80:83], a[196:199], v[36:39]\n" \
  "ds_read_b64_tr_b16 a[146:147], v228 offset:41472\n" \
  "v_cvt_pk_bf16_f32 v182, v182, v183\n" \
  "v_mfma_f32_16x16x32_bf16 v[40:43], a[84:87], a[192:195], v[40:43]\n" \
  "ds_read_b64_tr_b16 a[148:149], v229 offset:40960\n" \
  "v_accvgpr_write_b32 a212, v176\n" \
  "v_mfma_f32_16x16x32_bf16 v[44:47], a[84:87], a[196:199], v[44:47]\n" \
  "ds_read_b64_tr_b16 a[150:151], v229 offset:41472\n" \
  "v_accvgpr_write_b32 a213, v178\n" \
  "v_mfma_f32_16x16x32_bf16 v[48:51], a[88:91], a[192:195], v[48:51]\n" \
  "ds_read_b64_tr_b16 a[152:153], v230 offset:40960\n" \
  "v_accvgpr_write_b32 a214, v180\n" \
  "v_mfma_f32_16x16x32_bf16 v[52:55], a[88:91], a[196:199], v[52:55]\n" \
  "ds_read_b64_tr_b16 a[154:155], v230 offset:41472\n" \
  "v_accvgpr_write_b32 a215, v182\n" \
  "v_mfma_f32_16x16x32_bf16 v[56:59], a[92:95], a[192:195], v[56:59]\n" \
  "ds_read_b64_tr_b16 a[156:157], v231 offset:40960\n" \
  "v_cvt_pk_bf16_f32 v184, v184, v185\n" \
  "v_mfma_f32_16x16x32_bf16 v[60:63], a[92:95], a[196:199], v[60:63]\n" \
  "buffer_load_dwordx4 v241, s[48:51], s86 offen offset:1024 lds\n" \
  "v_mfma_f32_16x16x32_bf16 v[0:3], a[96:99], a[200:203], v[0:3]\n" \
  "ds_read_b64_tr_b16 a[158:159], v231 offset:41472\n" \
  "v_cvt_pk_bf16_f32 v186, v186, v187\n" \
  "v_mfma_f32_16x16x32_bf16 v[4:7], a[96:99], a[204:207], v[4:7]\n" \
  "ds_read_b64_tr_b16 a[160:161], v228 offset:36864\n" \
  "v_cvt_pk_bf16_f32 v188, v188, v189\n" \
  "v_mfma_f32_16x16x32_bf16 v[8:11], a[100:103], a[200:203], v[8:11]\n" \
  "ds_read_b64_tr_b16 a[162:163], v228 offset:37376\n" \
  "v_cvt_pk_bf16_f32 v190, v190, v191\n" \
  "v_mfma_f32_16x16x32_bf16 v[12:15], a[100:103], a[204:207], v[12:15]\n" \
  "ds_read_b64_tr_b16 a[164:165], v229 offset:36864\n" \
  "v_accvgpr_write_b32 a220, v184\n" \
  "v_mfma_f32_16x16x32_bf16 v[16:19], a[104:107], a[200:203], v[16:19]\n" \
  "ds_read_b64_tr_b16 a[166:167], v229 offset:37376\n" \
  "v_accvgpr_write_b32 a221, v186\n" \
  "v_mfma_f32_16x16x32_bf16 v[20:23], a[104:107], a[204:207], v[20:23]\n" \
  "ds_read_b64_tr_b16 a[168:169], v230 offset:36864\n" \
  "v_accvgpr_write_b32 a222, v188\n" \
  "v_mfma_f32_16x16x32_bf16 v[24:27], a[108:111], a[200:203], v[24:27]\n" \
  "ds_read_b64_tr_b16 a[170:171], v230 offset:37376\n" \
  "ds_read_b64_tr_b16 a[172:173], v231 offset:36864\n" \
  "v_mfma_f32_16x16x32_bf16 v[28:31], a[108:111], a[204:207], v[28:31]\n" \
  "ds_read_b64_tr_b16 a[174:175], v231 offset:37376\n" \
  "v_accvgpr_write_b32 a223, v190\n" \
  "v_mfma_f32_16x16x32_bf16 v[32:35], a[112:115], a[200:203], v[32:35]\n" \
  "buffer_load_dwordx4 v242, s[48:51], s86 offen offset:2048 lds\n" \
  "v_mfma_f32_16x16x32_bf16 v[36:39], a[112:115], a[204:207], v[36:39]\n" \
  "ds_read_b64_tr_b16 a[176:177], v228 offset:45056\n" \
  "v_mfma_f32_16x16x32_bf16 v[40:43], a[116:119], a[200:203], v[40:43]\n" \
  "ds_read_b64_tr_b16 a[178:179], v228 offset:45568\n" \
  "v_mfma_f32_16x16x32_bf16 v[44:47], a[116:119], a[204:207], v[44:47]\n" \
  "ds_read_b64_tr_b16 a[180:181], v229 offset:45056\n" \
  "v_mfma_f32_16x16x32_bf16 v[48:51], a[120:123], a[200:203], v[48:51]\n" \
  "ds_read_b64_tr_b16 a[182:183], v229 offset:45568\n" \
  "v_mfma_f32_16x16x32_bf16 v[52:55], a[120:123], a[204:207], v[52:55]\n" \
  "ds_read_b64_tr_b16 a[184:185], v230 offset:45056\n" \
  "v_mfma_f32_16x16x32_bf16 v[56:59], a[124:127], a[200:203], v[56:59]\n" \
  "ds_read_b64_tr_b16 a[186:187], v230 offset:45568\n" \
  "v_mfma_f32_16x16x32_bf16 v[60:63], a[124:127], a[204:207], v[60:63]\n" \
  "ds_read_b64_tr_b16 a[188:189], v231 offset:45056\n" \
  "v_mfma_f32_16x16x32_bf16 v[176:179], v[194:197], v[104:107], v[120:123]\n" \
  "ds_read_b64_tr_b16 a[190:191], v231 offset:45568\n" \
  "v_mfma_f32_16x16x32_bf16 v[180:183], v[192:195], v[104:107], v[120:123]\n" \
  "buffer_load_dwordx4 v243, s[48:51], s86 offen offset:3072 lds\n" \
  "v_mfma_f32_16x16x32_bf16 v[184:187], v[194:197], v[108:111], v[120:123]\n" \
  "ds_read_b64_tr_b16 a[64:65], v232 offset:32768\n" \
  "s_add_u32 m0, s75, 0x4000\n" \
  "v_mfma_f32_16x16x32_bf16 v[188:191], v[192:195], v[108:111], v[120:123]\n" \
  "ds_read_b64_tr_b16 a[66:67], v232 offset:33280\n" \
  "s_waitcnt lgkmcnt(5)\n" \
  "v_mfma_f32_16x16x32_bf16 v[0:3], a[128:131], a[208:211], v[0:3]\n" \
  "ds_read_b64_tr_b16 a[68:69], v233 offset:32768\n" \
  "v_mfma_f32_16x16x32_bf16 v[4:7], a[128:131], a[212:215], v[4:7]\n" \
  "ds_read_b64_tr_b16 a[70:71], v233 offset:33280\n" \
  "v_mfma_f32_16x16x32_bf16 v[8:11], a[132:135], a[208:211], v[8:11]\n" \
  "ds_read_b64_tr_b16 a[72:73], v234 offset:32768\n" \
  "v_mfma_f32_16x16x32_bf16 v[12:15], a[132:135], a[212:215], v[12:15]\n" \
  "ds_read_b64_tr_b16 a[74:75], v234 offset:33280\n" \
  "v_mfma_f32_16x16x32_bf16 v[16:19], a[136:139], a[208:211], v[16:19]\n" \
  "ds_read_b64_tr_b16 a[76:77], v235 offset:32768\n" \
  "v_mfma_f32_16x16x32_bf16 v[20:23], a[136:139], a[212:215], v[20:23]\n" \
  "ds_read_b64_tr_b16 a[78:79], v235 offset:33280\n" \
  "v_cvt_pk_bf16_f32 v176, v176, v177\n" \
  "v_mfma_f32_16x16x32_bf16 v[24:27], a[140:143], a[208:211], v[24:27]\n" \
  "buffer_load_dwordx4 v240, s[48:51], s87 offen lds\n" \
  "v_mfma_f32_16x16x32_bf16 v[28:31], a[140:143], a[212:215], v[28:31]\n" \
  "ds_read_b64_tr_b16 a[80:81], v232 offset:40960\n" \
  "v_cvt_pk_bf16_f32 v178, v178, v179\n" \
  "v_mfma_f32_16x16x32_bf16 v[32:35], a[144:147], a[208:211], v[32:35]\n" \
  "ds_read_b64_tr_b16 a[82:83], v232 offset:41472\n" \
  "v_cvt_pk_bf16_f32 v180, v180, v181\n" \
  "v_mfma_f32_16x16x32_bf16 v[36:39], a[144:147], a[212:215], v[36:39]\n" \
  "ds_read_b64_tr_b16 a[84:85], v233 offset:40960\n" \
  "ds_read_b64_tr_b16 a[86:87], v233 offset:41472\n" \
  "v_mfma_f32_16x16x32_bf16 v[40:43], a[148:151], a[208:211], v[40:43]\n" \
  "ds_read_b64_tr_b16 a[88:89], v234 offset:40960\n" \
  "v_cvt_pk_bf16_f32 v182, v182, v183\n" \
  "v_mfma_f32_16x16x32_bf16 v[44:47], a[148:151], a[212:215], v[44:47]\n" \
  "ds_read_b64_tr_b16 a[90:91], v234 offset:41472\n" \
  "v_accvgpr_write_b32 a228, v176\n" \
  "v_mfma_f32_16x16x32_bf16 v[48:51], a[152:155], a[208:211], v[48:51]\n" \
  "ds_read_b64_tr_b16 a[92:93], v235 offset:40960\n" \
  "v_accvgpr_write_b32 a229, v178\n" \
  "v_mfma_f32_16x16x32_bf16 v[52:55], a[152:155], a[212:215], v[52:55]\n" \
  "ds_read_b64_tr_b16 a[94:95], v235 offset:41472\n" \
  "v_accvgpr_write_b32 a230, v180\n" \
  "v_mfma_f32_16x16x32_bf16 v[56:59], a[156:159], a[208:211], v[56:59]\n" \
  "ds_read_b64_tr_b16 a[96:97], v232 offset:36864\n" \
  "v_accvgpr_write_b32 a231, v182\n" \
  "v_mfma_f32_16x16x32_bf16 v[60:63], a[156:159], a[212:215], v[60:63]\n" \
  "buffer_load_dwordx4 v241, s[48:51], s87 offen offset:1024 lds\n" \
  "v_mfma_f32_16x16x32_bf16 v[0:3], a[160:163], a[216:219], v[0:3]\n" \
  "ds_read_b64_tr_b16 a[98:99], v232 offset:37376\n" \
  "v_cvt_pk_bf16_f32 v184, v184, v185\n" \
  "v_mfma_f32_16x16x32_bf16 v[4:7], a[160:163], a[220:223], v[4:7]\n" \
  "ds_read_b64_tr_b16 a[100:101], v233 offset:36864\n" \
  "v_cvt_pk_bf16_f32 v186, v186, v187\n" \
  "v_mfma_f32_16x16x32_bf16 v[8:11], a[164:167], a[216:219], v[8:11]\n" \
  "ds_read_b64_tr_b16 a[102:103], v233 offset:37376\n" \
  "v_cvt_pk_bf16_f32 v188, v188, v189\n" \
  "v_mfma_f32_16x16x32_bf16 v[12:15], a[164:167], a[220:223], v[12:15]\n" \
  "ds_read_b64_tr_b16 a[104:105], v234 offset:36864\n" \
  "v_cvt_pk_bf16_f32 v190, v190, v191\n" \
  "v_mfma_f32_16x16x32_bf16 v[16:19], a[168:171], a[216:219], v[16:19]\n" \
  "ds_read_b64_tr_b16 a[106:107], v234 offset:37376\n" \
  "v_accvgpr_write_b32 a236, v184\n" \
  "v_mfma_f32_16x16x32_bf16 v[20:23], a[168:171], a[220:223], v[20:23]\n" \
  "ds_read_b64_tr_b16 a[108:109], v235 offset:36864\n" \
  "v_accvgpr_write_b32 a237, v186\n" \
  "v_mfma_f32_16x16x32_bf16 v[24:27], a[172:175], a[216:219], v[24:27]\n" \
  "ds_read_b64_tr_b16 a[110:111], v235 offset:37376\n" \
  "v_accvgpr_write_b32 a238, v188\n" \
  "v_mfma_f32_16x16x32_bf16 v[28:31], a[172:175], a[220:223], v[28:31]\n" \
  "ds_read_b64_tr_b16 a[112:113], v232 offset:45056\n" \
  "v_accvgpr_write_b32 a239, v190\n" \
  "v_mfma_f32_16x16x32_bf16 v[32:35], a[176:179], a[216:219], v[32:35]\n" \
  "buffer_load_dwordx4 v242, s[48:51], s87 offen offset:2048 lds\n" \
  "v_mfma_f32_16x16x32_bf16 v[36:39], a[176:179], a[220:223], v[36:39]\n" \
  "ds_read_b64_tr_b16 a[114:115], v232 offset:45568\n" \
  "v_mfma_f32_16x16x32_bf16 v[40:43], a[180:183], a[216:219], v[40:43]\n" \
  "ds_read_b64_tr_b16 a[116:117], v233 offset:45056\n" \
  "v_mfma_f32_16x16x32_bf16 v[44:47], a[180:183], a[220:223], v[44:47]\n" \
  "ds_read_b64_tr_b16 a[118:119], v233 offset:45568\n" \
  "s_waitcnt lgkmcnt(5)\n" \
  "v_mfma_f32_16x16x32_bf16 v[48:51], a[184:187], a[216:219], v[48:51]\n" \
  "ds_read_b64_tr_b16 a[120:121], v234 offset:45056\n" \
  "v_mfma_f32_16x16x32_bf16 v[52:55], a[184:187], a[220:223], v[52:55]\n" \
  "ds_read_b64_tr_b16 a[122:123], v234 offset:45568\n" \
  "v_mfma_f32_16x16x32_bf16 v[56:59], a[188:191], a[216:219], v[56:59]\n" \
  "ds_read_b64_tr_b16 a[124:125], v235 offset:45056\n" \
  "v_mfma_f32_16x16x32_bf16 v[60:63], a[188:191], a[220:223], v[60:63]\n" \
  "ds_read_b64_tr_b16 a[126:127], v235 offset:45568\n" \
  "v_mfma_f32_16x16x32_bf16 v[176:179], v[194:197], v[112:115], v[120:123]\n" \
  "v_mfma_f32_16x16x32_bf16 v[180:183], v[192:195], v[112:115], v[120:123]\n" \
  "buffer_load_dwordx4 v243, s[48:51], s87 offen offset:3072 lds\n" \
  "v_mfma_f32_16x16x32_bf16 v[184:187], v[194:197], v[116:119], v[120:123]\n" \
  "v_mfma_f32_16x16x32_bf16 v[188:191], v[192:195], v[116:119], v[120:123]\n" \
  "s_waitcnt vmcnt(16) lgkmcnt(0)\n" \
  "s_barrier\n" \
  "; HP [5, 6] ('F', 1, 5)\n" \
  "v_mfma_f32_16x16x32_bf16 v[0:3], a[64:67], a[224:227], v[0:3]\n" \
  "ds_read_b64_tr_b16 a[128:129], v228 offset:49152\n" \
  "ds_read_b64_tr_b16 a[130:131], v228 offset:49664\n" \
  "v_mfma_f32_16x16x32_bf16 v[4:7], a[64:67], a[228:231], v[4:7]\n" \
  "ds_read_b64_tr_b16 a[132:133], v229 offset:49152\n" \
  "s_add_u32 s86, s72, 0x118000\n" \
  "v_mfma_f32_16x16x32_bf16 v[8:11], a[68:71], a[224:227], v[8:11]\n" \
  "ds_read_b64_tr_b16 a[134:135], v229 offset:49664\n" \
  "s_add_u32 m0, s74, 0x8000\n" \
  "v_mfma_f32_16x16x32_bf16 v[12:15], a[68:71], a[228:231], v[12:15]\n" \
  "ds_read_b64_tr_b16 a[136:137], v230 offset:49152\n" \
  "s_add_u32 s87, s72, 0x150000\n" \
  "v_mfma_f32_16x16x32_bf16 v[16:19], a[72:75], a[224:227], v[16:19]\n" \
  "ds_read_b64_tr_b16 a[138:139], v230 offset:49664\n" \
  "ds_read_b64_tr_b16 a[140:141], v231 offset:49152\n" \
  "v_mfma_f32_16x16x32_bf16 v[20:23], a[72:75], a[228:231], v[20:23]\n" \
  "buffer_load_dwordx4 v240, s[48:51], s86 offen lds\n" \
  "v_mfma_f32_16x16x32_bf16 v[24:27], a[76:79], a[224:227], v[24:27]\n" \
  "ds_read_b64_tr_b16 a[142:143], v231 offset:49664\n" \
  "v_cvt_pk_bf16_f32 v176, v176, v177\n" \
  "v_mfma_f32_16x16x32_bf16 v[28:31], a[76:79], a[228:231], v[28:31]\n" \
  "ds_read_b64_tr_b16 a[144:145], v228 offset:57344\n" \
  "v_cvt_pk_bf16_f32 v178, v178, v179\n" \
  "v_mfma_f32_16x16x32_bf16 v[32:35], a[80:83], a[224:227], v[32:35]\n" \
  "ds_read_b64_tr_b16 a[146:147], v228 offset:57856\n" \
  "v_cvt_pk_bf16_f32 v180, v180, v181\n" \
  "v_mfma_f32_16x16x32_bf16 v[36:39], a[80:83], a[228:231], v[36:39]\n" \
  "ds_read_b64_tr_b16 a[148:149], v229 offset:57344\n" \
  "v_cvt_pk_bf16_f32 v182, v182, v183\n" \
  "v_mfma_f32_16x16x32_bf16 v[40:43], a[84:87], a[224:227], v[40:43]\n" \
  "ds_read_b64_tr_b16 a[150:151], v229 offset:57856\n" \
  "ds_read_b64_tr_b16 a[152:153], v230 offset:57344\n" \
  "v_mfma_f32_16x16x32_bf16 v[44:47], a[84:87], a[228:231], v[44:47]\n" \
  "ds_read_b64_tr_b16 a[154:155], v230 offset:57856\n" \
  "v_accvgpr_write_b32 a244, v176\n" \
  "v_mfma_f32_16x16x32_bf16 v[48:51], a[88:91], a[224:227], v[48:51]\n" \
  "ds_read_b64_tr_b16 a[156:157], v231 offset:57344\n" \
  "v_accvgpr_write_b32 a245, v178\n" \
  "v_mfma_f32_16x16x32_bf16 v[52:55], a[88:91], a[228:231], v[52:55]\n" \
  "buffer_load_dwordx4 v241, s[48:51], s86 offen offset:1024 lds\n" \
  "v_mfma_f32_16x16x32_bf16 v[56:59], a[92:95], a[224:227], v[56:59]\n" \
  "ds_read_b64_tr_b16 a[158:159], v231 offset:57856\n" \
  "v_accvgpr_write_b32 a246, v180\n" \
  "v_mfma_f32_16x16x32_bf16 v[60:63], a[92:95], a[228:231], v[60:63]\n" \
  "ds_read_b64_tr_b16 a[160:161], v228 offset:53248\n" \
  "v_accvgpr_write_b32 a247, v182\n" \
  "v_mfma_f32_16x16x32_bf16 v[0:3], a[96:99], a[232:235], v[0:3]\n" \
  "ds_read_b64_tr_b16 a[162:163], v228 offset:53760\n" \
  "ds_read_b64_tr_b16 a[164:165], v229 offset:53248\n" \
  "v_cvt_pk_bf16_f32 v184, v184, v185\n" \
  "v_mfma_f32_16x16x32_bf16 v[4:7], a[96:99], a[236:239], v[4:7]\n" \
  "ds_read_b64_tr_b16 a[166:167], v229 offset:53760\n" \
  "v_cvt_pk_bf16_f32 v186, v186, v187\n" \
  "v_mfma_f32_16x16x32_bf16 v[8:11], a[100:103], a[232:235], v[8:11]\n" \
  "ds_read_b64_tr_b16 a[168:169], v230 offset:53248\n" \
  "v_cvt_pk_bf16_f32 v188, v188, v189\n" \
  "v_mfma_f32_16x16x32_bf16 v[12:15], a[100:103], a[236:239], v[12:15]\n" \
  "ds_read_b64_tr_b16 a[170:171], v230 offset:53760\n" \
  "v_cvt_pk_bf16_f32 v190, v190, v191\n" \
  "v_mfma_f32_16x16x32_bf16 v[16:19], a[104:107], a[232:235], v[16:19]\n" \
  "ds_read_b64_tr_b16 a[172:173], v231 offset:53248\n" \
  "v_accvgpr_write_b32 a252, v184\n" \
  "v_mfma_f32_16x16x32_bf16 v[20:23], a[104:107], a[236:239], v[20:23]\n" \
  "buffer_load_dwordx4 v242, s[48:51], s86 offen offset:2048 lds\n" \
  "v_mfma_f32_16x16x32_bf16 v[24:27], a[108:111], a[232:235], v[24:27]\n" \
  "ds_read_b64_tr_b16 a[174:175], v231 offset:53760\n" \
  "ds_read_b64_tr_b16 a[176:177], v228 offset:61440\n" \
  "v_mfma_f32_16x16x32_bf16 v[28:31], a[108:111], a[236:239], v[28:31]\n" \
  "ds_read_b64_tr_b16 a[178:179], v228 offset:61952\n" \
  "v_accvgpr_write_b32 a253, v186\n" \
  "v_mfma_f32_16x16x32_bf16 v[32:35], a[112:115], a[232:235], v[32:35]\n" \
  "ds_read_b64_tr_b16 a[180:181], v229 offset:61440\n" \
  "v_accvgpr_write_b32 a254, v188\n" \
  "v_mfma_f32_16x16x32_bf16 v[36:39], a[112:115], a[236:239], v[36:39]\n" \
  "ds_read_b64_tr_b16 a[182:183], v229 offset:61952\n" \
  "v_accvgpr_write_b32 a255, v190\n" \
  "v_mfma_f32_16x16x32_bf16 v[40:43], a[116:119], a[232:235], v[40:43]\n" \
  "ds_read_b64_tr_b16 a[184:185], v230 offset:61440\n" \
  "v_mfma_f32_16x16x32_bf16 v[44:47], a[116:119], a[236:239], v[44:47]\n" \
  "ds_read_b64_tr_b16 a[186:187], v230 offset:61952\n" \
  "ds_read_b64_tr_b16 a[188:189], v231 offset:61440\n" \
  "v_mfma_f32_16x16x32_bf16 v[48:51], a[120:123], a[232:235], v[48:51]\n" \
  "ds_read_b64_tr_b16 a[190:191], v231 offset:61952\n" \
  "v_mfma_f32_16x16x32_bf16 v[52:55], a[120:123], a[236:239], v[52:55]\n" \
  "buffer_load_dwordx4 v243, s[48:51], s86 offen offset:3072 lds\n" \
  "v_mfma_f32_16x16x32_bf16 v[56:59], a[124:127], a[232:235], v[56:59]\n" \
  "ds_read_b64_tr_b16 a[64:65], v232 offset:49152\n" \
  "s_add_u32 m0, s75, 0x8000\n" \
  "v_mfma_f32_16x16x32_bf16 v[60:63], a[124:127], a[236:239], v[60:63]\n" \
  "ds_read_b64_tr_b16 a[66:67], v232 offset:49664\n" \
  "s_waitcnt lgkmcnt(6)\n" \
  "v_mfma_f32_16x16x32_bf16 v[0:3], a[128:131], a[240:243], v[0:3]\n" \
  "ds_read_b64_tr_b16 a[68:69], v233 offset:49152\n" \
  "ds_read_b64_tr_b16 a[70:71], v233 offset:49664\n" \
  "v_mfma_f32_16x16x32_bf16 v[4:7], a[128:131], a[244:247], v[4:7]\n" \
  "ds_read_b64_tr_b16 a[72:73], v234 offset:49152\n" \
  "v_mfma_f32_16x16x32_bf16 v[8:11], a[132:135], a[240:243], v[8:11]\n" \
  "ds_read_b64_tr_b16 a[74:75], v234 offset:49664\n" \
  "v_mfma_f32_16x16x32_bf16 v[12:15], a[132:135], a[244:247], v[12:15]\n" \
  "ds_read_b64_tr_b16 a[76:77], v235 offset:49152\n" \
  "v_mfma_f32_16x16x32_bf16 v[16:19], a[136:139], a[240:243], v[16:19]\n" \
  "ds_read_b64_tr_b16 a[78:79], v235 offset:49664\n" \
  "v_mfma_f32_16x16x32_bf16 v[20:23], a[136:139], a[244:247], v[20:23]\n" \
  "buffer_load_dwordx4 v240, s[48:51], s87 offen lds\n" \
  "v_mfma_f32_16x16x32_bf16 v[24:27], a[140:143], a[240:243], v[24:27]\n" \
  "ds_read_b64_tr_b16 a[80:81], v232 offset:57344\n" \
  "ds_read_b64_tr_b16 a[82:83], v232 offset:57856\n" \
  "v_mfma_f32_16x16x32_bf16 v[28:31], a[140:143], a[244:247], v[28:31]\n" \
  "ds_read_b64_tr_b16 a[84:85], v233 offset:57344\n" \
  "v_mfma_f32_16x16x32_bf16 v[32:35], a[144:147], a[240:243], v[32:35]\n" \
  "ds_read_b64_tr_b16 a[86:87], v233 offset:57856\n" \
  "v_mfma_f32_16x16x32_bf16 v[36:39], a[144:147], a[244:247], v[36:39]\n" \
  "ds_read_b64_tr_b16 a[88:89], v234 offset:57344\n" \
  "v_mfma_f32_16x16x32_bf16 v[40:43], a[148:151], a[240:243], v[40:43]\n" \
  "ds_read_b64_tr_b16 a[90:91], v234 offset:57856\n" \
  "v_mfma_f32_16x16x32_bf16 v[44:47], a[148:151], a[244:247], v[44:47]\n" \
  "ds_read_b64_tr_b16 a[92:93], v235 offset:57344\n" \
  "ds_read_b64_tr_b16 a[94:95], v235 offset:57856\n" \
  "v_mfma_f32_16x16x32_bf16 v[48:51], a[152:155], a[240:243], v[48:51]\n" \
  "ds_read_b64_tr_b16 a[96:97], v232 offset:53248\n" \
  "v_mfma_f32_16x16x32_bf16 v[52:55], a[152:155], a[244:247], v[52:55]\n" \
  "buffer_load_dwordx4 v241, s[48:51], s87 offen offset:1024 lds\n" \
  "v_mfma_f32_16x16x32_bf16 v[56:59], a[156:159], a[240:243], v[56:59]\n" \
  "ds_read_b64_tr_b16 a[98:99], v232 offset:53760\n" \
  "v_mfma_f32_16x16x32_bf16 v[60:63], a[156:159], a[244:247], v[60:63]\n" \
  "ds_read_b64_tr_b16 a[100:101], v233 offset:53248\n" \
  "v_mfma_f32_16x16x32_bf16 v[0:3], a[160:163], a[248:251], v[0:3]\n" \
  "ds_read_b64_tr_b16 a[102:103], v233 offset:53760\n" \
  "v_mfma_f32_16x16x32_bf16 v[4:7], a[160:163], a[252:255], v[4:7]\n" \
  "ds_read_b64_tr_b16 a[104:105], v234 offset:53248\n" \
  "ds_read_b64_tr_b16 a[106:107], v234 offset:53760\n" \
  "v_mfma_f32_16x16x32_bf16 v[8:11], a[164:167], a[248:251], v[8:11]\n" \
  "ds_read_b64_tr_b16 a[108:109], v235 offset:53248\n" \
  "v_mfma_f32_16x16x32_bf16 v[12:15], a[164:167], a[252:255], v[12:15]\n" \
  "ds_read_b64_tr_b16 a[110:111], v235 offset:53760\n" \
  "v_mfma_f32_16x16x32_bf16 v[16:19], a[168:171], a[248:251], v[16:19]\n" \
  "ds_read_b64_tr_b16 a[112:113], v232 offset:61440\n" \
  "v_mfma_f32_16x16x32_bf16 v[20:23], a[168:171], a[252:255], v[20:23]\n" \
  "buffer_load_dwordx4 v242, s[48:51], s87 offen offset:2048 lds\n" \
  "v_mfma_f32_16x16x32_bf16 v[24:27], a[172:175], a[248:251], v[24:27]\n" \
  "ds_read_b64_tr_b16 a[114:115], v232 offset:61952\n" \
  "v_mfma_f32_16x16x32_bf16 v[28:31], a[172:175], a[252:255], v[28:31]\n" \
  "ds_read_b64_tr_b16 a[116:117], v233 offset:61440\n" \
  "ds_read_b64_tr_b16 a[118:119], v233 offset:61952\n" \
  "v_mfma_f32_16x16x32_bf16 v[32:35], a[176:179], a[248:251], v[32:35]\n" \
  "ds_read_b64_tr_b16 a[120:121], v234 offset:61440\n" \
  "v_mfma_f32_16x16x32_bf16 v[36:39], a[176:179], a[252:255], v[36:39]\n" \
  "ds_read_b64_tr_b16 a[122:123], v234 offset:61952\n" \
  "v_mfma_f32_16x16x32_bf16 v[40:43], a[180:183], a[248:251], v[40:43]\n" \
  "ds_read_b64_tr_b16 a[124:125], v235 offset:61440\n" \
  "v_mfma_f32_16x16x32_bf16 v[44:47], a[180:183], a[252:255], v[44:47]\n" \
  "ds_read_b64_tr_b16 a[126:127], v235 offset:61952\n" \
  "s_waitcnt lgkmcnt(7)\n" \
  "v_mfma_f32_16x16x32_bf16 v[48:51], a[184:187], a[248:251], v[48:51]\n" \
  "v_mfma_f32_16x16x32_bf16 v[52:55], a[184:187], a[252:255], v[52:55]\n" \
  "buffer_load_dwordx4 v243, s[48:51], s87 offen offset:3072 lds\n" \
  "v_mfma_f32_16x16x32_bf16 v[56:59], a[188:191], a[248:251], v[56:59]\n" \
  "v_mfma_f32_16x16x32_bf16 v[60:63], a[188:191], a[252:255], v[60:63]\n" \
  "s_waitcnt vmcnt(16) lgkmcnt(0)\n" \
  "s_barrier\n" \
  "; HP [7, 8] ('F', 0, 0)\n" \
  "v_mfma_f32_16x16x32_bf16 v[64:67], a[64:67], v[128:131], 0\n" \
  "ds_read_b64_tr_b16 a[128:129], v228\n" \
  "ds_read_b64_tr_b16 a[130:131], v228 offset:512\n" \
  "v_mfma_f32_16x16x32_bf16 v[68:71], a[64:67], v[132:135], 0\n" \
  "ds_read_b64_tr_b16 a[132:133], v229\n" \
  "s_mov_b32 s86, 0\n" \
  "v_mfma_f32_16x16x32_bf16 v[72:75], a[68:71], v[128:131], 0\n" \
  "ds_read_b64_tr_b16 a[134:135], v229 offset:512\n" \
  "s_add_u32 m0, s74, 0xc000\n" \
  "v_mfma_f32_16x16x32_bf16 v[76:79], a[68:71], v[132:135], 0\n" \
  "ds_read_b64_tr_b16 a[136:137], v230\n" \
  "s_mov_b32 s87, 0x80000\n" \
  "v_mfma_f32_16x16x32_bf16 v[80:83], a[72:75], v[128:131], 0\n" \
  "ds_read_b64_tr_b16 a[138:139], v230 offset:512\n" \
  "ds_read_b64_tr_b16 a[140:141], v231\n" \
  "v_mfma_f32_16x16x32_bf16 v[84:87], a[72:75], v[132:135], 0\n" \
  "buffer_load_dwordx4 v236, s[44:47], s86 offen lds\n" \
  "v_mul_f32_e64 v176, v0, v244\n" \
  "v_mfma_f32_16x16x32_bf16 v[88:91], a[76:79], v[128:131], 0\n" \
  "ds_read_b64_tr_b16 a[142:143], v231 offset:512\n" \
  "v_mul_f32_e64 v177, v1, v244\n" \
  "v_mfma_f32_16x16x32_bf16 v[92:95], a[76:79], v[132:135], 0\n" \
  "ds_read_b64_tr_b16 a[144:145], v228 offset:8192\n" \
  "v_mul_f32_e64 v178, v2, v244\n" \
  "v_mfma_f32_16x16x32_bf16 v[96:99], a[80:83], v[128:131], 0\n" \
  "ds_read_b64_tr_b16 a[146:147], v228 offset:8704\n" \
  "v_mul_f32_e64 v179, v3, v244\n" \
  "v_mfma_f32_16x16x32_bf16 v[100:103], a[80:83], v[132:135], 0\n" \
  "ds_read_b64_tr_b16 a[148:149], v229 offset:8192\n" \
  "v_mul_f32_e64 v32, v0, v32\n" \
  "v_mfma_f32_16x16x32_bf16 v[104:107], a[84:87], v[128:131], 0\n" \
  "ds_read_b64_tr_b16 a[150:151], v229 offset:8704\n" \
  "ds_read_b64_tr_b16 a[152:153], v230 offset:8192\n" \
  "v_exp_f32_e64 v176, v176\n" \
  "v_mfma_f32_16x16x32_bf16 v[108:111], a[84:87], v[132:135], 0\n" \
  "ds_read_b64_tr_b16 a[154:155], v230 offset:8704\n" \
  "v_mul_f32 v33, v1, v33\n" \
  "v_exp_f32 v177, v177\n" \
  "v_mfma_f32_16x16x32_bf16 v[112:115], a[88:91], v[128:131], 0\n" \
  "ds_read_b64_tr_b16 a[156:157], v231 offset:8192\n" \
  "v_mul_f32_e64 v34, v2, v34\n" \
  "v_mfma_f32_16x16x32_bf16 v[116:119], a[88:91], v[132:135], 0\n" \
  "buffer_load_dwordx4 v237, s[44:47], s86 offen offset:1024 lds\n" \
  "v_exp_f32_e64 v178, v178\n" \
  "v_mfma_f32_16x16x32_bf16 v[120:123], a[92:95], v[128:131], 0\n" \
  "ds_read_b64_tr_b16 a[158:159], v231 offset:8704\n" \
  "v_mul_f32_e64 v35, v3, v35\n" \
  "v_mfma_f32_16x16x32_bf16 v[124:127], a[92:95], v[132:135], 0\n" \
  "ds_read_b64_tr_b16 a[160:161], v228 offset:4096\n" \
  "v_fma_f32 v176, v176, v246, v246\n" \
  "v_mfma_f32_16x16x32_bf16 v[64:67], a[96:99], v[136:139], v[64:67]\n" \
  "ds_read_b64_tr_b16 a[162:163], v228 offset:4608\n" \
  "ds_read_b64_tr_b16 a[164:165], v229 offset:4096\n" \
  "v_exp_f32_e64 v179, v179\n" \
  "v_mfma_f32_16x16x32_bf16 v[68:71], a[96:99], v[140:143], v[68:71]\n" \
  "ds_read_b64_tr_b16 a[166:167], v229 offset:4608\n" \
  "v_fma_f32 v177, v177, v246, v246\n" \
  "v_mfma_f32_16x16x32_bf16 v[72:75], a[100:103], v[136:139], v[72:75]\n" \
  "ds_read_b64_tr_b16 a[168:169], v230 offset:4096\n" \
  "v_fma_f32 v178, v178, v246, v246\n" \
  "v_fma_f32 v179, v179, v246, v246\n" \
  "v_mfma_f32_16x16x32_bf16 v[76:79], a[100:103], v[140:143], v[76:79]\n" \
  "ds_read_b64_tr_b16 a[170:171], v230 offset:4608\n" \
  "v_mul_f32_e64 v180, v4, v245\n" \
  "v_mfma_f32_16x16x32_bf16 v[80:83], a[104:107], v[136:139], v[80:83]\n" \
  "ds_read_b64_tr_b16 a[172:173], v231 offset:4096\n" \
  "v_mul_f32_e64 v181, v5, v245\n" \
  "v_mfma_f32_16x16x32_bf16 v[84:87], a[104:107], v[140:143], v[84:87]\n" \
  "buffer_load_dwordx4 v238, s[44:47], s86 offen offset:2048 lds\n" \
  "v_rcp_f32_e64 v176, v176\n" \
  "v_mfma_f32_16x16x32_bf16 v[88:91], a[108:111], v[136:139], v[88:91]\n" \
  "ds_read_b64_tr_b16 a[174:175], v231 offset:4608\n" \
  "ds_read_b64_tr_b16 a[176:177], v228 offset:12288\n" \
  "v_rcp_f32_e64 v177, v177\n" \
  "v_mfma_f32_16x16x32_bf16 v[92:95], a[108:111], v[140:143], v[92:95]\n" \
  "ds_read_b64_tr_b16 a[178:179], v228 offset:12800\n" \
  "v_mul_f32_e64 v32, v32, v176\n" \
  "v_mfma_f32_16x16x32_bf16 v[96:99], a[112:115], v[136:139], v[96:99]\n" \
  "ds_read_b64_tr_b16 a[180:181], v229 offset:12288\n" \
  "v_mul_f32_e64 v33, v33, v177\n" \
  "v_mfma_f32_16x16x32_bf16 v[100:103], a[112:115], v[140:143], v[100:103]\n" \
  "ds_read_b64_tr_b16 a[182:183], v229 offset:12800\n" \
  "v_cvt_pk_bf16_f32 v0, v32, v33\n" \
  "v_rcp_f32_e64 v178, v178\n" \
  "v_mfma_f32_16x16x32_bf16 v[104:107], a[116:119], v[136:139], v[104:107]\n" \
  "ds_read_b64_tr_b16 a[184:185], v230 offset:12288\n" \
  "v_mul_f32_e64 v34, v34, v178\n" \
  "v_mfma_f32_16x16x32_bf16 v[108:111], a[116:119], v[140:143], v[108:111]\n" \
  "ds_read_b64_tr_b16 a[186:187], v230 offset:12800\n" \
  "ds_read_b64_tr_b16 a[188:189], v231 offset:12288\n" \
  "v_rcp_f32_e64 v179, v179\n" \
  "v_mfma_f32_16x16x32_bf16 v[112:115], a[120:123], v[136:139], v[112:115]\n" \
  "ds_read_b64_tr_b16 a[190:191], v231 offset:12800\n" \
  "v_mul_f32_e64 v35, v35, v179\n" \
  "v_mfma_f32_16x16x32_bf16 v[116:119], a[120:123], v[140:143], v[116:119]\n" \
  "buffer_load_dwordx4 v239, s[44:47], s86 offen offset:3072 lds\n" \
  "v_cvt_pk_bf16_f32 v1, v34, v35\n" \
  "v_mfma_f32_16x16x32_bf16 v[120:123], a[124:127], v[136:139], v[120:123]\n" \
  "ds_read_b64_tr_b16 a[64:65], v232\n" \
  "v_mul_f32_e64 v182, v6, v245\n" \
  "v_mfma_f32_16x16x32_bf16 v[124:127], a[124:127], v[140:143], v[124:127]\n" \
  "ds_read_b64_tr_b16 a[66:67], v232 offset:512\n" \
  "s_waitcnt lgkmcnt(6)\n" \
  "v_mul_f32 v183, v7, v245\n" \
  "v_mfma_f32_16x16x32_bf16 v[64:67], a[128:131], v[144:147], v[64:67]\n" \
  "ds_read_b64_tr_b16 a[68:69], v233\n" \
  "ds_read_b64_tr_b16 a[70:71], v233 offset:512\n" \
  "v_exp_f32 v180, v180\n" \
  "v_exp_f32 v181, v181\n" \
  "v_mfma_f32_16x16x32_bf16 v[68:71], a[128:131], v[148:151], v[68:71]\n" \
  "ds_read_b64_tr_b16 a[72:73], v234\n" \
  "v_mul_f32_e64 v36, v4, v36\n" \
  "v_mfma_f32_16x16x32_bf16 v[72:75], a[132:135], v[144:147], v[72:75]\n" \
  "ds_read_b64_tr_b16 a[74:75], v234 offset:512\n" \
  "v_mul_f32_e64 v37, v5, v37\n" \
  "v_mfma_f32_16x16x32_bf16 v[76:79], a[132:135], v[148:151], v[76:79]\n" \
  "ds_read_b64_tr_b16 a[76:77], v235\n" \
  "v_mul_f32_e64 v38, v6, v38\n" \
  "v_mfma_f32_16x16x32_bf16 v[80:83], a[136:139], v[144:147], v[80:83]\n" \
  "ds_read_b64_tr_b16 a[78:79], v235 offset:512\n" \
  "s_add_u32 m0, s75, 0xc000\n" \
  "v_mul_f32_e64 v39, v7, v39\n" \
  "v_mfma_f32_16x16x32_bf16 v[84:87], a[136:139], v[148:151], v[84:87]\n" \
  "buffer_load_dwordx4 v236, s[44:47], s87 offen lds\n" \
  "v_exp_f32_e64 v182, v182\n" \
  "v_mfma_f32_16x16x32_bf16 v[88:91], a[140:143], v[144:147], v[88:91]\n" \
  "ds_read_b64_tr_b16 a[80:81], v232 offset:8192\n" \
  "ds_read_b64_tr_b16 a[82:83], v232 offset:8704\n" \
  "v_exp_f32_e64 v183, v183\n" \
  "v_mfma_f32_16x16x32_bf16 v[92:95], a[140:143], v[148:151], v[92:95]\n" \
  "ds_read_b64_tr_b16 a[84:85], v233 offset:8192\n" \
  "v_fma_f32 v180, v180, v247, v247\n" \
  "v_fma_f32 v181, v181, v247, v247\n" \
  "v_mfma_f32_16x16x32_bf16 v[96:99], a[144:147], v[144:147], v[96:99]\n" \
  "ds_read_b64_tr_b16 a[86:87], v233 offset:8704\n" \
  "v_fma_f32 v182, v182, v247, v247\n" \
  "v_mfma_f32_16x16x32_bf16 v[100:103], a[144:147], v[148:151], v[100:103]\n" \
  "ds_read_b64_tr_b16 a[88:89], v234 offset:8192\n" \
  "v_fma_f32 v183, v183, v247, v247\n" \
  "v_mfma_f32_16x16x32_bf16 v[104:107], a[148:151], v[144:147], v[104:107]\n" \
  "ds_read_b64_tr_b16 a[90:91], v234 offset:8704\n" \
  "v_mul_f32_e64 v184, v8, v244\n" \
  "v_mfma_f32_16x16x32_bf16 v[108:111], a[148:151], v[148:151], v[108:111]\n" \
  "ds_read_b64_tr_b16 a[92:93], v235 offset:8192\n" \
  "ds_read_b64_tr_b16 a[94:95], v235 offset:8704\n" \
  "v_rcp_f32_e64 v180, v180\n" \
  "v_mfma_f32_16x16x32_bf16 v[112:115], a[152:155], v[144:147], v[112:115]\n" \
  "ds_read_b64_tr_b16 a[96:97], v232 offset:4096\n" \
  "v_mul_f32_e64 v36, v36, v180\n" \
  "v_mfma_f32_16x16x32_bf16 v[116:119], a[152:155], v[148:151], v[116:119]\n" \
  "buffer_load_dwordx4 v237, s[44:47], s87 offen offset:1024 lds\n" \
  "v_rcp_f32_e64 v181, v181\n" \
  "v_mfma_f32_16x16x32_bf16 v[120:123], a[156:159], v[144:147], v[120:123]\n" \
  "ds_read_b64_tr_b16 a[98:99], v232 offset:4608\n" \
  "v_mul_f32 v37, v37, v181\n" \
  "v_rcp_f32 v182, v182\n" \
  "v_mfma_f32_16x16x32_bf16 v[124:127], a[156:159], v[148:151], v[124:127]\n" \
  "ds_read_b64_tr_b16 a[100:101], v233 offset:4096\n" \
  "v_mul_f32_e64 v38, v38, v182\n" \
  "v_mfma_f32_16x16x32_bf16 v[64:67], a[160:163], v[152:155], v[64:67]\n" \
  "ds_read_b64_tr_b16 a[102:103], v233 offset:4608\n" \
  "v_cvt_pk_bf16_f32 v4, v36, v37\n" \
  "v_mfma_f32_16x16x32_bf16 v[68:71], a[160:163], v[156:159], v[68:71]\n" \
  "ds_read_b64_tr_b16 a[104:105], v234 offset:4096\n" \
  "ds_read_b64_tr_b16 a[106:107], v234 offset:4608\n" \
  "v_rcp_f32_e64 v183, v183\n" \
  "v_mfma_f32_16x16x32_bf16 v[72:75], a[164:167], v[152:155], v[72:75]\n" \
  "ds_read_b64_tr_b16 a[108:109], v235 offset:4096\n" \
  "v_mul_f32_e64 v39, v39, v183\n" \
  "v_mfma_f32_16x16x32_bf16 v[76:79], a[164:167], v[156:159], v[76:79]\n" \
  "ds_read_b64_tr_b16 a[110:111], v235 offset:4608\n" \
  "v_cvt_pk_bf16_f32 v5, v38, v39\n" \
  "v_mfma_f32_16x16x32_bf16 v[80:83], a[168:171], v[152:155], v[80:83]\n" \
  "ds_read_b64_tr_b16 a[112:113], v232 offset:12288\n" \
  "v_mul_f32 v185, v9, v244\n" \
  "v_mul_f32 v186, v10, v244\n" \
  "v_mfma_f32_16x16x32_bf16 v[84:87], a[168:171], v[156:159], v[84:87]\n" \
  "buffer_load_dwordx4 v238, s[44:47], s87 offen offset:2048 lds\n" \
  "v_mul_f32_e64 v187, v11, v244\n" \
  "v_mfma_f32_16x16x32_bf16 v[88:91], a[172:175], v[152:155], v[88:91]\n" \
  "ds_read_b64_tr_b16 a[114:115], v232 offset:12800\n" \
  "v_mul_f32_e64 v40, v8, v40\n" \
  "v_mfma_f32_16x16x32_bf16 v[92:95], a[172:175], v[156:159], v[92:95]\n" \
  "ds_read_b64_tr_b16 a[116:117], v233 offset:12288\n" \
  "ds_read_b64_tr_b16 a[118:119], v233 offset:12800\n" \
  "v_exp_f32_e64 v184, v184\n" \
  "v_mfma_f32_16x16x32_bf16 v[96:99], a[176:179], v[152:155], v[96:99]\n" \
  "ds_read_b64_tr_b16 a[120:121], v234 offset:12288\n" \
  "v_mul_f32_e64 v41, v9, v41\n" \
  "v_mfma_f32_16x16x32_bf16 v[100:103], a[176:179], v[156:159], v[100:103]\n" \
  "ds_read_b64_tr_b16 a[122:123], v234 offset:12800\n" \
  "v_mul_f32_e64 v42, v10, v42\n" \
  "v_mfma_f32_16x16x32_bf16 v[104:107], a[180:183], v[152:155], v[104:107]\n" \
  "ds_read_b64_tr_b16 a[124:125], v235 offset:12288\n" \
  "v_mul_f32_e64 v43, v11, v43\n" \
  "v_mfma_f32_16x16x32_bf16 v[108:111], a[180:183], v[156:159], v[108:111]\n" \
  "ds_read_b64_tr_b16 a[126:127], v235 offset:12800\n" \
  "s_waitcnt lgkmcnt(7)\n" \
  "v_fma_f32 v184, v184, v246, v246\n" \
  "v_exp_f32 v185, v185\n" \
  "v_mfma_f32_16x16x32_bf16 v[112:115], a[184:187], v[152:155], v[112:115]\n" \
  "v_exp_f32_e64 v186, v186\n" \
  "v_mfma_f32_16x16x32_bf16 v[116:119], a[184:187], v[156:159], v[116:119]\n" \
  "buffer_load_dwordx4 v239, s[44:47], s87 offen offset:3072 lds\n" \
  "v_exp_f32_e64 v187, v187\n" \
  "v_mfma_f32_16x16x32_bf16 v[120:123], a[188:191], v[152:155], v[120:123]\n" \
  "v_fma_f32 v185, v185, v246, v246\n" \
  "v_fma_f32 v186, v186, v246, v246\n" \
  "v_mfma_f32_16x16x32_bf16 v[124:127], a[188:191], v[156:159], v[124:127]\n" \
  "v_fma_f32 v187, v187, v246, v246\n" \
  "v_mul_f32_e64 v188, v12, v245\n" \
  "s_waitcnt vmcnt(16) lgkmcnt(0)\n" \
  "s_barrier\n" \
  "; HP [9, 10] ('F', 0, 2)\n" \
  "v_mfma_f32_16x16x32_bf16 v[64:67], a[64:67], v[160:163], v[64:67]\n" \
  "ds_read_b64_tr_b16 a[128:129], v228 offset:16384\n" \
  "ds_read_b64_tr_b16 a[130:131], v228 offset:16896\n" \
  "v_rcp_f32_e64 v184, v184\n" \
  "v_mfma_f32_16x16x32_bf16 v[68:71], a[64:67], v[164:167], v[68:71]\n" \
  "ds_read_b64_tr_b16 a[132:133], v229 offset:16384\n" \
  "v_mul_f32_e64 v40, v40, v184\n" \
  "v_mfma_f32_16x16x32_bf16 v[72:75], a[68:71], v[160:163], v[72:75]\n" \
  "ds_read_b64_tr_b16 a[134:135], v229 offset:16896\n" \
  "v_mul_f32_e64 v189, v13, v245\n" \
  "v_mfma_f32_16x16x32_bf16 v[76:79], a[68:71], v[164:167], v[76:79]\n" \
  "ds_read_b64_tr_b16 a[136:137], v230 offset:16384\n" \
  "v_mul_f32_e64 v190, v14, v245\n" \
  "v_mfma_f32_16x16x32_bf16 v[80:83], a[72:75], v[160:163], v[80:83]\n" \
  "ds_read_b64_tr_b16 a[138:139], v230 offset:16896\n" \
  "ds_read_b64_tr_b16 a[140:141], v231 offset:16384\n" \
  "s_mov_b32 s86, 0x100000\n" \
  "s_add_u32 m0, s74, 0\n" \
  "v_rcp_f32 v185, v185\n" \
  "v_mfma_f32_16x16x32_bf16 v[84:87], a[72:75], v[164:167], v[84:87]\n" \
  "buffer_load_dwordx4 v236, s[44:47], s86 offen lds\n" \
  "v_rcp_f32_e64 v186, v186\n" \
  "v_mfma_f32_16x16x32_bf16 v[88:91], a[76:79], v[160:163], v[88:91]\n" \
  "ds_read_b64_tr_b16 a[142:143], v231 offset:16896\n" \
  "v_mul_f32_e64 v41, v41, v185\n" \
  "v_mfma_f32_16x16x32_bf16 v[92:95], a[76:79], v[164:167], v[92:95]\n" \
  "ds_read_b64_tr_b16 a[144:145], v228 offset:24576\n" \
  "v_mul_f32 v42, v42, v186\n" \
  "v_rcp_f32 v187, v187\n" \
  "v_mfma_f32_16x16x32_bf16 v[96:99], a[80:83], v[160:163], v[96:99]\n" \
  "ds_read_b64_tr_b16 a[146:147], v228 offset:25088\n" \
  "v_mul_f32_e64 v43, v43, v187\n" \
  "v_mfma_f32_16x16x32_bf16 v[100:103], a[80:83], v[164:167], v[100:103]\n" \
  "ds_read_b64_tr_b16 a[148:149], v229 offset:24576\n" \
  "v_cvt_pk_bf16_f32 v2, v40, v41\n" \
  "v_mfma_f32_16x16x32_bf16 v[104:107], a[84:87], v[160:163], v[104:107]\n" \
  "ds_read_b64_tr_b16 a[150:151], v229 offset:25088\n" \
  "ds_read_b64_tr_b16 a[152:153], v230 offset:24576\n" \
  "v_cvt_pk_bf16_f32 v3, v42, v43\n" \
  "v_mfma_f32_16x16x32_bf16 v[108:111], a[84:87], v[164:167], v[108:111]\n" \
  "buffer_store_dwordx4 v[0:3], v250, s[60:63], s81 offen offset:2048 sc1\n" \
  "v_mfma_f32_16x16x32_bf16 v[112:115], a[88:91], v[160:163], v[112:115]\n" \
  "ds_read_b64_tr_b16 a[154:155], v230 offset:25088\n" \
  "ds_read_b64_tr_b16 a[156:157], v231 offset:24576\n" \
  "v_mul_f32_e64 v191, v15, v245\n" \
  "v_mfma_f32_16x16x32_bf16 v[116:119], a[88:91], v[164:167], v[116:119]\n" \
  "buffer_load_dwordx4 v237, s[44:47], s86 offen offset:1024 lds\n" \
  "v_exp_f32_e64 v188, v188\n" \
  "v_mfma_f32_16x16x32_bf16 v[120:123], a[92:95], v[160:163], v[120:123]\n" \
  "ds_read_b64_tr_b16 a[158:159], v231 offset:25088\n" \
  "v_mul_f32 v44, v12, v44\n" \
  "v_exp_f32 v189, v189\n" \
  "v_mfma_f32_16x16x32_bf16 v[124:127], a[92:95], v[164:167], v[124:127]\n" \
  "ds_read_b64_tr_b16 a[160:161], v228 offset:20480\n" \
  "v_mul_f32_e64 v45, v13, v45\n" \
  "v_mfma_f32_16x16x32_bf16 v[64:67], a[96:99], v[168:171], v[64:67]\n" \
  "ds_read_b64_tr_b16 a[162:163], v228 offset:20992\n" \
  "ds_read_b64_tr_b16 a[164:165], v229 offset:20480\n" \
  "v_exp_f32_e64 v190, v190\n" \
  "v_mfma_f32_16x16x32_bf16 v[68:71], a[96:99], v[172:175], v[68:71]\n" \
  "ds_read_b64_tr_b16 a[166:167], v229 offset:20992\n" \
  "v_mul_f32_e64 v46, v14, v46\n" \
  "v_mfma_f32_16x16x32_bf16 v[72:75], a[100:103], v[168:171], v[72:75]\n" \
  "ds_read_b64_tr_b16 a[168:169], v230 offset:20480\n" \
  "v_mul_f32_e64 v47, v15, v47\n" \
  "v_mfma_f32_16x16x32_bf16 v[76:79], a[100:103], v[172:175], v[76:79]\n" \
  "ds_read_b64_tr_b16 a[170:171], v230 offset:20992\n" \
  "v_fma_f32 v188, v188, v247, v247\n" \
  "v_mfma_f32_16x16x32_bf16 v[80:83], a[104:107], v[168:171], v[80:83]\n" \
  "ds_read_b64_tr_b16 a[172:173], v231 offset:20480\n" \
  "v_fma_f32 v189, v189, v247, v247\n" \
  "v_mfma_f32_16x16x32_bf16 v[84:87], a[104:107], v[172:175], v[84:87]\n" \
  "buffer_load_dwordx4 v238, s[44:47], s86 offen offset:2048 lds\n" \
  "v_exp_f32_e64 v191, v191\n" \
  "v_mfma_f32_16x16x32_bf16 v[88:91], a[108:111], v[168:171], v[88:91]\n" \
  "ds_read_b64_tr_b16 a[174:175], v231 offset:20992\n" \
  "ds_read_b64_tr_b16 a[176:177], v228 offset:28672\n" \
  "v_fma_f32 v190, v190, v247, v247\n" \
  "v_fma_f32 v191, v191, v247, v247\n" \
  "v_mfma_f32_16x16x32_bf16 v[92:95], a[108:111], v[172:175], v[92:95]\n" \
  "ds_read_b64_tr_b16 a[178:179], v228 offset:29184\n" \
  "v_mul_f32_e64 v176, v16, v244\n" \
  "v_mfma_f32_16x16x32_bf16 v[96:99], a[112:115], v[168:171], v[96:99]\n" \
  "ds_read_b64_tr_b16 a[180:181], v229 offset:28672\n" \
  "v_mul_f32_e64 v177, v17, v244\n" \
  "v_mfma_f32_16x16x32_bf16 v[100:103], a[112:115], v[172:175], v[100:103]\n" \
  "ds_read_b64_tr_b16 a[182:183], v229 offset:29184\n" \
  "v_mul_f32_e64 v178, v18, v244\n" \
  "v_mfma_f32_16x16x32_bf16 v[104:107], a[116:119], v[168:171], v[104:107]\n" \
  "ds_read_b64_tr_b16 a[184:185], v230 offset:28672\n" \
  "v_mul_f32_e64 v179, v19, v244\n" \
  "v_mfma_f32_16x16x32_bf16 v[108:111], a[116:119], v[172:175], v[108:111]\n" \
  "ds_read_b64_tr_b16 a[186:187], v230 offset:29184\n" \
  "ds_read_b64_tr_b16 a[188:189], v231 offset:28672\n" \
  "v_rcp_f32_e64 v188, v188\n" \
  "v_mfma_f32_16x16x32_bf16 v[112:115], a[120:123], v[168:171], v[112:115]\n" \
  "ds_read_b64_tr_b16 a[190:191], v231 offset:29184\n" \
  "v_mul_f32_e64 v44, v44, v188\n" \
  "v_mfma_f32_16x16x32_bf16 v[116:119], a[120:123], v[172:175], v[116:119]\n" \
  "buffer_load_dwordx4 v239, s[44:47], s86 offen offset:3072 lds\n" \
  "v_rcp_f32 v189, v189\n" \
  "v_rcp_f32 v190, v190\n" \
  "v_mfma_f32_16x16x32_bf16 v[120:123], a[124:127], v[168:171], v[120:123]\n" \
  "ds_read_b64_tr_b16 a[64:65], v232 offset:16384\n" \
  "v_mul_f32_e64 v45, v45, v189\n" \
  "v_mfma_f32_16x16x32_bf16 v[124:127], a[124:127], v[172:175], v[124:127]\n" \
  "ds_read_b64_tr_b16 a[66:67], v232 offset:16896\n" \
  "s_waitcnt lgkmcnt(6)\n" \
  "v_mul_f32 v46, v46, v190\n" \
  "v_mfma_f32_16x16x32_bf16 v[64:67], a[128:131], a[192:195], v[64:67]\n" \
  "ds_read_b64_tr_b16 a[68:69], v233 offset:16384\n" \
  "ds_read_b64_tr_b16 a[70:71], v233 offset:16896\n" \
  "v_rcp_f32_e64 v191, v191\n" \
  "v_mfma_f32_16x16x32_bf16 v[68:71], a[128:131], a[196:199], v[68:71]\n" \
  "ds_read_b64_tr_b16 a[72:73], v234 offset:16384\n" \
  "v_mul_f32_e64 v47, v47, v191\n" \
  "v_mfma_f32_16x16x32_bf16 v[72:75], a[132:135], a[192:195], v[72:75]\n" \
  "ds_read_b64_tr_b16 a[74:75], v234 offset:16896\n" \
  "v_cvt_pk_bf16_f32 v6, v44, v45\n" \
  "v_mfma_f32_16x16x32_bf16 v[76:79], a[132:135], a[196:199], v[76:79]\n" \
  "ds_read_b64_tr_b16 a[76:77], v235 offset:16384\n" \
  "v_cvt_pk_bf16_f32 v7, v46, v47\n" \
  "v_mfma_f32_16x16x32_bf16 v[80:83], a[136:139], a[192:195], v[80:83]\n" \
  "s_mov_b32 s87, 0x180000\n" \
  "s_add_u32 m0, s75, 0\n" \
  "buffer_store_dwordx4 v[4:7], v250, s[60:63], s82 offen offset:2048 sc1\n" \
  "v_mfma_f32_16x16x32_bf16 v[84:87], a[136:139], a[196:199], v[84:87]\n" \
  "buffer_load_dwordx4 v236, s[44:47], s87 offen lds\n" \
  "v_exp_f32 v176, v176\n" \
  "v_exp_f32_e64 v177, v177\n" \
  "v_mfma_f32_16x16x32_bf16 v[88:91], a[140:143], a[192:195], v[88:91]\n" \
  "ds_read_b64_tr_b16 a[78:79], v235 offset:16896\n" \
  "ds_read_b64_tr_b16 a[80:81], v232 offset:24576\n" \
  "v_exp_f32_e64 v178, v178\n" \
  "v_mfma_f32_16x16x32_bf16 v[92:95], a[140:143], a[196:199], v[92:95]\n" \
  "ds_read_b64_tr_b16 a[82:83], v232 offset:25088\n" \
  "ds_read_b64_tr_b16 a[84:85], v233 offset:24576\n" \
  "v_exp_f32_e64 v179, v179\n" \
  "v_mfma_f32_16x16x32_bf16 v[96:99], a[144:147], a[192:195], v[96:99]\n" \
  "ds_read_b64_tr_b16 a[86:87], v233 offset:25088\n" \
  "v_mul_f32_e64 v48, v16, v48\n" \
  "v_mfma_f32_16x16x32_bf16 v[100:103], a[144:147], a[196:199], v[100:103]\n" \
  "ds_read_b64_tr_b16 a[88:89], v234 offset:24576\n" \
  "v_mul_f32_e64 v49, v17, v49\n" \
  "v_mfma_f32_16x16x32_bf16 v[104:107], a[148:151], a[192:195], v[104:107]\n" \
  "ds_read_b64_tr_b16 a[90:91], v234 offset:25088\n" \
  "v_mul_f32_e64 v50, v18, v50\n" \
  "v_mfma_f32_16x16x32_bf16 v[108:111], a[148:151], a[196:199], v[108:111]\n" \
  "ds_read_b64_tr_b16 a[92:93], v235 offset:24576\n" \
  "ds_read_b64_tr_b16 a[94:95], v235 offset:25088\n" \
  "v_mul_f32_e64 v51, v19, v51\n" \
  "v_mfma_f32_16x16x32_bf16 v[112:115], a[152:155], a[192:195], v[112:115]\n" \
  "ds_read_b64_tr_b16 a[96:97], v232 offset:20480\n" \
  "v_fma_f32 v176, v176, v246, v246\n" \
  "v_fma_f32 v177, v177, v246, v246\n" \
  "v_mfma_f32_16x16x32_bf16 v[116:119], a[152:155], a[196:199], v[116:119]\n" \
  "buffer_load_dwordx4 v237, s[44:47], s87 offen offset:1024 lds\n" \
  "v_fma_f32 v178, v178, v246, v246\n" \
  "v_mfma_f32_16x16x32_bf16 v[120:123], a[156:159], a[192:195], v[120:123]\n" \
  "ds_read_b64_tr_b16 a[98:99], v232 offset:20992\n" \
  "v_fma_f32 v179, v179, v246, v246\n" \
  "v_mfma_f32_16x16x32_bf16 v[124:127], a[156:159], a[196:199], v[124:127]\n" \
  "ds_read_b64_tr_b16 a[100:101], v233 offset:20480\n" \
  "v_mul_f32_e64 v180, v20, v245\n" \
  "v_mfma_f32_16x16x32_bf16 v[64:67], a[160:163], a[200:203], v[64:67]\n" \
  "ds_read_b64_tr_b16 a[102:103], v233 offset:20992\n" \
  "v_mul_f32_e64 v181, v21, v245\n" \
  "v_mfma_f32_16x16x32_bf16 v[68:71], a[160:163], a[204:207], v[68:71]\n" \
  "ds_read_b64_tr_b16 a[104:105], v234 offset:20480\n" \
  "ds_read_b64_tr_b16 a[106:107], v234 offset:20992\n" \
  "v_rcp_f32_e64 v176, v176\n" \
  "v_mfma_f32_16x16x32_bf16 v[72:75], a[164:167], a[200:203], v[72:75]\n" \
  "ds_read_b64_tr_b16 a[108:109], v235 offset:20480\n" \
  "v_mul_f32_e64 v48, v48, v176\n" \
  "v_mfma_f32_16x16x32_bf16 v[76:79], a[164:167], a[204:207], v[76:79]\n" \
  "ds_read_b64_tr_b16 a[110:111], v235 offset:20992\n" \
  "v_mul_f32 v182, v22, v245\n" \
  "v_rcp_f32 v177, v177\n" \
  "v_mfma_f32_16x16x32_bf16 v[80:83], a[168:171], a[200:203], v[80:83]\n" \
  "ds_read_b64_tr_b16 a[112:113], v232 offset:28672\n" \
  "v_mul_f32_e64 v49, v49, v177\n" \
  "v_mfma_f32_16x16x32_bf16 v[84:87], a[168:171], a[204:207], v[84:87]\n" \
  "buffer_load_dwordx4 v238, s[44:47], s87 offen offset:2048 lds\n" \
  "v_rcp_f32_e64 v178, v178\n" \
  "v_mfma_f32_16x16x32_bf16 v[88:91], a[172:175], a[200:203], v[88:91]\n" \
  "ds_read_b64_tr_b16 a[114:115], v232 offset:29184\n" \
  "v_mul_f32_e64 v50, v50, v178\n" \
  "v_mfma_f32_16x16x32_bf16 v[92:95], a[172:175], a[204:207], v[92:95]\n" \
  "ds_read_b64_tr_b16 a[116:117], v233 offset:28672\n" \
  "ds_read_b64_tr_b16 a[118:119], v233 offset:29184\n" \
  "v_rcp_f32_e64 v179, v179\n" \
  "v_mfma_f32_16x16x32_bf16 v[96:99], a[176:179], a[200:203], v[96:99]\n" \
  "ds_read_b64_tr_b16 a[120:121], v234 offset:28672\n" \
  "v_mul_f32_e64 v51, v51, v179\n" \
  "v_mfma_f32_16x16x32_bf16 v[100:103], a[176:179], a[204:207], v[100:103]\n" \
  "ds_read_b64_tr_b16 a[122:123], v234 offset:29184\n" \
  "v_cvt_pk_bf16_f32 v16, v48, v49\n" \
  "v_mfma_f32_16x16x32_bf16 v[104:107], a[180:183], a[200:203], v[104:107]\n" \
  "ds_read_b64_tr_b16 a[124:125], v235 offset:28672\n" \
  "v_cvt_pk_bf16_f32 v17, v50, v51\n" \
  "v_mfma_f32_16x16x32_bf16 v[108:111], a[180:183], a[204:207], v[108:111]\n" \
  "ds_read_b64_tr_b16 a[126:127], v235 offset:29184\n" \
  "s_waitcnt lgkmcnt(7)\n" \
  "v_mul_f32 v183, v23, v245\n" \
  "v_exp_f32_e64 v180, v180\n" \
  "v_mfma_f32_16x16x32_bf16 v[112:115], a[184:187], a[200:203], v[112:115]\n" \
  "v_exp_f32_e64 v181, v181\n" \
  "v_mfma_f32_16x16x32_bf16 v[116:119], a[184:187], a[204:207], v[116:119]\n" \
  "buffer_load_dwordx4 v239, s[44:47], s87 offen offset:3072 lds\n" \
  "v_exp_f32_e64 v182, v182\n" \
  "v_mfma_f32_16x16x32_bf16 v[120:123], a[188:191], a[200:203], v[120:123]\n" \
  "v_exp_f32_e64 v183, v183\n" \
  "v_mfma_f32_16x16x32_bf16 v[124:127], a[188:191], a[204:207], v[124:127]\n" \
  "v_mul_f32 v52, v20, v52\n" \
  "v_mul_f32 v53, v21, v53\n" \
  "s_waitcnt vmcnt(18) lgkmcnt(0)\n" \
  "s_barrier\n" \
  "; HP [11, 12] ('F', 0, 4)\n" \
  "v_mfma_f32_16x16x32_bf16 v[64:67], a[64:67], a[208:211], v[64:67]\n" \
  "ds_read_b64_tr_b16 a[128:129], v228 offset:32768\n" \
  "ds_read_b64_tr_b16 a[130:131], v228 offset:33280\n" \
  "v_mul_f32_e64 v54, v22, v54\n" \
  "v_mfma_f32_16x16x32_bf16 v[68:71], a[64:67], a[212:215], v[68:71]\n" \
  "ds_read_b64_tr_b16 a[132:133], v229 offset:32768\n" \
  "v_mul_f32_e64 v55, v23, v55\n" \
  "v_mfma_f32_16x16x32_bf16 v[72:75], a[68:71], a[208:211], v[72:75]\n" \
  "ds_read_b64_tr_b16 a[134:135], v229 offset:33280\n" \
  "v_fma_f32 v180, v180, v247, v247\n" \
  "v_mfma_f32_16x16x32_bf16 v[76:79], a[68:71], a[212:215], v[76:79]\n" \
  "ds_read_b64_tr_b16 a[136:137], v230 offset:32768\n" \
  "v_fma_f32 v181, v181, v247, v247\n" \
  "v_mfma_f32_16x16x32_bf16 v[80:83], a[72:75], a[208:211], v[80:83]\n" \
  "ds_read_b64_tr_b16 a[138:139], v230 offset:33280\n" \
  "ds_read_b64_tr_b16 a[140:141], v231 offset:32768\n" \
  "s_mov_b32 s86, 0x200000\n" \
  "s_add_u32 m0, s74, 0x4000\n" \
  "v_fma_f32 v182, v182, v247, v247\n" \
  "v_mfma_f32_16x16x32_bf16 v[84:87], a[72:75], a[212:215], v[84:87]\n" \
  "buffer_load_dwordx4 v236, s[44:47], s86 offen lds\n" \
  "v_fma_f32 v183, v183, v247, v247\n" \
  "v_mfma_f32_16x16x32_bf16 v[88:91], a[76:79], a[208:211], v[88:91]\n" \
  "ds_read_b64_tr_b16 a[142:143], v231 offset:33280\n" \
  "v_mul_f32_e64 v184, v24, v244\n" \
  "v_mfma_f32_16x16x32_bf16 v[92:95], a[76:79], a[212:215], v[92:95]\n" \
  "ds_read_b64_tr_b16 a[144:145], v228 offset:40960\n" \
  "v_mul_f32 v185, v25, v244\n" \
  "v_rcp_f32 v180, v180\n" \
  "v_mfma_f32_16x16x32_bf16 v[96:99], a[80:83], a[208:211], v[96:99]\n" \
  "ds_read_b64_tr_b16 a[146:147], v228 offset:41472\n" \
  "v_mul_f32_e64 v52, v52, v180\n" \
  "v_mfma_f32_16x16x32_bf16 v[100:103], a[80:83], a[212:215], v[100:103]\n" \
  "ds_read_b64_tr_b16 a[148:149], v229 offset:40960\n" \
  "v_mul_f32_e64 v186, v26, v244\n" \
  "v_mfma_f32_16x16x32_bf16 v[104:107], a[84:87], a[208:211], v[104:107]\n" \
  "ds_read_b64_tr_b16 a[150:151], v229 offset:41472\n" \
  "ds_read_b64_tr_b16 a[152:153], v230 offset:40960\n" \
  "v_rcp_f32_e64 v181, v181\n" \
  "v_mfma_f32_16x16x32_bf16 v[108:111], a[84:87], a[212:215], v[108:111]\n" \
  "ds_read_b64_tr_b16 a[154:155], v230 offset:41472\n" \
  "v_mul_f32_e64 v53, v53, v181\n" \
  "v_mfma_f32_16x16x32_bf16 v[112:115], a[88:91], a[208:211], v[112:115]\n" \
  "ds_read_b64_tr_b16 a[156:157], v231 offset:40960\n" \
  "v_cvt_pk_bf16_f32 v20, v52, v53\n" \
  "v_mfma_f32_16x16x32_bf16 v[116:119], a[88:91], a[212:215], v[116:119]\n" \
  "buffer_load_dwordx4 v237, s[44:47], s86 offen offset:1024 lds\n" \
  "v_rcp_f32_e64 v182, v182\n" \
  "v_mfma_f32_16x16x32_bf16 v[120:123], a[92:95], a[208:211], v[120:123]\n" \
  "ds_read_b64_tr_b16 a[158:159], v231 offset:41472\n" \
  "v_mul_f32_e64 v54, v54, v182\n" \
  "v_mfma_f32_16x16x32_bf16 v[124:127], a[92:95], a[212:215], v[124:127]\n" \
  "ds_read_b64_tr_b16 a[160:161], v228 offset:36864\n" \
  "v_mul_f32 v187, v27, v244\n" \
  "v_rcp_f32 v183, v183\n" \
  "v_mfma_f32_16x16x32_bf16 v[64:67], a[96:99], a[216:219], v[64:67]\n" \
  "ds_read_b64_tr_b16 a[162:163], v228 offset:37376\n" \
  "ds_read_b64_tr_b16 a[164:165], v229 offset:36864\n" \
  "v_mul_f32_e64 v55, v55, v183\n" \
  "v_mfma_f32_16x16x32_bf16 v[68:71], a[96:99], a[220:223], v[68:71]\n" \
  "ds_read_b64_tr_b16 a[166:167], v229 offset:37376\n" \
  "v_cvt_pk_bf16_f32 v21, v54, v55\n" \
  "v_mfma_f32_16x16x32_bf16 v[72:75], a[100:103], a[216:219], v[72:75]\n" \
  "ds_read_b64_tr_b16 a[168:169], v230 offset:36864\n" \
  "v_mul_f32_e64 v56, v24, v56\n" \
  "v_mfma_f32_16x16x32_bf16 v[76:79], a[100:103], a[220:223], v[76:79]\n" \
  "ds_read_b64_tr_b16 a[170:171], v230 offset:37376\n" \
  "v_mul_f32_e64 v57, v25, v57\n" \
  "v_mfma_f32_16x16x32_bf16 v[80:83], a[104:107], a[216:219], v[80:83]\n" \
  "ds_read_b64_tr_b16 a[172:173], v231 offset:36864\n" \
  "v_mul_f32_e64 v58, v26, v58\n" \
  "v_mfma_f32_16x16x32_bf16 v[84:87], a[104:107], a[220:223], v[84:87]\n" \
  "buffer_load_dwordx4 v238, s[44:47], s86 offen offset:2048 lds\n" \
  "v_exp_f32_e64 v184, v184\n" \
  "v_mfma_f32_16x16x32_bf16 v[88:91], a[108:111], a[216:219], v[88:91]\n" \
  "ds_read_b64_tr_b16 a[174:175], v231 offset:37376\n" \
  "ds_read_b64_tr_b16 a[176:177], v228 offset:45056\n" \
  "v_exp_f32_e64 v185, v185\n" \
  "v_mfma_f32_16x16x32_bf16 v[92:95], a[108:111], a[220:223], v[92:95]\n" \
  "ds_read_b64_tr_b16 a[178:179], v228 offset:45568\n" \
  "v_mul_f32 v59, v27, v59\n" \
  "v_exp_f32 v186, v186\n" \
  "v_mfma_f32_16x16x32_bf16 v[96:99], a[112:115], a[216:219], v[96:99]\n" \
  "ds_read_b64_tr_b16 a[180:181], v229 offset:45056\n" \
  "v_fma_f32 v184, v184, v246, v246\n" \
  "v_mfma_f32_16x16x32_bf16 v[100:103], a[112:115], a[220:223], v[100:103]\n" \
  "ds_read_b64_tr_b16 a[182:183], v229 offset:45568\n" \
  "v_fma_f32 v185, v185, v246, v246\n" \
  "v_mfma_f32_16x16x32_bf16 v[104:107], a[116:119], a[216:219], v[104:107]\n" \
  "ds_read_b64_tr_b16 a[184:185], v230 offset:45056\n" \
  "v_fma_f32 v186, v186, v246, v246\n" \
  "v_mfma_f32_16x16x32_bf16 v[108:111], a[116:119], a[220:223], v[108:111]\n" \
  "ds_read_b64_tr_b16 a[186:187], v230 offset:45568\n" \
  "ds_read_b64_tr_b16 a[188:189], v231 offset:45056\n" \
  "v_exp_f32_e64 v187, v187\n" \
  "v_mfma_f32_16x16x32_bf16 v[112:115], a[120:123], a[216:219], v[112:115]\n" \
  "ds_read_b64_tr_b16 a[190:191], v231 offset:45568\n" \
  "v_fma_f32 v187, v187, v246, v246\n" \
  "v_mfma_f32_16x16x32_bf16 v[116:119], a[120:123], a[220:223], v[116:119]\n" \
  "buffer_load_dwordx4 v239, s[44:47], s86 offen offset:3072 lds\n" \
  "v_rcp_f32_e64 v184, v184\n" \
  "v_mfma_f32_16x16x32_bf16 v[120:123], a[124:127], a[216:219], v[120:123]\n" \
  "ds_read_b64_tr_b16 a[64:65], v232 offset:32768\n" \
  "v_mul_f32 v56, v56, v184\n" \
  "v_rcp_f32 v185, v185\n" \
  "v_mfma_f32_16x16x32_bf16 v[124:127], a[124:127], a[220:223], v[124:127]\n" \
  "ds_read_b64_tr_b16 a[66:67], v232 offset:33280\n" \
  "s_waitcnt lgkmcnt(6)\n" \
  "v_mul_f32 v57, v57, v185\n" \
  "v_mfma_f32_16x16x32_bf16 v[64:67], a[128:131], a[224:227], v[64:67]\n" \
  "ds_read_b64_tr_b16 a[68:69], v233 offset:32768\n" \
  "ds_read_b64_tr_b16 a[70:71], v233 offset:33280\n" \
  "v_rcp_f32_e64 v186, v186\n" \
  "v_mfma_f32_16x16x32_bf16 v[68:71], a[128:131], a[228:231], v[68:71]\n" \
  "ds_read_b64_tr_b16 a[72:73], v234 offset:32768\n" \
  "v_mul_f32_e64 v58, v58, v186\n" \
  "v_mfma_f32_16x16x32_bf16 v[72:75], a[132:135], a[224:227], v[72:75]\n" \
  "ds_read_b64_tr_b16 a[74:75], v234 offset:33280\n" \
  "v_cvt_pk_bf16_f32 v18, v56, v57\n" \
  "v_mfma_f32_16x16x32_bf16 v[76:79], a[132:135], a[228:231], v[76:79]\n" \
  "ds_read_b64_tr_b16 a[76:77], v235 offset:32768\n" \
  "v_mul_f32_e64 v188, v28, v245\n" \
  "v_mfma_f32_16x16x32_bf16 v[80:83], a[136:139], a[224:227], v[80:83]\n" \
  "ds_read_b64_tr_b16 a[78:79], v235 offset:33280\n" \
  "s_mov_b32 s87, 0x280000\n" \
  "s_add_u32 m0, s75, 0x4000\n" \
  "v_mul_f32_e64 v189, v29, v245\n" \
  "v_mfma_f32_16x16x32_bf16 v[84:87], a[136:139], a[228:231], v[84:87]\n" \
  "buffer_load_dwordx4 v236, s[44:47], s87 offen lds\n" \
  "v_rcp_f32_e64 v187, v187\n" \
  "v_mfma_f32_16x16x32_bf16 v[88:91], a[140:143], a[224:227], v[88:91]\n" \
  "ds_read_b64_tr_b16 a[80:81], v232 offset:40960\n" \
  "ds_read_b64_tr_b16 a[82:83], v232 offset:41472\n" \
  "v_mul_f32_e64 v59, v59, v187\n" \
  "v_cvt_pk_bf16_f32 v19, v58, v59\n" \
  "v_mfma_f32_16x16x32_bf16 v[92:95], a[140:143], a[228:231], v[92:95]\n" \
  "buffer_store_dwordx4 v[16:19], v250, s[60:63], s81 offen offset:3072 sc1\n" \
  "v_mfma_f32_16x16x32_bf16 v[96:99], a[144:147], a[224:227], v[96:99]\n" \
  "ds_read_b64_tr_b16 a[84:85], v233 offset:40960\n" \
  "ds_read_b64_tr_b16 a[86:87], v233 offset:41472\n" \
  "v_mul_f32_e64 v190, v30, v245\n" \
  "v_mfma_f32_16x16x32_bf16 v[100:103], a[144:147], a[228:231], v[100:103]\n" \
  "ds_read_b64_tr_b16 a[88:89], v234 offset:40960\n" \
  "v_mul_f32_e64 v191, v31, v245\n" \
  "v_mfma_f32_16x16x32_bf16 v[104:107], a[148:151], a[224:227], v[104:107]\n" \
  "ds_read_b64_tr_b16 a[90:91], v234 offset:41472\n" \
  "v_mul_f32_e64 v60, v28, v60\n" \
  "v_mfma_f32_16x16x32_bf16 v[108:111], a[148:151], a[228:231], v[108:111]\n" \
  "ds_read_b64_tr_b16 a[92:93], v235 offset:40960\n" \
  "ds_read_b64_tr_b16 a[94:95], v235 offset:41472\n" \
  "v_exp_f32_e64 v188, v188\n" \
  "v_mfma_f32_16x16x32_bf16 v[112:115], a[152:155], a[224:227], v[112:115]\n" \
  "ds_read_b64_tr_b16 a[96:97], v232 offset:36864\n" \
  "v_mul_f32_e64 v61, v29, v61\n" \
  "v_mfma_f32_16x16x32_bf16 v[116:119], a[152:155], a[228:231], v[116:119]\n" \
  "buffer_load_dwordx4 v237, s[44:47], s87 offen offset:1024 lds\n" \
  "v_exp_f32_e64 v189, v189\n" \
  "v_mfma_f32_16x16x32_bf16 v[120:123], a[156:159], a[224:227], v[120:123]\n" \
  "ds_read_b64_tr_b16 a[98:99], v232 offset:37376\n" \
  "v_mul_f32 v62, v30, v62\n" \
  "v_exp_f32 v190, v190\n" \
  "v_mfma_f32_16x16x32_bf16 v[124:127], a[156:159], a[228:231], v[124:127]\n" \
  "ds_read_b64_tr_b16 a[100:101], v233 offset:36864\n" \
  "v_mul_f32_e64 v63, v31, v63\n" \
  "v_mfma_f32_16x16x32_bf16 v[64:67], a[160:163], a[232:235], v[64:67]\n" \
  "ds_read_b64_tr_b16 a[102:103], v233 offset:37376\n" \
  "v_fma_f32 v188, v188, v247, v247\n" \
  "v_mfma_f32_16x16x32_bf16 v[68:71], a[160:163], a[236:239], v[68:71]\n" \
  "ds_read_b64_tr_b16 a[104:105], v234 offset:36864\n" \
  "ds_read_b64_tr_b16 a[106:107], v234 offset:37376\n" \
  "v_exp_f32_e64 v191, v191\n" \
  "v_mfma_f32_16x16x32_bf16 v[72:75], a[164:167], a[232:235], v[72:75]\n" \
  "ds_read_b64_tr_b16 a[108:109], v235 offset:36864\n" \
  "v_fma_f32 v189, v189, v247, v247\n" \
  "v_mfma_f32_16x16x32_bf16 v[76:79], a[164:167], a[236:239], v[76:79]\n" \
  "ds_read_b64_tr_b16 a[110:111], v235 offset:37376\n" \
  "v_fma_f32 v190, v190, v247, v247\n" \
  "v_mfma_f32_16x16x32_bf16 v[80:83], a[168:171], a[232:235], v[80:83]\n" \
  "ds_read_b64_tr_b16 a[112:113], v232 offset:45056\n" \
  "v_fma_f32 v191, v191, v247, v247\n" \
  "v_mfma_f32_16x16x32_bf16 v[84:87], a[168:171], a[236:239], v[84:87]\n" \
  "buffer_load_dwordx4 v238, s[44:47], s87 offen offset:2048 lds\n" \
  "v_rcp_f32_e64 v188, v188\n" \
  "v_mfma_f32_16x16x32_bf16 v[88:91], a[172:175], a[232:235], v[88:91]\n" \
  "ds_read_b64_tr_b16 a[114:115], v232 offset:45568\n" \
  "v_mul_f32 v60, v60, v188\n" \
  "v_rcp_f32 v189, v189\n" \
  "v_mfma_f32_16x16x32_bf16 v[92:95], a[172:175], a[236:239], v[92:95]\n" \
  "ds_read_b64_tr_b16 a[116:117], v233 offset:45056\n" \
  "ds_read_b64_tr_b16 a[118:119], v233 offset:45568\n" \
  "v_rcp_f32_e64 v190, v190\n" \
  "v_mfma_f32_16x16x32_bf16 v[96:99], a[176:179], a[232:235], v[96:99]\n" \
  "ds_read_b64_tr_b16 a[120:121], v234 offset:45056\n" \
  "v_mul_f32_e64 v61, v61, v189\n" \
  "v_mfma_f32_16x16x32_bf16 v[100:103], a[176:179], a[236:239], v[100:103]\n" \
  "ds_read_b64_tr_b16 a[122:123], v234 offset:45568\n" \
  "v_mul_f32_e64 v62, v62, v190\n" \
  "v_mfma_f32_16x16x32_bf16 v[104:107], a[180:183], a[232:235], v[104:107]\n" \
  "ds_read_b64_tr_b16 a[124:125], v235 offset:45056\n" \
  "v_cvt_pk_bf16_f32 v22, v60, v61\n" \
  "v_mfma_f32_16x16x32_bf16 v[108:111], a[180:183], a[236:239], v[108:111]\n" \
  "ds_read_b64_tr_b16 a[126:127], v235 offset:45568\n" \
  "s_waitcnt lgkmcnt(7)\n" \
  "v_rcp_f32 v191, v191\n" \
  "v_mfma_f32_16x16x32_bf16 v[112:115], a[184:187], a[232:235], v[112:115]\n" \
  "v_mul_f32_e64 v63, v63, v191\n" \
  "v_cvt_pk_bf16_f32 v23, v62, v63\n" \
  "v_mfma_f32_16x16x32_bf16 v[116:119], a[184:187], a[236:239], v[116:119]\n" \
  "buffer_load_dwordx4 v239, s[44:47], s87 offen offset:3072 lds\n" \
  "buffer_store_dwordx4 v[20:23], v250, s[60:63], s82 offen offset:3072 sc1\n" \
  "v_mfma_f32_16x16x32_bf16 v[120:123], a[188:191], a[232:235], v[120:123]\n" \
  "v_mfma_f32_16x16x32_bf16 v[124:127], a[188:191], a[236:239], v[124:127]\n" \
  "s_waitcnt vmcnt(20) lgkmcnt(0)\n" \
  "s_barrier\n" \
  "s_nop 7\n" \
  "L_join_%=:\n" \
  "; HP [13, 14] ('F', 0, 6)\n" \
  "v_mfma_f32_16x16x32_bf16 v[64:67], a[64:67], a[240:243], v[64:67]\n" \
  "ds_read_b64_tr_b16 a[128:129], v228 offset:49152\n" \
  "ds_read_b64_tr_b16 a[130:131], v228 offset:49664\n" \
  "v_mfma_f32_16x16x32_bf16 v[68:71], a[64:67], a[244:247], v[68:71]\n" \
  "ds_read_b64_tr_b16 a[132:133], v229 offset:49152\n" \
  "s_mov_b32 s86, 0x300000\n" \
  "v_mfma_f32_16x16x32_bf16 v[72:75], a[68:71], a[240:243], v[72:75]\n" \
  "ds_read_b64_tr_b16 a[134:135], v229 offset:49664\n" \
  "s_add_u32 m0, s74, 0x8000\n" \
  "v_mfma_f32_16x16x32_bf16 v[76:79], a[68:71], a[244:247], v[76:79]\n" \
  "ds_read_b64_tr_b16 a[136:137], v230 offset:49152\n" \
  "s_mov_b32 s87, 0x100\n" \
  "v_mfma_f32_16x16x32_bf16 v[80:83], a[72:75], a[240:243], v[80:83]\n" \
  "ds_read_b64_tr_b16 a[138:139], v230 offset:49664\n" \
  "ds_read_b64_tr_b16 a[140:141], v231 offset:49152\n" \
  "v_mfma_f32_16x16x32_bf16 v[84:87], a[72:75], a[244:247], v[84:87]\n" \
  "buffer_load_dwordx4 v236, s[44:47], s86 offen lds\n" \
  "v_mfma_f32_16x16x32_bf16 v[88:91], a[76:79], a[240:243], v[88:91]\n" \
  "ds_read_b64_tr_b16 a[142:143], v231 offset:49664\n" \
  "v_mfma_f32_16x16x32_bf16 v[92:95], a[76:79], a[244:247], v[92:95]\n" \
  "ds_read_b64_tr_b16 a[144:145], v228 offset:57344\n" \
  "v_mfma_f32_16x16x32_bf16 v[96:99], a[80:83], a[240:243], v[96:99]\n" \
  "ds_read_b64_tr_b16 a[146:147], v228 offset:57856\n" \
  "v_mfma_f32_16x16x32_bf16 v[100:103], a[80:83], a[244:247], v[100:103]\n" \
  "ds_read_b64_tr_b16 a[148:149], v229 offset:57344\n" \
  "v_mfma_f32_16x16x32_bf16 v[104:107], a[84:87], a[240:243], v[104:107]\n" \
  "ds_read_b64_tr_b16 a[150:151], v229 offset:57856\n" \
  "ds_read_b64_tr_b16 a[152:153], v230 offset:57344\n" \
  "v_mfma_f32_16x16x32_bf16 v[108:111], a[84:87], a[244:247], v[108:111]\n" \
  "ds_read_b64_tr_b16 a[154:155], v230 offset:57856\n" \
  "v_mfma_f32_16x16x32_bf16 v[112:115], a[88:91], a[240:243], v[112:115]\n" \
  "ds_read_b64_tr_b16 a[156:157], v231 offset:57344\n" \
  "v_mfma_f32_16x16x32_bf16 v[116:119], a[88:91], a[244:247], v[116:119]\n" \
  "buffer_load_dwordx4 v237, s[44:47], s86 offen offset:1024 lds\n" \
  "v_mfma_f32_16x16x32_bf16 v[120:123], a[92:95], a[240:243], v[120:123]\n" \
  "ds_read_b64_tr_b16 a[158:159], v231 offset:57856\n" \
  "v_mfma_f32_16x16x32_bf16 v[124:127], a[92:95], a[244:247], v[124:127]\n" \
  "ds_read_b64_tr_b16 a[160:161], v228 offset:53248\n" \
  "v_mfma_f32_16x16x32_bf16 v[64:67], a[96:99], a[248:251], v[64:67]\n" \
  "ds_read_b64_tr_b16 a[162:163], v228 offset:53760\n" \
  "ds_read_b64_tr_b16 a[164:165], v229 offset:53248\n" \
  "v_mfma_f32_16x16x32_bf16 v[68:71], a[96:99], a[252:255], v[68:71]\n" \
  "ds_read_b64_tr_b16 a[166:167], v229 offset:53760\n" \
  "v_mfma_f32_16x16x32_bf16 v[72:75], a[100:103], a[248:251], v[72:75]\n" \
  "ds_read_b64_tr_b16 a[168:169], v230 offset:53248\n" \
  "v_mfma_f32_16x16x32_bf16 v[76:79], a[100:103], a[252:255], v[76:79]\n" \
  "ds_read_b64_tr_b16 a[170:171], v230 offset:53760\n" \
  "v_mfma_f32_16x16x32_bf16 v[80:83], a[104:107], a[248:251], v[80:83]\n" \
  "ds_read_b64_tr_b16 a[172:173], v231 offset:53248\n" \
  "v_mfma_f32_16x16x32_bf16 v[84:87], a[104:107], a[252:255], v[84:87]\n" \
  "buffer_load_dwordx4 v238, s[44:47], s86 offen offset:2048 lds\n" \
  "v_mfma_f32_16x16x32_bf16 v[88:91], a[108:111], a[248:251], v[88:91]\n" \
  "ds_read_b64_tr_b16 a[174:175], v231 offset:53760\n" \
  "ds_read_b64_tr_b16 a[176:177], v228 offset:61440\n" \
  "v_mfma_f32_16x16x32_bf16 v[92:95], a[108:111], a[252:255], v[92:95]\n" \
  "ds_read_b64_tr_b16 a[178:179], v228 offset:61952\n" \
  "v_mfma_f32_16x16x32_bf16 v[96:99], a[112:115], a[248:251], v[96:99]\n" \
  "ds_read_b64_tr_b16 a[180:181], v229 offset:61440\n" \
  "v_mfma_f32_16x16x32_bf16 v[100:103], a[112:115], a[252:255], v[100:103]\n" \
  "ds_read_b64_tr_b16 a[182:183], v229 offset:61952\n" \
  "v_mfma_f32_16x16x32_bf16 v[104:107], a[116:119], a[248:251], v[104:107]\n" \
  "ds_read_b64_tr_b16 a[184:185], v230 offset:61440\n" \
  "v_mfma_f32_16x16x32_bf16 v[108:111], a[116:119], a[252:255], v[108:111]\n" \
  "ds_read_b64_tr_b16 a[186:187], v230 offset:61952\n" \
  "ds_read_b64_tr_b16 a[188:189], v231 offset:61440\n" \
  "v_mfma_f32_16x16x32_bf16 v[112:115], a[120:123], a[248:251], v[112:115]\n" \
  "ds_read_b64_tr_b16 a[190:191], v231 offset:61952\n" \
  "v_mfma_f32_16x16x32_bf16 v[116:119], a[120:123], a[252:255], v[116:119]\n" \
  "buffer_load_dwordx4 v239, s[44:47], s86 offen offset:3072 lds\n" \
  "v_mfma_f32_16x16x32_bf16 v[120:123], a[124:127], a[248:251], v[120:123]\n" \
  "ds_read_b64_tr_b16 a[64:65], v232 offset:49152\n" \
  "s_add_u32 m0, s75, 0x8000\n" \
  "v_mfma_f32_16x16x32_bf16 v[124:127], a[124:127], a[252:255], v[124:127]\n" \
  "ds_read_b64_tr_b16 a[66:67], v232 offset:49664\n" \
  "s_waitcnt lgkmcnt(6)\n" \
  "v_mfma_f32_16x16x32_bf16 v[0:3], a[128:131], v[128:131], 0\n" \
  "ds_read_b64_tr_b16 a[68:69], v233 offset:49152\n" \
  "ds_read_b64_tr_b16 a[70:71], v233 offset:49664\n" \
  "v_mfma_f32_16x16x32_bf16 v[4:7], a[128:131], v[132:135], 0\n" \
  "ds_read_b64_tr_b16 a[72:73], v234 offset:49152\n" \
  "v_mfma_f32_16x16x32_bf16 v[8:11], a[132:135], v[128:131], 0\n" \
  "ds_read_b64_tr_b16 a[74:75], v234 offset:49664\n" \
  "v_mfma_f32_16x16x32_bf16 v[12:15], a[132:135], v[132:135], 0\n" \
  "ds_read_b64_tr_b16 a[76:77], v235 offset:49152\n" \
  "v_mfma_f32_16x16x32_bf16 v[16:19], a[136:139], v[128:131], 0\n" \
  "ds_read_b64_tr_b16 a[78:79], v235 offset:49664\n" \
  "s_waitcnt lgkmcnt(7)\n" \
  "v_mfma_f32_16x16x32_bf16 v[20:23], a[136:139], v[132:135], 0\n" \
  "buffer_load_dwordx4 v236, s[44:47], s87 offen lds\n" \
  "v_mfma_f32_16x16x32_bf16 v[24:27], a[140:143], v[128:131], 0\n" \
  "ds_read_b64_tr_b16 a[80:81], v232 offset:57344\n" \
  "ds_read_b64_tr_b16 a[82:83], v232 offset:57856\n" \
  "v_mul_f32 v176, v64, v244\n" \
  "v_mfma_f32_16x16x32_bf16 v[28:31], a[140:143], v[132:135], 0\n" \
  "ds_read_b64_tr_b16 a[84:85], v233 offset:57344\n" \
  "v_mul_f32_e64 v177, v65, v244\n" \
  "v_mfma_f32_16x16x32_bf16 v[32:35], a[144:147], v[128:131], 0\n" \
  "ds_read_b64_tr_b16 a[86:87], v233 offset:57856\n" \
  "v_mul_f32_e64 v178, v66, v244\n" \
  "v_mfma_f32_16x16x32_bf16 v[36:39], a[144:147], v[132:135], 0\n" \
  "ds_read_b64_tr_b16 a[88:89], v234 offset:57344\n" \
  "v_mul_f32_e64 v179, v67, v244\n" \
  "v_mfma_f32_16x16x32_bf16 v[40:43], a[148:151], v[128:131], 0\n" \
  "ds_read_b64_tr_b16 a[90:91], v234 offset:57856\n" \
  "v_mul_f32_e64 v96, v64, v96\n" \
  "v_mfma_f32_16x16x32_bf16 v[44:47], a[148:151], v[132:135], 0\n" \
  "ds_read_b64_tr_b16 a[92:93], v235 offset:57344\n" \
  "ds_read_b64_tr_b16 a[94:95], v235 offset:57856\n" \
  "v_exp_f32_e64 v176, v176\n" \
  "v_mfma_f32_16x16x32_bf16 v[48:51], a[152:155], v[128:131], 0\n" \
  "ds_read_b64_tr_b16 a[96:97], v232 offset:53248\n" \
  "v_mul_f32_e64 v97, v65, v97\n" \
  "v_mfma_f32_16x16x32_bf16 v[52:55], a[152:155], v[132:135], 0\n" \
  "buffer_load_dwordx4 v237, s[44:47], s87 offen offset:1024 lds\n" \
  "v_exp_f32_e64 v177, v177\n" \
  "v_mfma_f32_16x16x32_bf16 v[56:59], a[156:159], v[128:131], 0\n" \
  "ds_read_b64_tr_b16 a[98:99], v232 offset:53760\n" \
  "v_mul_f32_e64 v98, v66, v98\n" \
  "v_mfma_f32_16x16x32_bf16 v[60:63], a[156:159], v[132:135], 0\n" \
  "ds_read_b64_tr_b16 a[100:101], v233 offset:53248\n" \
  "v_mul_f32_e64 v99, v67, v99\n" \
  "v_mfma_f32_16x16x32_bf16 v[0:3], a[160:163], v[136:139], v[0:3]\n" \
  "ds_read_b64_tr_b16 a[102:103], v233 offset:53760\n" \
  "v_fma_f32 v176, v176, v246, v246\n" \
  "v_mfma_f32_16x16x32_bf16 v[4:7], a[160:163], v[140:143], v[4:7]\n" \
  "ds_read_b64_tr_b16 a[104:105], v234 offset:53248\n" \
  "ds_read_b64_tr_b16 a[106:107], v234 offset:53760\n" \
  "v_exp_f32_e64 v178, v178\n" \
  "v_mfma_f32_16x16x32_bf16 v[8:11], a[164:167], v[136:139], v[8:11]\n" \
  "ds_read_b64_tr_b16 a[108:109], v235 offset:53248\n" \
  "v_fma_f32 v177, v177, v246, v246\n" \
  "v_mfma_f32_16x16x32_bf16 v[12:15], a[164:167], v[140:143], v[12:15]\n" \
  "ds_read_b64_tr_b16 a[110:111], v235 offset:53760\n" \
  "v_fma_f32 v178, v178, v246, v246\n" \
  "v_mfma_f32_16x16x32_bf16 v[16:19], a[168:171], v[136:139], v[16:19]\n" \
  "ds_read_b64_tr_b16 a[112:113], v232 offset:61440\n" \
  "v_mul_f32_e64 v180, v68, v245\n" \
  "v_mfma_f32_16x16x32_bf16 v[20:23], a[168:171], v[140:143], v[20:23]\n" \
  "buffer_load_dwordx4 v238, s[44:47], s87 offen offset:2048 lds\n" \
  "v_exp_f32_e64 v179, v179\n" \
  "v_mfma_f32_16x16x32_bf16 v[24:27], a[172:175], v[136:139], v[24:27]\n" \
  "ds_read_b64_tr_b16 a[114:115], v232 offset:61952\n" \
  "v_fma_f32 v179, v179, v246, v246\n" \
  "v_mfma_f32_16x16x32_bf16 v[28:31], a[172:175], v[140:143], v[28:31]\n" \
  "ds_read_b64_tr_b16 a[116:117], v233 offset:61440\n" \
  "ds_read_b64_tr_b16 a[118:119], v233 offset:61952\n" \
  "v_rcp_f32_e64 v176, v176\n" \
  "v_mfma_f32_16x16x32_bf16 v[32:35], a[176:179], v[136:139], v[32:35]\n" \
  "ds_read_b64_tr_b16 a[120:121], v234 offset:61440\n" \
  "v_mul_f32_e64 v96, v96, v176\n" \
  "v_mfma_f32_16x16x32_bf16 v[36:39], a[176:179], v[140:143], v[36:39]\n" \
  "ds_read_b64_tr_b16 a[122:123], v234 offset:61952\n" \
  "v_mul_f32_e64 v181, v69, v245\n" \
  "v_mfma_f32_16x16x32_bf16 v[40:43], a[180:183], v[136:139], v[40:43]\n" \
  "ds_read_b64_tr_b16 a[124:125], v235 offset:61440\n" \
  "v_mul_f32_e64 v182, v70, v245\n" \
  "v_mfma_f32_16x16x32_bf16 v[44:47], a[180:183], v[140:143], v[44:47]\n" \
  "ds_read_b64_tr_b16 a[126:127], v235 offset:61952\n" \
  "v_mul_f32_e64 v183, v71, v245\n" \
  "v_mfma_f32_16x16x32_bf16 v[48:51], a[184:187], v[136:139], v[48:51]\n" \
  "v_rcp_f32_e64 v177, v177\n" \
  "v_mfma_f32_16x16x32_bf16 v[52:55], a[184:187], v[140:143], v[52:55]\n" \
  "buffer_load_dwordx4 v239, s[44:47], s87 offen offset:3072 lds\n" \
  "v_rcp_f32_e64 v178, v178\n" \
  "v_mfma_f32_16x16x32_bf16 v[56:59], a[188:191], v[136:139], v[56:59]\n" \
  "v_rcp_f32_e64 v179, v179\n" \
  "v_mfma_f32_16x16x32_bf16 v[60:63], a[188:191], v[140:143], v[60:63]\n" \
  "v_mul_f32 v97, v97, v177\n" \
  "v_mul_f32 v98, v98, v178\n" \
  "s_waitcnt vmcnt(18) lgkmcnt(0)\n" \
  "s_barrier\n" \
  "; HP [15, 16] ('Q', 0, 1)\n" \
  "v_mfma_f32_16x16x32_bf16 v[0:3], a[64:67], v[144:147], v[0:3]\n" \
  "ds_read_b64_tr_b16 a[128:129], v228\n" \
  "ds_read_b64_tr_b16 a[130:131], v228 offset:512\n" \
  "v_mfma_f32_16x16x32_bf16 v[4:7], a[64:67], v[148:151], v[4:7]\n" \
  "ds_read_b64_tr_b16 a[132:133], v229\n" \
  "v_mul_f32_e64 v99, v99, v179\n" \
  "v_mfma_f32_16x16x32_bf16 v[8:11], a[68:71], v[144:147], v[8:11]\n" \
  "ds_read_b64_tr_b16 a[134:135], v229 offset:512\n" \
  "v_cvt_pk_bf16_f32 v64, v96, v97\n" \
  "v_mfma_f32_16x16x32_bf16 v[12:15], a[68:71], v[148:151], v[12:15]\n" \
  "ds_read_b64_tr_b16 a[136:137], v230\n" \
  "v_cvt_pk_bf16_f32 v65, v98, v99\n" \
  "v_mfma_f32_16x16x32_bf16 v[16:19], a[72:75], v[144:147], v[16:19]\n" \
  "ds_read_b64_tr_b16 a[138:139], v230 offset:512\n" \
  "ds_read_b64_tr_b16 a[140:141], v231\n" \
  "s_mov_b32 s86, 0x80100\n" \
  "s_add_u32 m0, s74, 0xc000\n" \
  "v_exp_f32_e64 v180, v180\n" \
  "v_mfma_f32_16x16x32_bf16 v[20:23], a[72:75], v[148:151], v[20:23]\n" \
  "buffer_load_dwordx4 v236, s[44:47], s86 offen lds\n" \
  "v_exp_f32_e64 v181, v181\n" \
  "v_mfma_f32_16x16x32_bf16 v[24:27], a[76:79], v[144:147], v[24:27]\n" \
  "ds_read_b64_tr_b16 a[142:143], v231 offset:512\n" \
  "v_mul_f32_e64 v100, v68, v100\n" \
  "v_mfma_f32_16x16x32_bf16 v[28:31], a[76:79], v[148:151], v[28:31]\n" \
  "ds_read_b64_tr_b16 a[144:145], v228 offset:8192\n" \
  "v_mul_f32_e64 v101, v69, v101\n" \
  "v_mfma_f32_16x16x32_bf16 v[32:35], a[80:83], v[144:147], v[32:35]\n" \
  "ds_read_b64_tr_b16 a[146:147], v228 offset:8704\n" \
  "v_mul_f32_e64 v102, v70, v102\n" \
  "v_mfma_f32_16x16x32_bf16 v[36:39], a[80:83], v[148:151], v[36:39]\n" \
  "ds_read_b64_tr_b16 a[148:149], v229 offset:8192\n" \
  "v_mul_f32_e64 v103, v71, v103\n" \
  "v_mfma_f32_16x16x32_bf16 v[40:43], a[84:87], v[144:147], v[40:43]\n" \
  "ds_read_b64_tr_b16 a[150:151], v229 offset:8704\n" \
  "ds_read_b64_tr_b16 a[152:153], v230 offset:8192\n" \
  "v_exp_f32_e64 v182, v182\n" \
  "v_mfma_f32_16x16x32_bf16 v[44:47], a[84:87], v[148:151], v[44:47]\n" \
  "ds_read_b64_tr_b16 a[154:155], v230 offset:8704\n" \
  "v_fma_f32 v180, v180, v247, v247\n" \
  "v_mfma_f32_16x16x32_bf16 v[48:51], a[88:91], v[144:147], v[48:51]\n" \
  "ds_read_b64_tr_b16 a[156:157], v231 offset:8192\n" \
  "v_fma_f32 v181, v181, v247, v247\n" \
  "v_mfma_f32_16x16x32_bf16 v[52:55], a[88:91], v[148:151], v[52:55]\n" \
  "buffer_load_dwordx4 v237, s[44:47], s86 offen offset:1024 lds\n" \
  "v_exp_f32_e64 v183, v183\n" \
  "v_mfma_f32_16x16x32_bf16 v[56:59], a[92:95], v[144:147], v[56:59]\n" \
  "ds_read_b64_tr_b16 a[158:159], v231 offset:8704\n" \
  "v_fma_f32 v182, v182, v247, v247\n" \
  "v_mfma_f32_16x16x32_bf16 v[60:63], a[92:95], v[148:151], v[60:63]\n" \
  "ds_read_b64_tr_b16 a[160:161], v228 offset:4096\n" \
  "v_fma_f32 v183, v183, v247, v247\n" \
  "v_mfma_f32_16x16x32_bf16 v[0:3], a[96:99], v[152:155], v[0:3]\n" \
  "ds_read_b64_tr_b16 a[162:163], v228 offset:4608\n" \
  "ds_read_b64_tr_b16 a[164:165], v229 offset:4096\n" \
  "v_rcp_f32_e64 v180, v180\n" \
  "v_mfma_f32_16x16x32_bf16 v[4:7], a[96:99], v[156:159], v[4:7]\n" \
  "ds_read_b64_tr_b16 a[166:167], v229 offset:4608\n" \
  "v_mul_f32_e64 v100, v100, v180\n" \
  "v_mfma_f32_16x16x32_bf16 v[8:11], a[100:103], v[152:155], v[8:11]\n" \
  "ds_read_b64_tr_b16 a[168:169], v230 offset:4096\n" \
  "v_mul_f32_e64 v184, v72, v244\n" \
  "v_mfma_f32_16x16x32_bf16 v[12:15], a[100:103], v[156:159], v[12:15]\n" \
  "ds_read_b64_tr_b16 a[170:171], v230 offset:4608\n" \
  "v_mul_f32_e64 v185, v73, v244\n" \
  "v_mfma_f32_16x16x32_bf16 v[16:19], a[104:107], v[152:155], v[16:19]\n" \
  "ds_read_b64_tr_b16 a[172:173], v231 offset:4096\n" \
  "v_mul_f32_e64 v186, v74, v244\n" \
  "v_mfma_f32_16x16x32_bf16 v[20:23], a[104:107], v[156:159], v[20:23]\n" \
  "buffer_load_dwordx4 v238, s[44:47], s86 offen offset:2048 lds\n" \
  "v_rcp_f32_e64 v181, v181\n" \
  "v_mfma_f32_16x16x32_bf16 v[24:27], a[108:111], v[152:155], v[24:27]\n" \
  "ds_read_b64_tr_b16 a[174:175], v231 offset:4608\n" \
  "ds_read_b64_tr_b16 a[176:177], v228 offset:12288\n" \
  "v_rcp_f32_e64 v182, v182\n" \
  "v_mfma_f32_16x16x32_bf16 v[28:31], a[108:111], v[156:159], v[28:31]\n" \
  "ds_read_b64_tr_b16 a[178:179], v228 offset:12800\n" \
  "v_mul_f32_e64 v101, v101, v181\n" \
  "v_mfma_f32_16x16x32_bf16 v[32:35], a[112:115], v[152:155], v[32:35]\n" \
  "ds_read_b64_tr_b16 a[180:181], v229 offset:12288\n" \
  "v_mul_f32_e64 v102, v102, v182\n" \
  "v_mfma_f32_16x16x32_bf16 v[36:39], a[112:115], v[156:159], v[36:39]\n" \
  "ds_read_b64_tr_b16 a[182:183], v229 offset:12800\n" \
  "v_cvt_pk_bf16_f32 v68, v100, v101\n" \
  "v_mfma_f32_16x16x32_bf16 v[40:43], a[116:119], v[152:155], v[40:43]\n" \
  "ds_read_b64_tr_b16 a[184:185], v230 offset:12288\n" \
  "v_mul_f32_e64 v187, v75, v244\n" \
  "v_mfma_f32_16x16x32_bf16 v[44:47], a[116:119], v[156:159], v[44:47]\n" \
  "ds_read_b64_tr_b16 a[186:187], v230 offset:12800\n" \
  "ds_read_b64_tr_b16 a[188:189], v231 offset:12288\n" \
  "v_rcp_f32_e64 v183, v183\n" \
  "v_mfma_f32_16x16x32_bf16 v[48:51], a[120:123], v[152:155], v[48:51]\n" \
  "ds_read_b64_tr_b16 a[190:191], v231 offset:12800\n" \
  "v_mul_f32_e64 v103, v103, v183\n" \
  "v_mfma_f32_16x16x32_bf16 v[52:55], a[120:123], v[156:159], v[52:55]\n" \
  "buffer_load_dwordx4 v239, s[44:47], s86 offen offset:3072 lds\n" \
  "v_cvt_pk_bf16_f32 v69, v102, v103\n" \
  "v_mfma_f32_16x16x32_bf16 v[56:59], a[124:127], v[152:155], v[56:59]\n" \
  "ds_read_b64_tr_b16 a[64:65], v232\n" \
  "v_mul_f32_e64 v104, v72, v104\n" \
  "v_mfma_f32_16x16x32_bf16 v[60:63], a[124:127], v[156:159], v[60:63]\n" \
  "ds_read_b64_tr_b16 a[66:67], v232 offset:512\n" \
  "s_waitcnt lgkmcnt(6)\n" \
  "v_mul_f32 v105, v73, v105\n" \
  "v_mfma_f32_16x16x32_bf16 v[0:3], a[128:131], v[160:163], v[0:3]\n" \
  "ds_read_b64_tr_b16 a[68:69], v233\n" \
  "ds_read_b64_tr_b16 a[70:71], v233 offset:512\n" \
  "v_exp_f32_e64 v184, v184\n" \
  "v_mfma_f32_16x16x32_bf16 v[4:7], a[128:131], v[164:167], v[4:7]\n" \
  "ds_read_b64_tr_b16 a[72:73], v234\n" \
  "v_mul_f32_e64 v106, v74, v106\n" \
  "v_mfma_f32_16x16x32_bf16 v[8:11], a[132:135], v[160:163], v[8:11]\n" \
  "ds_read_b64_tr_b16 a[74:75], v234 offset:512\n" \
  "v_mul_f32_e64 v107, v75, v107\n" \
  "v_mfma_f32_16x16x32_bf16 v[12:15], a[132:135], v[164:167], v[12:15]\n" \
  "ds_read_b64_tr_b16 a[76:77], v235\n" \
  "v_fma_f32 v184, v184, v246, v246\n" \
  "v_mfma_f32_16x16x32_bf16 v[16:19], a[136:139], v[160:163], v[16:19]\n" \
  "ds_read_b64_tr_b16 a[78:79], v235 offset:512\n" \
  "s_mov_b32 s87, 0x100100\n" \
  "s_add_u32 m0, s75, 0xc000\n" \
  "v_mul_f32_e64 v188, v76, v245\n" \
  "v_mfma_f32_16x16x32_bf16 v[20:23], a[136:139], v[164:167], v[20:23]\n" \
  "buffer_load_dwordx4 v236, s[44:47], s87 offen lds\n" \
  "v_exp_f32_e64 v185, v185\n" \
  "v_mfma_f32_16x16x32_bf16 v[24:27], a[140:143], v[160:163], v[24:27]\n" \
  "ds_read_b64_tr_b16 a[80:81], v232 offset:8192\n" \
  "ds_read_b64_tr_b16 a[82:83], v232 offset:8704\n" \
  "v_mfma_f32_16x16x32_bf16 v[28:31], a[140:143], v[164:167], v[28:31]\n" \
  "ds_read_b64_tr_b16 a[84:85], v233 offset:8192\n" \
  "v_fma_f32 v185, v185, v246, v246\n" \
  "v_mfma_f32_16x16x32_bf16 v[32:35], a[144:147], v[160:163], v[32:35]\n" \
  "ds_read_b64_tr_b16 a[86:87], v233 offset:8704\n" \
  "v_mul_f32_e64 v189, v77, v245\n" \
  "v_mfma_f32_16x16x32_bf16 v[36:39], a[144:147], v[164:167], v[36:39]\n" \
  "ds_read_b64_tr_b16 a[88:89], v234 offset:8192\n" \
  "v_mul_f32_e64 v190, v78, v245\n" \
  "v_mfma_f32_16x16x32_bf16 v[40:43], a[148:151], v[160:163], v[40:43]\n" \
  "ds_read_b64_tr_b16 a[90:91], v234 offset:8704\n" \
  "v_mul_f32_e64 v191, v79, v245\n" \
  "v_mfma_f32_16x16x32_bf16 v[44:47], a[148:151], v[164:167], v[44:47]\n" \
  "ds_read_b64_tr_b16 a[92:93], v235 offset:8192\n" \
  "ds_read_b64_tr_b16 a[94:95], v235 offset:8704\n" \
  "v_exp_f32_e64 v186, v186\n" \
  "v_mfma_f32_16x16x32_bf16 v[48:51], a[152:155], v[160:163], v[48:51]\n" \
  "ds_read_b64_tr_b16 a[96:97], v232 offset:4096\n" \
  "v_fma_f32 v186, v186, v246, v246\n" \
  "v_mfma_f32_16x16x32_bf16 v[52:55], a[152:155], v[164:167], v[52:55]\n" \
  "buffer_load_dwordx4 v237, s[44:47], s87 offen offset:1024 lds\n" \
  "v_exp_f32_e64 v187, v187\n" \
  "v_mfma_f32_16x16x32_bf16 v[56:59], a[156:159], v[160:163], v[56:59]\n" \
  "ds_read_b64_tr_b16 a[98:99], v232 offset:4608\n" \
  "v_fma_f32 v187, v187, v246, v246\n" \
  "v_mfma_f32_16x16x32_bf16 v[60:63], a[156:159], v[164:167], v[60:63]\n" \
  "ds_read_b64_tr_b16 a[100:101], v233 offset:4096\n" \
  "v_mul_f32_e64 v108, v76, v108\n" \
  "v_mfma_f32_16x16x32_bf16 v[0:3], a[160:163], v[168:171], v[0:3]\n" \
  "ds_read_b64_tr_b16 a[102:103], v233 offset:4608\n" \
  "v_mul_f32_e64 v109, v77, v109\n" \
  "v_mfma_f32_16x16x32_bf16 v[4:7], a[160:163], v[172:175], v[4:7]\n" \
  "ds_read_b64_tr_b16 a[104:105], v234 offset:4096\n" \
  "ds_read_b64_tr_b16 a[106:107], v234 offset:4608\n" \
  "v_rcp_f32_e64 v184, v184\n" \
  "v_mfma_f32_16x16x32_bf16 v[8:11], a[164:167], v[168:171], v[8:11]\n" \
  "ds_read_b64_tr_b16 a[108:109], v235 offset:4096\n" \
  "v_mul_f32_e64 v104, v104, v184\n" \
  "v_mfma_f32_16x16x32_bf16 v[12:15], a[164:167], v[172:175], v[12:15]\n" \
  "ds_read_b64_tr_b16 a[110:111], v235 offset:4608\n" \
  "v_mul_f32_e64 v110, v78, v110\n" \
  "v_mfma_f32_16x16x32_bf16 v[16:19], a[168:171], v[168:171], v[16:19]\n" \
  "ds_read_b64_tr_b16 a[112:113], v232 offset:12288\n" \
  "v_mul_f32_e64 v111, v79, v111\n" \
  "v_mfma_f32_16x16x32_bf16 v[20:23], a[168:171], v[172:175], v[20:23]\n" \
  "buffer_load_dwordx4 v238, s[44:47], s87 offen offset:2048 lds\n" \
  "v_rcp_f32_e64 v185, v185\n" \
  "v_mfma_f32_16x16x32_bf16 v[24:27], a[172:175], v[168:171], v[24:27]\n" \
  "ds_read_b64_tr_b16 a[114:115], v232 offset:12800\n" \
  "v_mul_f32_e64 v105, v105, v185\n" \
  "v_mfma_f32_16x16x32_bf16 v[28:31], a[172:175], v[172:175], v[28:31]\n" \
  "ds_read_b64_tr_b16 a[116:117], v233 offset:12288\n" \
  "ds_read_b64_tr_b16 a[118:119], v233 offset:12800\n" \
  "v_rcp_f32_e64 v186, v186\n" \
  "v_mfma_f32_16x16x32_bf16 v[32:35], a[176:179], v[168:171], v[32:35]\n" \
  "ds_read_b64_tr_b16 a[120:121], v234 offset:12288\n" \
  "v_mul_f32_e64 v106, v106, v186\n" \
  "v_mfma_f32_16x16x32_bf16 v[36:39], a[176:179], v[172:175], v[36:39]\n" \
  "ds_read_b64_tr_b16 a[122:123], v234 offset:12800\n" \
  "v_cvt_pk_bf16_f32 v66, v104, v105\n" \
  "v_mfma_f32_16x16x32_bf16 v[40:43], a[180:183], v[168:171], v[40:43]\n" \
  "ds_read_b64_tr_b16 a[124:125], v235 offset:12288\n" \
  "v_mul_f32_e64 v176, v80, v244\n" \
  "v_mfma_f32_16x16x32_bf16 v[44:47], a[180:183], v[172:175], v[44:47]\n" \
  "ds_read_b64_tr_b16 a[126:127], v235 offset:12800\n" \
  "s_waitcnt lgkmcnt(7)\n" \
  "v_mul_f32 v177, v81, v244\n" \
  "v_mfma_f32_16x16x32_bf16 v[48:51], a[184:187], v[168:171], v[48:51]\n" \
  "v_rcp_f32_e64 v187, v187\n" \
  "v_mfma_f32_16x16x32_bf16 v[52:55], a[184:187], v[172:175], v[52:55]\n" \
  "buffer_load_dwordx4 v239, s[44:47], s87 offen offset:3072 lds\n" \
  "v_mul_f32_e64 v107, v107, v187\n" \
  "v_mfma_f32_16x16x32_bf16 v[56:59], a[188:191], v[168:171], v[56:59]\n" \
  "v_cvt_pk_bf16_f32 v67, v106, v107\n" \
  "v_mul_f32_e64 v178, v82, v244\n" \
  "v_mfma_f32_16x16x32_bf16 v[60:63], a[188:191], v[172:175], v[60:63]\n" \
  "buffer_store_dwordx4 v[64:67], v250, s[60:63], s81 offen sc1\n" \
  "s_waitcnt vmcnt(17) lgkmcnt(0)\n" \
  "s_barrier\n" \
  "; HP [17, 18] ('Q', 0, 3)\n" \
  "v_mfma_f32_16x16x32_bf16 v[0:3], a[64:67], a[192:195], v[0:3]\n" \
  "ds_read_b64_tr_b16 a[128:129], v228 offset:16384\n" \
  "ds_read_b64_tr_b16 a[130:131], v228 offset:16896\n" \
  "v_mfma_f32_16x16x32_bf16 v[4:7], a[64:67], a[196:199], v[4:7]\n" \
  "ds_read_b64_tr_b16 a[132:133], v229 offset:16384\n" \
  "v_mul_f32_e64 v179, v83, v244\n" \
  "v_mfma_f32_16x16x32_bf16 v[8:11], a[68:71], a[192:195], v[8:11]\n" \
  "ds_read_b64_tr_b16 a[134:135], v229 offset:16896\n" \
  "v_mul_f32_e64 v112, v80, v112\n" \
  "v_mfma_f32_16x16x32_bf16 v[12:15], a[68:71], a[196:199], v[12:15]\n" \
  "ds_read_b64_tr_b16 a[136:137], v230 offset:16384\n" \
  "v_mul_f32_e64 v113, v81, v113\n" \
  "v_mfma_f32_16x16x32_bf16 v[16:19], a[72:75], a[192:195], v[16:19]\n" \
  "ds_read_b64_tr_b16 a[138:139], v230 offset:16896\n" \
  "ds_read_b64_tr_b16 a[140:141], v231 offset:16384\n" \
  "s_mov_b32 s86, 0x180100\n" \
  "s_add_u32 m0, s74, 0\n" \
  "v_exp_f32 v188, v188\n" \
  "v_mfma_f32_16x16x32_bf16 v[20:23], a[72:75], a[196:199], v[20:23]\n" \
  "buffer_load_dwordx4 v236, s[44:47], s86 offen lds\n" \
  "v_exp_f32_e64 v189, v189\n" \
  "v_mfma_f32_16x16x32_bf16 v[24:27], a[76:79], a[192:195], v[24:27]\n" \
  "ds_read_b64_tr_b16 a[142:143], v231 offset:16896\n" \
  "v_fma_f32 v188, v188, v247, v247\n" \
  "v_mfma_f32_16x16x32_bf16 v[28:31], a[76:79], a[196:199], v[28:31]\n" \
  "ds_read_b64_tr_b16 a[144:145], v228 offset:24576\n" \
  "v_fma_f32 v189, v189, v247, v247\n" \
  "v_mfma_f32_16x16x32_bf16 v[32:35], a[80:83], a[192:195], v[32:35]\n" \
  "ds_read_b64_tr_b16 a[146:147], v228 offset:25088\n" \
  "v_mul_f32_e64 v114, v82, v114\n" \
  "v_mfma_f32_16x16x32_bf16 v[36:39], a[80:83], a[196:199], v[36:39]\n" \
  "ds_read_b64_tr_b16 a[148:149], v229 offset:24576\n" \
  "v_mul_f32_e64 v115, v83, v115\n" \
  "v_mfma_f32_16x16x32_bf16 v[40:43], a[84:87], a[192:195], v[40:43]\n" \
  "ds_read_b64_tr_b16 a[150:151], v229 offset:25088\n" \
  "ds_read_b64_tr_b16 a[152:153], v230 offset:24576\n" \
  "v_exp_f32_e64 v190, v190\n" \
  "v_mfma_f32_16x16x32_bf16 v[44:47], a[84:87], a[196:199], v[44:47]\n" \
  "ds_read_b64_tr_b16 a[154:155], v230 offset:25088\n" \
  "v_fma_f32 v190, v190, v247, v247\n" \
  "v_mfma_f32_16x16x32_bf16 v[48:51], a[88:91], a[192:195], v[48:51]\n" \
  "ds_read_b64_tr_b16 a[156:157], v231 offset:24576\n" \
  "s_mov_b32 s87, 0x200100\n" \
  "v_exp_f32_e64 v191, v191\n" \
  "v_mfma_f32_16x16x32_bf16 v[52:55], a[88:91], a[196:199], v[52:55]\n" \
  "buffer_load_dwordx4 v237, s[44:47], s86 offen offset:1024 lds\n" \
  "v_fma_f32 v191, v191, v247, v247\n" \
  "v_mfma_f32_16x16x32_bf16 v[56:59], a[92:95], a[192:195], v[56:59]\n" \
  "ds_read_b64_tr_b16 a[158:159], v231 offset:25088\n" \
  "v_rcp_f32_e64 v188, v188\n" \
  "v_mfma_f32_16x16x32_bf16 v[60:63], a[92:95], a[196:199], v[60:63]\n" \
  "ds_read_b64_tr_b16 a[160:161], v228 offset:20480\n" \
  "v_mul_f32_e64 v108, v108, v188\n" \
  "v_mfma_f32_16x16x32_bf16 v[0:3], a[96:99], a[200:203], v[0:3]\n" \
  "ds_read_b64_tr_b16 a[162:163], v228 offset:20992\n" \
  "ds_read_b64_tr_b16 a[164:165], v229 offset:20480\n" \
  "v_rcp_f32_e64 v189, v189\n" \
  "v_mfma_f32_16x16x32_bf16 v[4:7], a[96:99], a[204:207], v[4:7]\n" \
  "ds_read_b64_tr_b16 a[166:167], v229 offset:20992\n" \
  "v_mul_f32_e64 v109, v109, v189\n" \
  "v_mfma_f32_16x16x32_bf16 v[8:11], a[100:103], a[200:203], v[8:11]\n" \
  "ds_read_b64_tr_b16 a[168:169], v230 offset:20480\n" \
  "v_cvt_pk_bf16_f32 v70, v108, v109\n" \
  "v_mfma_f32_16x16x32_bf16 v[12:15], a[100:103], a[204:207], v[12:15]\n" \
  "ds_read_b64_tr_b16 a[170:171], v230 offset:20992\n" \
  "v_rcp_f32_e64 v190, v190\n" \
  "v_mfma_f32_16x16x32_bf16 v[16:19], a[104:107], a[200:203], v[16:19]\n" \
  "ds_read_b64_tr_b16 a[172:173], v231 offset:20480\n" \
  "v_mul_f32_e64 v110, v110, v190\n" \
  "v_mfma_f32_16x16x32_bf16 v[20:23], a[104:107], a[204:207], v[20:23]\n" \
  "buffer_load_dwordx4 v238, s[44:47], s86 offen offset:2048 lds\n" \
  "v_rcp_f32_e64 v191, v191\n" \
  "v_mfma_f32_16x16x32_bf16 v[24:27], a[108:111], a[200:203], v[24:27]\n" \
  "ds_read_b64_tr_b16 a[174:175], v231 offset:20992\n" \
  "ds_read_b64_tr_b16 a[176:177], v228 offset:28672\n" \
  "v_mul_f32_e64 v111, v111, v191\n" \
  "v_mfma_f32_16x16x32_bf16 v[28:31], a[108:111], a[204:207], v[28:31]\n" \
  "ds_read_b64_tr_b16 a[178:179], v228 offset:29184\n" \
  "v_cvt_pk_bf16_f32 v71, v110, v111\n" \
  "v_mfma_f32_16x16x32_bf16 v[32:35], a[112:115], a[200:203], v[32:35]\n" \
  "buffer_store_dwordx4 v[68:71], v250, s[60:63], s82 offen sc1\n" \
  "v_mfma_f32_16x16x32_bf16 v[36:39], a[112:115], a[204:207], v[36:39]\n" \
  "ds_read_b64_tr_b16 a[180:181], v229 offset:28672\n" \
  "ds_read_b64_tr_b16 a[182:183], v229 offset:29184\n" \
  "v_exp_f32_e64 v176, v176\n" \
  "v_mfma_f32_16x16x32_bf16 v[40:43], a[116:119], a[200:203], v[40:43]\n" \
  "ds_read_b64_tr_b16 a[184:185], v230 offset:28672\n" \
  "v_fma_f32 v176, v176, v246, v246\n" \
  "v_mfma_f32_16x16x32_bf16 v[44:47], a[116:119], a[204:207], v[44:47]\n" \
  "ds_read_b64_tr_b16 a[186:187], v230 offset:29184\n" \
  "ds_read_b64_tr_b16 a[188:189], v231 offset:28672\n" \
  "v_exp_f32_e64 v177, v177\n" \
  "v_mfma_f32_16x16x32_bf16 v[48:51], a[120:123], a[200:203], v[48:51]\n" \
  "ds_read_b64_tr_b16 a[190:191], v231 offset:29184\n" \
  "v_fma_f32 v177, v177, v246, v246\n" \
  "v_mfma_f32_16x16x32_bf16 v[52:55], a[120:123], a[204:207], v[52:55]\n" \
  "buffer_load_dwordx4 v239, s[44:47], s86 offen offset:3072 lds\n" \
  "v_exp_f32_e64 v178, v178\n" \
  "v_mfma_f32_16x16x32_bf16 v[56:59], a[124:127], a[200:203], v[56:59]\n" \
  "ds_read_b64_tr_b16 a[64:65], v232 offset:16384\n" \
  "v_fma_f32 v178, v178, v246, v246\n" \
  "v_mfma_f32_16x16x32_bf16 v[60:63], a[124:127], a[204:207], v[60:63]\n" \
  "ds_read_b64_tr_b16 a[66:67], v232 offset:16896\n" \
  "s_waitcnt lgkmcnt(6)\n" \
  "v_mul_f32 v180, v84, v245\n" \
  "v_mfma_f32_16x16x32_bf16 v[0:3], a[128:131], a[208:211], v[0:3]\n" \
  "ds_read_b64_tr_b16 a[68:69], v233 offset:16384\n" \
  "ds_read_b64_tr_b16 a[70:71], v233 offset:16896\n" \
  "v_mfma_f32_16x16x32_bf16 v[4:7], a[128:131], a[212:215], v[4:7]\n" \
  "ds_read_b64_tr_b16 a[72:73], v234 offset:16384\n" \
  "v_mul_f32_e64 v181, v85, v245\n" \
  "v_mfma_f32_16x16x32_bf16 v[8:11], a[132:135], a[208:211], v[8:11]\n" \
  "ds_read_b64_tr_b16 a[74:75], v234 offset:16896\n" \
  "v_mul_f32_e64 v182, v86, v245\n" \
  "v_mfma_f32_16x16x32_bf16 v[12:15], a[132:135], a[212:215], v[12:15]\n" \
  "ds_read_b64_tr_b16 a[76:77], v235 offset:16384\n" \
  "v_mul_f32_e64 v183, v87, v245\n" \
  "v_mfma_f32_16x16x32_bf16 v[16:19], a[136:139], a[208:211], v[16:19]\n" \
  "ds_read_b64_tr_b16 a[78:79], v235 offset:16896\n" \
  "s_add_u32 m0, s75, 0\n" \
  "v_mul_f32 v116, v84, v116\n" \
  "v_mfma_f32_16x16x32_bf16 v[20:23], a[136:139], a[212:215], v[20:23]\n" \
  "buffer_load_dwordx4 v236, s[44:47], s87 offen lds\n" \
  "v_exp_f32_e64 v179, v179\n" \
  "v_mfma_f32_16x16x32_bf16 v[24:27], a[140:143], a[208:211], v[24:27]\n" \
  "ds_read_b64_tr_b16 a[80:81], v232 offset:24576\n" \
  "ds_read_b64_tr_b16 a[82:83], v232 offset:25088\n" \
  "v_fma_f32 v179, v179, v246, v246\n" \
  "v_mfma_f32_16x16x32_bf16 v[28:31], a[140:143], a[212:215], v[28:31]\n" \
  "ds_read_b64_tr_b16 a[84:85], v233 offset:24576\n" \
  "v_mul_f32_e64 v117, v85, v117\n" \
  "v_mfma_f32_16x16x32_bf16 v[32:35], a[144:147], a[208:211], v[32:35]\n" \
  "ds_read_b64_tr_b16 a[86:87], v233 offset:25088\n" \
  "v_mul_f32_e64 v118, v86, v118\n" \
  "v_mfma_f32_16x16x32_bf16 v[36:39], a[144:147], a[212:215], v[36:39]\n" \
  "ds_read_b64_tr_b16 a[88:89], v234 offset:24576\n" \
  "v_mul_f32_e64 v119, v87, v119\n" \
  "v_mfma_f32_16x16x32_bf16 v[40:43], a[148:151], a[208:211], v[40:43]\n" \
  "ds_read_b64_tr_b16 a[90:91], v234 offset:25088\n" \
  "v_rcp_f32_e64 v176, v176\n" \
  "v_mfma_f32_16x16x32_bf16 v[44:47], a[148:151], a[212:215], v[44:47]\n" \
  "ds_read_b64_tr_b16 a[92:93], v235 offset:24576\n" \
  "ds_read_b64_tr_b16 a[94:95], v235 offset:25088\n" \
  "v_rcp_f32_e64 v177, v177\n" \
  "v_mfma_f32_16x16x32_bf16 v[48:51], a[152:155], a[208:211], v[48:51]\n" \
  "ds_read_b64_tr_b16 a[96:97], v232 offset:20480\n" \
  "v_mul_f32_e64 v112, v112, v176\n" \
  "v_mfma_f32_16x16x32_bf16 v[52:55], a[152:155], a[212:215], v[52:55]\n" \
  "buffer_load_dwordx4 v237, s[44:47], s87 offen offset:1024 lds\n" \
  "v_rcp_f32_e64 v178, v178\n" \
  "v_mfma_f32_16x16x32_bf16 v[56:59], a[156:159], a[208:211], v[56:59]\n" \
  "ds_read_b64_tr_b16 a[98:99], v232 offset:20992\n" \
  "v_mul_f32_e64 v113, v113, v177\n" \
  "v_mfma_f32_16x16x32_bf16 v[60:63], a[156:159], a[212:215], v[60:63]\n" \
  "ds_read_b64_tr_b16 a[100:101], v233 offset:20480\n" \
  "v_mul_f32_e64 v114, v114, v178\n" \
  "v_mfma_f32_16x16x32_bf16 v[0:3], a[160:163], a[216:219], v[0:3]\n" \
  "ds_read_b64_tr_b16 a[102:103], v233 offset:20992\n" \
  "v_cvt_pk_bf16_f32 v80, v112, v113\n" \
  "v_mfma_f32_16x16x32_bf16 v[4:7], a[160:163], a[220:223], v[4:7]\n" \
  "ds_read_b64_tr_b16 a[104:105], v234 offset:20480\n" \
  "ds_read_b64_tr_b16 a[106:107], v234 offset:20992\n" \
  "v_rcp_f32_e64 v179, v179\n" \
  "v_mfma_f32_16x16x32_bf16 v[8:11], a[164:167], a[216:219], v[8:11]\n" \
  "ds_read_b64_tr_b16 a[108:109], v235 offset:20480\n" \
  "v_mul_f32_e64 v115, v115, v179\n" \
  "v_mfma_f32_16x16x32_bf16 v[12:15], a[164:167], a[220:223], v[12:15]\n" \
  "ds_read_b64_tr_b16 a[110:111], v235 offset:20992\n" \
  "v_cvt_pk_bf16_f32 v81, v114, v115\n" \
  "v_mfma_f32_16x16x32_bf16 v[16:19], a[168:171], a[216:219], v[16:19]\n" \
  "ds_read_b64_tr_b16 a[112:113], v232 offset:28672\n" \
  "v_mul_f32_e64 v184, v88, v244\n" \
  "v_mfma_f32_16x16x32_bf16 v[20:23], a[168:171], a[220:223], v[20:23]\n" \
  "buffer_load_dwordx4 v238, s[44:47], s87 offen offset:2048 lds\n" \
  "v_exp_f32_e64 v180, v180\n" \
  "v_mfma_f32_16x16x32_bf16 v[24:27], a[172:175], a[216:219], v[24:27]\n" \
  "ds_read_b64_tr_b16 a[114:115], v232 offset:29184\n" \
  "v_fma_f32 v180, v180, v247, v247\n" \
  "v_mfma_f32_16x16x32_bf16 v[28:31], a[172:175], a[220:223], v[28:31]\n" \
  "ds_read_b64_tr_b16 a[116:117], v233 offset:28672\n" \
  "ds_read_b64_tr_b16 a[118:119], v233 offset:29184\n" \
  "v_exp_f32_e64 v181, v181\n" \
  "v_mfma_f32_16x16x32_bf16 v[32:35], a[176:179], a[216:219], v[32:35]\n" \
  "ds_read_b64_tr_b16 a[120:121], v234 offset:28672\n" \
  "v_fma_f32 v181, v181, v247, v247\n" \
  "v_mfma_f32_16x16x32_bf16 v[36:39], a[176:179], a[220:223], v[36:39]\n" \
  "ds_read_b64_tr_b16 a[122:123], v234 offset:29184\n" \
  "v_mul_f32_e64 v185, v89, v244\n" \
  "v_mfma_f32_16x16x32_bf16 v[40:43], a[180:183], a[216:219], v[40:43]\n" \
  "ds_read_b64_tr_b16 a[124:125], v235 offset:28672\n" \
  "v_mul_f32_e64 v186, v90, v244\n" \
  "v_mfma_f32_16x16x32_bf16 v[44:47], a[180:183], a[220:223], v[44:47]\n" \
  "ds_read_b64_tr_b16 a[126:127], v235 offset:29184\n" \
  "s_waitcnt lgkmcnt(7)\n" \
  "v_mul_f32 v187, v91, v244\n" \
  "v_mfma_f32_16x16x32_bf16 v[48:51], a[184:187], a[216:219], v[48:51]\n" \
  "v_exp_f32_e64 v182, v182\n" \
  "v_mfma_f32_16x16x32_bf16 v[52:55], a[184:187], a[220:223], v[52:55]\n" \
  "buffer_load_dwordx4 v239, s[44:47], s87 offen offset:3072 lds\n" \
  "v_exp_f32_e64 v183, v183\n" \
  "v_mfma_f32_16x16x32_bf16 v[56:59], a[188:191], a[216:219], v[56:59]\n" \
  "v_fma_f32 v182, v182, v247, v247\n" \
  "v_fma_f32 v183, v183, v247, v247\n" \
  "v_mfma_f32_16x16x32_bf16 v[60:63], a[188:191], a[220:223], v[60:63]\n" \
  "v_rcp_f32_e64 v180, v180\n" \
  "s_waitcnt vmcnt(18) lgkmcnt(0)\n" \
  "s_barrier\n" \
  "; HP [19, 20] ('Q', 0, 5)\n" \
  "v_mfma_f32_16x16x32_bf16 v[0:3], a[64:67], a[224:227], v[0:3]\n" \
  "ds_read_b64_tr_b16 a[128:129], v228 offset:32768\n" \
  "ds_read_b64_tr_b16 a[130:131], v228 offset:33280\n" \
  "v_mfma_f32_16x16x32_bf16 v[4:7], a[64:67], a[228:231], v[4:7]\n" \
  "ds_read_b64_tr_b16 a[132:133], v229 offset:32768\n" \
  "v_mul_f32_e64 v116, v116, v180\n" \
  "v_mfma_f32_16x16x32_bf16 v[8:11], a[68:71], a[224:227], v[8:11]\n" \
  "ds_read_b64_tr_b16 a[134:135], v229 offset:33280\n" \
  "v_mul_f32_e64 v120, v88, v120\n" \
  "v_mfma_f32_16x16x32_bf16 v[12:15], a[68:71], a[228:231], v[12:15]\n" \
  "ds_read_b64_tr_b16 a[136:137], v230 offset:32768\n" \
  "v_mul_f32_e64 v121, v89, v121\n" \
  "v_mfma_f32_16x16x32_bf16 v[16:19], a[72:75], a[224:227], v[16:19]\n" \
  "ds_read_b64_tr_b16 a[138:139], v230 offset:33280\n" \
  "ds_read_b64_tr_b16 a[140:141], v231 offset:32768\n" \
  "s_mov_b32 s86, 0x280100\n" \
  "s_add_u32 m0, s74, 0x4000\n" \
  "v_rcp_f32_e64 v181, v181\n" \
  "v_mfma_f32_16x16x32_bf16 v[20:23], a[72:75], a[228:231], v[20:23]\n" \
  "buffer_load_dwordx4 v236, s[44:47], s86 offen lds\n" \
  "v_rcp_f32_e64 v182, v182\n" \
  "v_mfma_f32_16x16x32_bf16 v[24:27], a[76:79], a[224:227], v[24:27]\n" \
  "ds_read_b64_tr_b16 a[142:143], v231 offset:33280\n" \
  "v_mul_f32_e64 v117, v117, v181\n" \
  "v_mfma_f32_16x16x32_bf16 v[28:31], a[76:79], a[228:231], v[28:31]\n" \
  "ds_read_b64_tr_b16 a[144:145], v228 offset:40960\n" \
  "v_mul_f32_e64 v118, v118, v182\n" \
  "v_mfma_f32_16x16x32_bf16 v[32:35], a[80:83], a[224:227], v[32:35]\n" \
  "ds_read_b64_tr_b16 a[146:147], v228 offset:41472\n" \
  "v_cvt_pk_bf16_f32 v84, v116, v117\n" \
  "v_mfma_f32_16x16x32_bf16 v[36:39], a[80:83], a[228:231], v[36:39]\n" \
  "ds_read_b64_tr_b16 a[148:149], v229 offset:40960\n" \
  "v_mul_f32_e64 v122, v90, v122\n" \
  "v_mfma_f32_16x16x32_bf16 v[40:43], a[84:87], a[224:227], v[40:43]\n" \
  "ds_read_b64_tr_b16 a[150:151], v229 offset:41472\n" \
  "ds_read_b64_tr_b16 a[152:153], v230 offset:40960\n" \
  "v_rcp_f32_e64 v183, v183\n" \
  "v_mfma_f32_16x16x32_bf16 v[44:47], a[84:87], a[228:231], v[44:47]\n" \
  "ds_read_b64_tr_b16 a[154:155], v230 offset:41472\n" \
  "v_mul_f32_e64 v119, v119, v183\n" \
  "v_mfma_f32_16x16x32_bf16 v[48:51], a[88:91], a[224:227], v[48:51]\n" \
  "ds_read_b64_tr_b16 a[156:157], v231 offset:40960\n" \
  "v_cvt_pk_bf16_f32 v85, v118, v119\n" \
  "v_mfma_f32_16x16x32_bf16 v[52:55], a[88:91], a[228:231], v[52:55]\n" \
  "buffer_load_dwordx4 v237, s[44:47], s86 offen offset:1024 lds\n" \
  "v_exp_f32_e64 v184, v184\n" \
  "v_mfma_f32_16x16x32_bf16 v[56:59], a[92:95], a[224:227], v[56:59]\n" \
  "ds_read_b64_tr_b16 a[158:159], v231 offset:41472\n" \
  "v_mul_f32_e64 v123, v91, v123\n" \
  "v_mfma_f32_16x16x32_bf16 v[60:63], a[92:95], a[228:231], v[60:63]\n" \
  "ds_read_b64_tr_b16 a[160:161], v228 offset:36864\n" \
  "v_fma_f32 v184, v184, v246, v246\n" \
  "v_mfma_f32_16x16x32_bf16 v[0:3], a[96:99], a[232:235], v[0:3]\n" \
  "ds_read_b64_tr_b16 a[162:163], v228 offset:37376\n" \
  "ds_read_b64_tr_b16 a[164:165], v229 offset:36864\n" \
  "v_exp_f32_e64 v185, v185\n" \
  "v_mfma_f32_16x16x32_bf16 v[4:7], a[96:99], a[236:239], v[4:7]\n" \
  "ds_read_b64_tr_b16 a[166:167], v229 offset:37376\n" \
  "v_fma_f32 v185, v185, v246, v246\n" \
  "v_mfma_f32_16x16x32_bf16 v[8:11], a[100:103], a[232:235], v[8:11]\n" \
  "ds_read_b64_tr_b16 a[168:169], v230 offset:36864\n" \
  "v_mul_f32_e64 v188, v92, v245\n" \
  "v_mfma_f32_16x16x32_bf16 v[12:15], a[100:103], a[236:239], v[12:15]\n" \
  "ds_read_b64_tr_b16 a[170:171], v230 offset:37376\n" \
  "v_mul_f32_e64 v189, v93, v245\n" \
  "v_mfma_f32_16x16x32_bf16 v[16:19], a[104:107], a[232:235], v[16:19]\n" \
  "ds_read_b64_tr_b16 a[172:173], v231 offset:36864\n" \
  "v_mul_f32_e64 v190, v94, v245\n" \
  "v_mfma_f32_16x16x32_bf16 v[20:23], a[104:107], a[236:239], v[20:23]\n" \
  "buffer_load_dwordx4 v238, s[44:47], s86 offen offset:2048 lds\n" \
  "v_mfma_f32_16x16x32_bf16 v[24:27], a[108:111], a[232:235], v[24:27]\n" \
  "ds_read_b64_tr_b16 a[174:175], v231 offset:37376\n" \
  "ds_read_b64_tr_b16 a[176:177], v228 offset:45056\n" \
  "v_exp_f32_e64 v186, v186\n" \
  "v_mfma_f32_16x16x32_bf16 v[28:31], a[108:111], a[236:239], v[28:31]\n" \
  "ds_read_b64_tr_b16 a[178:179], v228 offset:45568\n" \
  "v_fma_f32 v186, v186, v246, v246\n" \
  "v_mfma_f32_16x16x32_bf16 v[32:35], a[112:115], a[232:235], v[32:35]\n" \
  "ds_read_b64_tr_b16 a[180:181], v229 offset:45056\n" \
  "v_mul_f32_e64 v191, v95, v245\n" \
  "v_mfma_f32_16x16x32_bf16 v[36:39], a[112:115], a[236:239], v[36:39]\n" \
  "ds_read_b64_tr_b16 a[182:183], v229 offset:45568\n" \
  "v_mul_f32_e64 v124, v92, v124\n" \
  "v_mfma_f32_16x16x32_bf16 v[40:43], a[116:119], a[232:235], v[40:43]\n" \
  "ds_read_b64_tr_b16 a[184:185], v230 offset:45056\n" \
  "v_mul_f32_e64 v125, v93, v125\n" \
  "v_mfma_f32_16x16x32_bf16 v[44:47], a[116:119], a[236:239], v[44:47]\n" \
  "ds_read_b64_tr_b16 a[186:187], v230 offset:45568\n" \
  "ds_read_b64_tr_b16 a[188:189], v231 offset:45056\n" \
  "v_exp_f32_e64 v187, v187\n" \
  "v_mfma_f32_16x16x32_bf16 v[48:51], a[120:123], a[232:235], v[48:51]\n" \
  "ds_read_b64_tr_b16 a[190:191], v231 offset:45568\n" \
  "v_fma_f32 v187, v187, v246, v246\n" \
  "v_mfma_f32_16x16x32_bf16 v[52:55], a[120:123], a[236:239], v[52:55]\n" \
  "buffer_load_dwordx4 v239, s[44:47], s86 offen offset:3072 lds\n" \
  "v_rcp_f32_e64 v184, v184\n" \
  "v_mfma_f32_16x16x32_bf16 v[56:59], a[124:127], a[232:235], v[56:59]\n" \
  "ds_read_b64_tr_b16 a[64:65], v232 offset:32768\n" \
  "v_mul_f32_e64 v120, v120, v184\n" \
  "v_mfma_f32_16x16x32_bf16 v[60:63], a[124:127], a[236:239], v[60:63]\n" \
  "ds_read_b64_tr_b16 a[66:67], v232 offset:33280\n" \
  "s_waitcnt lgkmcnt(6)\n" \
  "v_mul_f32 v126, v94, v126\n" \
  "v_mfma_f32_16x16x32_bf16 v[0:3], a[128:131], a[240:243], v[0:3]\n" \
  "ds_read_b64_tr_b16 a[68:69], v233 offset:32768\n" \
  "ds_read_b64_tr_b16 a[70:71], v233 offset:33280\n" \
  "v_rcp_f32_e64 v185, v185\n" \
  "v_mfma_f32_16x16x32_bf16 v[4:7], a[128:131], a[244:247], v[4:7]\n" \
  "ds_read_b64_tr_b16 a[72:73], v234 offset:32768\n" \
  "v_mul_f32_e64 v121, v121, v185\n" \
  "v_mfma_f32_16x16x32_bf16 v[8:11], a[132:135], a[240:243], v[8:11]\n" \
  "ds_read_b64_tr_b16 a[74:75], v234 offset:33280\n" \
  "v_cvt_pk_bf16_f32 v82, v120, v121\n" \
  "v_mfma_f32_16x16x32_bf16 v[12:15], a[132:135], a[244:247], v[12:15]\n" \
  "ds_read_b64_tr_b16 a[76:77], v235 offset:32768\n" \
  "v_mul_f32_e64 v127, v95, v127\n" \
  "v_mfma_f32_16x16x32_bf16 v[16:19], a[136:139], a[240:243], v[16:19]\n" \
  "ds_read_b64_tr_b16 a[78:79], v235 offset:33280\n" \
  "s_mov_b32 s87, 0x300100\n" \
  "s_add_u32 m0, s75, 0x4000\n" \
  "v_rcp_f32_e64 v186, v186\n" \
  "v_mfma_f32_16x16x32_bf16 v[20:23], a[136:139], a[244:247], v[20:23]\n" \
  "buffer_load_dwordx4 v236, s[44:47], s87 offen lds\n" \
  "v_rcp_f32_e64 v187, v187\n" \
  "v_mfma_f32_16x16x32_bf16 v[24:27], a[140:143], a[240:243], v[24:27]\n" \
  "ds_read_b64_tr_b16 a[80:81], v232 offset:40960\n" \
  "ds_read_b64_tr_b16 a[82:83], v232 offset:41472\n" \
  "v_mul_f32_e64 v122, v122, v186\n" \
  "v_mfma_f32_16x16x32_bf16 v[28:31], a[140:143], a[244:247], v[28:31]\n" \
  "ds_read_b64_tr_b16 a[84:85], v233 offset:40960\n" \
  "v_mul_f32_e64 v123, v123, v187\n" \
  "v_mfma_f32_16x16x32_bf16 v[32:35], a[144:147], a[240:243], v[32:35]\n" \
  "ds_read_b64_tr_b16 a[86:87], v233 offset:41472\n" \
  "v_cvt_pk_bf16_f32 v83, v122, v123\n" \
  "v_mfma_f32_16x16x32_bf16 v[36:39], a[144:147], a[244:247], v[36:39]\n" \
  "buffer_store_dwordx4 v[80:83], v250, s[60:63], s81 offen offset:1024 sc1\n" \
  "v_mfma_f32_16x16x32_bf16 v[40:43], a[148:151], a[240:243], v[40:43]\n" \
  "ds_read_b64_tr_b16 a[88:89], v234 offset:40960\n" \
  "ds_read_b64_tr_b16 a[90:91], v234 offset:41472\n" \
  "v_mfma_f32_16x16x32_bf16 v[44:47], a[148:151], a[244:247], v[44:47]\n" \
  "ds_read_b64_tr_b16 a[92:93], v235 offset:40960\n" \
  "ds_read_b64_tr_b16 a[94:95], v235 offset:41472\n" \
  "v_exp_f32_e64 v188, v188\n" \
  "v_mfma_f32_16x16x32_bf16 v[48:51], a[152:155], a[240:243], v[48:51]\n" \
  "ds_read_b64_tr_b16 a[96:97], v232 offset:36864\n" \
  "v_fma_f32 v188, v188, v247, v247\n" \
  "v_mfma_f32_16x16x32_bf16 v[52:55], a[152:155], a[244:247], v[52:55]\n" \
  "buffer_load_dwordx4 v237, s[44:47], s87 offen offset:1024 lds\n" \
  "v_exp_f32_e64 v189, v189\n" \
  "v_mfma_f32_16x16x32_bf16 v[56:59], a[156:159], a[240:243], v[56:59]\n" \
  "ds_read_b64_tr_b16 a[98:99], v232 offset:37376\n" \
  "v_fma_f32 v189, v189, v247, v247\n" \
  "v_mfma_f32_16x16x32_bf16 v[60:63], a[156:159], a[244:247], v[60:63]\n" \
  "ds_read_b64_tr_b16 a[100:101], v233 offset:36864\n" \
  "v_exp_f32_e64 v190, v190\n" \
  "v_mfma_f32_16x16x32_bf16 v[0:3], a[160:163], a[248:251], v[0:3]\n" \
  "ds_read_b64_tr_b16 a[102:103], v233 offset:37376\n" \
  "v_fma_f32 v190, v190, v247, v247\n" \
  "v_mfma_f32_16x16x32_bf16 v[4:7], a[160:163], a[252:255], v[4:7]\n" \
  "ds_read_b64_tr_b16 a[104:105], v234 offset:36864\n" \
  "ds_read_b64_tr_b16 a[106:107], v234 offset:37376\n" \
  "v_exp_f32_e64 v191, v191\n" \
  "v_mfma_f32_16x16x32_bf16 v[8:11], a[164:167], a[248:251], v[8:11]\n" \
  "ds_read_b64_tr_b16 a[108:109], v235 offset:36864\n" \
  "v_fma_f32 v191, v191, v247, v247\n" \
  "v_mfma_f32_16x16x32_bf16 v[12:15], a[164:167], a[252:255], v[12:15]\n" \
  "ds_read_b64_tr_b16 a[110:111], v235 offset:37376\n" \
  "v_rcp_f32_e64 v188, v188\n" \
  "v_mfma_f32_16x16x32_bf16 v[16:19], a[168:171], a[248:251], v[16:19]\n" \
  "ds_read_b64_tr_b16 a[112:113], v232 offset:45056\n" \
  "v_mul_f32_e64 v124, v124, v188\n" \
  "v_mfma_f32_16x16x32_bf16 v[20:23], a[168:171], a[252:255], v[20:23]\n" \
  "buffer_load_dwordx4 v238, s[44:47], s87 offen offset:2048 lds\n" \
  "v_rcp_f32_e64 v189, v189\n" \
  "v_mfma_f32_16x16x32_bf16 v[24:27], a[172:175], a[248:251], v[24:27]\n" \
  "ds_read_b64_tr_b16 a[114:115], v232 offset:45568\n" \
  "v_mul_f32_e64 v125, v125, v189\n" \
  "v_mfma_f32_16x16x32_bf16 v[28:31], a[172:175], a[252:255], v[28:31]\n" \
  "ds_read_b64_tr_b16 a[116:117], v233 offset:45056\n" \
  "ds_read_b64_tr_b16 a[118:119], v233 offset:45568\n" \
  "v_rcp_f32_e64 v190, v190\n" \
  "v_mfma_f32_16x16x32_bf16 v[32:35], a[176:179], a[248:251], v[32:35]\n" \
  "ds_read_b64_tr_b16 a[120:121], v234 offset:45056\n" \
  "v_mul_f32_e64 v126, v126, v190\n" \
  "v_mfma_f32_16x16x32_bf16 v[36:39], a[176:179], a[252:255], v[36:39]\n" \
  "ds_read_b64_tr_b16 a[122:123], v234 offset:45568\n" \
  "v_cvt_pk_bf16_f32 v86, v124, v125\n" \
  "v_mfma_f32_16x16x32_bf16 v[40:43], a[180:183], a[248:251], v[40:43]\n" \
  "ds_read_b64_tr_b16 a[124:125], v235 offset:45056\n" \
  "v_rcp_f32_e64 v191, v191\n" \
  "v_mfma_f32_16x16x32_bf16 v[44:47], a[180:183], a[252:255], v[44:47]\n" \
  "ds_read_b64_tr_b16 a[126:127], v235 offset:45568\n" \
  "s_waitcnt lgkmcnt(7)\n" \
  "v_mul_f32 v127, v127, v191\n" \
  "v_mfma_f32_16x16x32_bf16 v[48:51], a[184:187], a[248:251], v[48:51]\n" \
  "v_cvt_pk_bf16_f32 v87, v126, v127\n" \
  "v_mfma_f32_16x16x32_bf16 v[52:55], a[184:187], a[252:255], v[52:55]\n" \
  "buffer_load_dwordx4 v239, s[44:47], s87 offen offset:3072 lds\n" \
  "buffer_store_dwordx4 v[84:87], v250, s[60:63], s82 offen offset:1024 sc1\n" \
  "v_mfma_f32_16x16x32_bf16 v[56:59], a[188:191], a[248:251], v[56:59]\n" \
  "v_mfma_f32_16x16x32_bf16 v[60:63], a[188:191], a[252:255], v[60:63]\n" \
  "s_waitcnt vmcnt(20) lgkmcnt(0)\n" \
  "s_barrier\n" \
  "; HP [21, 22] ('Q', 1, 0)\n" \
  "v_mfma_f32_16x16x32_bf16 v[64:67], a[64:67], v[128:131], 0\n" \
  "ds_read_b64_tr_b16 a[128:129], v228 offset:49152\n" \
  "ds_read_b64_tr_b16 a[130:131], v228 offset:49664\n" \
  "v_mfma_f32_16x16x32_bf16 v[68:71], a[64:67], v[132:135], 0\n" \
  "ds_read_b64_tr_b16 a[132:133], v229 offset:49152\n" \
  "s_mov_b32 s72, 0\n" \
  "v_mfma_f32_16x16x32_bf16 v[72:75], a[68:71], v[128:131], 0\n" \
  "ds_read_b64_tr_b16 a[134:135], v229 offset:49664\n" \
  "s_add_u32 m0, s74, 0x8000\n" \
  "v_mfma_f32_16x16x32_bf16 v[76:79], a[68:71], v[132:135], 0\n" \
  "ds_read_b64_tr_b16 a[136:137], v230 offset:49152\n" \
  "v_mfma_f32_16x16x32_bf16 v[80:83], a[72:75], v[128:131], 0\n" \
  "ds_read_b64_tr_b16 a[138:139], v230 offset:49664\n" \
  "ds_read_b64_tr_b16 a[140:141], v231 offset:49152\n" \
  "v_mfma_f32_16x16x32_bf16 v[84:87], a[72:75], v[132:135], 0\n" \
  "buffer_load_dwordx4 v220, s[36:39], s72 offen lds\n" \
  "v_mfma_f32_16x16x32_bf16 v[88:91], a[76:79], v[128:131], 0\n" \
  "ds_read_b64_tr_b16 a[142:143], v231 offset:49664\n" \
  "v_mul_f32 v0, v0, v248\n" \
  "v_mfma_f32_16x16x32_bf16 v[92:95], a[76:79], v[132:135], 0\n" \
  "ds_read_b64_tr_b16 a[144:145], v228 offset:57344\n" \
  "v_mul_f32_e64 v1, v1, v248\n" \
  "v_mfma_f32_16x16x32_bf16 v[96:99], a[80:83], v[128:131], 0\n" \
  "ds_read_b64_tr_b16 a[146:147], v228 offset:57856\n" \
  "v_mul_f32_e64 v2, v2, v248\n" \
  "v_mfma_f32_16x16x32_bf16 v[100:103], a[80:83], v[132:135], 0\n" \
  "ds_read_b64_tr_b16 a[148:149], v229 offset:57344\n" \
  "v_mul_f32_e64 v3, v3, v248\n" \
  "v_mfma_f32_16x16x32_bf16 v[104:107], a[84:87], v[128:131], 0\n" \
  "ds_read_b64_tr_b16 a[150:151], v229 offset:57856\n" \
  "ds_read_b64_tr_b16 a[152:153], v230 offset:57344\n" \
  "v_cvt_pk_bf16_f32 v0, v0, v1\n" \
  "v_mfma_f32_16x16x32_bf16 v[108:111], a[84:87], v[132:135], 0\n" \
  "ds_read_b64_tr_b16 a[154:155], v230 offset:57856\n" \
  "v_cvt_pk_bf16_f32 v1, v2, v3\n" \
  "v_mfma_f32_16x16x32_bf16 v[112:115], a[88:91], v[128:131], 0\n" \
  "ds_read_b64_tr_b16 a[156:157], v231 offset:57344\n" \
  "v_mul_f32_e64 v8, v8, v248\n" \
  "v_mfma_f32_16x16x32_bf16 v[116:119], a[88:91], v[132:135], 0\n" \
  "buffer_load_dwordx4 v221, s[36:39], s72 offen offset:1024 lds\n" \
  "v_mfma_f32_16x16x32_bf16 v[120:123], a[92:95], v[128:131], 0\n" \
  "ds_read_b64_tr_b16 a[158:159], v231 offset:57856\n" \
  "v_mul_f32_e64 v9, v9, v248\n" \
  "v_mfma_f32_16x16x32_bf16 v[124:127], a[92:95], v[132:135], 0\n" \
  "ds_read_b64_tr_b16 a[160:161], v228 offset:53248\n" \
  "v_mul_f32_e64 v10, v10, v248\n" \
  "v_mfma_f32_16x16x32_bf16 v[64:67], a[96:99], v[136:139], v[64:67]\n" \
  "ds_read_b64_tr_b16 a[162:163], v228 offset:53760\n" \
  "ds_read_b64_tr_b16 a[164:165], v229 offset:53248\n" \
  "v_mul_f32_e64 v11, v11, v248\n" \
  "v_mfma_f32_16x16x32_bf16 v[68:71], a[96:99], v[140:143], v[68:71]\n" \
  "ds_read_b64_tr_b16 a[166:167], v229 offset:53760\n" \
  "v_cvt_pk_bf16_f32 v8, v8, v9\n" \
  "v_mfma_f32_16x16x32_bf16 v[72:75], a[100:103], v[136:139], v[72:75]\n" \
  "ds_read_b64_tr_b16 a[168:169], v230 offset:53248\n" \
  "v_cvt_pk_bf16_f32 v9, v10, v11\n" \
  "v_mfma_f32_16x16x32_bf16 v[76:79], a[100:103], v[140:143], v[76:79]\n" \
  "ds_read_b64_tr_b16 a[170:171], v230 offset:53760\n" \
  "v_permlane32_swap_b32 v0, v8\n" \
  "v_mfma_f32_16x16x32_bf16 v[80:83], a[104:107], v[136:139], v[80:83]\n" \
  "ds_read_b64_tr_b16 a[172:173], v231 offset:53248\n" \
  "v_permlane32_swap_b32 v1, v9\n" \
  "v_mfma_f32_16x16x32_bf16 v[84:87], a[104:107], v[140:143], v[84:87]\n" \
  "buffer_load_dwordx4 v222, s[36:39], s72 offen offset:2048 lds\n" \
  "v_mfma_f32_16x16x32_bf16 v[88:91], a[108:111], v[136:139], v[88:91]\n" \
  "ds_read_b64_tr_b16 a[174:175], v231 offset:53760\n" \
  "ds_read_b64_tr_b16 a[176:177], v228 offset:61440\n" \
  "v_permlane16_swap_b32 v0, v8\n" \
  "v_mfma_f32_16x16x32_bf16 v[92:95], a[108:111], v[140:143], v[92:95]\n" \
  "ds_read_b64_tr_b16 a[178:179], v228 offset:61952\n" \
  "v_permlane16_swap_b32 v1, v9\n" \
  "v_mfma_f32_16x16x32_bf16 v[96:99], a[112:115], v[136:139], v[96:99]\n" \
  "ds_read_b64_tr_b16 a[180:181], v229 offset:61440\n" \
  "v_accvgpr_write_b32 a0, v0\n" \
  "v_mfma_f32_16x16x32_bf16 v[100:103], a[112:115], v[140:143], v[100:103]\n" \
  "ds_read_b64_tr_b16 a[182:183], v229 offset:61952\n" \
  "v_accvgpr_write_b32 a1, v1\n" \
  "v_mfma_f32_16x16x32_bf16 v[104:107], a[116:119], v[136:139], v[104:107]\n" \
  "ds_read_b64_tr_b16 a[184:185], v230 offset:61440\n" \
  "v_accvgpr_write_b32 a2, v8\n" \
  "v_mfma_f32_16x16x32_bf16 v[108:111], a[116:119], v[140:143], v[108:111]\n" \
  "ds_read_b64_tr_b16 a[186:187], v230 offset:61952\n" \
  "ds_read_b64_tr_b16 a[188:189], v231 offset:61440\n" \
  "v_mfma_f32_16x16x32_bf16 v[112:115], a[120:123], v[136:139], v[112:115]\n" \
  "ds_read_b64_tr_b16 a[190:191], v231 offset:61952\n" \
  "v_accvgpr_write_b32 a3, v9\n" \
  "v_mfma_f32_16x16x32_bf16 v[116:119], a[120:123], v[140:143], v[116:119]\n" \
  "buffer_load_dwordx4 v223, s[36:39], s72 offen offset:3072 lds\n" \
  "v_mul_f32_e64 v4, v4, v249\n" \
  "v_mfma_f32_16x16x32_bf16 v[120:123], a[124:127], v[136:139], v[120:123]\n" \
  "ds_read_b64_tr_b16 a[64:65], v232 offset:49152\n" \
  "v_mul_f32_e64 v5, v5, v249\n" \
  "v_mfma_f32_16x16x32_bf16 v[124:127], a[124:127], v[140:143], v[124:127]\n" \
  "ds_read_b64_tr_b16 a[66:67], v232 offset:49664\n" \
  "s_waitcnt lgkmcnt(6)\n" \
  "v_mul_f32 v6, v6, v249\n" \
  "v_mfma_f32_16x16x32_bf16 v[64:67], a[128:131], v[144:147], v[64:67]\n" \
  "ds_read_b64_tr_b16 a[68:69], v233 offset:49152\n" \
  "ds_read_b64_tr_b16 a[70:71], v233 offset:49664\n" \
  "v_mul_f32_e64 v7, v7, v249\n" \
  "v_mfma_f32_16x16x32_bf16 v[68:71], a[128:131], v[148:151], v[68:71]\n" \
  "ds_read_b64_tr_b16 a[72:73], v234 offset:49152\n" \
  "v_cvt_pk_bf16_f32 v4, v4, v5\n" \
  "v_mfma_f32_16x16x32_bf16 v[72:75], a[132:135], v[144:147], v[72:75]\n" \
  "ds_read_b64_tr_b16 a[74:75], v234 offset:49664\n" \
  "v_cvt_pk_bf16_f32 v5, v6, v7\n" \
  "v_mfma_f32_16x16x32_bf16 v[76:79], a[132:135], v[148:151], v[76:79]\n" \
  "ds_read_b64_tr_b16 a[76:77], v235 offset:49152\n" \
  "v_mul_f32_e64 v12, v12, v249\n" \
  "v_mfma_f32_16x16x32_bf16 v[80:83], a[136:139], v[144:147], v[80:83]\n" \
  "ds_read_b64_tr_b16 a[78:79], v235 offset:49664\n" \
  "s_mov_b32 s72, 0x4000\n" \
  "s_add_u32 m0, s75, 0x8000\n" \
  "v_mul_f32_e64 v13, v13, v249\n" \
  "v_mfma_f32_16x16x32_bf16 v[84:87], a[136:139], v[148:151], v[84:87]\n" \
  "buffer_load_dwordx4 v220, s[36:39], s72 offen lds\n" \
  "v_mfma_f32_16x16x32_bf16 v[88:91], a[140:143], v[144:147], v[88:91]\n" \
  "ds_read_b64_tr_b16 a[80:81], v232 offset:57344\n" \
  "ds_read_b64_tr_b16 a[82:83], v232 offset:57856\n" \
  "v_mul_f32_e64 v14, v14, v249\n" \
  "v_mfma_f32_16x16x32_bf16 v[92:95], a[140:143], v[148:151], v[92:95]\n" \
  "ds_read_b64_tr_b16 a[84:85], v233 offset:57344\n" \
  "v_mul_f32_e64 v15, v15, v249\n" \
  "v_mfma_f32_16x16x32_bf16 v[96:99], a[144:147], v[144:147], v[96:99]\n" \
  "ds_read_b64_tr_b16 a[86:87], v233 offset:57856\n" \
  "v_cvt_pk_bf16_f32 v12, v12, v13\n" \
  "v_mfma_f32_16x16x32_bf16 v[100:103], a[144:147], v[148:151], v[100:103]\n" \
  "ds_read_b64_tr_b16 a[88:89], v234 offset:57344\n" \
  "v_cvt_pk_bf16_f32 v13, v14, v15\n" \
  "v_mfma_f32_16x16x32_bf16 v[104:107], a[148:151], v[144:147], v[104:107]\n" \
  "ds_read_b64_tr_b16 a[90:91], v234 offset:57856\n" \
  "v_permlane32_swap_b32 v4, v12\n" \
  "v_mfma_f32_16x16x32_bf16 v[108:111], a[148:151], v[148:151], v[108:111]\n" \
  "ds_read_b64_tr_b16 a[92:93], v235 offset:57344\n" \
  "ds_read_b64_tr_b16 a[94:95], v235 offset:57856\n" \
  "v_mfma_f32_16x16x32_bf16 v[112:115], a[152:155], v[144:147], v[112:115]\n" \
  "ds_read_b64_tr_b16 a[96:97], v232 offset:53248\n" \
  "v_permlane32_swap_b32 v5, v13\n" \
  "v_mfma_f32_16x16x32_bf16 v[116:119], a[152:155], v[148:151], v[116:119]\n" \
  "buffer_load_dwordx4 v221, s[36:39], s72 offen offset:1024 lds\n" \
  "v_permlane16_swap_b32 v4, v12\n" \
  "v_mfma_f32_16x16x32_bf16 v[120:123], a[156:159], v[144:147], v[120:123]\n" \
  "ds_read_b64_tr_b16 a[98:99], v232 offset:53760\n" \
  "v_permlane16_swap_b32 v5, v13\n" \
  "v_mfma_f32_16x16x32_bf16 v[124:127], a[156:159], v[148:151], v[124:127]\n" \
  "ds_read_b64_tr_b16 a[100:101], v233 offset:53248\n" \
  "v_accvgpr_write_b32 a32, v4\n" \
  "v_mfma_f32_16x16x32_bf16 v[64:67], a[160:163], v[152:155], v[64:67]\n" \
  "ds_read_b64_tr_b16 a[102:103], v233 offset:53760\n" \
  "v_accvgpr_write_b32 a33, v5\n" \
  "v_mfma_f32_16x16x32_bf16 v[68:71], a[160:163], v[156:159], v[68:71]\n" \
  "ds_read_b64_tr_b16 a[104:105], v234 offset:53248\n" \
  "ds_read_b64_tr_b16 a[106:107], v234 offset:53760\n" \
  "v_mfma_f32_16x16x32_bf16 v[72:75], a[164:167], v[152:155], v[72:75]\n" \
  "ds_read_b64_tr_b16 a[108:109], v235 offset:53248\n" \
  "v_accvgpr_write_b32 a34, v12\n" \
  "v_mfma_f32_16x16x32_bf16 v[76:79], a[164:167], v[156:159], v[76:79]\n" \
  "ds_read_b64_tr_b16 a[110:111], v235 offset:53760\n" \
  "v_accvgpr_write_b32 a35, v13\n" \
  "v_mfma_f32_16x16x32_bf16 v[80:83], a[168:171], v[152:155], v[80:83]\n" \
  "ds_read_b64_tr_b16 a[112:113], v232 offset:61440\n" \
  "v_mul_f32_e64 v16, v16, v248\n" \
  "v_mfma_f32_16x16x32_bf16 v[84:87], a[168:171], v[156:159], v[84:87]\n" \
  "buffer_load_dwordx4 v222, s[36:39], s72 offen offset:2048 lds\n" \
  "v_mul_f32_e64 v17, v17, v248\n" \
  "v_mfma_f32_16x16x32_bf16 v[88:91], a[172:175], v[152:155], v[88:91]\n" \
  "ds_read_b64_tr_b16 a[114:115], v232 offset:61952\n" \
  "v_mul_f32_e64 v18, v18, v248\n" \
  "v_mfma_f32_16x16x32_bf16 v[92:95], a[172:175], v[156:159], v[92:95]\n" \
  "ds_read_b64_tr_b16 a[116:117], v233 offset:61440\n" \
  "ds_read_b64_tr_b16 a[118:119], v233 offset:61952\n" \
  "v_mul_f32_e64 v19, v19, v248\n" \
  "v_mfma_f32_16x16x32_bf16 v[96:99], a[176:179], v[152:155], v[96:99]\n" \
  "ds_read_b64_tr_b16 a[120:121], v234 offset:61440\n" \
  "v_cvt_pk_bf16_f32 v16, v16, v17\n" \
  "v_mfma_f32_16x16x32_bf16 v[100:103], a[176:179], v[156:159], v[100:103]\n" \
  "ds_read_b64_tr_b16 a[122:123], v234 offset:61952\n" \
  "v_cvt_pk_bf16_f32 v17, v18, v19\n" \
  "v_mfma_f32_16x16x32_bf16 v[104:107], a[180:183], v[152:155], v[104:107]\n" \
  "ds_read_b64_tr_b16 a[124:125], v235 offset:61440\n" \
  "v_mul_f32_e64 v24, v24, v248\n" \
  "v_mfma_f32_16x16x32_bf16 v[108:111], a[180:183], v[156:159], v[108:111]\n" \
  "ds_read_b64_tr_b16 a[126:127], v235 offset:61952\n" \
  "s_waitcnt lgkmcnt(7)\n" \
  "v_mul_f32 v25, v25, v248\n" \
  "v_mfma_f32_16x16x32_bf16 v[112:115], a[184:187], v[152:155], v[112:115]\n" \
  "v_mul_f32 v26, v26, v248\n" \
  "v_mul_f32 v27, v27, v248\n" \
  "v_mfma_f32_16x16x32_bf16 v[116:119], a[184:187], v[156:159], v[116:119]\n" \
  "buffer_load_dwordx4 v223, s[36:39], s72 offen offset:3072 lds\n" \
  "v_mfma_f32_16x16x32_bf16 v[120:123], a[188:191], v[152:155], v[120:123]\n" \
  "v_cvt_pk_bf16_f32 v24, v24, v25\n" \
  "v_cvt_pk_bf16_f32 v25, v26, v27\n" \
  "v_mfma_f32_16x16x32_bf16 v[124:127], a[188:191], v[156:159], v[124:127]\n" \
  "v_mul_f32 v20, v20, v249\n" \
  "v_permlane32_swap_b32 v16, v24\n" \
  "s_waitcnt vmcnt(18) lgkmcnt(0)\n" \
  "s_barrier\n" \
  "; HP [23, 24] ('Q', 1, 2)\n" \
  "v_mfma_f32_16x16x32_bf16 v[64:67], a[64:67], v[160:163], v[64:67]\n" \
  "ds_read_b64_tr_b16 a[128:129], v228\n" \
  "ds_read_b64_tr_b16 a[130:131], v228 offset:512\n" \
  "v_mfma_f32_16x16x32_bf16 v[68:71], a[64:67], v[164:167], v[68:71]\n" \
  "ds_read_b64_tr_b16 a[132:133], v229\n" \
  "v_permlane32_swap_b32 v17, v25\n" \
  "v_mfma_f32_16x16x32_bf16 v[72:75], a[68:71], v[160:163], v[72:75]\n" \
  "ds_read_b64_tr_b16 a[134:135], v229 offset:512\n" \
  "v_permlane16_swap_b32 v16, v24\n" \
  "v_mfma_f32_16x16x32_bf16 v[76:79], a[68:71], v[164:167], v[76:79]\n" \
  "ds_read_b64_tr_b16 a[136:137], v230\n" \
  "v_permlane16_swap_b32 v17, v25\n" \
  "v_mfma_f32_16x16x32_bf16 v[80:83], a[72:75], v[160:163], v[80:83]\n" \
  "ds_read_b64_tr_b16 a[138:139], v230 offset:512\n" \
  "ds_read_b64_tr_b16 a[140:141], v231\n" \
  "s_mov_b32 s73, 0\n" \
  "s_add_u32 m0, s74, 0xc000\n" \
  "v_accvgpr_write_b32 a4, v16\n" \
  "v_mfma_f32_16x16x32_bf16 v[84:87], a[72:75], v[164:167], v[84:87]\n" \
  "buffer_load_dwordx4 v224, s[40:43], s73 offen lds\n" \
  "v_accvgpr_write_b32 a5, v17\n" \
  "v_mfma_f32_16x16x32_bf16 v[88:91], a[76:79], v[160:163], v[88:91]\n" \
  "ds_read_b64_tr_b16 a[142:143], v231 offset:512\n" \
  "v_accvgpr_write_b32 a6, v24\n" \
  "v_mfma_f32_16x16x32_bf16 v[92:95], a[76:79], v[164:167], v[92:95]\n" \
  "ds_read_b64_tr_b16 a[144:145], v228 offset:8192\n" \
  "v_accvgpr_write_b32 a7, v25\n" \
  "v_mfma_f32_16x16x32_bf16 v[96:99], a[80:83], v[160:163], v[96:99]\n" \
  "ds_read_b64_tr_b16 a[146:147], v228 offset:8704\n" \
  "v_mul_f32_e64 v21, v21, v249\n" \
  "v_mfma_f32_16x16x32_bf16 v[100:103], a[80:83], v[164:167], v[100:103]\n" \
  "ds_read_b64_tr_b16 a[148:149], v229 offset:8192\n" \
  "v_mul_f32_e64 v22, v22, v249\n" \
  "v_mfma_f32_16x16x32_bf16 v[104:107], a[84:87], v[160:163], v[104:107]\n" \
  "ds_read_b64_tr_b16 a[150:151], v229 offset:8704\n" \
  "ds_read_b64_tr_b16 a[152:153], v230 offset:8192\n" \
  "v_mfma_f32_16x16x32_bf16 v[108:111], a[84:87], v[164:167], v[108:111]\n" \
  "ds_read_b64_tr_b16 a[154:155], v230 offset:8704\n" \
  "v_mul_f32_e64 v23, v23, v249\n" \
  "v_mfma_f32_16x16x32_bf16 v[112:115], a[88:91], v[160:163], v[112:115]\n" \
  "ds_read_b64_tr_b16 a[156:157], v231 offset:8192\n" \
  "v_cvt_pk_bf16_f32 v20, v20, v21\n" \
  "v_mfma_f32_16x16x32_bf16 v[116:119], a[88:91], v[164:167], v[116:119]\n" \
  "buffer_load_dwordx4 v225, s[40:43], s73 offen offset:1024 lds\n" \
  "v_mfma_f32_16x16x32_bf16 v[120:123], a[92:95], v[160:163], v[120:123]\n" \
  "ds_read_b64_tr_b16 a[158:159], v231 offset:8704\n" \
  "v_cvt_pk_bf16_f32 v21, v22, v23\n" \
  "v_mfma_f32_16x16x32_bf16 v[124:127], a[92:95], v[164:167], v[124:127]\n" \
  "ds_read_b64_tr_b16 a[160:161], v228 offset:4096\n" \
  "v_mul_f32_e64 v28, v28, v249\n" \
  "v_mfma_f32_16x16x32_bf16 v[64:67], a[96:99], v[168:171], v[64:67]\n" \
  "ds_read_b64_tr_b16 a[162:163], v228 offset:4608\n" \
  "ds_read_b64_tr_b16 a[164:165], v229 offset:4096\n" \
  "v_mul_f32_e64 v29, v29, v249\n" \
  "v_mfma_f32_16x16x32_bf16 v[68:71], a[96:99], v[172:175], v[68:71]\n" \
  "ds_read_b64_tr_b16 a[166:167], v229 offset:4608\n" \
  "v_mul_f32_e64 v30, v30, v249\n" \
  "v_mfma_f32_16x16x32_bf16 v[72:75], a[100:103], v[168:171], v[72:75]\n" \
  "ds_read_b64_tr_b16 a[168:169], v230 offset:4096\n" \
  "v_mul_f32_e64 v31, v31, v249\n" \
  "v_mfma_f32_16x16x32_bf16 v[76:79], a[100:103], v[172:175], v[76:79]\n" \
  "ds_read_b64_tr_b16 a[170:171], v230 offset:4608\n" \
  "v_cvt_pk_bf16_f32 v28, v28, v29\n" \
  "v_mfma_f32_16x16x32_bf16 v[80:83], a[104:107], v[168:171], v[80:83]\n" \
  "ds_read_b64_tr_b16 a[172:173], v231 offset:4096\n" \
  "v_cvt_pk_bf16_f32 v29, v30, v31\n" \
  "v_mfma_f32_16x16x32_bf16 v[84:87], a[104:107], v[172:175], v[84:87]\n" \
  "buffer_load_dwordx4 v226, s[40:43], s73 offen offset:2048 lds\n" \
  "v_mfma_f32_16x16x32_bf16 v[88:91], a[108:111], v[168:171], v[88:91]\n" \
  "ds_read_b64_tr_b16 a[174:175], v231 offset:4608\n" \
  "ds_read_b64_tr_b16 a[176:177], v228 offset:12288\n" \
  "v_permlane32_swap_b32 v20, v28\n" \
  "v_mfma_f32_16x16x32_bf16 v[92:95], a[108:111], v[172:175], v[92:95]\n" \
  "ds_read_b64_tr_b16 a[178:179], v228 offset:12800\n" \
  "v_permlane32_swap_b32 v21, v29\n" \
  "v_mfma_f32_16x16x32_bf16 v[96:99], a[112:115], v[168:171], v[96:99]\n" \
  "ds_read_b64_tr_b16 a[180:181], v229 offset:12288\n" \
  "v_permlane16_swap_b32 v20, v28\n" \
  "v_mfma_f32_16x16x32_bf16 v[100:103], a[112:115], v[172:175], v[100:103]\n" \
  "ds_read_b64_tr_b16 a[182:183], v229 offset:12800\n" \
  "v_permlane16_swap_b32 v21, v29\n" \
  "v_mfma_f32_16x16x32_bf16 v[104:107], a[116:119], v[168:171], v[104:107]\n" \
  "ds_read_b64_tr_b16 a[184:185], v230 offset:12288\n" \
  "v_accvgpr_write_b32 a36, v20\n" \
  "v_mfma_f32_16x16x32_bf16 v[108:111], a[116:119], v[172:175], v[108:111]\n" \
  "ds_read_b64_tr_b16 a[186:187], v230 offset:12800\n" \
  "ds_read_b64_tr_b16 a[188:189], v231 offset:12288\n" \
  "v_mfma_f32_16x16x32_bf16 v[112:115], a[120:123], v[168:171], v[112:115]\n" \
  "ds_read_b64_tr_b16 a[190:191], v231 offset:12800\n" \
  "v_accvgpr_write_b32 a37, v21\n" \
  "v_mfma_f32_16x16x32_bf16 v[116:119], a[120:123], v[172:175], v[116:119]\n" \
  "buffer_load_dwordx4 v227, s[40:43], s73 offen offset:3072 lds\n" \
  "v_accvgpr_write_b32 a38, v28\n" \
  "v_mfma_f32_16x16x32_bf16 v[120:123], a[124:127], v[168:171], v[120:123]\n" \
  "ds_read_b64_tr_b16 a[64:65], v232\n" \
  "v_accvgpr_write_b32 a39, v29\n" \
  "v_mfma_f32_16x16x32_bf16 v[124:127], a[124:127], v[172:175], v[124:127]\n" \
  "ds_read_b64_tr_b16 a[66:67], v232 offset:512\n" \
  "s_waitcnt lgkmcnt(6)\n" \
  "v_mul_f32 v32, v32, v248\n" \
  "v_mfma_f32_16x16x32_bf16 v[64:67], a[128:131], a[192:195], v[64:67]\n" \
  "ds_read_b64_tr_b16 a[68:69], v233\n" \
  "ds_read_b64_tr_b16 a[70:71], v233 offset:512\n" \
  "v_mfma_f32_16x16x32_bf16 v[68:71], a[128:131], a[196:199], v[68:71]\n" \
  "ds_read_b64_tr_b16 a[72:73], v234\n" \
  "v_mul_f32_e64 v33, v33, v248\n" \
  "v_mfma_f32_16x16x32_bf16 v[72:75], a[132:135], a[192:195], v[72:75]\n" \
  "ds_read_b64_tr_b16 a[74:75], v234 offset:512\n" \
  "v_mul_f32_e64 v34, v34, v248\n" \
  "v_mfma_f32_16x16x32_bf16 v[76:79], a[132:135], a[196:199], v[76:79]\n" \
  "ds_read_b64_tr_b16 a[76:77], v235\n" \
  "v_mul_f32_e64 v35, v35, v248\n" \
  "v_mfma_f32_16x16x32_bf16 v[80:83], a[136:139], a[192:195], v[80:83]\n" \
  "ds_read_b64_tr_b16 a[78:79], v235 offset:512\n" \
  "s_mov_b32 s72, 0x8000\n" \
  "s_add_u32 m0, s75, 0xc000\n" \
  "v_cvt_pk_bf16_f32 v32, v32, v33\n" \
  "v_mfma_f32_16x16x32_bf16 v[84:87], a[136:139], a[196:199], v[84:87]\n" \
  "buffer_load_dwordx4 v220, s[36:39], s72 offen lds\n" \
  "v_mfma_f32_16x16x32_bf16 v[88:91], a[140:143], a[192:195], v[88:91]\n" \
  "ds_read_b64_tr_b16 a[80:81], v232 offset:8192\n" \
  "ds_read_b64_tr_b16 a[82:83], v232 offset:8704\n" \
  "v_cvt_pk_bf16_f32 v33, v34, v35\n" \
  "v_mfma_f32_16x16x32_bf16 v[92:95], a[140:143], a[196:199], v[92:95]\n" \
  "ds_read_b64_tr_b16 a[84:85], v233 offset:8192\n" \
  "v_mul_f32_e64 v40, v40, v248\n" \
  "v_mfma_f32_16x16x32_bf16 v[96:99], a[144:147], a[192:195], v[96:99]\n" \
  "ds_read_b64_tr_b16 a[86:87], v233 offset:8704\n" \
  "v_mul_f32_e64 v41, v41, v248\n" \
  "v_mfma_f32_16x16x32_bf16 v[100:103], a[144:147], a[196:199], v[100:103]\n" \
  "ds_read_b64_tr_b16 a[88:89], v234 offset:8192\n" \
  "v_mul_f32_e64 v42, v42, v248\n" \
  "v_mfma_f32_16x16x32_bf16 v[104:107], a[148:151], a[192:195], v[104:107]\n" \
  "ds_read_b64_tr_b16 a[90:91], v234 offset:8704\n" \
  "v_mul_f32_e64 v43, v43, v248\n" \
  "v_mfma_f32_16x16x32_bf16 v[108:111], a[148:151], a[196:199], v[108:111]\n" \
  "ds_read_b64_tr_b16 a[92:93], v235 offset:8192\n" \
  "ds_read_b64_tr_b16 a[94:95], v235 offset:8704\n" \
  "v_cvt_pk_bf16_f32 v40, v40, v41\n" \
  "v_mfma_f32_16x16x32_bf16 v[112:115], a[152:155], a[192:195], v[112:115]\n" \
  "ds_read_b64_tr_b16 a[96:97], v232 offset:4096\n" \
  "v_cvt_pk_bf16_f32 v41, v42, v43\n" \
  "v_mfma_f32_16x16x32_bf16 v[116:119], a[152:155], a[196:199], v[116:119]\n" \
  "buffer_load_dwordx4 v221, s[36:39], s72 offen offset:1024 lds\n" \
  "v_mfma_f32_16x16x32_bf16 v[120:123], a[156:159], a[192:195], v[120:123]\n" \
  "ds_read_b64_tr_b16 a[98:99], v232 offset:4608\n" \
  "v_permlane32_swap_b32 v32, v40\n" \
  "v_mfma_f32_16x16x32_bf16 v[124:127], a[156:159], a[196:199], v[124:127]\n" \
  "ds_read_b64_tr_b16 a[100:101], v233 offset:4096\n" \
  "v_permlane32_swap_b32 v33, v41\n" \
  "v_mfma_f32_16x16x32_bf16 v[64:67], a[160:163], a[200:203], v[64:67]\n" \
  "ds_read_b64_tr_b16 a[102:103], v233 offset:4608\n" \
  "v_permlane16_swap_b32 v32, v40\n" \
  "v_mfma_f32_16x16x32_bf16 v[68:71], a[160:163], a[204:207], v[68:71]\n" \
  "ds_read_b64_tr_b16 a[104:105], v234 offset:4096\n" \
  "ds_read_b64_tr_b16 a[106:107], v234 offset:4608\n" \
  "v_permlane16_swap_b32 v33, v41\n" \
  "v_mfma_f32_16x16x32_bf16 v[72:75], a[164:167], a[200:203], v[72:75]\n" \
  "ds_read_b64_tr_b16 a[108:109], v235 offset:4096\n" \
  "v_accvgpr_write_b32 a8, v32\n" \
  "v_mfma_f32_16x16x32_bf16 v[76:79], a[164:167], a[204:207], v[76:79]\n" \
  "ds_read_b64_tr_b16 a[110:111], v235 offset:4608\n" \
  "v_accvgpr_write_b32 a9, v33\n" \
  "v_mfma_f32_16x16x32_bf16 v[80:83], a[168:171], a[200:203], v[80:83]\n" \
  "ds_read_b64_tr_b16 a[112:113], v232 offset:12288\n" \
  "v_accvgpr_write_b32 a10, v40\n" \
  "v_mfma_f32_16x16x32_bf16 v[84:87], a[168:171], a[204:207], v[84:87]\n" \
  "buffer_load_dwordx4 v222, s[36:39], s72 offen offset:2048 lds\n" \
  "v_mfma_f32_16x16x32_bf16 v[88:91], a[172:175], a[200:203], v[88:91]\n" \
  "ds_read_b64_tr_b16 a[114:115], v232 offset:12800\n" \
  "v_accvgpr_write_b32 a11, v41\n" \
  "v_mfma_f32_16x16x32_bf16 v[92:95], a[172:175], a[204:207], v[92:95]\n" \
  "ds_read_b64_tr_b16 a[116:117], v233 offset:12288\n" \
  "ds_read_b64_tr_b16 a[118:119], v233 offset:12800\n" \
  "v_mul_f32_e64 v36, v36, v249\n" \
  "v_mfma_f32_16x16x32_bf16 v[96:99], a[176:179], a[200:203], v[96:99]\n" \
  "ds_read_b64_tr_b16 a[120:121], v234 offset:12288\n" \
  "v_mul_f32_e64 v37, v37, v249\n" \
  "v_mfma_f32_16x16x32_bf16 v[100:103], a[176:179], a[204:207], v[100:103]\n" \
  "ds_read_b64_tr_b16 a[122:123], v234 offset:12800\n" \
  "v_mul_f32_e64 v38, v38, v249\n" \
  "v_mfma_f32_16x16x32_bf16 v[104:107], a[180:183], a[200:203], v[104:107]\n" \
  "ds_read_b64_tr_b16 a[124:125], v235 offset:12288\n" \
  "v_mul_f32_e64 v39, v39, v249\n" \
  "v_mfma_f32_16x16x32_bf16 v[108:111], a[180:183], a[204:207], v[108:111]\n" \
  "ds_read_b64_tr_b16 a[126:127], v235 offset:12800\n" \
  "s_waitcnt lgkmcnt(7)\n" \
  "v_cvt_pk_bf16_f32 v36, v36, v37\n" \
  "v_mfma_f32_16x16x32_bf16 v[112:115], a[184:187], a[200:203], v[112:115]\n" \
  "v_cvt_pk_bf16_f32 v37, v38, v39\n" \
  "v_mul_f32 v44, v44, v249\n" \
  "v_mfma_f32_16x16x32_bf16 v[116:119], a[184:187], a[204:207], v[116:119]\n" \
  "buffer_load_dwordx4 v223, s[36:39], s72 offen offset:3072 lds\n" \
  "v_mfma_f32_16x16x32_bf16 v[120:123], a[188:191], a[200:203], v[120:123]\n" \
  "v_mul_f32 v45, v45, v249\n" \
  "v_mul_f32 v46, v46, v249\n" \
  "v_mfma_f32_16x16x32_bf16 v[124:127], a[188:191], a[204:207], v[124:127]\n" \
  "v_mul_f32_e64 v47, v47, v249\n" \
  "v_cvt_pk_bf16_f32 v44, v44, v45\n" \
  "s_waitcnt vmcnt(16) lgkmcnt(0)\n" \
  "s_barrier\n" \
  "v_mov_b32 v246, s79\n" \
  "v_mov_b32 v247, s80\n" \
  "v_add_u32_e64 v247, 1, v247\n" \
  "buffer_store_dwordx2 v[246:247], v254, s[68:71], s78 offen sc1\n" \
  "; HP [25, 26] ('Q', 1, 4)\n" \
  "v_mfma_f32_16x16x32_bf16 v[64:67], a[64:67], a[208:211], v[64:67]\n" \
  "ds_read_b64_tr_b16 a[128:129], v228 offset:16384\n" \
  "ds_read_b64_tr_b16 a[130:131], v228 offset:16896\n" \
  "v_mfma_f32_16x16x32_bf16 v[68:71], a[64:67], a[212:215], v[68:71]\n" \
  "ds_read_b64_tr_b16 a[132:133], v229 offset:16384\n" \
  "v_cvt_pk_bf16_f32 v45, v46, v47\n" \
  "v_mfma_f32_16x16x32_bf16 v[72:75], a[68:71], a[208:211], v[72:75]\n" \
  "ds_read_b64_tr_b16 a[134:135], v229 offset:16896\n" \
  "v_permlane32_swap_b32 v36, v44\n" \
  "v_mfma_f32_16x16x32_bf16 v[76:79], a[68:71], a[212:215], v[76:79]\n" \
  "ds_read_b64_tr_b16 a[136:137], v230 offset:16384\n" \
  "v_permlane32_swap_b32 v37, v45\n" \
  "v_mfma_f32_16x16x32_bf16 v[80:83], a[72:75], a[208:211], v[80:83]\n" \
  "ds_read_b64_tr_b16 a[138:139], v230 offset:16896\n" \
  "ds_read_b64_tr_b16 a[140:141], v231 offset:16384\n" \
  "s_mov_b32 s73, 0x4000\n" \
  "s_add_u32 m0, s74, 0\n" \
  "v_mfma_f32_16x16x32_bf16 v[84:87], a[72:75], a[212:215], v[84:87]\n" \
  "buffer_load_dwordx4 v224, s[40:43], s73 offen lds\n" \
  "v_permlane16_swap_b32 v36, v44\n" \
  "v_mfma_f32_16x16x32_bf16 v[88:91], a[76:79], a[208:211], v[88:91]\n" \
  "ds_read_b64_tr_b16 a[142:143], v231 offset:16896\n" \
  "v_permlane16_swap_b32 v37, v45\n" \
  "v_mfma_f32_16x16x32_bf16 v[92:95], a[76:79], a[212:215], v[92:95]\n" \
  "ds_read_b64_tr_b16 a[144:145], v228 offset:24576\n" \
  "v_accvgpr_write_b32 a40, v36\n" \
  "v_mfma_f32_16x16x32_bf16 v[96:99], a[80:83], a[208:211], v[96:99]\n" \
  "ds_read_b64_tr_b16 a[146:147], v228 offset:25088\n" \
  "v_accvgpr_write_b32 a41, v37\n" \
  "v_mfma_f32_16x16x32_bf16 v[100:103], a[80:83], a[212:215], v[100:103]\n" \
  "ds_read_b64_tr_b16 a[148:149], v229 offset:24576\n" \
  "v_accvgpr_write_b32 a42, v44\n" \
  "v_mfma_f32_16x16x32_bf16 v[104:107], a[84:87], a[208:211], v[104:107]\n" \
  "ds_read_b64_tr_b16 a[150:151], v229 offset:25088\n" \
  "ds_read_b64_tr_b16 a[152:153], v230 offset:24576\n" \
  "v_mfma_f32_16x16x32_bf16 v[108:111], a[84:87], a[212:215], v[108:111]\n" \
  "ds_read_b64_tr_b16 a[154:155], v230 offset:25088\n" \
  "v_accvgpr_write_b32 a43, v45\n" \
  "v_mfma_f32_16x16x32_bf16 v[112:115], a[88:91], a[208:211], v[112:115]\n" \
  "ds_read_b64_tr_b16 a[156:157], v231 offset:24576\n" \
  "v_mul_f32 v48, v48, v248\n" \
  "v_mfma_f32_16x16x32_bf16 v[116:119], a[88:91], a[212:215], v[116:119]\n" \
  "buffer_load_dwordx4 v225, s[40:43], s73 offen offset:1024 lds\n" \
  "v_mul_f32_e64 v49, v49, v248\n" \
  "v_mfma_f32_16x16x32_bf16 v[120:123], a[92:95], a[208:211], v[120:123]\n" \
  "ds_read_b64_tr_b16 a[158:159], v231 offset:25088\n" \
  "v_mul_f32_e64 v50, v50, v248\n" \
  "v_mfma_f32_16x16x32_bf16 v[124:127], a[92:95], a[212:215], v[124:127]\n" \
  "ds_read_b64_tr_b16 a[160:161], v228 offset:20480\n" \
  "v_mul_f32_e64 v51, v51, v248\n" \
  "v_mfma_f32_16x16x32_bf16 v[64:67], a[96:99], a[216:219], v[64:67]\n" \
  "ds_read_b64_tr_b16 a[162:163], v228 offset:20992\n" \
  "ds_read_b64_tr_b16 a[164:165], v229 offset:20480\n" \
  "v_mfma_f32_16x16x32_bf16 v[68:71], a[96:99], a[220:223], v[68:71]\n" \
  "ds_read_b64_tr_b16 a[166:167], v229 offset:20992\n" \
  "v_cvt_pk_bf16_f32 v48, v48, v49\n" \
  "v_mfma_f32_16x16x32_bf16 v[72:75], a[100:103], a[216:219], v[72:75]\n" \
  "ds_read_b64_tr_b16 a[168:169], v230 offset:20480\n" \
  "v_cvt_pk_bf16_f32 v49, v50, v51\n" \
  "v_mfma_f32_16x16x32_bf16 v[76:79], a[100:103], a[220:223], v[76:79]\n" \
  "ds_read_b64_tr_b16 a[170:171], v230 offset:20992\n" \
  "v_mul_f32_e64 v56, v56, v248\n" \
  "v_mfma_f32_16x16x32_bf16 v[80:83], a[104:107], a[216:219], v[80:83]\n" \
  "ds_read_b64_tr_b16 a[172:173], v231 offset:20480\n" \
  "v_mul_f32_e64 v57, v57, v248\n" \
  "v_mfma_f32_16x16x32_bf16 v[84:87], a[104:107], a[220:223], v[84:87]\n" \
  "buffer_load_dwordx4 v226, s[40:43], s73 offen offset:2048 lds\n" \
  "v_mfma_f32_16x16x32_bf16 v[88:91], a[108:111], a[216:219], v[88:91]\n" \
  "ds_read_b64_tr_b16 a[174:175], v231 offset:20992\n" \
  "ds_read_b64_tr_b16 a[176:177], v228 offset:28672\n" \
  "v_mul_f32_e64 v58, v58, v248\n" \
  "v_mfma_f32_16x16x32_bf16 v[92:95], a[108:111], a[220:223], v[92:95]\n" \
  "ds_read_b64_tr_b16 a[178:179], v228 offset:29184\n" \
  "v_mul_f32_e64 v59, v59, v248\n" \
  "v_mfma_f32_16x16x32_bf16 v[96:99], a[112:115], a[216:219], v[96:99]\n" \
  "ds_read_b64_tr_b16 a[180:181], v229 offset:28672\n" \
  "v_cvt_pk_bf16_f32 v56, v56, v57\n" \
  "v_mfma_f32_16x16x32_bf16 v[100:103], a[112:115], a[220:223], v[100:103]\n" \
  "ds_read_b64_tr_b16 a[182:183], v229 offset:29184\n" \
  "v_cvt_pk_bf16_f32 v57, v58, v59\n" \
  "v_mfma_f32_16x16x32_bf16 v[104:107], a[116:119], a[216:219], v[104:107]\n" \
  "ds_read_b64_tr_b16 a[184:185], v230 offset:28672\n" \
  "v_permlane32_swap_b32 v48, v56\n" \
  "v_mfma_f32_16x16x32_bf16 v[108:111], a[116:119], a[220:223], v[108:111]\n" \
  "ds_read_b64_tr_b16 a[186:187], v230 offset:29184\n" \
  "ds_read_b64_tr_b16 a[188:189], v231 offset:28672\n" \
  "v_mfma_f32_16x16x32_bf16 v[112:115], a[120:123], a[216:219], v[112:115]\n" \
  "ds_read_b64_tr_b16 a[190:191], v231 offset:29184\n" \
  "v_permlane32_swap_b32 v49, v57\n" \
  "v_mfma_f32_16x16x32_bf16 v[116:119], a[120:123], a[220:223], v[116:119]\n" \
  "buffer_load_dwordx4 v227, s[40:43], s73 offen offset:3072 lds\n" \
  "v_mfma_f32_16x16x32_bf16 v[120:123], a[124:127], a[216:219], v[120:123]\n" \
  "ds_read_b64_tr_b16 a[64:65], v232 offset:16384\n" \
  "v_permlane16_swap_b32 v48, v56\n" \
  "v_mfma_f32_16x16x32_bf16 v[124:127], a[124:127], a[220:223], v[124:127]\n" \
  "ds_read_b64_tr_b16 a[66:67], v232 offset:16896\n" \
  "s_waitcnt lgkmcnt(6)\n" \
  "v_permlane16_swap_b32 v49, v57\n" \
  "v_mfma_f32_16x16x32_bf16 v[64:67], a[128:131], a[224:227], v[64:67]\n" \
  "ds_read_b64_tr_b16 a[96:97], v232 offset:20480\n" \
  "ds_read_b64_tr_b16 a[98:99], v232 offset:20992\n" \
  "v_accvgpr_write_b32 a12, v48\n" \
  "v_mfma_f32_16x16x32_bf16 v[68:71], a[128:131], a[228:231], v[68:71]\n" \
  "ds_read_b64_tr_b16 a[68:69], v233 offset:16384\n" \
  "v_accvgpr_write_b32 a13, v49\n" \
  "v_mfma_f32_16x16x32_bf16 v[72:75], a[132:135], a[224:227], v[72:75]\n" \
  "ds_read_b64_tr_b16 a[70:71], v233 offset:16896\n" \
  "v_accvgpr_write_b32 a14, v56\n" \
  "v_mfma_f32_16x16x32_bf16 v[76:79], a[132:135], a[228:231], v[76:79]\n" \
  "ds_read_b64_tr_b16 a[100:101], v233 offset:20480\n" \
  "v_accvgpr_write_b32 a15, v57\n" \
  "v_mfma_f32_16x16x32_bf16 v[80:83], a[136:139], a[224:227], v[80:83]\n" \
  "ds_read_b64_tr_b16 a[102:103], v233 offset:20992\n" \
  "s_mov_b32 s72, 0xc000\n" \
  "s_add_u32 m0, s75, 0\n" \
  "v_mul_f32_e64 v52, v52, v249\n" \
  "v_mfma_f32_16x16x32_bf16 v[84:87], a[136:139], a[228:231], v[84:87]\n" \
  "buffer_load_dwordx4 v220, s[36:39], s72 offen lds\n" \
  "v_mfma_f32_16x16x32_bf16 v[88:91], a[140:143], a[224:227], v[88:91]\n" \
  "ds_read_b64_tr_b16 a[72:73], v234 offset:16384\n" \
  "ds_read_b64_tr_b16 a[74:75], v234 offset:16896\n" \
  "v_mfma_f32_16x16x32_bf16 v[92:95], a[140:143], a[228:231], v[92:95]\n" \
  "ds_read_b64_tr_b16 a[104:105], v234 offset:20480\n" \
  "v_mul_f32_e64 v53, v53, v249\n" \
  "v_mfma_f32_16x16x32_bf16 v[96:99], a[144:147], a[224:227], v[96:99]\n" \
  "ds_read_b64_tr_b16 a[106:107], v234 offset:20992\n" \
  "v_mul_f32_e64 v54, v54, v249\n" \
  "v_mfma_f32_16x16x32_bf16 v[100:103], a[144:147], a[228:231], v[100:103]\n" \
  "ds_read_b64_tr_b16 a[76:77], v235 offset:16384\n" \
  "v_mul_f32_e64 v55, v55, v249\n" \
  "v_mfma_f32_16x16x32_bf16 v[104:107], a[148:151], a[224:227], v[104:107]\n" \
  "ds_read_b64_tr_b16 a[78:79], v235 offset:16896\n" \
  "v_cvt_pk_bf16_f32 v52, v52, v53\n" \
  "v_mfma_f32_16x16x32_bf16 v[108:111], a[148:151], a[228:231], v[108:111]\n" \
  "ds_read_b64_tr_b16 a[108:109], v235 offset:20480\n" \
  "ds_read_b64_tr_b16 a[110:111], v235 offset:20992\n" \
  "v_mfma_f32_16x16x32_bf16 v[112:115], a[152:155], a[224:227], v[112:115]\n" \
  "ds_read_b64_tr_b16 a[80:81], v232 offset:24576\n" \
  "v_cvt_pk_bf16_f32 v53, v54, v55\n" \
  "v_mfma_f32_16x16x32_bf16 v[116:119], a[152:155], a[228:231], v[116:119]\n" \
  "buffer_load_dwordx4 v221, s[36:39], s72 offen offset:1024 lds\n" \
  "v_mul_f32_e64 v60, v60, v249\n" \
  "v_mfma_f32_16x16x32_bf16 v[120:123], a[156:159], a[224:227], v[120:123]\n" \
  "ds_read_b64_tr_b16 a[82:83], v232 offset:25088\n" \
  "v_mul_f32_e64 v61, v61, v249\n" \
  "v_mfma_f32_16x16x32_bf16 v[124:127], a[156:159], a[228:231], v[124:127]\n" \
  "ds_read_b64_tr_b16 a[112:113], v232 offset:28672\n" \
  "v_mul_f32_e64 v62, v62, v249\n" \
  "v_mfma_f32_16x16x32_bf16 v[64:67], a[160:163], a[232:235], v[64:67]\n" \
  "ds_read_b64_tr_b16 a[114:115], v232 offset:29184\n" \
  "v_mul_f32_e64 v63, v63, v249\n" \
  "v_mfma_f32_16x16x32_bf16 v[68:71], a[160:163], a[236:239], v[68:71]\n" \
  "ds_read_b64_tr_b16 a[84:85], v233 offset:24576\n" \
  "ds_read_b64_tr_b16 a[86:87], v233 offset:25088\n" \
  "v_mfma_f32_16x16x32_bf16 v[72:75], a[164:167], a[232:235], v[72:75]\n" \
  "ds_read_b64_tr_b16 a[116:117], v233 offset:28672\n" \
  "v_cvt_pk_bf16_f32 v60, v60, v61\n" \
  "v_mfma_f32_16x16x32_bf16 v[76:79], a[164:167], a[236:239], v[76:79]\n" \
  "ds_read_b64_tr_b16 a[118:119], v233 offset:29184\n" \
  "v_cvt_pk_bf16_f32 v61, v62, v63\n" \
  "v_mfma_f32_16x16x32_bf16 v[80:83], a[168:171], a[232:235], v[80:83]\n" \
  "ds_read_b64_tr_b16 a[88:89], v234 offset:24576\n" \
  "v_permlane32_swap_b32 v52, v60\n" \
  "v_mfma_f32_16x16x32_bf16 v[84:87], a[168:171], a[236:239], v[84:87]\n" \
  "buffer_load_dwordx4 v222, s[36:39], s72 offen offset:2048 lds\n" \
  "v_mfma_f32_16x16x32_bf16 v[88:91], a[172:175], a[232:235], v[88:91]\n" \
  "ds_read_b64_tr_b16 a[90:91], v234 offset:25088\n" \
  "v_permlane32_swap_b32 v53, v61\n" \
  "v_mfma_f32_16x16x32_bf16 v[92:95], a[172:175], a[236:239], v[92:95]\n" \
  "ds_read_b64_tr_b16 a[120:121], v234 offset:28672\n" \
  "ds_read_b64_tr_b16 a[122:123], v234 offset:29184\n" \
  "v_permlane16_swap_b32 v52, v60\n" \
  "v_mfma_f32_16x16x32_bf16 v[96:99], a[176:179], a[232:235], v[96:99]\n" \
  "ds_read_b64_tr_b16 a[92:93], v235 offset:24576\n" \
  "v_permlane16_swap_b32 v53, v61\n" \
  "v_mfma_f32_16x16x32_bf16 v[100:103], a[176:179], a[236:239], v[100:103]\n" \
  "ds_read_b64_tr_b16 a[94:95], v235 offset:25088\n" \
  "v_accvgpr_write_b32 a44, v52\n" \
  "v_mfma_f32_16x16x32_bf16 v[104:107], a[180:183], a[232:235], v[104:107]\n" \
  "ds_read_b64_tr_b16 a[124:125], v235 offset:28672\n" \
  "v_accvgpr_write_b32 a45, v53\n" \
  "v_mfma_f32_16x16x32_bf16 v[108:111], a[180:183], a[236:239], v[108:111]\n" \
  "ds_read_b64_tr_b16 a[126:127], v235 offset:29184\n" \
  "s_waitcnt lgkmcnt(7)\n" \
  "v_accvgpr_write_b32 a46, v60\n" \
  "v_mfma_f32_16x16x32_bf16 v[112:115], a[184:187], a[232:235], v[112:115]\n" \
  "v_accvgpr_write_b32 a47, v61\n" \
  "v_mfma_f32_16x16x32_bf16 v[116:119], a[184:187], a[236:239], v[116:119]\n" \
  "buffer_load_dwordx4 v223, s[36:39], s72 offen offset:3072 lds\n" \
  "v_mfma_f32_16x16x32_bf16 v[120:123], a[188:191], a[232:235], v[120:123]\n" \
  "v_mfma_f32_16x16x32_bf16 v[124:127], a[188:191], a[236:239], v[124:127]\n" \
  "s_waitcnt vmcnt(17) lgkmcnt(0)\n" \
  "s_barrier\n" \
  "; HP [27, 28] ('Q', 1, 6)\n" \
  "v_mfma_f32_16x16x32_bf16 v[64:67], a[64:67], a[240:243], v[64:67]\n" \
  "ds_read_b128 a[128:131], v208 offset:32768\n" \
  "v_readlane_b32 s94, v255, 54\n" \
  "v_mfma_f32_16x16x32_bf16 v[68:71], a[64:67], a[244:247], v[68:71]\n" \
  "ds_read_b128 a[160:163], v208 offset:40960\n" \
  "v_lshlrev_b32 v234, 6, v253\n" \
  "v_mfma_f32_16x16x32_bf16 v[64:67], a[96:99], a[248:251], v[64:67]\n" \
  "s_mov_b32 s73, 0x8000\n" \
  "v_add_u32_e64 v234, s94, v234\n" \
  "v_readlane_b32 s95, v255, 36\n" \
  "v_mfma_f32_16x16x32_bf16 v[68:71], a[96:99], a[252:255], v[68:71]\n" \
  "ds_read_b128 a[132:135], v209 offset:32768\n" \
  "v_lshlrev_b32_e64 v235, 6, v253\n" \
  "v_mfma_f32_16x16x32_bf16 v[72:75], a[68:71], a[240:243], v[72:75]\n" \
  "ds_read_b128 a[164:167], v209 offset:40960\n" \
  "s_add_u32 m0, s74, 0x4000\n" \
  "v_add_u32_e64 v235, s95, v235\n" \
  "v_mfma_f32_16x16x32_bf16 v[76:79], a[68:71], a[244:247], v[76:79]\n" \
  "buffer_load_dwordx4 v224, s[40:43], s73 offen lds\n" \
  "v_mfma_f32_16x16x32_bf16 v[72:75], a[100:103], a[248:251], v[72:75]\n" \
  "s_mov_b32 s72, 0x10000\n" \
  "v_min_u32 v235, L_rare0_%=-L_begin_%=, v235\n" \
  "v_mov_b32_e64 v244, v250\n" \
  "v_mfma_f32_16x16x32_bf16 v[76:79], a[100:103], a[252:255], v[76:79]\n" \
  "ds_read_b128 a[136:139], v210 offset:32768\n" \
  "v_add_u32 v245, 0x38000, v250\n" \
  "v_mfma_f32_16x16x32_bf16 v[80:83], a[72:75], a[240:243], v[80:83]\n" \
  "v_readlane_b32 s94, v255, 52\n" \
  "v_and_b32_e64 v242, 15, v253\n" \
  "v_mfma_f32_16x16x32_bf16 v[84:87], a[72:75], a[244:247], v[84:87]\n" \
  "ds_read_b128 a[168:171], v210 offset:40960\n" \
  "v_lshlrev_b32_e64 v242, 7, v242\n" \
  "v_mfma_f32_16x16x32_bf16 v[80:83], a[104:107], a[248:251], v[80:83]\n" \
  "ds_read_b128 a[140:143], v211 offset:32768\n" \
  "v_add_u32_e64 v242, s94, v242\n" \
  "v_mfma_f32_16x16x32_bf16 v[84:87], a[104:107], a[252:255], v[84:87]\n" \
  "v_mfma_f32_16x16x32_bf16 v[88:91], a[76:79], a[240:243], v[88:91]\n" \
  "ds_read_b128 a[172:175], v211 offset:40960\n" \
  "v_mul_f32_e64 v64, v64, v248\n" \
  "v_mfma_f32_16x16x32_bf16 v[92:95], a[76:79], a[244:247], v[92:95]\n" \
  "buffer_load_dwordx4 v225, s[40:43], s73 offen offset:1024 lds\n" \
  "v_mul_f32_e64 v68, v68, v249\n" \
  "v_mfma_f32_16x16x32_bf16 v[88:91], a[108:111], a[248:251], v[88:91]\n" \
  "v_mul_f32 v65, v65, v248\n" \
  "v_mul_f32 v69, v69, v249\n" \
  "v_mfma_f32_16x16x32_bf16 v[92:95], a[108:111], a[252:255], v[92:95]\n" \
  "ds_read_b128 a[144:147], v208 offset:33024\n" \
  "v_mul_f32_e64 v66, v66, v248\n" \
  "v_mfma_f32_16x16x32_bf16 v[96:99], a[80:83], a[240:243], v[96:99]\n" \
  "ds_read_b128 a[176:179], v208 offset:41216\n" \
  "v_mul_f32_e64 v70, v70, v249\n" \
  "v_mfma_f32_16x16x32_bf16 v[100:103], a[80:83], a[244:247], v[100:103]\n" \
  "v_mul_f32 v67, v67, v248\n" \
  "v_mul_f32 v71, v71, v249\n" \
  "v_mfma_f32_16x16x32_bf16 v[96:99], a[112:115], a[248:251], v[96:99]\n" \
  "ds_read_b128 a[148:151], v209 offset:33024\n" \
  "v_cvt_pk_bf16_f32 v64, v64, v65\n" \
  "v_mfma_f32_16x16x32_bf16 v[100:103], a[112:115], a[252:255], v[100:103]\n" \
  "v_cvt_pk_bf16_f32 v68, v68, v69\n" \
  "v_cvt_pk_bf16_f32 v65, v66, v67\n" \
  "v_mfma_f32_16x16x32_bf16 v[104:107], a[84:87], a[240:243], v[104:107]\n" \
  "ds_read_b128 a[180:183], v209 offset:41216\n" \
  "v_cvt_pk_bf16_f32 v69, v70, v71\n" \
  "v_mul_f32_e64 v80, v80, v248\n" \
  "v_mfma_f32_16x16x32_bf16 v[108:111], a[84:87], a[244:247], v[108:111]\n" \
  "buffer_load_dwordx4 v226, s[40:43], s73 offen offset:2048 lds\n" \
  "v_mul_f32_e64 v84, v84, v249\n" \
  "v_mfma_f32_16x16x32_bf16 v[104:107], a[116:119], a[248:251], v[104:107]\n" \
  "ds_read_b128 a[152:155], v210 offset:33024\n" \
  "v_mul_f32 v72, v72, v248\n" \
  "v_mul_f32 v81, v81, v248\n" \
  "v_mfma_f32_16x16x32_bf16 v[108:111], a[116:119], a[252:255], v[108:111]\n" \
  "v_mul_f32 v76, v76, v249\n" \
  "v_mul_f32 v73, v73, v248\n" \
  "v_mul_f32 v85, v85, v249\n" \
  "v_mul_f32 v82, v82, v248\n" \
  "v_mfma_f32_16x16x32_bf16 v[112:115], a[88:91], a[240:243], v[112:115]\n" \
  "ds_read_b128 a[184:187], v210 offset:41216\n" \
  "v_mul_f32 v77, v77, v249\n" \
  "v_mul_f32 v86, v86, v249\n" \
  "v_mfma_f32_16x16x32_bf16 v[116:119], a[88:91], a[244:247], v[116:119]\n" \
  "v_mul_f32 v74, v74, v248\n" \
  "v_mul_f32 v78, v78, v249\n" \
  "v_mul_f32_e64 v83, v83, v248\n" \
  "v_mfma_f32_16x16x32_bf16 v[112:115], a[120:123], a[248:251], v[112:115]\n" \
  "ds_read_b128 a[156:159], v211 offset:33024\n" \
  "v_mul_f32 v75, v75, v248\n" \
  "v_mul_f32 v87, v87, v249\n" \
  "v_cvt_pk_bf16_f32 v80, v80, v81\n" \
  "v_mfma_f32_16x16x32_bf16 v[116:119], a[120:123], a[252:255], v[116:119]\n" \
  "ds_read_b128 a[188:191], v211 offset:41216\n" \
  "v_mul_f32_e64 v79, v79, v249\n" \
  "v_cvt_pk_bf16_f32 v84, v84, v85\n" \
  "v_mfma_f32_16x16x32_bf16 v[120:123], a[92:95], a[240:243], v[120:123]\n" \
  "s_waitcnt lgkmcnt(3)\n" \
  "v_cvt_pk_bf16_f32 v72, v72, v73\n" \
  "v_cvt_pk_bf16_f32 v76, v76, v77\n" \
  "v_cvt_pk_bf16_f32 v81, v82, v83\n" \
  "v_mul_f32 v96, v96, v248\n" \
  "v_mfma_f32_16x16x32_bf16 v[124:127], a[92:95], a[244:247], v[124:127]\n" \
  "buffer_load_dwordx4 v227, s[40:43], s73 offen offset:3072 lds\n" \
  "v_cvt_pk_bf16_f32 v85, v86, v87\n" \
  "v_mul_f32_e64 v100, v100, v249\n" \
  "v_mfma_f32_16x16x32_bf16 v[120:123], a[124:127], a[248:251], v[120:123]\n" \
  "v_subrev_u32 v208, 0x10000, v208\n" \
  "v_subrev_u32 v209, 0x10000, v209\n" \
  "v_subrev_u32 v210, 0x10000, v210\n" \
  "v_subrev_u32 v211, 0x10000, v211\n" \
  "ds_read_b128 a[64:67], v208 offset:32768\n" \
  "v_cvt_pk_bf16_f32 v73, v74, v75\n" \
  "v_mul_f32 v88, v88, v248\n" \
  "v_mul_f32 v92, v92, v249\n" \
  "v_mul_f32 v97, v97, v248\n" \
  "v_mul_f32 v101, v101, v249\n" \
  "v_mfma_f32_16x16x32_bf16 v[124:127], a[124:127], a[252:255], v[124:127]\n" \
  "s_add_u32 m0, s75, 0x4000\n" \
  "v_cvt_pk_bf16_f32 v77, v78, v79\n" \
  "v_permlane32_swap_b32 v64, v72\n" \
  "v_mul_f32 v89, v89, v248\n" \
  "v_mul_f32_e64 v98, v98, v248\n" \
  "v_mfma_f32_16x16x32_bf16 v[128:131], a[128:131], a[0:3], 0\n" \
  "ds_read_b128 a[68:71], v209 offset:32768\n" \
  "v_permlane32_swap_b32 v68, v76\n" \
  "v_mul_f32 v93, v93, v249\n" \
  "v_mul_f32 v102, v102, v249\n" \
  "v_mul_f32 v99, v99, v248\n" \
  "v_mfma_f32_16x16x32_bf16 v[132:135], a[128:131], a[32:35], 0\n" \
  "ds_read_b128 a[72:75], v210 offset:32768\n" \
  "v_permlane32_swap_b32 v65, v73\n" \
  "v_mul_f32 v90, v90, v248\n" \
  "v_mul_f32 v94, v94, v249\n" \
  "v_mul_f32 v103, v103, v249\n" \
  "v_mfma_f32_16x16x32_bf16 v[136:139], a[160:163], a[0:3], 0\n" \
  "v_permlane32_swap_b32 v69, v77\n" \
  "v_permlane16_swap_b32 v64, v72\n" \
  "v_mul_f32_e64 v91, v91, v248\n" \
  "v_cvt_pk_bf16_f32 v96, v96, v97\n" \
  "v_cvt_pk_bf16_f32 v100, v100, v101\n" \
  "v_mfma_f32_16x16x32_bf16 v[140:143], a[160:163], a[32:35], 0\n" \
  "ds_read_b128 a[76:79], v211 offset:32768\n" \
  "v_permlane16_swap_b32 v68, v76\n" \
  "v_mul_f32 v95, v95, v249\n" \
  "v_cvt_pk_bf16_f32 v97, v98, v99\n" \
  "v_mfma_f32_16x16x32_bf16 v[128:131], a[132:135], a[4:7], v[128:131]\n" \
  "v_permlane16_swap_b32 v65, v73\n" \
  "v_permlane16_swap_b32 v69, v77\n" \
  "v_cvt_pk_bf16_f32 v88, v88, v89\n" \
  "v_cvt_pk_bf16_f32 v101, v102, v103\n" \
  "v_mul_f32 v104, v104, v248\n" \
  "v_mul_f32 v112, v112, v248\n" \
  "v_mfma_f32_16x16x32_bf16 v[132:135], a[132:135], a[36:39], v[132:135]\n" \
  "buffer_load_dwordx4 v220, s[36:39], s72 offen lds\n" \
  "v_cvt_pk_bf16_f32 v92, v92, v93\n" \
  "v_cvt_pk_bf16_f32 v89, v90, v91\n" \
  "v_mul_f32 v108, v108, v249\n" \
  "v_mul_f32 v116, v116, v249\n" \
  "v_mul_f32_e64 v113, v113, v248\n" \
  "v_mfma_f32_16x16x32_bf16 v[136:139], a[164:167], a[4:7], v[136:139]\n" \
  "ds_read_b128 a[80:83], v208 offset:33024\n" \
  "v_accvgpr_write_b32 a16, v64\n" \
  "v_cvt_pk_bf16_f32 v93, v94, v95\n" \
  "v_mul_f32 v105, v105, v248\n" \
  "v_mul_f32 v109, v109, v249\n" \
  "v_mul_f32 v117, v117, v249\n" \
  "v_mul_f32 v114, v114, v248\n" \
  "v_mfma_f32_16x16x32_bf16 v[140:143], a[164:167], a[36:39], v[140:143]\n" \
  "ds_read_b128 a[84:87], v209 offset:33024\n" \
  "v_accvgpr_write_b32 a48, v68\n" \
  "v_permlane32_swap_b32 v80, v88\n" \
  "v_mul_f32 v106, v106, v248\n" \
  "v_mul_f32_e64 v118, v118, v249\n" \
  "v_mfma_f32_16x16x32_bf16 v[128:131], a[136:139], a[8:11], v[128:131]\n" \
  "v_accvgpr_write_b32 a17, v65\n" \
  "v_accvgpr_write_b32 a49, v69\n" \
  "v_permlane32_swap_b32 v84, v92\n" \
  "v_permlane32_swap_b32 v81, v89\n" \
  "v_mul_f32 v110, v110, v249\n" \
  "v_mul_f32 v107, v107, v248\n" \
  "v_mul_f32 v115, v115, v248\n" \
  "v_mul_f32 v119, v119, v249\n" \
  "v_mfma_f32_16x16x32_bf16 v[132:135], a[136:139], a[40:43], v[132:135]\n" \
  "ds_read_b128 a[88:91], v210 offset:33024\n" \
  "v_accvgpr_write_b32 a18, v72\n" \
  "v_permlane32_swap_b32 v85, v93\n" \
  "v_mul_f32 v111, v111, v249\n" \
  "v_cvt_pk_bf16_f32 v112, v112, v113\n" \
  "v_cvt_pk_bf16_f32 v116, v116, v117\n" \
  "v_mfma_f32_16x16x32_bf16 v[136:139], a[168:171], a[8:11], v[136:139]\n" \
  "v_accvgpr_write_b32 a50, v76\n" \
  "v_accvgpr_write_b32 a19, v73\n" \
  "v_permlane16_swap_b32 v80, v88\n" \
  "v_cvt_pk_bf16_f32 v104, v104, v105\n" \
  "v_cvt_pk_bf16_f32 v108, v108, v109\n" \
  "v_cvt_pk_bf16_f32 v113, v114, v115\n" \
  "v_cvt_pk_bf16_f32 v117, v118, v119\n" \
  "v_mfma_f32_16x16x32_bf16 v[140:143], a[168:171], a[40:43], v[140:143]\n" \
  "ds_read_b128 a[92:95], v211 offset:33024\n" \
  "v_accvgpr_write_b32 a51, v77\n" \
  "v_permlane16_swap_b32 v84, v92\n" \
  "v_cvt_pk_bf16_f32 v105, v106, v107\n" \
  "v_mul_f32_e64 v120, v120, v248\n" \
  "v_mfma_f32_16x16x32_bf16 v[128:131], a[140:143], a[12:15], v[128:131]\n" \
  "ds_read_b128 a[96:99], v208 offset:40960\n" \
  "v_permlane16_swap_b32 v81, v89\n" \
  "v_permlane16_swap_b32 v85, v93\n" \
  "v_cvt_pk_bf16_f32 v109, v110, v111\n" \
  "v_permlane32_swap_b32 v96, v104\n" \
  "v_mul_f32 v124, v124, v249\n" \
  "v_mul_f32_e64 v121, v121, v248\n" \
  "v_mfma_f32_16x16x32_bf16 v[132:135], a[140:143], a[44:47], v[132:135]\n" \
  "buffer_load_dwordx4 v221, s[36:39], s72 offen offset:1024 lds\n" \
  "v_accvgpr_write_b32 a20, v80\n" \
  "v_permlane32_swap_b32 v100, v108\n" \
  "v_mul_f32 v125, v125, v249\n" \
  "v_mul_f32_e64 v122, v122, v248\n" \
  "v_mfma_f32_16x16x32_bf16 v[136:139], a[172:175], a[12:15], v[136:139]\n" \
  "v_accvgpr_write_b32 a52, v84\n" \
  "v_accvgpr_write_b32 a21, v81\n" \
  "v_permlane32_swap_b32 v97, v105\n" \
  "v_permlane32_swap_b32 v101, v109\n" \
  "v_mul_f32 v126, v126, v249\n" \
  "v_mul_f32 v123, v123, v248\n" \
  "v_mfma_f32_16x16x32_bf16 v[140:143], a[172:175], a[44:47], v[140:143]\n" \
  "ds_read_b128 a[100:103], v209 offset:40960\n" \
  "v_accvgpr_write_b32 a53, v85\n" \
  "v_permlane16_swap_b32 v96, v104\n" \
  "v_mul_f32 v127, v127, v249\n" \
  "v_mfma_f32_16x16x32_bf16 v[128:131], a[144:147], a[16:19], v[128:131]\n" \
  "v_accvgpr_write_b32 a22, v88\n" \
  "v_accvgpr_write_b32 a54, v92\n" \
  "v_permlane16_swap_b32 v100, v108\n" \
  "v_permlane16_swap_b32 v97, v105\n" \
  "v_cvt_pk_bf16_f32 v120, v120, v121\n" \
  "v_cvt_pk_bf16_f32 v124, v124, v125\n" \
  "v_mfma_f32_16x16x32_bf16 v[132:135], a[144:147], a[48:51], v[132:135]\n" \
  "ds_read_b128 a[104:107], v210 offset:40960\n" \
  "v_accvgpr_write_b32 a23, v89\n" \
  "v_permlane16_swap_b32 v101, v109\n" \
  "v_cvt_pk_bf16_f32 v121, v122, v123\n" \
  "v_cvt_pk_bf16_f32 v125, v126, v127\n" \
  "v_mfma_f32_16x16x32_bf16 v[136:139], a[176:179], a[16:19], v[136:139]\n" \
  "ds_read_b128 a[108:111], v211 offset:40960\n" \
  "v_accvgpr_write_b32 a55, v93\n" \
  "v_accvgpr_write_b32 a24, v96\n" \
  "v_accvgpr_write_b32 a56, v100\n" \
  "v_permlane32_swap_b32 v112, v120\n" \
  "v_permlane32_swap_b32 v116, v124\n" \
  "v_mfma_f32_16x16x32_bf16 v[140:143], a[176:179], a[48:51], v[140:143]\n" \
  "v_accvgpr_write_b32 a25, v97\n" \
  "v_accvgpr_write_b32 a57, v101\n" \
  "v_permlane32_swap_b32 v113, v121\n" \
  "v_mfma_f32_16x16x32_bf16 v[128:131], a[148:151], a[20:23], v[128:131]\n" \
  "ds_read_b128 a[112:115], v208 offset:41216\n" \
  "v_accvgpr_write_b32 a26, v104\n" \
  "v_permlane32_swap_b32 v117, v125\n" \
  "v_permlane16_swap_b32 v112, v120\n" \
  "v_mfma_f32_16x16x32_bf16 v[132:135], a[148:151], a[52:55], v[132:135]\n" \
  "buffer_load_dwordx4 v222, s[36:39], s72 offen offset:2048 lds\n" \
  "v_accvgpr_write_b32 a58, v108\n" \
  "v_permlane16_swap_b32 v116, v124\n" \
  "v_permlane16_swap_b32 v113, v121\n" \
  "v_mfma_f32_16x16x32_bf16 v[136:139], a[180:183], a[20:23], v[136:139]\n" \
  "v_accvgpr_write_b32 a27, v105\n" \
  "v_accvgpr_write_b32 a59, v109\n" \
  "v_permlane16_swap_b32 v117, v125\n" \
  "v_accvgpr_write_b32 a28, v112\n" \
  "v_mfma_f32_16x16x32_bf16 v[140:143], a[180:183], a[52:55], v[140:143]\n" \
  "ds_read_b128 a[116:119], v209 offset:41216\n" \
  "v_accvgpr_write_b32 a60, v116\n" \
  "v_mfma_f32_16x16x32_bf16 v[128:131], a[152:155], a[24:27], v[128:131]\n" \
  "ds_read_b128 a[120:123], v210 offset:41216\n" \
  "v_accvgpr_write_b32 a29, v113\n" \
  "v_accvgpr_write_b32 a61, v117\n" \
  "v_mfma_f32_16x16x32_bf16 v[132:135], a[152:155], a[56:59], v[132:135]\n" \
  "s_waitcnt lgkmcnt(3)\n" \
  "v_accvgpr_write_b32 a30, v120\n" \
  "v_accvgpr_write_b32 a62, v124\n" \
  "v_mfma_f32_16x16x32_bf16 v[136:139], a[184:187], a[24:27], v[136:139]\n" \
  "ds_read_b128 a[124:127], v211 offset:41216\n" \
  "v_accvgpr_write_b32 a31, v121\n" \
  "v_accvgpr_write_b32 a63, v125\n" \
  "v_mfma_f32_16x16x32_bf16 v[140:143], a[184:187], a[56:59], v[140:143]\n" \
  "v_mfma_f32_16x16x32_bf16 v[128:131], a[156:159], a[28:31], v[128:131]\n" \
  "v_mfma_f32_16x16x32_bf16 v[132:135], a[156:159], a[60:63], v[132:135]\n" \
  "buffer_load_dwordx4 v223, s[36:39], s72 offen offset:3072 lds\n" \
  "v_mfma_f32_16x16x32_bf16 v[136:139], a[188:191], a[28:31], v[136:139]\n" \
  "v_mfma_f32_16x16x32_bf16 v[140:143], a[188:191], a[60:63], v[140:143]\n" \
  "s_waitcnt vmcnt(17) lgkmcnt(0)\n" \
  "s_barrier\n" \
  "; attn phase 0\n" \
  "v_mfma_f32_16x16x32_bf16 v[144:147], a[64:67], a[0:3], 0\n" \
  "ds_read_b64_tr_b16 a[128:129], v212 offset:49152\n" \
  "s_mov_b32 s73, 0xc000\n" \
  "v_mfma_f32_16x16x32_bf16 v[148:151], a[64:67], a[32:35], 0\n" \
  "ds_read_b64_tr_b16 a[130:131], v212 offset:57344\n" \
  "s_add_u32 m0, s74, 0x8000\n" \
  "v_mfma_f32_16x16x32_bf16 v[144:147], a[68:71], a[4:7], v[144:147]\n" \
  "ds_read_b64_tr_b16 a[132:133], v213 offset:49152\n" \
  "v_mov_b32 v172, 0x3f803f80\n" \
  "v_mov_b32 v173, 0x3f803f80\n" \
  "v_mfma_f32_16x16x32_bf16 v[148:151], a[68:71], a[36:39], v[148:151]\n" \
  "ds_read_b64_tr_b16 a[134:135], v213 offset:57344\n" \
  "v_mov_b32 v174, 0x3f803f80\n" \
  "v_mov_b32 v175, 0x3f803f80\n" \
  "v_mfma_f32_16x16x32_bf16 v[144:147], a[72:75], a[8:11], v[144:147]\n" \
  "ds_read_b64_tr_b16 a[136:137], v214 offset:49152\n" \
  "v_max3_f32 v168, v128, v129, v130\n" \
  "v_max3_f32 v168, v168, v131, v136\n" \
  "v_max3_f32 v168, v168, v137, v138\n" \
  "v_mfma_f32_16x16x32_bf16 v[148:151], a[72:75], a[40:43], v[148:151]\n" \
  "buffer_load_dwordx4 v224, s[40:43], s73 offen lds\n" \
  "v_max_f32 v168, v168, v139\n" \
  "v_mov_b32 v170, v168\n" \
  "v_mfma_f32_16x16x32_bf16 v[144:147], a[76:79], a[12:15], v[144:147]\n" \
  "ds_read_b64_tr_b16 a[138:139], v214 offset:57344\n" \
  "v_permlane32_swap_b32 v168, v170\n" \
  "v_max_f32 v168, v168, v170\n" \
  "v_mfma_f32_16x16x32_bf16 v[148:151], a[76:79], a[44:47], v[148:151]\n" \
  "ds_read_b64_tr_b16 a[140:141], v215 offset:49152\n" \
  "v_mov_b32_e64 v170, v168\n" \
  "v_max3_f32 v169, v132, v133, v134\n" \
  "v_max3_f32 v169, v169, v135, v140\n" \
  "v_mfma_f32_16x16x32_bf16 v[144:147], a[80:83], a[16:19], v[144:147]\n" \
  "s_mov_b32 s72, 0x14000\n" \
  "v_permlane16_swap_b32 v168, v170\n" \
  "v_max_f32 v168, v168, v170\n" \
  "v_mfma_f32_16x16x32_bf16 v[148:151], a[80:83], a[48:51], v[148:151]\n" \
  "ds_read_b64_tr_b16 a[142:143], v215 offset:57344\n" \
  "v_add_f32 v168, 0x41a00000, v168\n" \
  "v_max3_f32 v169, v169, v141, v142\n" \
  "v_mfma_f32_16x16x32_bf16 v[144:147], a[84:87], a[20:23], v[144:147]\n" \
  "ds_read_b64_tr_b16 a[144:145], v216 offset:49152\n" \
  "v_max_f32 v169, v169, v143\n" \
  "v_mov_b32 v171, v169\n" \
  "v_sub_f32_e64 v128, v128, v168\n" \
  "v_mfma_f32_16x16x32_bf16 v[148:151], a[84:87], a[52:55], v[148:151]\n" \
  "ds_read_b64_tr_b16 a[146:147], v216 offset:57344\n" \
  "v_permlane32_swap_b32 v169, v171\n" \
  "v_max_f32 v169, v169, v171\n" \
  "v_mfma_f32_16x16x32_bf16 v[144:147], a[88:91], a[24:27], v[144:147]\n" \
  "ds_read_b64_tr_b16 a[148:149], v217 offset:49152\n" \
  "v_mov_b32 v171, v169\n" \
  "v_sub_f32 v129, v129, v168\n" \
  "v_mfma_f32_16x16x32_bf16 v[148:151], a[88:91], a[56:59], v[148:151]\n" \
  "ds_read_b64_tr_b16 a[150:151], v217 offset:57344\n" \
  "v_permlane16_swap_b32 v169, v171\n" \
  "v_max_f32 v169, v169, v171\n" \
  "v_add_f32 v169, 0x41a00000, v169\n" \
  "v_mfma_f32_16x16x32_bf16 v[144:147], a[92:95], a[28:31], v[144:147]\n" \
  "buffer_load_dwordx4 v225, s[40:43], s73 offen offset:1024 lds\n" \
  "v_sub_f32 v130, v130, v168\n" \
  "v_sub_f32 v131, v131, v168\n" \
  "v_mfma_f32_16x16x32_bf16 v[148:151], a[92:95], a[60:63], v[148:151]\n" \
  "ds_read_b64_tr_b16 a[152:153], v218 offset:49152\n" \
  "v_sub_f32 v132, v132, v169\n" \
  "v_sub_f32 v133, v133, v169\n" \
  "v_sub_f32_e64 v134, v134, v169\n" \
  "v_mfma_f32_16x16x32_bf16 v[152:155], a[96:99], a[0:3], 0\n" \
  "ds_read_b64_tr_b16 a[154:155], v218 offset:57344\n" \
  "v_sub_f32 v135, v135, v169\n" \
  "v_sub_f32 v136, v136, v168\n" \
  "v_mfma_f32_16x16x32_bf16 v[156:159], a[96:99], a[32:35], 0\n" \
  "v_sub_f32 v137, v137, v168\n" \
  "v_sub_f32 v138, v138, v168\n" \
  "v_mfma_f32_16x16x32_bf16 v[152:155], a[100:103], a[4:7], v[152:155]\n" \
  "ds_read_b64_tr_b16 a[156:157], v219 offset:49152\n" \
  "v_sub_f32 v139, v139, v168\n" \
  "v_sub_f32 v140, v140, v169\n" \
  "v_sub_f32_e64 v141, v141, v169\n" \
  "v_mfma_f32_16x16x32_bf16 v[156:159], a[100:103], a[36:39], v[156:159]\n" \
  "ds_read_b64_tr_b16 a[158:159], v219 offset:57344\n" \
  "v_sub_f32 v142, v142, v169\n" \
  "v_sub_f32 v143, v143, v169\n" \
  "v_mfma_f32_16x16x32_bf16 v[152:155], a[104:107], a[8:11], v[152:155]\n" \
  "ds_read_b64_tr_b16 a[160:161], v212 offset:49408\n" \
  "v_exp_f32 v192, v128\n" \
  "v_exp_f32 v193, v129\n" \
  "v_mfma_f32_16x16x32_bf16 v[156:159], a[104:107], a[40:43], v[156:159]\n" \
  "ds_read_b64_tr_b16 a[162:163], v212 offset:57600\n" \
  "v_cvt_pk_bf16_f32 v176, v192, v193\n" \
  "v_exp_f32 v194, v130\n" \
  "v_exp_f32 v195, v131\n" \
  "v_mfma_f32_16x16x32_bf16 v[152:155], a[108:111], a[12:15], v[152:155]\n" \
  "buffer_load_dwordx4 v226, s[40:43], s73 offen offset:2048 lds\n" \
  "v_exp_f32 v196, v132\n" \
  "v_exp_f32 v197, v133\n" \
  "v_mfma_f32_16x16x32_bf16 v[156:159], a[108:111], a[44:47], v[156:159]\n" \
  "ds_read_b64_tr_b16 a[164:165], v213 offset:49408\n" \
  "v_cvt_pk_bf16_f32 v177, v194, v195\n" \
  "v_exp_f32_e64 v198, v134\n" \
  "v_mfma_f32_16x16x32_bf16 v[152:155], a[112:115], a[16:19], v[152:155]\n" \
  "ds_read_b64_tr_b16 a[166:167], v213 offset:57600\n" \
  "v_cvt_pk_bf16_f32 v180, v196, v197\n" \
  "v_exp_f32 v199, v135\n" \
  "v_exp_f32 v192, v136\n" \
  "v_mfma_f32_16x16x32_bf16 v[156:159], a[112:115], a[48:51], v[156:159]\n" \
  "ds_read_b64_tr_b16 a[168:169], v214 offset:49408\n" \
  "v_cvt_pk_bf16_f32 v181, v198, v199\n" \
  "v_exp_f32_e64 v193, v137\n" \
  "v_mfma_f32_16x16x32_bf16 v[152:155], a[116:119], a[20:23], v[152:155]\n" \
  "s_waitcnt lgkmcnt(4)\n" \
  "v_exp_f32 v194, v138\n" \
  "v_exp_f32_e64 v195, v139\n" \
  "v_mfma_f32_16x16x32_bf16 v[156:159], a[116:119], a[52:55], v[156:159]\n" \
  "ds_read_b64_tr_b16 a[170:171], v214 offset:57600\n" \
  "v_cvt_pk_bf16_f32 v178, v192, v193\n" \
  "v_exp_f32 v196, v140\n" \
  "v_exp_f32 v197, v141\n" \
  "v_mfma_f32_16x16x32_bf16 v[152:155], a[120:123], a[24:27], v[152:155]\n" \
  "ds_read_b64_tr_b16 a[172:173], v215 offset:49408\n" \
  "v_cvt_pk_bf16_f32 v179, v194, v195\n" \
  "v_exp_f32_e64 v198, v142\n" \
  "v_mfma_f32_16x16x32_bf16 v[156:159], a[120:123], a[56:59], v[156:159]\n" \
  "ds_read_b64_tr_b16 a[174:175], v215 offset:57600\n" \
  "v_cvt_pk_bf16_f32 v182, v196, v197\n" \
  "v_exp_f32 v199, v143\n" \
  "s_nop 0\n" \
  "v_cvt_pk_bf16_f32 v183, v198, v199\n" \
  "v_mfma_f32_16x16x32_bf16 v[152:155], a[124:127], a[28:31], v[152:155]\n" \
  "buffer_load_dwordx4 v227, s[40:43], s73 offen offset:3072 lds\n" \
  "v_mfma_f32_16x16x32_bf16 v[156:159], a[124:127], a[60:63], v[156:159]\n" \
  "ds_read_b64_tr_b16 a[176:177], v216 offset:49408\n" \
  "s_add_u32 m0, s75, 0x8000\n" \
  "v_mfma_f32_16x16x32_bf16 v[0:3], a[128:131], v[176:179], 0\n" \
  "ds_read_b64_tr_b16 a[178:179], v216 offset:57600\n" \
  "v_mfma_f32_16x16x32_bf16 v[4:7], a[128:131], v[180:183], 0\n" \
  "ds_read_b64_tr_b16 a[180:181], v217 offset:49408\n" \
  "v_mfma_f32_16x16x32_bf16 v[8:11], a[132:135], v[176:179], 0\n" \
  "ds_read_b64_tr_b16 a[182:183], v217 offset:57600\n" \
  "v_mfma_f32_16x16x32_bf16 v[12:15], a[132:135], v[180:183], 0\n" \
  "v_mfma_f32_16x16x32_bf16 v[16:19], a[136:139], v[176:179], 0\n" \
  "ds_read_b64_tr_b16 a[184:185], v218 offset:49408\n" \
  "v_mfma_f32_16x16x32_bf16 v[20:23], a[136:139], v[180:183], 0\n" \
  "ds_read_b64_tr_b16 a[186:187], v218 offset:57600\n" \
  "v_sub_f32_e64 v160, 0, v168\n" \
  "v_mfma_f32_16x16x32_bf16 v[24:27], a[140:143], v[176:179], 0\n" \
  "buffer_load_dwordx4 v220, s[36:39], s72 offen lds\n" \
  "v_sub_f32_e64 v161, 0, v168\n" \
  "v_mfma_f32_16x16x32_bf16 v[28:31], a[140:143], v[180:183], 0\n" \
  "ds_read_b64_tr_b16 a[188:189], v219 offset:49408\n" \
  "v_sub_f32_e64 v162, 0, v168\n" \
  "v_mfma_f32_16x16x32_bf16 v[32:35], a[144:147], v[176:179], 0\n" \
  "ds_read_b64_tr_b16 a[190:191], v219 offset:57600\n" \
  "v_sub_f32_e64 v163, 0, v168\n" \
  "v_mfma_f32_16x16x32_bf16 v[36:39], a[144:147], v[180:183], 0\n" \
  "ds_read_b128 a[64:67], v208 offset:49152\n" \
  "v_sub_f32 v164, 0, v169\n" \
  "v_sub_f32 v165, 0, v169\n" \
  "v_mfma_f32_16x16x32_bf16 v[40:43], a[148:151], v[176:179], 0\n" \
  "ds_read_b128 a[68:71], v209 offset:49152\n" \
  "v_sub_f32_e64 v166, 0, v169\n" \
  "v_mfma_f32_16x16x32_bf16 v[44:47], a[148:151], v[180:183], 0\n" \
  "ds_read_b128 a[72:75], v210 offset:49152\n" \
  "v_sub_f32_e64 v167, 0, v169\n" \
  "v_mfma_f32_16x16x32_bf16 v[48:51], a[152:155], v[176:179], 0\n" \
  "s_waitcnt lgkmcnt(5)\n" \
  "v_sub_f32 v144, v144, v168\n" \
  "v_sub_f32_e64 v145, v145, v168\n" \
  "v_mfma_f32_16x16x32_bf16 v[52:55], a[152:155], v[180:183], 0\n" \
  "ds_read_b128 a[76:79], v211 offset:49152\n" \
  "v_sub_f32_e64 v146, v146, v168\n" \
  "v_mfma_f32_16x16x32_bf16 v[56:59], a[156:159], v[176:179], 0\n" \
  "ds_read_b128 a[80:83], v208 offset:49408\n" \
  "v_sub_f32_e64 v147, v147, v168\n" \
  "v_mfma_f32_16x16x32_bf16 v[60:63], a[156:159], v[180:183], 0\n" \
  "buffer_load_dwordx4 v221, s[36:39], s72 offen offset:1024 lds\n" \
  "v_sub_f32_e64 v148, v148, v169\n" \
  "v_mfma_f32_16x16x32_bf16 v[64:67], a[160:163], v[176:179], 0\n" \
  "ds_read_b128 a[84:87], v209 offset:49408\n" \
  "v_sub_f32_e64 v149, v149, v169\n" \
  "v_mfma_f32_16x16x32_bf16 v[68:71], a[160:163], v[180:183], 0\n" \
  "ds_read_b128 a[88:91], v210 offset:49408\n" \
  "v_sub_f32 v150, v150, v169\n" \
  "v_sub_f32 v151, v151, v169\n" \
  "v_mfma_f32_16x16x32_bf16 v[72:75], a[164:167], v[176:179], 0\n" \
  "ds_read_b128 a[92:95], v211 offset:49408\n" \
  "v_sub_f32_e64 v152, v152, v168\n" \
  "v_mfma_f32_16x16x32_bf16 v[76:79], a[164:167], v[180:183], 0\n" \
  "ds_read_b128 a[96:99], v208 offset:57344\n" \
  "v_sub_f32_e64 v153, v153, v168\n" \
  "v_mfma_f32_16x16x32_bf16 v[80:83], a[168:171], v[176:179], 0\n" \
  "ds_read_b128 a[100:103], v209 offset:57344\n" \
  "v_sub_f32_e64 v154, v154, v168\n" \
  "v_mfma_f32_16x16x32_bf16 v[84:87], a[168:171], v[180:183], 0\n" \
  "v_sub_f32 v155, v155, v168\n" \
  "v_sub_f32 v156, v156, v169\n" \
  "v_mfma_f32_16x16x32_bf16 v[88:91], a[172:175], v[176:179], 0\n" \
  "ds_read_b128 a[104:107], v210 offset:57344\n" \
  "v_sub_f32_e64 v157, v157, v169\n" \
  "v_mfma_f32_16x16x32_bf16 v[92:95], a[172:175], v[180:183], 0\n" \
  "buffer_load_dwordx4 v222, s[36:39], s72 offen offset:2048 lds\n" \
  "v_sub_f32_e64 v158, v158, v169\n" \
  "v_mfma_f32_16x16x32_bf16 v[96:99], a[176:179], v[176:179], 0\n" \
  "ds_read_b128 a[108:111], v211 offset:57344\n" \
  "v_sub_f32_e64 v159, v159, v169\n" \
  "v_mfma_f32_16x16x32_bf16 v[100:103], a[176:179], v[180:183], 0\n" \
  "ds_read_b128 a[112:115], v208 offset:57600\n" \
  "v_exp_f32 v192, v144\n" \
  "v_exp_f32 v193, v145\n" \
  "v_mfma_f32_16x16x32_bf16 v[104:107], a[180:183], v[176:179], 0\n" \
  "ds_read_b128 a[116:119], v209 offset:57600\n" \
  "v_cvt_pk_bf16_f32 v184, v192, v193\n" \
  "v_mfma_f32_16x16x32_bf16 v[108:111], a[180:183], v[180:183], 0\n" \
  "ds_read_b128 a[120:123], v210 offset:57600\n" \
  "v_exp_f32_e64 v194, v146\n" \
  "v_mfma_f32_16x16x32_bf16 v[112:115], a[184:187], v[176:179], 0\n" \
  "ds_read_b128 a[124:127], v211 offset:57600\n" \
  "v_exp_f32_e64 v195, v147\n" \
  "v_mfma_f32_16x16x32_bf16 v[116:119], a[184:187], v[180:183], 0\n" \
  "s_waitcnt lgkmcnt(5)\n" \
  "v_exp_f32 v196, v148\n" \
  "v_exp_f32_e64 v197, v149\n" \
  "v_mfma_f32_16x16x32_bf16 v[120:123], a[188:191], v[176:179], 0\n" \
  "v_cvt_pk_bf16_f32 v185, v194, v195\n" \
  "v_cvt_pk_bf16_f32 v188, v196, v197\n" \
  "v_mfma_f32_16x16x32_bf16 v[124:127], a[188:191], v[180:183], 0\n" \
  "buffer_load_dwordx4 v223, s[36:39], s72 offen offset:3072 lds\n" \
  "v_mfma_f32_16x16x32_bf16 v[200:203], v[172:175], v[176:179], 0\n" \
  "v_exp_f32_e64 v198, v150\n" \
  "v_mfma_f32_16x16x32_bf16 v[204:207], v[172:175], v[180:183], 0\n" \
  "v_exp_f32 v199, v151\n" \
  "s_nop 0\n" \
  "v_cvt_pk_bf16_f32 v189, v198, v199\n" \
  "s_waitcnt vmcnt(16) lgkmcnt(0)\n" \
  "s_barrier\n" \
  "; attn phase 1\n" \
  "v_mfma_f32_16x16x32_bf16 v[128:131], a[64:67], a[0:3], v[160:163]\n" \
  "ds_read_b64_tr_b16 a[128:129], v212\n" \
  "ds_read_b64_tr_b16 a[130:131], v212 offset:8192\n" \
  "buffer_load_dword v170, v234, s[52:55], 0 offen\n" \
  "v_mfma_f32_16x16x32_bf16 v[132:135], a[64:67], a[32:35], v[164:167]\n" \
  "s_mov_b32 s73, 0x10000\n" \
  "v_exp_f32_e64 v192, v152\n" \
  "v_mfma_f32_16x16x32_bf16 v[128:131], a[68:71], a[4:7], v[128:131]\n" \
  "ds_read_b64_tr_b16 a[132:133], v213\n" \
  "v_or3_b32 v248, v184, v185, v188\n" \
  "v_mfma_f32_16x16x32_bf16 v[132:135], a[68:71], a[36:39], v[132:135]\n" \
  "ds_read_b64_tr_b16 a[134:135], v213 offset:8192\n" \
  "v_readlane_b32 s94, v255, 53\n" \
  "v_mfma_f32_16x16x32_bf16 v[128:131], a[72:75], a[8:11], v[128:131]\n" \
  "ds_read_b64_tr_b16 a[136:137], v214\n" \
  "v_min_u32_e64 v251, 25, v253\n" \
  "v_mfma_f32_16x16x32_bf16 v[132:135], a[72:75], a[40:43], v[132:135]\n" \
  "s_add_u32 m0, s74, 0xc000\n" \
  "v_exp_f32_e64 v193, v153\n" \
  "v_mfma_f32_16x16x32_bf16 v[128:131], a[76:79], a[12:15], v[128:131]\n" \
  "ds_read_b64_tr_b16 a[138:139], v214 offset:8192\n" \
  "v_cvt_pk_bf16_f32 v186, v192, v193\n" \
  "v_mfma_f32_16x16x32_bf16 v[132:135], a[76:79], a[44:47], v[132:135]\n" \
  "buffer_load_dwordx4 v224, s[40:43], s73 offen lds\n" \
  "v_mfma_f32_16x16x32_bf16 v[128:131], a[80:83], a[16:19], v[128:131]\n" \
  "ds_read_b64_tr_b16 a[140:141], v215\n" \
  "ds_read_b64_tr_b16 a[142:143], v215 offset:8192\n" \
  "v_mfma_f32_16x16x32_bf16 v[132:135], a[80:83], a[48:51], v[132:135]\n" \
  "s_mov_b32 s72, 0x18000\n" \
  "v_exp_f32_e64 v194, v154\n" \
  "v_mfma_f32_16x16x32_bf16 v[128:131], a[84:87], a[20:23], v[128:131]\n" \
  "ds_read_b64_tr_b16 a[144:145], v216\n" \
  "v_or3_b32 v248, v248, v189, v186\n" \
  "v_mfma_f32_16x16x32_bf16 v[132:135], a[84:87], a[52:55], v[132:135]\n" \
  "ds_read_b64_tr_b16 a[146:147], v216 offset:8192\n" \
  "v_lshlrev_b32_e64 v251, 6, v251\n" \
  "v_mfma_f32_16x16x32_bf16 v[128:131], a[88:91], a[24:27], v[128:131]\n" \
  "ds_read_b64_tr_b16 a[148:149], v217\n" \
  "v_add_u32_e64 v251, s94, v251\n" \
  "v_mfma_f32_16x16x32_bf16 v[132:135], a[88:91], a[56:59], v[132:135]\n" \
  "v_exp_f32_e64 v195, v155\n" \
  "v_mfma_f32_16x16x32_bf16 v[128:131], a[92:95], a[28:31], v[128:131]\n" \
  "ds_read_b64_tr_b16 a[150:151], v217 offset:8192\n" \
  "v_cvt_pk_bf16_f32 v187, v194, v195\n" \
  "v_mfma_f32_16x16x32_bf16 v[132:135], a[92:95], a[60:63], v[132:135]\n" \
  "buffer_load_dwordx4 v225, s[40:43], s73 offen offset:1024 lds\n" \
  "v_mfma_f32_16x16x32_bf16 v[136:139], a[96:99], a[0:3], v[160:163]\n" \
  "ds_read_b128 a[64:67], v208\n" \
  "ds_read_b64_tr_b16 a[152:153], v218\n" \
  "v_mfma_f32_16x16x32_bf16 v[140:143], a[96:99], a[32:35], v[164:167]\n" \
  "v_exp_f32_e64 v196, v156\n" \
  "v_mfma_f32_16x16x32_bf16 v[136:139], a[100:103], a[4:7], v[136:139]\n" \
  "ds_read_b64_tr_b16 a[154:155], v218 offset:8192\n" \
  "v_min_u32 v251, 0x187ffc, v251\n" \
  "v_mfma_f32_16x16x32_bf16 v[140:143], a[100:103], a[36:39], v[140:143]\n" \
  "ds_read_b128 a[68:71], v209\n" \
  "v_mfma_f32_16x16x32_bf16 v[136:139], a[104:107], a[8:11], v[136:139]\n" \
  "ds_read_b64_tr_b16 a[156:157], v219\n" \
  "v_mfma_f32_16x16x32_bf16 v[140:143], a[104:107], a[40:43], v[140:143]\n" \
  "v_exp_f32_e64 v197, v157\n" \
  "v_mfma_f32_16x16x32_bf16 v[136:139], a[108:111], a[12:15], v[136:139]\n" \
  "ds_read_b64_tr_b16 a[158:159], v219 offset:8192\n" \
  "v_cvt_pk_bf16_f32 v190, v196, v197\n" \
  "v_mfma_f32_16x16x32_bf16 v[140:143], a[108:111], a[44:47], v[140:143]\n" \
  "buffer_load_dwordx4 v226, s[40:43], s73 offen offset:2048 lds\n" \
  "v_mfma_f32_16x16x32_bf16 v[136:139], a[112:115], a[16:19], v[136:139]\n" \
  "ds_read_b128 a[72:75], v210\n" \
  "ds_read_b64_tr_b16 a[160:161], v212 offset:256\n" \
  "v_mfma_f32_16x16x32_bf16 v[140:143], a[112:115], a[48:51], v[140:143]\n" \
  "ds_read_b64_tr_b16 a[162:163], v212 offset:8448\n" \
  "v_or3_b32 v248, v248, v187, v190\n" \
  "v_mfma_f32_16x16x32_bf16 v[136:139], a[116:119], a[20:23], v[136:139]\n" \
  "ds_read_b128 a[76:79], v211\n" \
  "v_mfma_f32_16x16x32_bf16 v[140:143], a[116:119], a[52:55], v[140:143]\n" \
  "ds_read_b64_tr_b16 a[164:165], v213 offset:256\n" \
  "v_mfma_f32_16x16x32_bf16 v[136:139], a[120:123], a[24:27], v[136:139]\n" \
  "ds_read_b64_tr_b16 a[166:167], v213 offset:8448\n" \
  "s_waitcnt lgkmcnt(6)\n" \
  "v_mfma_f32_16x16x32_bf16 v[140:143], a[120:123], a[56:59], v[140:143]\n" \
  "ds_read_b128 a[80:83], v208 offset:256\n" \
  "v_exp_f32 v198, v158\n" \
  "v_exp_f32 v199, v159\n" \
  "s_nop 0\n" \
  "v_cvt_pk_bf16_f32 v191, v198, v199\n" \
  "v_mfma_f32_16x16x32_bf16 v[136:139], a[124:127], a[28:31], v[136:139]\n" \
  "ds_read_b64_tr_b16 a[168:169], v214 offset:256\n" \
  "v_bitop3_b32 v248, v248, v191, s76 bitop3:0xa8\n" \
  "v_cmp_ne_u32_e64 vcc, 0, v248\n" \
  "v_mfma_f32_16x16x32_bf16 v[140:143], a[124:127], a[60:63], v[140:143]\n" \
  "s_cbranch_vccnz L_rare_p1_%=\n" \
  "L_back_p1_%=:\n" \
  "v_mfma_f32_16x16x32_bf16 v[0:3], a[128:131], v[184:187], v[0:3]\n" \
  "ds_read_b64_tr_b16 a[170:171], v214 offset:8448\n" \
  "ds_read_b128 a[84:87], v209 offset:256\n" \
  "v_mfma_f32_16x16x32_bf16 v[4:7], a[128:131], v[188:191], v[4:7]\n" \
  "v_exp_f32 v192, v128\n" \
  "v_mfma_f32_16x16x32_bf16 v[8:11], a[132:135], v[184:187], v[8:11]\n" \
  "ds_read_b64_tr_b16 a[172:173], v215 offset:256\n" \
  "v_mfma_f32_16x16x32_bf16 v[12:15], a[132:135], v[188:191], v[12:15]\n" \
  "ds_read_b64_tr_b16 a[174:175], v215 offset:8448\n" \
  "v_mfma_f32_16x16x32_bf16 v[16:19], a[136:139], v[184:187], v[16:19]\n" \
  "ds_read_b128 a[88:91], v210 offset:256\n" \
  "v_mfma_f32_16x16x32_bf16 v[20:23], a[136:139], v[188:191], v[20:23]\n" \
  "v_exp_f32_e64 v193, v129\n" \
  "v_mfma_f32_16x16x32_bf16 v[24:27], a[140:143], v[184:187], v[24:27]\n" \
  "ds_read_b64_tr_b16 a[176:177], v216 offset:256\n" \
  "v_cvt_pk_bf16_f32 v176, v192, v193\n" \
  "v_mfma_f32_16x16x32_bf16 v[28:31], a[140:143], v[188:191], v[28:31]\n" \
  "buffer_load_dwordx4 v227, s[40:43], s73 offen offset:3072 lds\n" \
  "v_mfma_f32_16x16x32_bf16 v[32:35], a[144:147], v[184:187], v[32:35]\n" \
  "ds_read_b64_tr_b16 a[178:179], v216 offset:8448\n" \
  "ds_read_b128 a[92:95], v211 offset:256\n" \
  "v_mfma_f32_16x16x32_bf16 v[36:39], a[144:147], v[188:191], v[36:39]\n" \
  "s_add_u32 m0, s75, 0xc000\n" \
  "v_exp_f32_e64 v194, v130\n" \
  "v_mfma_f32_16x16x32_bf16 v[40:43], a[148:151], v[184:187], v[40:43]\n" \
  "ds_read_b64_tr_b16 a[180:181], v217 offset:256\n" \
  "v_mfma_f32_16x16x32_bf16 v[44:47], a[148:151], v[188:191], v[44:47]\n" \
  "ds_read_b64_tr_b16 a[182:183], v217 offset:8448\n" \
  "v_mfma_f32_16x16x32_bf16 v[48:51], a[152:155], v[184:187], v[48:51]\n" \
  "ds_read_b128 a[96:99], v208 offset:8192\n" \
  "v_mfma_f32_16x16x32_bf16 v[52:55], a[152:155], v[188:191], v[52:55]\n" \
  "s_waitcnt lgkmcnt(5)\n" \
  "v_exp_f32 v195, v131\n" \
  "v_mfma_f32_16x16x32_bf16 v[56:59], a[156:159], v[184:187], v[56:59]\n" \
  "ds_read_b64_tr_b16 a[184:185], v218 offset:256\n" \
  "v_cvt_pk_bf16_f32 v177, v194, v195\n" \
  "v_mfma_f32_16x16x32_bf16 v[60:63], a[156:159], v[188:191], v[60:63]\n" \
  "buffer_load_dwordx4 v220, s[36:39], s72 offen lds\n" \
  "v_mfma_f32_16x16x32_bf16 v[64:67], a[160:163], v[184:187], v[64:67]\n" \
  "ds_read_b64_tr_b16 a[186:187], v218 offset:8448\n" \
  "ds_read_b128 a[100:103], v209 offset:8192\n" \
  "v_mfma_f32_16x16x32_bf16 v[68:71], a[160:163], v[188:191], v[68:71]\n" \
  "v_exp_f32_e64 v196, v132\n" \
  "v_mfma_f32_16x16x32_bf16 v[72:75], a[164:167], v[184:187], v[72:75]\n" \
  "ds_read_b64_tr_b16 a[188:189], v219 offset:256\n" \
  "v_mfma_f32_16x16x32_bf16 v[76:79], a[164:167], v[188:191], v[76:79]\n" \
  "ds_read_b64_tr_b16 a[190:191], v219 offset:8448\n" \
  "v_mfma_f32_16x16x32_bf16 v[80:83], a[168:171], v[184:187], v[80:83]\n" \
  "ds_read_b128 a[104:107], v210 offset:8192\n" \
  "v_mfma_f32_16x16x32_bf16 v[84:87], a[168:171], v[188:191], v[84:87]\n" \
  "s_waitcnt lgkmcnt(5)\n" \
  "v_exp_f32 v197, v133\n" \
  "v_mfma_f32_16x16x32_bf16 v[88:91], a[172:175], v[184:187], v[88:91]\n" \
  "ds_read_b128 a[108:111], v211 offset:8192\n" \
  "v_cvt_pk_bf16_f32 v180, v196, v197\n" \
  "v_mfma_f32_16x16x32_bf16 v[92:95], a[172:175], v[188:191], v[92:95]\n" \
  "buffer_load_dwordx4 v221, s[36:39], s72 offen offset:1024 lds\n" \
  "v_mfma_f32_16x16x32_bf16 v[96:99], a[176:179], v[184:187], v[96:99]\n" \
  "ds_read_b128 a[112:115], v208 offset:8448\n" \
  "ds_read_b128 a[116:119], v209 offset:8448\n" \
  "v_mfma_f32_16x16x32_bf16 v[100:103], a[176:179], v[188:191], v[100:103]\n" \
  "ds_read_b128 a[120:123], v210 offset:8448\n" \
  "v_mfma_f32_16x16x32_bf16 v[104:107], a[180:183], v[184:187], v[104:107]\n" \
  "ds_read_b128 a[124:127], v211 offset:8448\n" \
  "s_waitcnt lgkmcnt(5)\n" \
  "v_mfma_f32_16x16x32_bf16 v[108:111], a[180:183], v[188:191], v[108:111]\n" \
  "buffer_load_dwordx4 v222, s[36:39], s72 offen offset:2048 lds\n" \
  "v_mfma_f32_16x16x32_bf16 v[112:115], a[184:187], v[184:187], v[112:115]\n" \
  "v_exp_f32 v198, v134\n" \
  "v_mfma_f32_16x16x32_bf16 v[116:119], a[184:187], v[188:191], v[116:119]\n" \
  "buffer_load_dwordx4 v223, s[36:39], s72 offen offset:3072 lds\n" \
  "v_mfma_f32_16x16x32_bf16 v[120:123], a[188:191], v[184:187], v[120:123]\n" \
  "v_exp_f32_e64 v199, v135\n" \
  "v_mfma_f32_16x16x32_bf16 v[124:127], a[188:191], v[188:191], v[124:127]\n" \
  "v_exp_f32_e64 v192, v136\n" \
  "global_load_dword v168, v251, s[26:27]\n" \
  "v_mfma_f32_16x16x32_bf16 v[200:203], v[172:175], v[184:187], v[200:203]\n" \
  "v_exp_f32_e64 v193, v137\n" \
  "v_mfma_f32_16x16x32_bf16 v[204:207], v[172:175], v[188:191], v[204:207]\n" \
  "s_waitcnt vmcnt(18) lgkmcnt(0)\n" \
  "s_barrier\n" \
  "s_mov_b32 s77, 14\n" \
  "L_loop_%=:\n" \
  "; attn phase 2\n" \
  "v_mfma_f32_16x16x32_bf16 v[144:147], a[64:67], a[0:3], v[160:163]\n" \
  "ds_read_b64_tr_b16 a[128:129], v212 offset:16384\n" \
  "ds_read_b64_tr_b16 a[130:131], v212 offset:24576\n" \
  "v_mfma_f32_16x16x32_bf16 v[148:151], a[64:67], a[32:35], v[164:167]\n" \
  "s_add_u32 s73, s73, 0x4000\n" \
  "v_exp_f32 v194, v138\n" \
  "v_mfma_f32_16x16x32_bf16 v[144:147], a[68:71], a[4:7], v[144:147]\n" \
  "ds_read_b64_tr_b16 a[132:133], v213 offset:16384\n" \
  "v_cvt_pk_bf16_f32 v181, v198, v199\n" \
  "v_mfma_f32_16x16x32_bf16 v[148:151], a[68:71], a[36:39], v[148:151]\n" \
  "ds_read_b64_tr_b16 a[134:135], v213 offset:24576\n" \
  "v_cvt_pk_bf16_f32 v178, v192, v193\n" \
  "v_mfma_f32_16x16x32_bf16 v[144:147], a[72:75], a[8:11], v[144:147]\n" \
  "ds_read_b64_tr_b16 a[136:137], v214 offset:16384\n" \
  "v_or3_b32 v248, v176, v177, v180\n" \
  "v_mfma_f32_16x16x32_bf16 v[148:151], a[72:75], a[40:43], v[148:151]\n" \
  "s_add_u32 m0, s74, 0\n" \
  "v_exp_f32 v195, v139\n" \
  "v_mfma_f32_16x16x32_bf16 v[144:147], a[76:79], a[12:15], v[144:147]\n" \
  "ds_read_b64_tr_b16 a[138:139], v214 offset:24576\n" \
  "v_cvt_pk_bf16_f32 v179, v194, v195\n" \
  "v_mfma_f32_16x16x32_bf16 v[148:151], a[76:79], a[44:47], v[148:151]\n" \
  "buffer_load_dwordx4 v224, s[40:43], s73 offen lds\n" \
  "v_mfma_f32_16x16x32_bf16 v[144:147], a[80:83], a[16:19], v[144:147]\n" \
  "ds_read_b64_tr_b16 a[140:141], v215 offset:16384\n" \
  "ds_read_b64_tr_b16 a[142:143], v215 offset:24576\n" \
  "v_mfma_f32_16x16x32_bf16 v[148:151], a[80:83], a[48:51], v[148:151]\n" \
  "s_add_u32 s72, s72, 0x4000\n" \
  "v_exp_f32_e64 v196, v140\n" \
  "v_mfma_f32_16x16x32_bf16 v[144:147], a[84:87], a[20:23], v[144:147]\n" \
  "ds_read_b64_tr_b16 a[144:145], v216 offset:16384\n" \
  "v_or3_b32 v248, v248, v181, v178\n" \
  "v_mfma_f32_16x16x32_bf16 v[148:151], a[84:87], a[52:55], v[148:151]\n" \
  "ds_read_b64_tr_b16 a[146:147], v216 offset:24576\n" \
  "v_mfma_f32_16x16x32_bf16 v[144:147], a[88:91], a[24:27], v[144:147]\n" \
  "ds_read_b64_tr_b16 a[148:149], v217 offset:16384\n" \
  "v_mfma_f32_16x16x32_bf16 v[148:151], a[88:91], a[56:59], v[148:151]\n" \
  "v_exp_f32_e64 v197, v141\n" \
  "v_mfma_f32_16x16x32_bf16 v[144:147], a[92:95], a[28:31], v[144:147]\n" \
  "ds_read_b64_tr_b16 a[150:151], v217 offset:24576\n" \
  "v_cvt_pk_bf16_f32 v182, v196, v197\n" \
  "v_mfma_f32_16x16x32_bf16 v[148:151], a[92:95], a[60:63], v[148:151]\n" \
  "buffer_load_dwordx4 v225, s[40:43], s73 offen offset:1024 lds\n" \
  "v_mfma_f32_16x16x32_bf16 v[152:155], a[96:99], a[0:3], v[160:163]\n" \
  "ds_read_b128 a[64:67], v208 offset:16384\n" \
  "ds_read_b64_tr_b16 a[152:153], v218 offset:16384\n" \
  "v_mfma_f32_16x16x32_bf16 v[156:159], a[96:99], a[32:35], v[164:167]\n" \
  "v_exp_f32_e64 v198, v142\n" \
  "v_mfma_f32_16x16x32_bf16 v[152:155], a[100:103], a[4:7], v[152:155]\n" \
  "ds_read_b64_tr_b16 a[154:155], v218 offset:24576\n" \
  "v_or3_b32 v248, v248, v179, v182\n" \
  "v_mfma_f32_16x16x32_bf16 v[156:159], a[100:103], a[36:39], v[156:159]\n" \
  "ds_read_b128 a[68:71], v209 offset:16384\n" \
  "v_mfma_f32_16x16x32_bf16 v[152:155], a[104:107], a[8:11], v[152:155]\n" \
  "ds_read_b64_tr_b16 a[156:157], v219 offset:16384\n" \
  "v_mfma_f32_16x16x32_bf16 v[156:159], a[104:107], a[40:43], v[156:159]\n" \
  "v_exp_f32_e64 v199, v143\n" \
  "v_mfma_f32_16x16x32_bf16 v[152:155], a[108:111], a[12:15], v[152:155]\n" \
  "ds_read_b64_tr_b16 a[158:159], v219 offset:24576\n" \
  "v_cvt_pk_bf16_f32 v183, v198, v199\n" \
  "v_mfma_f32_16x16x32_bf16 v[156:159], a[108:111], a[44:47], v[156:159]\n" \
  "buffer_load_dwordx4 v226, s[40:43], s73 offen offset:2048 lds\n" \
  "v_mfma_f32_16x16x32_bf16 v[152:155], a[112:115], a[16:19], v[152:155]\n" \
  "ds_read_b128 a[72:75], v210 offset:16384\n" \
  "ds_read_b64_tr_b16 a[160:161], v212 offset:16640\n" \
  "v_mfma_f32_16x16x32_bf16 v[156:159], a[112:115], a[48:51], v[156:159]\n" \
  "ds_read_b64_tr_b16 a[162:163], v212 offset:24832\n" \
  "v_bitop3_b32 v248, v248, v183, s76 bitop3:0xa8\n" \
  "v_mfma_f32_16x16x32_bf16 v[152:155], a[116:119], a[20:23], v[152:155]\n" \
  "ds_read_b128 a[76:79], v211 offset:16384\n" \
  "v_cmp_ne_u32_e64 vcc, 0, v248\n" \
  "v_mfma_f32_16x16x32_bf16 v[156:159], a[116:119], a[52:55], v[156:159]\n" \
  "ds_read_b64_tr_b16 a[164:165], v213 offset:16640\n" \
  "v_mfma_f32_16x16x32_bf16 v[152:155], a[120:123], a[24:27], v[152:155]\n" \
  "ds_read_b64_tr_b16 a[166:167], v213 offset:24832\n" \
  "v_mfma_f32_16x16x32_bf16 v[156:159], a[120:123], a[56:59], v[156:159]\n" \
  "ds_read_b128 a[80:83], v208 offset:16640\n" \
  "v_mfma_f32_16x16x32_bf16 v[152:155], a[124:127], a[28:31], v[152:155]\n" \
  "ds_read_b64_tr_b16 a[168:169], v214 offset:16640\n" \
  "s_waitcnt lgkmcnt(6)\n" \
  "v_mfma_f32_16x16x32_bf16 v[156:159], a[124:127], a[60:63], v[156:159]\n" \
  "s_cbranch_vccnz L_rare_2_%=\n" \
  "L_back_2_%=:\n" \
  "v_mfma_f32_16x16x32_bf16 v[0:3], a[128:131], v[176:179], v[0:3]\n" \
  "ds_read_b64_tr_b16 a[170:171], v214 offset:24832\n" \
  "ds_read_b128 a[84:87], v209 offset:16640\n" \
  "v_mfma_f32_16x16x32_bf16 v[4:7], a[128:131], v[180:183], v[4:7]\n" \
  "v_exp_f32_e64 v192, v144\n" \
  "v_mfma_f32_16x16x32_bf16 v[8:11], a[132:135], v[176:179], v[8:11]\n" \
  "ds_read_b64_tr_b16 a[172:173], v215 offset:16640\n" \
  "v_mfma_f32_16x16x32_bf16 v[12:15], a[132:135], v[180:183], v[12:15]\n" \
  "ds_read_b64_tr_b16 a[174:175], v215 offset:24832\n" \
  "v_mfma_f32_16x16x32_bf16 v[16:19], a[136:139], v[176:179], v[16:19]\n" \
  "ds_read_b128 a[88:91], v210 offset:16640\n" \
  "v_mfma_f32_16x16x32_bf16 v[20:23], a[136:139], v[180:183], v[20:23]\n" \
  "v_exp_f32_e64 v193, v145\n" \
  "v_mfma_f32_16x16x32_bf16 v[24:27], a[140:143], v[176:179], v[24:27]\n" \
  "ds_read_b64_tr_b16 a[176:177], v216 offset:16640\n" \
  "v_cvt_pk_bf16_f32 v184, v192, v193\n" \
  "v_mfma_f32_16x16x32_bf16 v[28:31], a[140:143], v[180:183], v[28:31]\n" \
  "buffer_load_dwordx4 v227, s[40:43], s73 offen offset:3072 lds\n" \
  "v_mfma_f32_16x16x32_bf16 v[32:35], a[144:147], v[176:179], v[32:35]\n" \
  "ds_read_b64_tr_b16 a[178:179], v216 offset:24832\n" \
  "ds_read_b128 a[92:95], v211 offset:16640\n" \
  "v_mfma_f32_16x16x32_bf16 v[36:39], a[144:147], v[180:183], v[36:39]\n" \
  "s_add_u32 m0, s75, 0\n" \
  "v_exp_f32 v194, v146\n" \
  "v_mfma_f32_16x16x32_bf16 v[40:43], a[148:151], v[176:179], v[40:43]\n" \
  "ds_read_b64_tr_b16 a[180:181], v217 offset:16640\n" \
  "v_mfma_f32_16x16x32_bf16 v[44:47], a[148:151], v[180:183], v[44:47]\n" \
  "ds_read_b64_tr_b16 a[182:183], v217 offset:24832\n" \
  "v_mfma_f32_16x16x32_bf16 v[48:51], a[152:155], v[176:179], v[48:51]\n" \
  "ds_read_b128 a[96:99], v208 offset:24576\n" \
  "v_mfma_f32_16x16x32_bf16 v[52:55], a[152:155], v[180:183], v[52:55]\n" \
  "s_waitcnt lgkmcnt(5)\n" \
  "v_exp_f32 v195, v147\n" \
  "v_mfma_f32_16x16x32_bf16 v[56:59], a[156:159], v[176:179], v[56:59]\n" \
  "ds_read_b64_tr_b16 a[184:185], v218 offset:16640\n" \
  "v_cvt_pk_bf16_f32 v185, v194, v195\n" \
  "v_mfma_f32_16x16x32_bf16 v[60:63], a[156:159], v[180:183], v[60:63]\n" \
  "buffer_load_dwordx4 v220, s[36:39], s72 offen lds\n" \
  "v_mfma_f32_16x16x32_bf16 v[64:67], a[160:163], v[176:179], v[64:67]\n" \
  "ds_read_b64_tr_b16 a[186:187], v218 offset:24832\n" \
  "ds_read_b128 a[100:103], v209 offset:24576\n" \
  "v_mfma_f32_16x16x32_bf16 v[68:71], a[160:163], v[180:183], v[68:71]\n" \
  "v_exp_f32_e64 v196, v148\n" \
  "v_mfma_f32_16x16x32_bf16 v[72:75], a[164:167], v[176:179], v[72:75]\n" \
  "ds_read_b64_tr_b16 a[188:189], v219 offset:16640\n" \
  "v_mfma_f32_16x16x32_bf16 v[76:79], a[164:167], v[180:183], v[76:79]\n" \
  "ds_read_b64_tr_b16 a[190:191], v219 offset:24832\n" \
  "v_mfma_f32_16x16x32_bf16 v[80:83], a[168:171], v[176:179], v[80:83]\n" \
  "ds_read_b128 a[104:107], v210 offset:24576\n" \
  "v_mfma_f32_16x16x32_bf16 v[84:87], a[168:171], v[180:183], v[84:87]\n" \
  "s_waitcnt lgkmcnt(5)\n" \
  "v_exp_f32 v197, v149\n" \
  "v_mfma_f32_16x16x32_bf16 v[88:91], a[172:175], v[176:179], v[88:91]\n" \
  "ds_read_b128 a[108:111], v211 offset:24576\n" \
  "v_cvt_pk_bf16_f32 v188, v196, v197\n" \
  "v_mfma_f32_16x16x32_bf16 v[92:95], a[172:175], v[180:183], v[92:95]\n" \
  "buffer_load_dwordx4 v221, s[36:39], s72 offen offset:1024 lds\n" \
  "v_mfma_f32_16x16x32_bf16 v[96:99], a[176:179], v[176:179], v[96:99]\n" \
  "ds_read_b128 a[112:115], v208 offset:24832\n" \
  "ds_read_b128 a[116:119], v209 offset:24832\n" \
  "v_mfma_f32_16x16x32_bf16 v[100:103], a[176:179], v[180:183], v[100:103]\n" \
  "ds_read_b128 a[120:123], v210 offset:24832\n" \
  "v_mfma_f32_16x16x32_bf16 v[104:107], a[180:183], v[176:179], v[104:107]\n" \
  "ds_read_b128 a[124:127], v211 offset:24832\n" \
  "s_waitcnt lgkmcnt(5)\n" \
  "v_mfma_f32_16x16x32_bf16 v[108:111], a[180:183], v[180:183], v[108:111]\n" \
  "buffer_load_dwordx4 v222, s[36:39], s72 offen offset:2048 lds\n" \
  "v_mfma_f32_16x16x32_bf16 v[112:115], a[184:187], v[176:179], v[112:115]\n" \
  "v_exp_f32 v198, v150\n" \
  "v_mfma_f32_16x16x32_bf16 v[116:119], a[184:187], v[180:183], v[116:119]\n" \
  "buffer_load_dwordx4 v223, s[36:39], s72 offen offset:3072 lds\n" \
  "v_mfma_f32_16x16x32_bf16 v[120:123], a[188:191], v[176:179], v[120:123]\n" \
  "v_exp_f32_e64 v199, v151\n" \
  "v_mfma_f32_16x16x32_bf16 v[124:127], a[188:191], v[180:183], v[124:127]\n" \
  "v_exp_f32_e64 v192, v152\n" \
  "v_mfma_f32_16x16x32_bf16 v[200:203], v[172:175], v[176:179], v[200:203]\n" \
  "v_exp_f32_e64 v193, v153\n" \
  "v_mfma_f32_16x16x32_bf16 v[204:207], v[172:175], v[180:183], v[204:207]\n" \
  "s_waitcnt vmcnt(16) lgkmcnt(0)\n" \
  "s_barrier\n" \
  "; attn phase 3\n" \
  "v_mfma_f32_16x16x32_bf16 v[128:131], a[64:67], a[0:3], v[160:163]\n" \
  "ds_read_b64_tr_b16 a[128:129], v212 offset:32768\n" \
  "ds_read_b64_tr_b16 a[130:131], v212 offset:40960\n" \
  "v_mfma_f32_16x16x32_bf16 v[132:135], a[64:67], a[32:35], v[164:167]\n" \
  "s_add_u32 s73, s73, 0x4000\n" \
  "v_exp_f32_e64 v194, v154\n" \
  "v_mfma_f32_16x16x32_bf16 v[128:131], a[68:71], a[4:7], v[128:131]\n" \
  "ds_read_b64_tr_b16 a[132:133], v213 offset:32768\n" \
  "v_cvt_pk_bf16_f32 v189, v198, v199\n" \
  "v_mfma_f32_16x16x32_bf16 v[132:135], a[68:71], a[36:39], v[132:135]\n" \
  "ds_read_b64_tr_b16 a[134:135], v213 offset:40960\n" \
  "v_cvt_pk_bf16_f32 v186, v192, v193\n" \
  "v_mfma_f32_16x16x32_bf16 v[128:131], a[72:75], a[8:11], v[128:131]\n" \
  "ds_read_b64_tr_b16 a[136:137], v214 offset:32768\n" \
  "v_or3_b32 v248, v184, v185, v188\n" \
  "v_mfma_f32_16x16x32_bf16 v[132:135], a[72:75], a[40:43], v[132:135]\n" \
  "s_add_u32 m0, s74, 0x4000\n" \
  "v_exp_f32_e64 v195, v155\n" \
  "v_mfma_f32_16x16x32_bf16 v[128:131], a[76:79], a[12:15], v[128:131]\n" \
  "ds_read_b64_tr_b16 a[138:139], v214 offset:40960\n" \
  "v_cvt_pk_bf16_f32 v187, v194, v195\n" \
  "v_mfma_f32_16x16x32_bf16 v[132:135], a[76:79], a[44:47], v[132:135]\n" \
  "buffer_load_dwordx4 v224, s[40:43], s73 offen lds\n" \
  "v_mfma_f32_16x16x32_bf16 v[128:131], a[80:83], a[16:19], v[128:131]\n" \
  "ds_read_b64_tr_b16 a[140:141], v215 offset:32768\n" \
  "ds_read_b64_tr_b16 a[142:143], v215 offset:40960\n" \
  "v_mfma_f32_16x16x32_bf16 v[132:135], a[80:83], a[48:51], v[132:135]\n" \
  "s_add_u32 s72, s72, 0x4000\n" \
  "v_exp_f32_e64 v196, v156\n" \
  "v_mfma_f32_16x16x32_bf16 v[128:131], a[84:87], a[20:23], v[128:131]\n" \
  "ds_read_b64_tr_b16 a[144:145], v216 offset:32768\n" \
  "v_or3_b32 v248, v248, v189, v186\n" \
  "v_mfma_f32_16x16x32_bf16 v[132:135], a[84:87], a[52:55], v[132:135]\n" \
  "ds_read_b64_tr_b16 a[146:147], v216 offset:40960\n" \
  "v_mfma_f32_16x16x32_bf16 v[128:131], a[88:91], a[24:27], v[128:131]\n" \
  "ds_read_b64_tr_b16 a[148:149], v217 offset:32768\n" \
  "v_mfma_f32_16x16x32_bf16 v[132:135], a[88:91], a[56:59], v[132:135]\n" \
  "v_exp_f32_e64 v197, v157\n" \
  "v_mfma_f32_16x16x32_bf16 v[128:131], a[92:95], a[28:31], v[128:131]\n" \
  "ds_read_b64_tr_b16 a[150:151], v217 offset:40960\n" \
  "v_cvt_pk_bf16_f32 v190, v196, v197\n" \
  "v_mfma_f32_16x16x32_bf16 v[132:135], a[92:95], a[60:63], v[132:135]\n" \
  "buffer_load_dwordx4 v225, s[40:43], s73 offen offset:1024 lds\n" \
  "v_mfma_f32_16x16x32_bf16 v[136:139], a[96:99], a[0:3], v[160:163]\n" \
  "ds_read_b128 a[64:67], v208 offset:32768\n" \
  "ds_read_b64_tr_b16 a[152:153], v218 offset:32768\n" \
  "v_mfma_f32_16x16x32_bf16 v[140:143], a[96:99], a[32:35], v[164:167]\n" \
  "v_exp_f32_e64 v198, v158\n" \
  "v_mfma_f32_16x16x32_bf16 v[136:139], a[100:103], a[4:7], v[136:139]\n" \
  "ds_read_b64_tr_b16 a[154:155], v218 offset:40960\n" \
  "v_or3_b32 v248, v248, v187, v190\n" \
  "v_mfma_f32_16x16x32_bf16 v[140:143], a[100:103], a[36:39], v[140:143]\n" \
  "ds_read_b128 a[68:71], v209 offset:32768\n" \
  "v_mfma_f32_16x16x32_bf16 v[136:139], a[104:107], a[8:11], v[136:139]\n" \
  "ds_read_b64_tr_b16 a[156:157], v219 offset:32768\n" \
  "v_mfma_f32_16x16x32_bf16 v[140:143], a[104:107], a[40:43], v[140:143]\n" \
  "v_exp_f32_e64 v199, v159\n" \
  "v_mfma_f32_16x16x32_bf16 v[136:139], a[108:111], a[12:15], v[136:139]\n" \
  "ds_read_b64_tr_b16 a[158:159], v219 offset:40960\n" \
  "v_cvt_pk_bf16_f32 v191, v198, v199\n" \
  "v_mfma_f32_16x16x32_bf16 v[140:143], a[108:111], a[44:47], v[140:143]\n" \
  "buffer_load_dwordx4 v226, s[40:43], s73 offen offset:2048 lds\n" \
  "v_mfma_f32_16x16x32_bf16 v[136:139], a[112:115], a[16:19], v[136:139]\n" \
  "ds_read_b128 a[72:75], v210 offset:32768\n" \
  "ds_read_b64_tr_b16 a[160:161], v212 offset:33024\n" \
  "v_mfma_f32_16x16x32_bf16 v[140:143], a[112:115], a[48:51], v[140:143]\n" \
  "ds_read_b64_tr_b16 a[162:163], v212 offset:41216\n" \
  "v_bitop3_b32 v248, v248, v191, s76 bitop3:0xa8\n" \
  "v_mfma_f32_16x16x32_bf16 v[136:139], a[116:119], a[20:23], v[136:139]\n" \
  "ds_read_b128 a[76:79], v211 offset:32768\n" \
  "v_cmp_ne_u32_e64 vcc, 0, v248\n" \
  "v_mfma_f32_16x16x32_bf16 v[140:143], a[116:119], a[52:55], v[140:143]\n" \
  "ds_read_b64_tr_b16 a[164:165], v213 offset:33024\n" \
  "v_mfma_f32_16x16x32_bf16 v[136:139], a[120:123], a[24:27], v[136:139]\n" \
  "ds_read_b64_tr_b16 a[166:167], v213 offset:41216\n" \
  "v_mfma_f32_16x16x32_bf16 v[140:143], a[120:123], a[56:59], v[140:143]\n" \
  "ds_read_b128 a[80:83], v208 offset:33024\n" \
  "v_mfma_f32_16x16x32_bf16 v[136:139], a[124:127], a[28:31], v[136:139]\n" \
  "ds_read_b64_tr_b16 a[168:169], v214 offset:33024\n" \
  "s_waitcnt lgkmcnt(6)\n" \
  "v_mfma_f32_16x16x32_bf16 v[140:143], a[124:127], a[60:63], v[140:143]\n" \
  "s_cbranch_vccnz L_rare_3_%=\n" \
  "L_back_3_%=:\n" \
  "v_mfma_f32_16x16x32_bf16 v[0:3], a[128:131], v[184:187], v[0:3]\n" \
  "ds_read_b64_tr_b16 a[170:171], v214 offset:41216\n" \
  "ds_read_b128 a[84:87], v209 offset:33024\n" \
  "v_mfma_f32_16x16x32_bf16 v[4:7], a[128:131], v[188:191], v[4:7]\n" \
  "v_exp_f32_e64 v192, v128\n" \
  "v_mfma_f32_16x16x32_bf16 v[8:11], a[132:135], v[184:187], v[8:11]\n" \
  "ds_read_b64_tr_b16 a[172:173], v215 offset:33024\n" \
  "v_mfma_f32_16x16x32_bf16 v[12:15], a[132:135], v[188:191], v[12:15]\n" \
  "ds_read_b64_tr_b16 a[174:175], v215 offset:41216\n" \
  "v_mfma_f32_16x16x32_bf16 v[16:19], a[136:139], v[184:187], v[16:19]\n" \
  "ds_read_b128 a[88:91], v210 offset:33024\n" \
  "v_mfma_f32_16x16x32_bf16 v[20:23], a[136:139], v[188:191], v[20:23]\n" \
  "v_exp_f32_e64 v193, v129\n" \
  "v_mfma_f32_16x16x32_bf16 v[24:27], a[140:143], v[184:187], v[24:27]\n" \
  "ds_read_b64_tr_b16 a[176:177], v216 offset:33024\n" \
  "v_cvt_pk_bf16_f32 v176, v192, v193\n" \
  "v_mfma_f32_16x16x32_bf16 v[28:31], a[140:143], v[188:191], v[28:31]\n" \
  "buffer_load_dwordx4 v227, s[40:43], s73 offen offset:3072 lds\n" \
  "v_mfma_f32_16x16x32_bf16 v[32:35], a[144:147], v[184:187], v[32:35]\n" \
  "ds_read_b64_tr_b16 a[178:179], v216 offset:41216\n" \
  "ds_read_b128 a[92:95], v211 offset:33024\n" \
  "v_mfma_f32_16x16x32_bf16 v[36:39], a[144:147], v[188:191], v[36:39]\n" \
  "s_add_u32 m0, s75, 0x4000\n" \
  "v_exp_f32_e64 v194, v130\n" \
  "v_mfma_f32_16x16x32_bf16 v[40:43], a[148:151], v[184:187], v[40:43]\n" \
  "ds_read_b64_tr_b16 a[180:181], v217 offset:33024\n" \
  "v_mfma_f32_16x16x32_bf16 v[44:47], a[148:151], v[188:191], v[44:47]\n" \
  "ds_read_b64_tr_b16 a[182:183], v217 offset:41216\n" \
  "v_mfma_f32_16x16x32_bf16 v[48:51], a[152:155], v[184:187], v[48:51]\n" \
  "ds_read_b128 a[96:99], v208 offset:40960\n" \
  "v_mfma_f32_16x16x32_bf16 v[52:55], a[152:155], v[188:191], v[52:55]\n" \
  "s_waitcnt lgkmcnt(5)\n" \
  "v_exp_f32 v195, v131\n" \
  "v_mfma_f32_16x16x32_bf16 v[56:59], a[156:159], v[184:187], v[56:59]\n" \
  "ds_read_b64_tr_b16 a[184:185], v218 offset:33024\n" \
  "v_cvt_pk_bf16_f32 v177, v194, v195\n" \
  "v_mfma_f32_16x16x32_bf16 v[60:63], a[156:159], v[188:191], v[60:63]\n" \
  "buffer_load_dwordx4 v220, s[36:39], s72 offen lds\n" \
  "v_mfma_f32_16x16x32_bf16 v[64:67], a[160:163], v[184:187], v[64:67]\n" \
  "ds_read_b64_tr_b16 a[186:187], v218 offset:41216\n" \
  "ds_read_b128 a[100:103], v209 offset:40960\n" \
  "v_mfma_f32_16x16x32_bf16 v[68:71], a[160:163], v[188:191], v[68:71]\n" \
  "v_exp_f32_e64 v196, v132\n" \
  "v_mfma_f32_16x16x32_bf16 v[72:75], a[164:167], v[184:187], v[72:75]\n" \
  "ds_read_b64_tr_b16 a[188:189], v219 offset:33024\n" \
  "v_mfma_f32_16x16x32_bf16 v[76:79], a[164:167], v[188:191], v[76:79]\n" \
  "ds_read_b64_tr_b16 a[190:191], v219 offset:41216\n" \
  "v_mfma_f32_16x16x32_bf16 v[80:83], a[168:171], v[184:187], v[80:83]\n" \
  "ds_read_b128 a[104:107], v210 offset:40960\n" \
  "v_mfma_f32_16x16x32_bf16 v[84:87], a[168:171], v[188:191], v[84:87]\n" \
  "s_waitcnt lgkmcnt(5)\n" \
  "v_exp_f32 v197, v133\n" \
  "v_mfma_f32_16x16x32_bf16 v[88:91], a[172:175], v[184:187], v[88:91]\n" \
  "ds_read_b128 a[108:111], v211 offset:40960\n" \
  "v_cvt_pk_bf16_f32 v180, v196, v197\n" \
  "v_mfma_f32_16x16x32_bf16 v[92:95], a[172:175], v[188:191], v[92:95]\n" \
  "buffer_load_dwordx4 v221, s[36:39], s72 offen offset:1024 lds\n" \
  "v_mfma_f32_16x16x32_bf16 v[96:99], a[176:179], v[184:187], v[96:99]\n" \
  "ds_read_b128 a[112:115], v208 offset:41216\n" \
  "ds_read_b128 a[116:119], v209 offset:41216\n" \
  "v_mfma_f32_16x16x32_bf16 v[100:103], a[176:179], v[188:191], v[100:103]\n" \
  "ds_read_b128 a[120:123], v210 offset:41216\n" \
  "v_mfma_f32_16x16x32_bf16 v[104:107], a[180:183], v[184:187], v[104:107]\n" \
  "ds_read_b128 a[124:127], v211 offset:41216\n" \
  "s_waitcnt lgkmcnt(5)\n" \
  "v_mfma_f32_16x16x32_bf16 v[108:111], a[180:183], v[188:191], v[108:111]\n" \
  "buffer_load_dwordx4 v222, s[36:39], s72 offen offset:2048 lds\n" \
  "v_mfma_f32_16x16x32_bf16 v[112:115], a[184:187], v[184:187], v[112:115]\n" \
  "v_exp_f32 v198, v134\n" \
  "v_mfma_f32_16x16x32_bf16 v[116:119], a[184:187], v[188:191], v[116:119]\n" \
  "buffer_load_dwordx4 v223, s[36:39], s72 offen offset:3072 lds\n" \
  "v_mfma_f32_16x16x32_bf16 v[120:123], a[188:191], v[184:187], v[120:123]\n" \
  "v_exp_f32_e64 v199, v135\n" \
  "v_mfma_f32_16x16x32_bf16 v[124:127], a[188:191], v[188:191], v[124:127]\n" \
  "v_exp_f32_e64 v192, v136\n" \
  "v_mfma_f32_16x16x32_bf16 v[200:203], v[172:175], v[184:187], v[200:203]\n" \
  "v_exp_f32_e64 v193, v137\n" \
  "v_mfma_f32_16x16x32_bf16 v[204:207], v[172:175], v[188:191], v[204:207]\n" \
  "s_waitcnt vmcnt(16) lgkmcnt(0)\n" \
  "s_barrier\n" \
  "; attn phase 4\n" \
  "v_mfma_f32_16x16x32_bf16 v[144:147], a[64:67], a[0:3], v[160:163]\n" \
  "ds_read_b64_tr_b16 a[128:129], v212 offset:49152\n" \
  "ds_read_b64_tr_b16 a[130:131], v212 offset:57344\n" \
  "v_mfma_f32_16x16x32_bf16 v[148:151], a[64:67], a[32:35], v[164:167]\n" \
  "s_add_u32 s73, s73, 0x4000\n" \
  "v_exp_f32_e64 v194, v138\n" \
  "v_mfma_f32_16x16x32_bf16 v[144:147], a[68:71], a[4:7], v[144:147]\n" \
  "ds_read_b64_tr_b16 a[132:133], v213 offset:49152\n" \
  "v_cvt_pk_bf16_f32 v181, v198, v199\n" \
  "v_mfma_f32_16x16x32_bf16 v[148:151], a[68:71], a[36:39], v[148:151]\n" \
  "ds_read_b64_tr_b16 a[134:135], v213 offset:57344\n" \
  "v_cvt_pk_bf16_f32 v178, v192, v193\n" \
  "v_mfma_f32_16x16x32_bf16 v[144:147], a[72:75], a[8:11], v[144:147]\n" \
  "ds_read_b64_tr_b16 a[136:137], v214 offset:49152\n" \
  "v_or3_b32 v248, v176, v177, v180\n" \
  "v_mfma_f32_16x16x32_bf16 v[148:151], a[72:75], a[40:43], v[148:151]\n" \
  "s_add_u32 m0, s74, 0x8000\n" \
  "v_exp_f32_e64 v195, v139\n" \
  "v_mfma_f32_16x16x32_bf16 v[144:147], a[76:79], a[12:15], v[144:147]\n" \
  "ds_read_b64_tr_b16 a[138:139], v214 offset:57344\n" \
  "v_cvt_pk_bf16_f32 v179, v194, v195\n" \
  "v_mfma_f32_16x16x32_bf16 v[148:151], a[76:79], a[44:47], v[148:151]\n" \
  "buffer_load_dwordx4 v224, s[40:43], s73 offen lds\n" \
  "v_mfma_f32_16x16x32_bf16 v[144:147], a[80:83], a[16:19], v[144:147]\n" \
  "ds_read_b64_tr_b16 a[140:141], v215 offset:49152\n" \
  "ds_read_b64_tr_b16 a[142:143], v215 offset:57344\n" \
  "v_mfma_f32_16x16x32_bf16 v[148:151], a[80:83], a[48:51], v[148:151]\n" \
  "s_add_u32 s72, s72, 0x4000\n" \
  "v_exp_f32_e64 v196, v140\n" \
  "v_mfma_f32_16x16x32_bf16 v[144:147], a[84:87], a[20:23], v[144:147]\n" \
  "ds_read_b64_tr_b16 a[144:145], v216 offset:49152\n" \
  "v_or3_b32 v248, v248, v181, v178\n" \
  "v_mfma_f32_16x16x32_bf16 v[148:151], a[84:87], a[52:55], v[148:151]\n" \
  "ds_read_b64_tr_b16 a[146:147], v216 offset:57344\n" \
  "v_mfma_f32_16x16x32_bf16 v[144:147], a[88:91], a[24:27], v[144:147]\n" \
  "ds_read_b64_tr_b16 a[148:149], v217 offset:49152\n" \
  "v_mfma_f32_16x16x32_bf16 v[148:151], a[88:91], a[56:59], v[148:151]\n" \
  "v_exp_f32_e64 v197, v141\n" \
  "v_mfma_f32_16x16x32_bf16 v[144:147], a[92:95], a[28:31], v[144:147]\n" \
  "ds_read_b64_tr_b16 a[150:151], v217 offset:57344\n" \
  "v_cvt_pk_bf16_f32 v182, v196, v197\n" \
  "v_mfma_f32_16x16x32_bf16 v[148:151], a[92:95], a[60:63], v[148:151]\n" \
  "buffer_load_dwordx4 v225, s[40:43], s73 offen offset:1024 lds\n" \
  "v_mfma_f32_16x16x32_bf16 v[152:155], a[96:99], a[0:3], v[160:163]\n" \
  "ds_read_b128 a[64:67], v208 offset:49152\n" \
  "ds_read_b64_tr_b16 a[152:153], v218 offset:49152\n" \
  "v_mfma_f32_16x16x32_bf16 v[156:159], a[96:99], a[32:35], v[164:167]\n" \
  "v_exp_f32_e64 v198, v142\n" \
  "v_mfma_f32_16x16x32_bf16 v[152:155], a[100:103], a[4:7], v[152:155]\n" \
  "ds_read_b64_tr_b16 a[154:155], v218 offset:57344\n" \
  "v_or3_b32 v248, v248, v179, v182\n" \
  "v_mfma_f32_16x16x32_bf16 v[156:159], a[100:103], a[36:39], v[156:159]\n" \
  "ds_read_b128 a[68:71], v209 offset:49152\n" \
  "v_mfma_f32_16x16x32_bf16 v[152:155], a[104:107], a[8:11], v[152:155]\n" \
  "ds_read_b64_tr_b16 a[156:157], v219 offset:49152\n" \
  "v_mfma_f32_16x16x32_bf16 v[156:159], a[104:107], a[40:43], v[156:159]\n" \
  "v_exp_f32_e64 v199, v143\n" \
  "v_mfma_f32_16x16x32_bf16 v[152:155], a[108:111], a[12:15], v[152:155]\n" \
  "ds_read_b64_tr_b16 a[158:159], v219 offset:57344\n" \
  "v_cvt_pk_bf16_f32 v183, v198, v199\n" \
  "v_mfma_f32_16x16x32_bf16 v[156:159], a[108:111], a[44:47], v[156:159]\n" \
  "buffer_load_dwordx4 v226, s[40:43], s73 offen offset:2048 lds\n" \
  "v_mfma_f32_16x16x32_bf16 v[152:155], a[112:115], a[16:19], v[152:155]\n" \
  "ds_read_b128 a[72:75], v210 offset:49152\n" \
  "ds_read_b64_tr_b16 a[160:161], v212 offset:49408\n" \
  "v_mfma_f32_16x16x32_bf16 v[156:159], a[112:115], a[48:51], v[156:159]\n" \
  "ds_read_b64_tr_b16 a[162:163], v212 offset:57600\n" \
  "v_bitop3_b32 v248, v248, v183, s76 bitop3:0xa8\n" \
  "v_mfma_f32_16x16x32_bf16 v[152:155], a[116:119], a[20:23], v[152:155]\n" \
  "ds_read_b128 a[76:79], v211 offset:49152\n" \
  "v_cmp_ne_u32_e64 vcc, 0, v248\n" \
  "v_mfma_f32_16x16x32_bf16 v[156:159], a[116:119], a[52:55], v[156:159]\n" \
  "ds_read_b64_tr_b16 a[164:165], v213 offset:49408\n" \
  "v_mfma_f32_16x16x32_bf16 v[152:155], a[120:123], a[24:27], v[152:155]\n" \
  "ds_read_b64_tr_b16 a[166:167], v213 offset:57600\n" \
  "v_mfma_f32_16x16x32_bf16 v[156:159], a[120:123], a[56:59], v[156:159]\n" \
  "ds_read_b128 a[80:83], v208 offset:49408\n" \
  "v_mfma_f32_16x16x32_bf16 v[152:155], a[124:127], a[28:31], v[152:155]\n" \
  "ds_read_b64_tr_b16 a[168:169], v214 offset:49408\n" \
  "s_waitcnt lgkmcnt(6)\n" \
  "v_mfma_f32_16x16x32_bf16 v[156:159], a[124:127], a[60:63], v[156:159]\n" \
  "s_cbranch_vccnz L_rare_0_%=\n" \
  "L_back_0_%=:\n" \
  "v_mfma_f32_16x16x32_bf16 v[0:3], a[128:131], v[176:179], v[0:3]\n" \
  "ds_read_b64_tr_b16 a[170:171], v214 offset:57600\n" \
  "ds_read_b128 a[84:87], v209 offset:49408\n" \
  "v_mfma_f32_16x16x32_bf16 v[4:7], a[128:131], v[180:183], v[4:7]\n" \
  "v_exp_f32_e64 v192, v144\n" \
  "v_mfma_f32_16x16x32_bf16 v[8:11], a[132:135], v[176:179], v[8:11]\n" \
  "ds_read_b64_tr_b16 a[172:173], v215 offset:49408\n" \
  "v_mfma_f32_16x16x32_bf16 v[12:15], a[132:135], v[180:183], v[12:15]\n" \
  "ds_read_b64_tr_b16 a[174:175], v215 offset:57600\n" \
  "v_mfma_f32_16x16x32_bf16 v[16:19], a[136:139], v[176:179], v[16:19]\n" \
  "ds_read_b128 a[88:91], v210 offset:49408\n" \
  "v_mfma_f32_16x16x32_bf16 v[20:23], a[136:139], v[180:183], v[20:23]\n" \
  "v_exp_f32_e64 v193, v145\n" \
  "v_mfma_f32_16x16x32_bf16 v[24:27], a[140:143], v[176:179], v[24:27]\n" \
  "ds_read_b64_tr_b16 a[176:177], v216 offset:49408\n" \
  "v_cvt_pk_bf16_f32 v184, v192, v193\n" \
  "v_mfma_f32_16x16x32_bf16 v[28:31], a[140:143], v[180:183], v[28:31]\n" \
  "buffer_load_dwordx4 v227, s[40:43], s73 offen offset:3072 lds\n" \
  "v_mfma_f32_16x16x32_bf16 v[32:35], a[144:147], v[176:179], v[32:35]\n" \
  "ds_read_b64_tr_b16 a[178:179], v216 offset:57600\n" \
  "ds_read_b128 a[92:95], v211 offset:49408\n" \
  "v_mfma_f32_16x16x32_bf16 v[36:39], a[144:147], v[180:183], v[36:39]\n" \
  "s_add_u32 m0, s75, 0x8000\n" \
  "v_exp_f32_e64 v194, v146\n" \
  "v_mfma_f32_16x16x32_bf16 v[40:43], a[148:151], v[176:179], v[40:43]\n" \
  "ds_read_b64_tr_b16 a[180:181], v217 offset:49408\n" \
  "v_mfma_f32_16x16x32_bf16 v[44:47], a[148:151], v[180:183], v[44:47]\n" \
  "ds_read_b64_tr_b16 a[182:183], v217 offset:57600\n" \
  "v_mfma_f32_16x16x32_bf16 v[48:51], a[152:155], v[176:179], v[48:51]\n" \
  "ds_read_b128 a[96:99], v208 offset:57344\n" \
  "v_mfma_f32_16x16x32_bf16 v[52:55], a[152:155], v[180:183], v[52:55]\n" \
  "s_waitcnt lgkmcnt(5)\n" \
  "v_exp_f32 v195, v147\n" \
  "v_mfma_f32_16x16x32_bf16 v[56:59], a[156:159], v[176:179], v[56:59]\n" \
  "ds_read_b64_tr_b16 a[184:185], v218 offset:49408\n" \
  "v_cvt_pk_bf16_f32 v185, v194, v195\n" \
  "v_mfma_f32_16x16x32_bf16 v[60:63], a[156:159], v[180:183], v[60:63]\n" \
  "buffer_load_dwordx4 v220, s[36:39], s72 offen lds\n" \
  "v_mfma_f32_16x16x32_bf16 v[64:67], a[160:163], v[176:179], v[64:67]\n" \
  "ds_read_b64_tr_b16 a[186:187], v218 offset:57600\n" \
  "ds_read_b128 a[100:103], v209 offset:57344\n" \
  "v_mfma_f32_16x16x32_bf16 v[68:71], a[160:163], v[180:183], v[68:71]\n" \
  "v_exp_f32_e64 v196, v148\n" \
  "v_mfma_f32_16x16x32_bf16 v[72:75], a[164:167], v[176:179], v[72:75]\n" \
  "ds_read_b64_tr_b16 a[188:189], v219 offset:49408\n" \
  "v_mfma_f32_16x16x32_bf16 v[76:79], a[164:167], v[180:183], v[76:79]\n" \
  "ds_read_b64_tr_b16 a[190:191], v219 offset:57600\n" \
  "v_mfma_f32_16x16x32_bf16 v[80:83], a[168:171], v[176:179], v[80:83]\n" \
  "ds_read_b128 a[104:107], v210 offset:57344\n" \
  "v_mfma_f32_16x16x32_bf16 v[84:87], a[168:171], v[180:183], v[84:87]\n" \
  "s_waitcnt lgkmcnt(5)\n" \
  "v_exp_f32 v197, v149\n" \
  "v_mfma_f32_16x16x32_bf16 v[88:91], a[172:175], v[176:179], v[88:91]\n" \
  "ds_read_b128 a[108:111], v211 offset:57344\n" \
  "v_cvt_pk_bf16_f32 v188, v196, v197\n" \
  "v_mfma_f32_16x16x32_bf16 v[92:95], a[172:175], v[180:183], v[92:95]\n" \
  "buffer_load_dwordx4 v221, s[36:39], s72 offen offset:1024 lds\n" \
  "v_mfma_f32_16x16x32_bf16 v[96:99], a[176:179], v[176:179], v[96:99]\n" \
  "ds_read_b128 a[112:115], v208 offset:57600\n" \
  "ds_read_b128 a[116:119], v209 offset:57600\n" \
  "v_mfma_f32_16x16x32_bf16 v[100:103], a[176:179], v[180:183], v[100:103]\n" \
  "ds_read_b128 a[120:123], v210 offset:57600\n" \
  "v_mfma_f32_16x16x32_bf16 v[104:107], a[180:183], v[176:179], v[104:107]\n" \
  "ds_read_b128 a[124:127], v211 offset:57600\n" \
  "s_waitcnt lgkmcnt(5)\n" \
  "v_mfma_f32_16x16x32_bf16 v[108:111], a[180:183], v[180:183], v[108:111]\n" \
  "buffer_load_dwordx4 v222, s[36:39], s72 offen offset:2048 lds\n" \
  "v_mfma_f32_16x16x32_bf16 v[112:115], a[184:187], v[176:179], v[112:115]\n" \
  "v_exp_f32 v198, v150\n" \
  "v_mfma_f32_16x16x32_bf16 v[116:119], a[184:187], v[180:183], v[116:119]\n" \
  "buffer_load_dwordx4 v223, s[36:39], s72 offen offset:3072 lds\n" \
  "v_mfma_f32_16x16x32_bf16 v[120:123], a[188:191], v[176:179], v[120:123]\n" \
  "v_exp_f32_e64 v199, v151\n" \
  "v_mfma_f32_16x16x32_bf16 v[124:127], a[188:191], v[180:183], v[124:127]\n" \
  "v_exp_f32_e64 v192, v152\n" \
  "v_mfma_f32_16x16x32_bf16 v[200:203], v[172:175], v[176:179], v[200:203]\n" \
  "v_exp_f32_e64 v193, v153\n" \
  "v_mfma_f32_16x16x32_bf16 v[204:207], v[172:175], v[180:183], v[204:207]\n" \
  "s_waitcnt vmcnt(16) lgkmcnt(0)\n" \
  "s_barrier\n" \
  "; attn phase 5\n" \
  "v_mfma_f32_16x16x32_bf16 v[128:131], a[64:67], a[0:3], v[160:163]\n" \
  "ds_read_b64_tr_b16 a[128:129], v212\n" \
  "ds_read_b64_tr_b16 a[130:131], v212 offset:8192\n" \
  "v_mfma_f32_16x16x32_bf16 v[132:135], a[64:67], a[32:35], v[164:167]\n" \
  "s_add_u32 s73, s73, 0x4000\n" \
  "v_exp_f32_e64 v194, v154\n" \
  "v_mfma_f32_16x16x32_bf16 v[128:131], a[68:71], a[4:7], v[128:131]\n" \
  "ds_read_b64_tr_b16 a[132:133], v213\n" \
  "v_cvt_pk_bf16_f32 v189, v198, v199\n" \
  "v_mfma_f32_16x16x32_bf16 v[132:135], a[68:71], a[36:39], v[132:135]\n" \
  "ds_read_b64_tr_b16 a[134:135], v213 offset:8192\n" \
  "v_cvt_pk_bf16_f32 v186, v192, v193\n" \
  "v_mfma_f32_16x16x32_bf16 v[128:131], a[72:75], a[8:11], v[128:131]\n" \
  "ds_read_b64_tr_b16 a[136:137], v214\n" \
  "v_or3_b32 v248, v184, v185, v188\n" \
  "v_mfma_f32_16x16x32_bf16 v[132:135], a[72:75], a[40:43], v[132:135]\n" \
  "s_add_u32 m0, s74, 0xc000\n" \
  "v_exp_f32_e64 v195, v155\n" \
  "v_mfma_f32_16x16x32_bf16 v[128:131], a[76:79], a[12:15], v[128:131]\n" \
  "ds_read_b64_tr_b16 a[138:139], v214 offset:8192\n" \
  "v_cvt_pk_bf16_f32 v187, v194, v195\n" \
  "v_mfma_f32_16x16x32_bf16 v[132:135], a[76:79], a[44:47], v[132:135]\n" \
  "buffer_load_dwordx4 v224, s[40:43], s73 offen lds\n" \
  "v_mfma_f32_16x16x32_bf16 v[128:131], a[80:83], a[16:19], v[128:131]\n" \
  "ds_read_b64_tr_b16 a[140:141], v215\n" \
  "ds_read_b64_tr_b16 a[142:143], v215 offset:8192\n" \
  "v_mfma_f32_16x16x32_bf16 v[132:135], a[80:83], a[48:51], v[132:135]\n" \
  "s_add_u32 s72, s72, 0x4000\n" \
  "v_exp_f32_e64 v196, v156\n" \
  "v_mfma_f32_16x16x32_bf16 v[128:131], a[84:87], a[20:23], v[128:131]\n" \
  "ds_read_b64_tr_b16 a[144:145], v216\n" \
  "v_or3_b32 v248, v248, v189, v186\n" \
  "v_mfma_f32_16x16x32_bf16 v[132:135], a[84:87], a[52:55], v[132:135]\n" \
  "ds_read_b64_tr_b16 a[146:147], v216 offset:8192\n" \
  "v_mfma_f32_16x16x32_bf16 v[128:131], a[88:91], a[24:27], v[128:131]\n" \
  "ds_read_b64_tr_b16 a[148:149], v217\n" \
  "v_mfma_f32_16x16x32_bf16 v[132:135], a[88:91], a[56:59], v[132:135]\n" \
  "v_exp_f32_e64 v197, v157\n" \
  "v_mfma_f32_16x16x32_bf16 v[128:131], a[92:95], a[28:31], v[128:131]\n" \
  "ds_read_b64_tr_b16 a[150:151], v217 offset:8192\n" \
  "v_cvt_pk_bf16_f32 v190, v196, v197\n" \
  "v_mfma_f32_16x16x32_bf16 v[132:135], a[92:95], a[60:63], v[132:135]\n" \
  "buffer_load_dwordx4 v225, s[40:43], s73 offen offset:1024 lds\n" \
  "v_mfma_f32_16x16x32_bf16 v[136:139], a[96:99], a[0:3], v[160:163]\n" \
  "ds_read_b128 a[64:67], v208\n" \
  "ds_read_b64_tr_b16 a[152:153], v218\n" \
  "v_mfma_f32_16x16x32_bf16 v[140:143], a[96:99], a[32:35], v[164:167]\n" \
  "v_exp_f32_e64 v198, v158\n" \
  "v_mfma_f32_16x16x32_bf16 v[136:139], a[100:103], a[4:7], v[136:139]\n" \
  "ds_read_b64_tr_b16 a[154:155], v218 offset:8192\n" \
  "v_or3_b32 v248, v248, v187, v190\n" \
  "v_mfma_f32_16x16x32_bf16 v[140:143], a[100:103], a[36:39], v[140:143]\n" \
  "ds_read_b128 a[68:71], v209\n" \
  "v_mfma_f32_16x16x32_bf16 v[136:139], a[104:107], a[8:11], v[136:139]\n" \
  "ds_read_b64_tr_b16 a[156:157], v219\n" \
  "v_mfma_f32_16x16x32_bf16 v[140:143], a[104:107], a[40:43], v[140:143]\n" \
  "v_exp_f32_e64 v199, v159\n" \
  "v_mfma_f32_16x16x32_bf16 v[136:139], a[108:111], a[12:15], v[136:139]\n" \
  "ds_read_b64_tr_b16 a[158:159], v219 offset:8192\n" \
  "v_cvt_pk_bf16_f32 v191, v198, v199\n" \
  "v_mfma_f32_16x16x32_bf16 v[140:143], a[108:111], a[44:47], v[140:143]\n" \
  "buffer_load_dwordx4 v226, s[40:43], s73 offen offset:2048 lds\n" \
  "v_mfma_f32_16x16x32_bf16 v[136:139], a[112:115], a[16:19], v[136:139]\n" \
  "ds_read_b128 a[72:75], v210\n" \
  "ds_read_b64_tr_b16 a[160:161], v212 offset:256\n" \
  "v_mfma_f32_16x16x32_bf16 v[140:143], a[112:115], a[48:51], v[140:143]\n" \
  "ds_read_b64_tr_b16 a[162:163], v212 offset:8448\n" \
  "v_bitop3_b32 v248, v248, v191, s76 bitop3:0xa8\n" \
  "v_mfma_f32_16x16x32_bf16 v[136:139], a[116:119], a[20:23], v[136:139]\n" \
  "ds_read_b128 a[76:79], v211\n" \
  "v_cmp_ne_u32_e64 vcc, 0, v248\n" \
  "v_mfma_f32_16x16x32_bf16 v[140:143], a[116:119], a[52:55], v[140:143]\n" \
  "ds_read_b64_tr_b16 a[164:165], v213 offset:256\n" \
  "v_mfma_f32_16x16x32_bf16 v[136:139], a[120:123], a[24:27], v[136:139]\n" \
  "ds_read_b64_tr_b16 a[166:167], v213 offset:8448\n" \
  "v_mfma_f32_16x16x32_bf16 v[140:143], a[120:123], a[56:59], v[140:143]\n" \
  "ds_read_b128 a[80:83], v208 offset:256\n" \
  "v_mfma_f32_16x16x32_bf16 v[136:139], a[124:127], a[28:31], v[136:139]\n" \
  "ds_read_b64_tr_b16 a[168:169], v214 offset:256\n" \
  "s_waitcnt lgkmcnt(6)\n" \
  "v_mfma_f32_16x16x32_bf16 v[140:143], a[124:127], a[60:63], v[140:143]\n" \
  "s_cbranch_vccnz L_rare_1_%=\n" \
  "L_back_1_%=:\n" \
  "v_mfma_f32_16x16x32_bf16 v[0:3], a[128:131], v[184:187], v[0:3]\n" \
  "ds_read_b64_tr_b16 a[170:171], v214 offset:8448\n" \
  "ds_read_b128 a[84:87], v209 offset:256\n" \
  "v_mfma_f32_16x16x32_bf16 v[4:7], a[128:131], v[188:191], v[4:7]\n" \
  "v_exp_f32_e64 v192, v128\n" \
  "v_mfma_f32_16x16x32_bf16 v[8:11], a[132:135], v[184:187], v[8:11]\n" \
  "ds_read_b64_tr_b16 a[172:173], v215 offset:256\n" \
  "v_mfma_f32_16x16x32_bf16 v[12:15], a[132:135], v[188:191], v[12:15]\n" \
  "ds_read_b64_tr_b16 a[174:175], v215 offset:8448\n" \
  "v_mfma_f32_16x16x32_bf16 v[16:19], a[136:139], v[184:187], v[16:19]\n" \
  "ds_read_b128 a[88:91], v210 offset:256\n" \
  "v_mfma_f32_16x16x32_bf16 v[20:23], a[136:139], v[188:191], v[20:23]\n" \
  "v_exp_f32_e64 v193, v129\n" \
  "v_mfma_f32_16x16x32_bf16 v[24:27], a[140:143], v[184:187], v[24:27]\n" \
  "ds_read_b64_tr_b16 a[176:177], v216 offset:256\n" \
  "v_cvt_pk_bf16_f32 v176, v192, v193\n" \
  "v_mfma_f32_16x16x32_bf16 v[28:31], a[140:143], v[188:191], v[28:31]\n" \
  "buffer_load_dwordx4 v227, s[40:43], s73 offen offset:3072 lds\n" \
  "v_mfma_f32_16x16x32_bf16 v[32:35], a[144:147], v[184:187], v[32:35]\n" \
  "ds_read_b64_tr_b16 a[178:179], v216 offset:8448\n" \
  "ds_read_b128 a[92:95], v211 offset:256\n" \
  "v_mfma_f32_16x16x32_bf16 v[36:39], a[144:147], v[188:191], v[36:39]\n" \
  "s_add_u32 m0, s75, 0xc000\n" \
  "v_exp_f32_e64 v194, v130\n" \
  "v_mfma_f32_16x16x32_bf16 v[40:43], a[148:151], v[184:187], v[40:43]\n" \
  "ds_read_b64_tr_b16 a[180:181], v217 offset:256\n" \
  "v_mfma_f32_16x16x32_bf16 v[44:47], a[148:151], v[188:191], v[44:47]\n" \
  "ds_read_b64_tr_b16 a[182:183], v217 offset:8448\n" \
  "v_mfma_f32_16x16x32_bf16 v[48:51], a[152:155], v[184:187], v[48:51]\n" \
  "ds_read_b128 a[96:99], v208 offset:8192\n" \
  "v_mfma_f32_16x16x32_bf16 v[52:55], a[152:155], v[188:191], v[52:55]\n" \
  "s_waitcnt lgkmcnt(5)\n" \
  "v_exp_f32 v195, v131\n" \
  "v_mfma_f32_16x16x32_bf16 v[56:59], a[156:159], v[184:187], v[56:59]\n" \
  "ds_read_b64_tr_b16 a[184:185], v218 offset:256\n" \
  "v_cvt_pk_bf16_f32 v177, v194, v195\n" \
  "v_mfma_f32_16x16x32_bf16 v[60:63], a[156:159], v[188:191], v[60:63]\n" \
  "buffer_load_dwordx4 v220, s[36:39], s72 offen lds\n" \
  "v_mfma_f32_16x16x32_bf16 v[64:67], a[160:163], v[184:187], v[64:67]\n" \
  "ds_read_b64_tr_b16 a[186:187], v218 offset:8448\n" \
  "ds_read_b128 a[100:103], v209 offset:8192\n" \
  "v_mfma_f32_16x16x32_bf16 v[68:71], a[160:163], v[188:191], v[68:71]\n" \
  "v_exp_f32_e64 v196, v132\n" \
  "v_mfma_f32_16x16x32_bf16 v[72:75], a[164:167], v[184:187], v[72:75]\n" \
  "ds_read_b64_tr_b16 a[188:189], v219 offset:256\n" \
  "v_mfma_f32_16x16x32_bf16 v[76:79], a[164:167], v[188:191], v[76:79]\n" \
  "ds_read_b64_tr_b16 a[190:191], v219 offset:8448\n" \
  "v_mfma_f32_16x16x32_bf16 v[80:83], a[168:171], v[184:187], v[80:83]\n" \
  "ds_read_b128 a[104:107], v210 offset:8192\n" \
  "v_mfma_f32_16x16x32_bf16 v[84:87], a[168:171], v[188:191], v[84:87]\n" \
  "s_waitcnt lgkmcnt(5)\n" \
  "v_exp_f32 v197, v133\n" \
  "v_mfma_f32_16x16x32_bf16 v[88:91], a[172:175], v[184:187], v[88:91]\n" \
  "ds_read_b128 a[108:111], v211 offset:8192\n" \
  "v_cvt_pk_bf16_f32 v180, v196, v197\n" \
  "v_mfma_f32_16x16x32_bf16 v[92:95], a[172:175], v[188:191], v[92:95]\n" \
  "buffer_load_dwordx4 v221, s[36:39], s72 offen offset:1024 lds\n" \
  "v_mfma_f32_16x16x32_bf16 v[96:99], a[176:179], v[184:187], v[96:99]\n" \
  "ds_read_b128 a[112:115], v208 offset:8448\n" \
  "ds_read_b128 a[116:119], v209 offset:8448\n" \
  "v_mfma_f32_16x16x32_bf16 v[100:103], a[176:179], v[188:191], v[100:103]\n" \
  "ds_read_b128 a[120:123], v210 offset:8448\n" \
  "v_mfma_f32_16x16x32_bf16 v[104:107], a[180:183], v[184:187], v[104:107]\n" \
  "ds_read_b128 a[124:127], v211 offset:8448\n" \
  "s_waitcnt lgkmcnt(5)\n" \
  "v_mfma_f32_16x16x32_bf16 v[108:111], a[180:183], v[188:191], v[108:111]\n" \
  "buffer_load_dwordx4 v222, s[36:39], s72 offen offset:2048 lds\n" \
  "v_mfma_f32_16x16x32_bf16 v[112:115], a[184:187], v[184:187], v[112:115]\n" \
  "v_exp_f32 v198, v134\n" \
  "v_mfma_f32_16x16x32_bf16 v[116:119], a[184:187], v[188:191], v[116:119]\n" \
  "buffer_load_dwordx4 v223, s[36:39], s72 offen offset:3072 lds\n" \
  "v_mfma_f32_16x16x32_bf16 v[120:123], a[188:191], v[184:187], v[120:123]\n" \
  "v_exp_f32_e64 v199, v135\n" \
  "v_mfma_f32_16x16x32_bf16 v[124:127], a[188:191], v[188:191], v[124:127]\n" \
  "v_exp_f32_e64 v192, v136\n" \
  "v_mfma_f32_16x16x32_bf16 v[200:203], v[172:175], v[184:187], v[200:203]\n" \
  "v_exp_f32_e64 v193, v137\n" \
  "v_mfma_f32_16x16x32_bf16 v[204:207], v[172:175], v[188:191], v[204:207]\n" \
  "s_waitcnt vmcnt(16) lgkmcnt(0)\n" \
  "s_barrier\n" \
  "s_sub_u32 s77, s77, 1\n" \
  "s_cmp_lg_u32 s77, 0\n" \
  "s_cbranch_scc1 L_loop_%=\n" \
  "s_and_b32 s95, %1, 1\n" \
  "s_mul_i32 s95, s95, 0xe000\n" \
  "; attn phase 58\n" \
  "v_mfma_f32_16x16x32_bf16 v[144:147], a[64:67], a[0:3], v[160:163]\n" \
  "ds_read_b64_tr_b16 a[128:129], v212 offset:16384\n" \
  "ds_read_b64_tr_b16 a[130:131], v212 offset:24576\n" \
  "global_load_dword v171, v235, s[34:35]\n" \
  "v_mfma_f32_16x16x32_bf16 v[148:151], a[64:67], a[32:35], v[164:167]\n" \
  "s_mov_b32 s73, 0xf4000\n" \
  "v_exp_f32_e64 v194, v138\n" \
  "v_mfma_f32_16x16x32_bf16 v[144:147], a[68:71], a[4:7], v[144:147]\n" \
  "ds_read_b64_tr_b16 a[132:133], v213 offset:16384\n" \
  "v_cvt_pk_bf16_f32 v181, v198, v199\n" \
  "v_mfma_f32_16x16x32_bf16 v[148:151], a[68:71], a[36:39], v[148:151]\n" \
  "ds_read_b64_tr_b16 a[134:135], v213 offset:24576\n" \
  "v_cvt_pk_bf16_f32 v178, v192, v193\n" \
  "buffer_load_dwordx2 v[240:241], v242, s[68:71], 0 offen sc1\n" \
  "v_mfma_f32_16x16x32_bf16 v[144:147], a[72:75], a[8:11], v[144:147]\n" \
  "ds_read_b64_tr_b16 a[136:137], v214 offset:16384\n" \
  "v_or3_b32 v248, v176, v177, v180\n" \
  "v_mfma_f32_16x16x32_bf16 v[148:151], a[72:75], a[40:43], v[148:151]\n" \
  "s_add_u32 m0, s74, 0\n" \
  "v_exp_f32 v195, v139\n" \
  "v_readlane_b32 s60, v255, 40\n" \
  "v_mfma_f32_16x16x32_bf16 v[144:147], a[76:79], a[12:15], v[144:147]\n" \
  "ds_read_b64_tr_b16 a[138:139], v214 offset:24576\n" \
  "v_cvt_pk_bf16_f32 v179, v194, v195\n" \
  "v_mfma_f32_16x16x32_bf16 v[148:151], a[76:79], a[44:47], v[148:151]\n" \
  "buffer_load_dwordx4 v224, s[40:43], s73 offen lds\n" \
  "v_readlane_b32 s61, v255, 41\n" \
  "v_mfma_f32_16x16x32_bf16 v[144:147], a[80:83], a[16:19], v[144:147]\n" \
  "ds_read_b64_tr_b16 a[140:141], v215 offset:16384\n" \
  "ds_read_b64_tr_b16 a[142:143], v215 offset:24576\n" \
  "v_mfma_f32_16x16x32_bf16 v[148:151], a[80:83], a[48:51], v[148:151]\n" \
  "s_mov_b32 s72, 0xfc000\n" \
  "v_exp_f32_e64 v196, v140\n" \
  "v_readlane_b32 s62, v255, 42\n" \
  "v_mfma_f32_16x16x32_bf16 v[144:147], a[84:87], a[20:23], v[144:147]\n" \
  "ds_read_b64_tr_b16 a[144:145], v216 offset:16384\n" \
  "v_or3_b32 v248, v248, v181, v178\n" \
  "v_mfma_f32_16x16x32_bf16 v[148:151], a[84:87], a[52:55], v[148:151]\n" \
  "ds_read_b64_tr_b16 a[146:147], v216 offset:24576\n" \
  "v_readlane_b32 s44, v255, 43\n" \
  "v_mfma_f32_16x16x32_bf16 v[144:147], a[88:91], a[24:27], v[144:147]\n" \
  "ds_read_b64_tr_b16 a[148:149], v217 offset:16384\n" \
  "v_readlane_b32 s45, v255, 44\n" \
  "v_mfma_f32_16x16x32_bf16 v[148:151], a[88:91], a[56:59], v[148:151]\n" \
  "v_exp_f32_e64 v197, v141\n" \
  "v_mfma_f32_16x16x32_bf16 v[144:147], a[92:95], a[28:31], v[144:147]\n" \
  "ds_read_b64_tr_b16 a[150:151], v217 offset:24576\n" \
  "v_cvt_pk_bf16_f32 v182, v196, v197\n" \
  "v_mfma_f32_16x16x32_bf16 v[148:151], a[92:95], a[60:63], v[148:151]\n" \
  "buffer_load_dwordx4 v225, s[40:43], s73 offen offset:1024 lds\n" \
  "v_mfma_f32_16x16x32_bf16 v[152:155], a[96:99], a[0:3], v[160:163]\n" \
  "ds_read_b128 a[64:67], v208 offset:16384\n" \
  "ds_read_b64_tr_b16 a[152:153], v218 offset:16384\n" \
  "v_readlane_b32 s46, v255, 45\n" \
  "v_mfma_f32_16x16x32_bf16 v[156:159], a[96:99], a[32:35], v[164:167]\n" \
  "v_exp_f32_e64 v198, v142\n" \
  "v_mfma_f32_16x16x32_bf16 v[152:155], a[100:103], a[4:7], v[152:155]\n" \
  "ds_read_b64_tr_b16 a[154:155], v218 offset:24576\n" \
  "v_or3_b32 v248, v248, v179, v182\n" \
  "v_readlane_b32 s56, v255, 46\n" \
  "v_mfma_f32_16x16x32_bf16 v[156:159], a[100:103], a[36:39], v[156:159]\n" \
  "ds_read_b128 a[68:71], v209 offset:16384\n" \
  "v_readlane_b32 s57, v255, 47\n" \
  "v_mfma_f32_16x16x32_bf16 v[152:155], a[104:107], a[8:11], v[152:155]\n" \
  "ds_read_b64_tr_b16 a[156:157], v219 offset:16384\n" \
  "v_readlane_b32 s58, v255, 48\n" \
  "v_mfma_f32_16x16x32_bf16 v[156:159], a[104:107], a[40:43], v[156:159]\n" \
  "s_waitcnt lgkmcnt(5)\n" \
  "v_exp_f32 v199, v143\n" \
  "v_mfma_f32_16x16x32_bf16 v[152:155], a[108:111], a[12:15], v[152:155]\n" \
  "ds_read_b64_tr_b16 a[158:159], v219 offset:24576\n" \
  "v_cvt_pk_bf16_f32 v183, v198, v199\n" \
  "v_mfma_f32_16x16x32_bf16 v[156:159], a[108:111], a[44:47], v[156:159]\n" \
  "buffer_load_dwordx4 v226, s[40:43], s73 offen offset:2048 lds\n" \
  "v_mfma_f32_16x16x32_bf16 v[152:155], a[112:115], a[16:19], v[152:155]\n" \
  "ds_read_b128 a[72:75], v210 offset:16384\n" \
  "ds_read_b64_tr_b16 a[160:161], v212 offset:16640\n" \
  "v_readlane_b32 s48, v255, 49\n" \
  "v_mfma_f32_16x16x32_bf16 v[156:159], a[112:115], a[48:51], v[156:159]\n" \
  "ds_read_b64_tr_b16 a[162:163], v212 offset:24832\n" \
  "v_bitop3_b32 v248, v248, v183, s76 bitop3:0xa8\n" \
  "v_mfma_f32_16x16x32_bf16 v[152:155], a[116:119], a[20:23], v[152:155]\n" \
  "ds_read_b128 a[76:79], v211 offset:16384\n" \
  "v_cmp_ne_u32_e64 vcc, 0, v248\n" \
  "v_mfma_f32_16x16x32_bf16 v[156:159], a[116:119], a[52:55], v[156:159]\n" \
  "ds_read_b64_tr_b16 a[164:165], v213 offset:16640\n" \
  "v_readlane_b32 s49, v255, 50\n" \
  "v_mfma_f32_16x16x32_bf16 v[152:155], a[120:123], a[24:27], v[152:155]\n" \
  "ds_read_b64_tr_b16 a[166:167], v213 offset:24832\n" \
  "v_readlane_b32 s50, v255, 51\n" \
  "v_mfma_f32_16x16x32_bf16 v[156:159], a[120:123], a[56:59], v[156:159]\n" \
  "ds_read_b128 a[80:83], v208 offset:16640\n" \
  "v_and_b32_e64 v243, 15, v253\n" \
  "v_mfma_f32_16x16x32_bf16 v[152:155], a[124:127], a[28:31], v[152:155]\n" \
  "ds_read_b64_tr_b16 a[168:169], v214 offset:16640\n" \
  "v_mul_u32_u24 v243, 0x380, v243\n" \
  "v_mfma_f32_16x16x32_bf16 v[156:159], a[124:127], a[60:63], v[156:159]\n" \
  "s_cbranch_vccnz L_rare_p58_%=\n" \
  "L_back_p58_%=:\n" \
  "v_mfma_f32_16x16x32_bf16 v[0:3], a[128:131], v[176:179], v[0:3]\n" \
  "ds_read_b64_tr_b16 a[170:171], v214 offset:24832\n" \
  "ds_read_b128 a[84:87], v209 offset:16640\n" \
  "v_mfma_f32_16x16x32_bf16 v[4:7], a[128:131], v[180:183], v[4:7]\n" \
  "v_exp_f32 v192, v144\n" \
  "v_mfma_f32_16x16x32_bf16 v[8:11], a[132:135], v[176:179], v[8:11]\n" \
  "ds_read_b64_tr_b16 a[172:173], v215 offset:16640\n" \
  "v_lshrrev_b32_e64 v251, 4, v253\n" \
  "v_mfma_f32_16x16x32_bf16 v[12:15], a[132:135], v[180:183], v[12:15]\n" \
  "ds_read_b64_tr_b16 a[174:175], v215 offset:24832\n" \
  "v_lshl_add_u32 v243, v251, 3, v243\n" \
  "v_mfma_f32_16x16x32_bf16 v[16:19], a[136:139], v[176:179], v[16:19]\n" \
  "ds_read_b128 a[88:91], v210 offset:16640\n" \
  "v_lshrrev_b32_e64 v251, 3, v253\n" \
  "v_mfma_f32_16x16x32_bf16 v[20:23], a[136:139], v[180:183], v[20:23]\n" \
  "s_waitcnt lgkmcnt(5)\n" \
  "v_exp_f32 v193, v145\n" \
  "v_mfma_f32_16x16x32_bf16 v[24:27], a[140:143], v[176:179], v[24:27]\n" \
  "ds_read_b64_tr_b16 a[176:177], v216 offset:16640\n" \
  "v_cvt_pk_bf16_f32 v184, v192, v193\n" \
  "v_mfma_f32_16x16x32_bf16 v[28:31], a[140:143], v[180:183], v[28:31]\n" \
  "buffer_load_dwordx4 v227, s[40:43], s73 offen offset:3072 lds\n" \
  "v_mfma_f32_16x16x32_bf16 v[32:35], a[144:147], v[176:179], v[32:35]\n" \
  "ds_read_b64_tr_b16 a[178:179], v216 offset:24832\n" \
  "ds_read_b128 a[92:95], v211 offset:16640\n" \
  "v_mfma_f32_16x16x32_bf16 v[36:39], a[144:147], v[180:183], v[36:39]\n" \
  "s_add_u32 m0, s75, 0\n" \
  "v_exp_f32 v194, v146\n" \
  "v_mfma_f32_16x16x32_bf16 v[40:43], a[148:151], v[176:179], v[40:43]\n" \
  "ds_read_b64_tr_b16 a[180:181], v217 offset:16640\n" \
  "v_mul_u32_u24 v251, 0x380, v251\n" \
  "v_mfma_f32_16x16x32_bf16 v[44:47], a[148:151], v[180:183], v[44:47]\n" \
  "ds_read_b64_tr_b16 a[182:183], v217 offset:24832\n" \
  "v_add_u32_e64 v251, s85, v251\n" \
  "v_mfma_f32_16x16x32_bf16 v[48:51], a[152:155], v[176:179], v[48:51]\n" \
  "ds_read_b128 a[96:99], v208 offset:24576\n" \
  "v_lshrrev_b32_e64 v252, 3, v253\n" \
  "v_mfma_f32_16x16x32_bf16 v[52:55], a[152:155], v[180:183], v[52:55]\n" \
  "v_exp_f32_e64 v195, v147\n" \
  "v_mfma_f32_16x16x32_bf16 v[56:59], a[156:159], v[176:179], v[56:59]\n" \
  "ds_read_b64_tr_b16 a[184:185], v218 offset:16640\n" \
  "v_cvt_pk_bf16_f32 v185, v194, v195\n" \
  "v_mfma_f32_16x16x32_bf16 v[60:63], a[156:159], v[180:183], v[60:63]\n" \
  "buffer_load_dwordx4 v220, s[36:39], s72 offen lds\n" \
  "v_mfma_f32_16x16x32_bf16 v[64:67], a[160:163], v[176:179], v[64:67]\n" \
  "ds_read_b64_tr_b16 a[186:187], v218 offset:24832\n" \
  "ds_read_b128 a[100:103], v209 offset:24576\n" \
  "v_and_b32_e64 v252, 6, v252\n" \
  "v_mfma_f32_16x16x32_bf16 v[68:71], a[160:163], v[180:183], v[68:71]\n" \
  "s_waitcnt lgkmcnt(4)\n" \
  "v_exp_f32 v196, v148\n" \
  "v_mfma_f32_16x16x32_bf16 v[72:75], a[164:167], v[176:179], v[72:75]\n" \
  "ds_read_b64_tr_b16 a[188:189], v219 offset:16640\n" \
  "v_xor_b32_e64 v252, v252, v253\n" \
  "v_mfma_f32_16x16x32_bf16 v[76:79], a[164:167], v[180:183], v[76:79]\n" \
  "ds_read_b64_tr_b16 a[190:191], v219 offset:24832\n" \
  "v_and_b32_e64 v252, 7, v252\n" \
  "v_mfma_f32_16x16x32_bf16 v[80:83], a[168:171], v[176:179], v[80:83]\n" \
  "ds_read_b128 a[104:107], v210 offset:24576\n" \
  "v_lshl_add_u32 v251, v252, 4, v251\n" \
  "v_mfma_f32_16x16x32_bf16 v[84:87], a[168:171], v[180:183], v[84:87]\n" \
  "v_exp_f32_e64 v197, v149\n" \
  "v_mfma_f32_16x16x32_bf16 v[88:91], a[172:175], v[176:179], v[88:91]\n" \
  "ds_read_b128 a[108:111], v211 offset:24576\n" \
  "v_cvt_pk_bf16_f32 v188, v196, v197\n" \
  "v_mfma_f32_16x16x32_bf16 v[92:95], a[172:175], v[180:183], v[92:95]\n" \
  "buffer_load_dwordx4 v221, s[36:39], s72 offen offset:1024 lds\n" \
  "v_mfma_f32_16x16x32_bf16 v[96:99], a[176:179], v[176:179], v[96:99]\n" \
  "ds_read_b128 a[112:115], v208 offset:24832\n" \
  "ds_read_b128 a[116:119], v209 offset:24832\n" \
  "v_mfma_f32_16x16x32_bf16 v[100:103], a[176:179], v[180:183], v[100:103]\n" \
  "ds_read_b128 a[120:123], v210 offset:24832\n" \
  "v_add_u32_e64 v236, 0, v251\n" \
  "v_mfma_f32_16x16x32_bf16 v[104:107], a[180:183], v[176:179], v[104:107]\n" \
  "ds_read_b128 a[124:127], v211 offset:24832\n" \
  "v_add_u32 v237, 0x1800, v251\n" \
  "v_mfma_f32_16x16x32_bf16 v[108:111], a[180:183], v[180:183], v[108:111]\n" \
  "buffer_load_dwordx4 v222, s[36:39], s72 offen offset:2048 lds\n" \
  "s_waitcnt lgkmcnt(5)\n" \
  "v_mfma_f32_16x16x32_bf16 v[112:115], a[184:187], v[176:179], v[112:115]\n" \
  "v_exp_f32 v198, v150\n" \
  "v_mfma_f32_16x16x32_bf16 v[116:119], a[184:187], v[180:183], v[116:119]\n" \
  "buffer_load_dwordx4 v223, s[36:39], s72 offen offset:3072 lds\n" \
  "v_add_u32 v238, 0x3000, v251\n" \
  "v_mfma_f32_16x16x32_bf16 v[120:123], a[188:191], v[176:179], v[120:123]\n" \
  "v_exp_f32_e64 v199, v151\n" \
  "v_mfma_f32_16x16x32_bf16 v[124:127], a[188:191], v[180:183], v[124:127]\n" \
  "v_exp_f32_e64 v192, v152\n" \
  "v_add_u32 v239, 0x4800, v251\n" \
  "v_mfma_f32_16x16x32_bf16 v[200:203], v[172:175], v[176:179], v[200:203]\n" \
  "v_exp_f32_e64 v193, v153\n" \
  "v_mfma_f32_16x16x32_bf16 v[204:207], v[172:175], v[180:183], v[204:207]\n" \
  "s_waitcnt vmcnt(18) lgkmcnt(0)\n" \
  "s_barrier\n" \
  "; attn phase 59\n" \
  "v_mfma_f32_16x16x32_bf16 v[128:131], a[64:67], a[0:3], v[160:163]\n" \
  "ds_read_b64_tr_b16 a[128:129], v212 offset:32768\n" \
  "ds_read_b64_tr_b16 a[130:131], v212 offset:40960\n" \
  "v_mfma_f32_16x16x32_bf16 v[132:135], a[64:67], a[32:35], v[164:167]\n" \
  "s_mov_b32 s73, 0xf8000\n" \
  "v_exp_f32_e64 v194, v154\n" \
  "v_mfma_f32_16x16x32_bf16 v[128:131], a[68:71], a[4:7], v[128:131]\n" \
  "ds_read_b64_tr_b16 a[132:133], v213 offset:32768\n" \
  "v_cvt_pk_bf16_f32 v189, v198, v199\n" \
  "v_mfma_f32_16x16x32_bf16 v[132:135], a[68:71], a[36:39], v[132:135]\n" \
  "ds_read_b64_tr_b16 a[134:135], v213 offset:40960\n" \
  "v_cvt_pk_bf16_f32 v186, v192, v193\n" \
  "v_mfma_f32_16x16x32_bf16 v[128:131], a[72:75], a[8:11], v[128:131]\n" \
  "ds_read_b64_tr_b16 a[136:137], v214 offset:32768\n" \
  "v_or3_b32 v248, v184, v185, v188\n" \
  "v_mfma_f32_16x16x32_bf16 v[132:135], a[72:75], a[40:43], v[132:135]\n" \
  "s_add_u32 m0, s74, 0x4000\n" \
  "v_exp_f32_e64 v195, v155\n" \
  "v_mfma_f32_16x16x32_bf16 v[128:131], a[76:79], a[12:15], v[128:131]\n" \
  "ds_read_b64_tr_b16 a[138:139], v214 offset:40960\n" \
  "v_cvt_pk_bf16_f32 v187, v194, v195\n" \
  "v_mfma_f32_16x16x32_bf16 v[132:135], a[76:79], a[44:47], v[132:135]\n" \
  "buffer_load_dwordx4 v224, s[40:43], s73 offen lds\n" \
  "v_mfma_f32_16x16x32_bf16 v[128:131], a[80:83], a[16:19], v[128:131]\n" \
  "ds_read_b64_tr_b16 a[140:141], v215 offset:32768\n" \
  "ds_read_b64_tr_b16 a[142:143], v215 offset:40960\n" \
  "v_mfma_f32_16x16x32_bf16 v[132:135], a[80:83], a[48:51], v[132:135]\n" \
  "v_exp_f32_e64 v196, v156\n" \
  "v_mfma_f32_16x16x32_bf16 v[128:131], a[84:87], a[20:23], v[128:131]\n" \
  "ds_read_b64_tr_b16 a[144:145], v216 offset:32768\n" \
  "v_or3_b32 v248, v248, v189, v186\n" \
  "v_mfma_f32_16x16x32_bf16 v[132:135], a[84:87], a[52:55], v[132:135]\n" \
  "ds_read_b64_tr_b16 a[146:147], v216 offset:40960\n" \
  "v_readlane_b32 s36, v255, 37\n" \
  "v_mfma_f32_16x16x32_bf16 v[128:131], a[88:91], a[24:27], v[128:131]\n" \
  "ds_read_b64_tr_b16 a[148:149], v217 offset:32768\n" \
  "v_readlane_b32 s37, v255, 38\n" \
  "v_mfma_f32_16x16x32_bf16 v[132:135], a[88:91], a[56:59], v[132:135]\n" \
  "v_exp_f32_e64 v197, v157\n" \
  "v_mfma_f32_16x16x32_bf16 v[128:131], a[92:95], a[28:31], v[128:131]\n" \
  "ds_read_b64_tr_b16 a[150:151], v217 offset:40960\n" \
  "v_cvt_pk_bf16_f32 v190, v196, v197\n" \
  "v_mfma_f32_16x16x32_bf16 v[132:135], a[92:95], a[60:63], v[132:135]\n" \
  "buffer_load_dwordx4 v225, s[40:43], s73 offen offset:1024 lds\n" \
  "v_mfma_f32_16x16x32_bf16 v[136:139], a[96:99], a[0:3], v[160:163]\n" \
  "ds_read_b128 a[64:67], v208 offset:32768\n" \
  "ds_read_b64_tr_b16 a[152:153], v218 offset:32768\n" \
  "v_mfma_f32_16x16x32_bf16 v[140:143], a[96:99], a[32:35], v[164:167]\n" \
  "v_exp_f32_e64 v198, v158\n" \
  "v_mfma_f32_16x16x32_bf16 v[136:139], a[100:103], a[4:7], v[136:139]\n" \
  "ds_read_b64_tr_b16 a[154:155], v218 offset:40960\n" \
  "v_or3_b32 v248, v248, v187, v190\n" \
  "v_mfma_f32_16x16x32_bf16 v[140:143], a[100:103], a[36:39], v[140:143]\n" \
  "ds_read_b128 a[68:71], v209 offset:32768\n" \
  "v_readlane_b32 s38, v255, 39\n" \
  "v_mfma_f32_16x16x32_bf16 v[136:139], a[104:107], a[8:11], v[136:139]\n" \
  "ds_read_b64_tr_b16 a[156:157], v219 offset:32768\n" \
  "v_add_u32_e64 v222, s95, v250\n" \
  "v_mfma_f32_16x16x32_bf16 v[140:143], a[104:107], a[40:43], v[140:143]\n" \
  "v_exp_f32_e64 v199, v159\n" \
  "v_mfma_f32_16x16x32_bf16 v[136:139], a[108:111], a[12:15], v[136:139]\n" \
  "ds_read_b64_tr_b16 a[158:159], v219 offset:40960\n" \
  "v_cvt_pk_bf16_f32 v191, v198, v199\n" \
  "v_mfma_f32_16x16x32_bf16 v[140:143], a[108:111], a[44:47], v[140:143]\n" \
  "buffer_load_dwordx4 v226, s[40:43], s73 offen offset:2048 lds\n" \
  "v_mfma_f32_16x16x32_bf16 v[136:139], a[112:115], a[16:19], v[136:139]\n" \
  "ds_read_b128 a[72:75], v210 offset:32768\n" \
  "ds_read_b64_tr_b16 a[160:161], v212 offset:33024\n" \
  "v_mfma_f32_16x16x32_bf16 v[140:143], a[112:115], a[48:51], v[140:143]\n" \
  "ds_read_b64_tr_b16 a[162:163], v212 offset:41216\n" \
  "v_bitop3_b32 v248, v248, v191, s76 bitop3:0xa8\n" \
  "v_mfma_f32_16x16x32_bf16 v[136:139], a[116:119], a[20:23], v[136:139]\n" \
  "ds_read_b128 a[76:79], v211 offset:32768\n" \
  "v_cmp_ne_u32_e64 vcc, 0, v248\n" \
  "v_mfma_f32_16x16x32_bf16 v[140:143], a[116:119], a[52:55], v[140:143]\n" \
  "ds_read_b64_tr_b16 a[164:165], v213 offset:33024\n" \
  "v_subrev_u32_e64 v223, s95, v250\n" \
  "v_mfma_f32_16x16x32_bf16 v[136:139], a[120:123], a[24:27], v[136:139]\n" \
  "ds_read_b64_tr_b16 a[166:167], v213 offset:41216\n" \
  "v_add_u32 v223, 0xe000, v223\n" \
  "v_mfma_f32_16x16x32_bf16 v[140:143], a[120:123], a[56:59], v[140:143]\n" \
  "ds_read_b128 a[80:83], v208 offset:33024\n" \
  "v_mfma_f32_16x16x32_bf16 v[136:139], a[124:127], a[28:31], v[136:139]\n" \
  "ds_read_b64_tr_b16 a[168:169], v214 offset:33024\n" \
  "s_waitcnt lgkmcnt(6)\n" \
  "v_mfma_f32_16x16x32_bf16 v[140:143], a[124:127], a[60:63], v[140:143]\n" \
  "s_cbranch_vccnz L_rare_p59_%=\n" \
  "L_back_p59_%=:\n" \
  "v_mfma_f32_16x16x32_bf16 v[0:3], a[128:131], v[184:187], v[0:3]\n" \
  "ds_read_b64_tr_b16 a[170:171], v214 offset:41216\n" \
  "ds_read_b128 a[84:87], v209 offset:33024\n" \
  "v_mfma_f32_16x16x32_bf16 v[4:7], a[128:131], v[188:191], v[4:7]\n" \
  "v_exp_f32_e64 v192, v128\n" \
  "v_mfma_f32_16x16x32_bf16 v[8:11], a[132:135], v[184:187], v[8:11]\n" \
  "ds_read_b64_tr_b16 a[172:173], v215 offset:33024\n" \
  "v_mfma_f32_16x16x32_bf16 v[12:15], a[132:135], v[188:191], v[12:15]\n" \
  "ds_read_b64_tr_b16 a[174:175], v215 offset:41216\n" \
  "v_mfma_f32_16x16x32_bf16 v[16:19], a[136:139], v[184:187], v[16:19]\n" \
  "ds_read_b128 a[88:91], v210 offset:33024\n" \
  "v_mfma_f32_16x16x32_bf16 v[20:23], a[136:139], v[188:191], v[20:23]\n" \
  "v_exp_f32_e64 v193, v129\n" \
  "v_mfma_f32_16x16x32_bf16 v[24:27], a[140:143], v[184:187], v[24:27]\n" \
  "ds_read_b64_tr_b16 a[176:177], v216 offset:33024\n" \
  "v_cvt_pk_bf16_f32 v176, v192, v193\n" \
  "v_mfma_f32_16x16x32_bf16 v[28:31], a[140:143], v[188:191], v[28:31]\n" \
  "buffer_load_dwordx4 v227, s[40:43], s73 offen offset:3072 lds\n" \
  "v_mfma_f32_16x16x32_bf16 v[32:35], a[144:147], v[184:187], v[32:35]\n" \
  "ds_read_b64_tr_b16 a[178:179], v216 offset:41216\n" \
  "ds_read_b128 a[92:95], v211 offset:33024\n" \
  "v_mfma_f32_16x16x32_bf16 v[36:39], a[144:147], v[188:191], v[36:39]\n" \
  "s_mov_b32 s73, 0xfc000\n" \
  "v_exp_f32_e64 v194, v130\n" \
  "v_mfma_f32_16x16x32_bf16 v[40:43], a[148:151], v[184:187], v[40:43]\n" \
  "ds_read_b64_tr_b16 a[180:181], v217 offset:33024\n" \
  "s_add_u32 m0, s75, 0x4000\n" \
  "v_mfma_f32_16x16x32_bf16 v[44:47], a[148:151], v[188:191], v[44:47]\n" \
  "ds_read_b64_tr_b16 a[182:183], v217 offset:41216\n" \
  "v_mfma_f32_16x16x32_bf16 v[48:51], a[152:155], v[184:187], v[48:51]\n" \
  "ds_read_b128 a[96:99], v208 offset:40960\n" \
  "v_mfma_f32_16x16x32_bf16 v[52:55], a[152:155], v[188:191], v[52:55]\n" \
  "s_waitcnt lgkmcnt(5)\n" \
  "v_exp_f32 v195, v131\n" \
  "v_mfma_f32_16x16x32_bf16 v[56:59], a[156:159], v[184:187], v[56:59]\n" \
  "ds_read_b64_tr_b16 a[184:185], v218 offset:33024\n" \
  "v_cvt_pk_bf16_f32 v177, v194, v195\n" \
  "v_mfma_f32_16x16x32_bf16 v[60:63], a[156:159], v[188:191], v[60:63]\n" \
  "buffer_load_dwordx4 v224, s[40:43], s73 offen lds\n" \
  "v_mfma_f32_16x16x32_bf16 v[64:67], a[160:163], v[184:187], v[64:67]\n" \
  "ds_read_b64_tr_b16 a[186:187], v218 offset:41216\n" \
  "ds_read_b128 a[100:103], v209 offset:40960\n" \
  "v_mfma_f32_16x16x32_bf16 v[68:71], a[160:163], v[188:191], v[68:71]\n" \
  "v_exp_f32_e64 v196, v132\n" \
  "v_mfma_f32_16x16x32_bf16 v[72:75], a[164:167], v[184:187], v[72:75]\n" \
  "ds_read_b64_tr_b16 a[188:189], v219 offset:33024\n" \
  "v_mfma_f32_16x16x32_bf16 v[76:79], a[164:167], v[188:191], v[76:79]\n" \
  "ds_read_b64_tr_b16 a[190:191], v219 offset:41216\n" \
  "v_mfma_f32_16x16x32_bf16 v[80:83], a[168:171], v[184:187], v[80:83]\n" \
  "ds_read_b128 a[104:107], v210 offset:40960\n" \
  "v_mfma_f32_16x16x32_bf16 v[84:87], a[168:171], v[188:191], v[84:87]\n" \
  "s_waitcnt lgkmcnt(5)\n" \
  "v_exp_f32 v197, v133\n" \
  "v_mfma_f32_16x16x32_bf16 v[88:91], a[172:175], v[184:187], v[88:91]\n" \
  "ds_read_b128 a[108:111], v211 offset:40960\n" \
  "v_cvt_pk_bf16_f32 v180, v196, v197\n" \
  "v_mfma_f32_16x16x32_bf16 v[92:95], a[172:175], v[188:191], v[92:95]\n" \
  "buffer_load_dwordx4 v225, s[40:43], s73 offen offset:1024 lds\n" \
  "v_mfma_f32_16x16x32_bf16 v[96:99], a[176:179], v[184:187], v[96:99]\n" \
  "ds_read_b128 a[112:115], v208 offset:41216\n" \
  "ds_read_b128 a[116:119], v209 offset:41216\n" \
  "v_mfma_f32_16x16x32_bf16 v[100:103], a[176:179], v[188:191], v[100:103]\n" \
  "ds_read_b128 a[120:123], v210 offset:41216\n" \
  "v_mfma_f32_16x16x32_bf16 v[104:107], a[180:183], v[184:187], v[104:107]\n" \
  "ds_read_b128 a[124:127], v211 offset:41216\n" \
  "s_waitcnt lgkmcnt(5)\n" \
  "v_mfma_f32_16x16x32_bf16 v[108:111], a[180:183], v[188:191], v[108:111]\n" \
  "buffer_load_dwordx4 v226, s[40:43], s73 offen offset:2048 lds\n" \
  "v_mfma_f32_16x16x32_bf16 v[112:115], a[184:187], v[184:187], v[112:115]\n" \
  "v_exp_f32 v198, v134\n" \
  "v_mfma_f32_16x16x32_bf16 v[116:119], a[184:187], v[188:191], v[116:119]\n" \
  "buffer_load_dwordx4 v227, s[40:43], s73 offen offset:3072 lds\n" \
  "v_mfma_f32_16x16x32_bf16 v[120:123], a[188:191], v[184:187], v[120:123]\n" \
  "v_exp_f32_e64 v199, v135\n" \
  "v_mfma_f32_16x16x32_bf16 v[124:127], a[188:191], v[188:191], v[124:127]\n" \
  "v_exp_f32_e64 v192, v136\n" \
  "v_mfma_f32_16x16x32_bf16 v[200:203], v[172:175], v[184:187], v[200:203]\n" \
  "v_exp_f32_e64 v193, v137\n" \
  "v_mfma_f32_16x16x32_bf16 v[204:207], v[172:175], v[188:191], v[204:207]\n" \
  "s_waitcnt vmcnt(16) lgkmcnt(0)\n" \
  "s_barrier\n" \
  "s_add_u32 s95, s80, 1\n" \
  "v_subrev_u32 v251, s95, v241\n" \
  "v_cmp_lt_u32 s[92:93], 8, v251\n" \
  "v_cmp_ne_u32_e64 vcc, s79, v240\n" \
  "s_or_b64 vcc, vcc, s[92:93]\n" \
  "s_cbranch_vccnz L_slowh_%=\n" \
  "L_sbackh_%=:\n" \
  "; attn phase 60\n" \
  "v_mfma_f32_16x16x32_bf16 v[144:147], a[64:67], a[0:3], v[160:163]\n" \
  "ds_read_b64_tr_b16 a[128:129], v212 offset:49152\n" \
  "ds_read_b64_tr_b16 a[130:131], v212 offset:57344\n" \
  "v_mfma_f32_16x16x32_bf16 v[148:151], a[64:67], a[32:35], v[164:167]\n" \
  "s_mov_b32 s86, 0\n" \
  "v_exp_f32 v194, v138\n" \
  "v_mfma_f32_16x16x32_bf16 v[144:147], a[68:71], a[4:7], v[144:147]\n" \
  "ds_read_b64_tr_b16 a[132:133], v213 offset:49152\n" \
  "v_cvt_pk_bf16_f32 v181, v198, v199\n" \
  "v_mfma_f32_16x16x32_bf16 v[148:151], a[68:71], a[36:39], v[148:151]\n" \
  "ds_read_b64_tr_b16 a[134:135], v213 offset:57344\n" \
  "v_cvt_pk_bf16_f32 v178, v192, v193\n" \
  "v_mfma_f32_16x16x32_bf16 v[144:147], a[72:75], a[8:11], v[144:147]\n" \
  "ds_read_b64_tr_b16 a[136:137], v214 offset:49152\n" \
  "v_or3_b32 v248, v176, v177, v180\n" \
  "v_mfma_f32_16x16x32_bf16 v[148:151], a[72:75], a[40:43], v[148:151]\n" \
  "s_add_u32 m0, s74, 0x8000\n" \
  "v_exp_f32_e64 v195, v139\n" \
  "v_mfma_f32_16x16x32_bf16 v[144:147], a[76:79], a[12:15], v[144:147]\n" \
  "ds_read_b64_tr_b16 a[138:139], v214 offset:57344\n" \
  "v_cvt_pk_bf16_f32 v179, v194, v195\n" \
  "v_mfma_f32_16x16x32_bf16 v[148:151], a[76:79], a[44:47], v[148:151]\n" \
  "buffer_load_dwordx4 v236, s[52:55], s86 offen lds\n" \
  "v_mfma_f32_16x16x32_bf16 v[144:147], a[80:83], a[16:19], v[144:147]\n" \
  "ds_read_b64_tr_b16 a[140:141], v215 offset:49152\n" \
  "ds_read_b64_tr_b16 a[142:143], v215 offset:57344\n" \
  "s_mov_b32 s88, 0\n" \
  "v_mfma_f32_16x16x32_bf16 v[148:151], a[80:83], a[48:51], v[148:151]\n" \
  "s_mov_b32 s87, 0x1c000\n" \
  "v_exp_f32 v196, v140\n" \
  "v_mfma_f32_16x16x32_bf16 v[144:147], a[84:87], a[20:23], v[144:147]\n" \
  "ds_read_b64_tr_b16 a[144:145], v216 offset:49152\n" \
  "v_or3_b32 v248, v248, v181, v178\n" \
  "v_mfma_f32_16x16x32_bf16 v[148:151], a[84:87], a[52:55], v[148:151]\n" \
  "ds_read_b64_tr_b16 a[146:147], v216 offset:57344\n" \
  "v_mfma_f32_16x16x32_bf16 v[144:147], a[88:91], a[24:27], v[144:147]\n" \
  "ds_read_b64_tr_b16 a[148:149], v217 offset:49152\n" \
  "v_mfma_f32_16x16x32_bf16 v[148:151], a[88:91], a[56:59], v[148:151]\n" \
  "v_exp_f32_e64 v197, v141\n" \
  "v_mfma_f32_16x16x32_bf16 v[144:147], a[92:95], a[28:31], v[144:147]\n" \
  "ds_read_b64_tr_b16 a[150:151], v217 offset:57344\n" \
  "v_cvt_pk_bf16_f32 v182, v196, v197\n" \
  "v_mfma_f32_16x16x32_bf16 v[148:151], a[92:95], a[60:63], v[148:151]\n" \
  "buffer_load_dwordx4 v237, s[52:55], s86 offen offset:1024 lds\n" \
  "v_mfma_f32_16x16x32_bf16 v[152:155], a[96:99], a[0:3], v[160:163]\n" \
  "ds_read_b128 a[64:67], v208 offset:49152\n" \
  "ds_read_b64_tr_b16 a[152:153], v218 offset:49152\n" \
  "buffer_load_dwordx4 a[192:195], v222, s[60:63], s88 offen sc1\n" \
  "v_mfma_f32_16x16x32_bf16 v[156:159], a[96:99], a[32:35], v[164:167]\n" \
  "v_exp_f32_e64 v198, v142\n" \
  "v_mfma_f32_16x16x32_bf16 v[152:155], a[100:103], a[4:7], v[152:155]\n" \
  "ds_read_b64_tr_b16 a[154:155], v218 offset:57344\n" \
  "v_or3_b32 v248, v248, v179, v182\n" \
  "v_mfma_f32_16x16x32_bf16 v[156:159], a[100:103], a[36:39], v[156:159]\n" \
  "ds_read_b128 a[68:71], v209 offset:49152\n" \
  "v_mfma_f32_16x16x32_bf16 v[152:155], a[104:107], a[8:11], v[152:155]\n" \
  "ds_read_b64_tr_b16 a[156:157], v219 offset:49152\n" \
  "v_mfma_f32_16x16x32_bf16 v[156:159], a[104:107], a[40:43], v[156:159]\n" \
  "v_exp_f32_e64 v199, v143\n" \
  "v_mfma_f32_16x16x32_bf16 v[152:155], a[108:111], a[12:15], v[152:155]\n" \
  "ds_read_b64_tr_b16 a[158:159], v219 offset:57344\n" \
  "v_cvt_pk_bf16_f32 v183, v198, v199\n" \
  "v_mfma_f32_16x16x32_bf16 v[156:159], a[108:111], a[44:47], v[156:159]\n" \
  "buffer_load_dwordx4 v238, s[52:55], s86 offen offset:2048 lds\n" \
  "v_mfma_f32_16x16x32_bf16 v[152:155], a[112:115], a[16:19], v[152:155]\n" \
  "ds_read_b128 a[72:75], v210 offset:49152\n" \
  "ds_read_b64_tr_b16 a[160:161], v212 offset:49408\n" \
  "buffer_load_dwordx4 a[196:199], v223, s[60:63], s88 offen sc1\n" \
  "v_mfma_f32_16x16x32_bf16 v[156:159], a[112:115], a[48:51], v[156:159]\n" \
  "ds_read_b64_tr_b16 a[162:163], v212 offset:57600\n" \
  "v_bitop3_b32 v248, v248, v183, s76 bitop3:0xa8\n" \
  "v_mfma_f32_16x16x32_bf16 v[152:155], a[116:119], a[20:23], v[152:155]\n" \
  "ds_read_b128 a[76:79], v211 offset:49152\n" \
  "v_cmp_ne_u32_e64 vcc, 0, v248\n" \
  "v_mfma_f32_16x16x32_bf16 v[156:159], a[116:119], a[52:55], v[156:159]\n" \
  "ds_read_b64_tr_b16 a[164:165], v213 offset:49408\n" \
  "v_mfma_f32_16x16x32_bf16 v[152:155], a[120:123], a[24:27], v[152:155]\n" \
  "ds_read_b64_tr_b16 a[166:167], v213 offset:57600\n" \
  "v_mfma_f32_16x16x32_bf16 v[156:159], a[120:123], a[56:59], v[156:159]\n" \
  "ds_read_b128 a[80:83], v208 offset:49408\n" \
  "v_mfma_f32_16x16x32_bf16 v[152:155], a[124:127], a[28:31], v[152:155]\n" \
  "ds_read_b64_tr_b16 a[168:169], v214 offset:49408\n" \
  "s_waitcnt lgkmcnt(6)\n" \
  "v_mfma_f32_16x16x32_bf16 v[156:159], a[124:127], a[60:63], v[156:159]\n" \
  "s_cbranch_vccnz L_rare_p60_%=\n" \
  "L_back_p60_%=:\n" \
  "v_mfma_f32_16x16x32_bf16 v[0:3], a[128:131], v[176:179], v[0:3]\n" \
  "ds_read_b64_tr_b16 a[170:171], v214 offset:57600\n" \
  "ds_read_b128 a[84:87], v209 offset:49408\n" \
  "buffer_load_dwordx4 a[200:203], v222, s[60:63], s88 offen offset:1024 sc1\n" \
  "v_mfma_f32_16x16x32_bf16 v[4:7], a[128:131], v[180:183], v[4:7]\n" \
  "v_exp_f32_e64 v192, v144\n" \
  "v_mfma_f32_16x16x32_bf16 v[8:11], a[132:135], v[176:179], v[8:11]\n" \
  "ds_read_b64_tr_b16 a[172:173], v215 offset:49408\n" \
  "v_mfma_f32_16x16x32_bf16 v[12:15], a[132:135], v[180:183], v[12:15]\n" \
  "ds_read_b64_tr_b16 a[174:175], v215 offset:57600\n" \
  "v_mfma_f32_16x16x32_bf16 v[16:19], a[136:139], v[176:179], v[16:19]\n" \
  "ds_read_b128 a[88:91], v210 offset:49408\n" \
  "v_mfma_f32_16x16x32_bf16 v[20:23], a[136:139], v[180:183], v[20:23]\n" \
  "v_exp_f32_e64 v193, v145\n" \
  "v_mfma_f32_16x16x32_bf16 v[24:27], a[140:143], v[176:179], v[24:27]\n" \
  "ds_read_b64_tr_b16 a[176:177], v216 offset:49408\n" \
  "v_cvt_pk_bf16_f32 v184, v192, v193\n" \
  "v_mfma_f32_16x16x32_bf16 v[28:31], a[140:143], v[180:183], v[28:31]\n" \
  "buffer_load_dwordx4 v239, s[52:55], s86 offen offset:3072 lds\n" \
  "v_mfma_f32_16x16x32_bf16 v[32:35], a[144:147], v[176:179], v[32:35]\n" \
  "ds_read_b64_tr_b16 a[178:179], v216 offset:57600\n" \
  "ds_read_b128 a[92:95], v211 offset:49408\n" \
  "buffer_load_dwordx4 a[204:207], v223, s[60:63], s88 offen offset:1024 sc1\n" \
  "v_mfma_f32_16x16x32_bf16 v[36:39], a[144:147], v[180:183], v[36:39]\n" \
  "s_add_u32 m0, s75, 0x8000\n" \
  "v_exp_f32_e64 v194, v146\n" \
  "v_mfma_f32_16x16x32_bf16 v[40:43], a[148:151], v[176:179], v[40:43]\n" \
  "ds_read_b64_tr_b16 a[180:181], v217 offset:49408\n" \
  "v_mfma_f32_16x16x32_bf16 v[44:47], a[148:151], v[180:183], v[44:47]\n" \
  "ds_read_b64_tr_b16 a[182:183], v217 offset:57600\n" \
  "v_mfma_f32_16x16x32_bf16 v[48:51], a[152:155], v[176:179], v[48:51]\n" \
  "ds_read_b128 a[96:99], v208 offset:57344\n" \
  "v_mfma_f32_16x16x32_bf16 v[52:55], a[152:155], v[180:183], v[52:55]\n" \
  "s_waitcnt lgkmcnt(5)\n" \
  "v_exp_f32 v195, v147\n" \
  "v_mfma_f32_16x16x32_bf16 v[56:59], a[156:159], v[176:179], v[56:59]\n" \
  "ds_read_b64_tr_b16 a[184:185], v218 offset:49408\n" \
  "v_cvt_pk_bf16_f32 v185, v194, v195\n" \
  "v_mfma_f32_16x16x32_bf16 v[60:63], a[156:159], v[180:183], v[60:63]\n" \
  "buffer_load_dwordx4 v236, s[52:55], s87 offen lds\n" \
  "v_mfma_f32_16x16x32_bf16 v[64:67], a[160:163], v[176:179], v[64:67]\n" \
  "ds_read_b64_tr_b16 a[186:187], v218 offset:57600\n" \
  "ds_read_b128 a[100:103], v209 offset:57344\n" \
  "s_mov_b32 s88, 0x1000\n" \
  "v_mfma_f32_16x16x32_bf16 v[68:71], a[160:163], v[180:183], v[68:71]\n" \
  "v_exp_f32_e64 v196, v148\n" \
  "v_mfma_f32_16x16x32_bf16 v[72:75], a[164:167], v[176:179], v[72:75]\n" \
  "ds_read_b64_tr_b16 a[188:189], v219 offset:49408\n" \
  "v_mfma_f32_16x16x32_bf16 v[76:79], a[164:167], v[180:183], v[76:79]\n" \
  "ds_read_b64_tr_b16 a[190:191], v219 offset:57600\n" \
  "v_mfma_f32_16x16x32_bf16 v[80:83], a[168:171], v[176:179], v[80:83]\n" \
  "ds_read_b128 a[104:107], v210 offset:57344\n" \
  "v_mfma_f32_16x16x32_bf16 v[84:87], a[168:171], v[180:183], v[84:87]\n" \
  "s_waitcnt lgkmcnt(5)\n" \
  "v_exp_f32 v197, v149\n" \
  "v_mfma_f32_16x16x32_bf16 v[88:91], a[172:175], v[176:179], v[88:91]\n" \
  "ds_read_b128 a[108:111], v211 offset:57344\n" \
  "v_cvt_pk_bf16_f32 v188, v196, v197\n" \
  "v_mfma_f32_16x16x32_bf16 v[92:95], a[172:175], v[180:183], v[92:95]\n" \
  "buffer_load_dwordx4 v237, s[52:55], s87 offen offset:1024 lds\n" \
  "v_mfma_f32_16x16x32_bf16 v[96:99], a[176:179], v[176:179], v[96:99]\n" \
  "ds_read_b128 a[112:115], v208 offset:57600\n" \
  "ds_read_b128 a[116:119], v209 offset:57600\n" \
  "buffer_load_dwordx4 a[208:211], v222, s[60:63], s88 offen sc1\n" \
  "v_mfma_f32_16x16x32_bf16 v[100:103], a[176:179], v[180:183], v[100:103]\n" \
  "ds_read_b128 a[120:123], v210 offset:57600\n" \
  "v_mfma_f32_16x16x32_bf16 v[104:107], a[180:183], v[176:179], v[104:107]\n" \
  "ds_read_b128 a[124:127], v211 offset:57600\n" \
  "s_waitcnt lgkmcnt(5)\n" \
  "v_mfma_f32_16x16x32_bf16 v[108:111], a[180:183], v[180:183], v[108:111]\n" \
  "buffer_load_dwordx4 v238, s[52:55], s87 offen offset:2048 lds\n" \
  "v_mfma_f32_16x16x32_bf16 v[112:115], a[184:187], v[176:179], v[112:115]\n" \
  "v_exp_f32 v198, v150\n" \
  "v_mfma_f32_16x16x32_bf16 v[116:119], a[184:187], v[180:183], v[116:119]\n" \
  "buffer_load_dwordx4 v239, s[52:55], s87 offen offset:3072 lds\n" \
  "v_mfma_f32_16x16x32_bf16 v[120:123], a[188:191], v[176:179], v[120:123]\n" \
  "v_exp_f32_e64 v199, v151\n" \
  "v_mfma_f32_16x16x32_bf16 v[124:127], a[188:191], v[180:183], v[124:127]\n" \
  "v_exp_f32_e64 v192, v152\n" \
  "buffer_load_dwordx4 a[212:215], v223, s[60:63], s88 offen sc1\n" \
  "v_mfma_f32_16x16x32_bf16 v[200:203], v[172:175], v[176:179], v[200:203]\n" \
  "v_exp_f32_e64 v193, v153\n" \
  "v_mfma_f32_16x16x32_bf16 v[204:207], v[172:175], v[180:183], v[204:207]\n" \
  "s_waitcnt vmcnt(22) lgkmcnt(0)\n" \
  "s_barrier\n" \
  "; attn phase 61\n" \
  "v_mfma_f32_16x16x32_bf16 v[128:131], a[64:67], a[0:3], v[160:163]\n" \
  "ds_read_b64_tr_b16 a[128:129], v212\n" \
  "ds_read_b64_tr_b16 a[130:131], v212 offset:8192\n" \
  "v_mfma_f32_16x16x32_bf16 v[132:135], a[64:67], a[32:35], v[164:167]\n" \
  "s_mov_b32 s86, 0x80\n" \
  "v_exp_f32_e64 v194, v154\n" \
  "v_mfma_f32_16x16x32_bf16 v[128:131], a[68:71], a[4:7], v[128:131]\n" \
  "ds_read_b64_tr_b16 a[132:133], v213\n" \
  "v_cvt_pk_bf16_f32 v189, v198, v199\n" \
  "v_mfma_f32_16x16x32_bf16 v[132:135], a[68:71], a[36:39], v[132:135]\n" \
  "ds_read_b64_tr_b16 a[134:135], v213 offset:8192\n" \
  "v_cvt_pk_bf16_f32 v186, v192, v193\n" \
  "buffer_load_dwordx4 a[216:219], v222, s[60:63], s88 offen offset:1024 sc1\n" \
  "v_mfma_f32_16x16x32_bf16 v[128:131], a[72:75], a[8:11], v[128:131]\n" \
  "ds_read_b64_tr_b16 a[136:137], v214\n" \
  "v_or3_b32 v248, v184, v185, v188\n" \
  "v_mfma_f32_16x16x32_bf16 v[132:135], a[72:75], a[40:43], v[132:135]\n" \
  "s_add_u32 m0, s74, 0xc000\n" \
  "v_exp_f32_e64 v195, v155\n" \
  "v_mfma_f32_16x16x32_bf16 v[128:131], a[76:79], a[12:15], v[128:131]\n" \
  "ds_read_b64_tr_b16 a[138:139], v214 offset:8192\n" \
  "v_cvt_pk_bf16_f32 v187, v194, v195\n" \
  "buffer_load_dwordx4 a[220:223], v223, s[60:63], s88 offen offset:1024 sc1\n" \
  "v_mfma_f32_16x16x32_bf16 v[132:135], a[76:79], a[44:47], v[132:135]\n" \
  "buffer_load_dwordx4 v236, s[52:55], s86 offen lds\n" \
  "v_mfma_f32_16x16x32_bf16 v[128:131], a[80:83], a[16:19], v[128:131]\n" \
  "ds_read_b64_tr_b16 a[140:141], v215\n" \
  "ds_read_b64_tr_b16 a[142:143], v215 offset:8192\n" \
  "s_mov_b32 s88, 0x2000\n" \
  "v_mfma_f32_16x16x32_bf16 v[132:135], a[80:83], a[48:51], v[132:135]\n" \
  "s_mov_b32 s87, 0x1c080\n" \
  "v_exp_f32_e64 v196, v156\n" \
  "v_mfma_f32_16x16x32_bf16 v[128:131], a[84:87], a[20:23], v[128:131]\n" \
  "ds_read_b64_tr_b16 a[144:145], v216\n" \
  "v_or3_b32 v248, v248, v189, v186\n" \
  "v_mfma_f32_16x16x32_bf16 v[132:135], a[84:87], a[52:55], v[132:135]\n" \
  "ds_read_b64_tr_b16 a[146:147], v216 offset:8192\n" \
  "v_lshrrev_b32_e64 v251, 2, v253\n" \
  "v_mfma_f32_16x16x32_bf16 v[128:131], a[88:91], a[24:27], v[128:131]\n" \
  "ds_read_b64_tr_b16 a[148:149], v217\n" \
  "v_lshlrev_b32_e64 v251, 7, v251\n" \
  "v_mfma_f32_16x16x32_bf16 v[132:135], a[88:91], a[56:59], v[132:135]\n" \
  "v_exp_f32_e64 v197, v157\n" \
  "v_mfma_f32_16x16x32_bf16 v[128:131], a[92:95], a[28:31], v[128:131]\n" \
  "ds_read_b64_tr_b16 a[150:151], v217 offset:8192\n" \
  "v_cvt_pk_bf16_f32 v190, v196, v197\n" \
  "v_mfma_f32_16x16x32_bf16 v[132:135], a[92:95], a[60:63], v[132:135]\n" \
  "buffer_load_dwordx4 v237, s[52:55], s86 offen offset:1024 lds\n" \
  "v_mfma_f32_16x16x32_bf16 v[136:139], a[96:99], a[0:3], v[160:163]\n" \
  "ds_read_b128 a[64:67], v208\n" \
  "ds_read_b64_tr_b16 a[152:153], v218\n" \
  "buffer_load_dwordx4 a[224:227], v222, s[60:63], s88 offen sc1\n" \
  "v_mfma_f32_16x16x32_bf16 v[140:143], a[96:99], a[32:35], v[164:167]\n" \
  "v_exp_f32_e64 v198, v158\n" \
  "v_mfma_f32_16x16x32_bf16 v[136:139], a[100:103], a[4:7], v[136:139]\n" \
  "ds_read_b64_tr_b16 a[154:155], v218 offset:8192\n" \
  "v_or3_b32 v248, v248, v187, v190\n" \
  "buffer_load_dwordx4 a[228:231], v223, s[60:63], s88 offen sc1\n" \
  "v_mfma_f32_16x16x32_bf16 v[140:143], a[100:103], a[36:39], v[140:143]\n" \
  "ds_read_b128 a[68:71], v209\n" \
  "v_and_b32_e64 v252, 3, v253\n" \
  "v_mfma_f32_16x16x32_bf16 v[136:139], a[104:107], a[8:11], v[136:139]\n" \
  "ds_read_b64_tr_b16 a[156:157], v219\n" \
  "v_lshl_add_u32 v251, v252, 3, v251\n" \
  "v_mfma_f32_16x16x32_bf16 v[140:143], a[104:107], a[40:43], v[140:143]\n" \
  "s_waitcnt lgkmcnt(5)\n" \
  "v_exp_f32 v199, v159\n" \
  "v_mfma_f32_16x16x32_bf16 v[136:139], a[108:111], a[12:15], v[136:139]\n" \
  "ds_read_b64_tr_b16 a[158:159], v219 offset:8192\n" \
  "v_cvt_pk_bf16_f32 v191, v198, v199\n" \
  "v_mfma_f32_16x16x32_bf16 v[140:143], a[108:111], a[44:47], v[140:143]\n" \
  "buffer_load_dwordx4 v238, s[52:55], s86 offen offset:2048 lds\n" \
  "v_mfma_f32_16x16x32_bf16 v[136:139], a[112:115], a[16:19], v[136:139]\n" \
  "ds_read_b128 a[72:75], v210\n" \
  "ds_read_b64_tr_b16 a[160:161], v212 offset:256\n" \
  "v_mfma_f32_16x16x32_bf16 v[140:143], a[112:115], a[48:51], v[140:143]\n" \
  "ds_read_b64_tr_b16 a[162:163], v212 offset:8448\n" \
  "v_bitop3_b32 v248, v248, v191, s76 bitop3:0xa8\n" \
  "v_mfma_f32_16x16x32_bf16 v[136:139], a[116:119], a[20:23], v[136:139]\n" \
  "ds_read_b128 a[76:79], v211\n" \
  "v_cmp_ne_u32_e64 vcc, 0, v248\n" \
  "buffer_load_dwordx4 a[232:235], v222, s[60:63], s88 offen offset:1024 sc1\n" \
  "v_mfma_f32_16x16x32_bf16 v[140:143], a[116:119], a[52:55], v[140:143]\n" \
  "ds_read_b64_tr_b16 a[164:165], v213 offset:256\n" \
  "v_add_u32_e64 v251, s84, v251\n" \
  "v_mfma_f32_16x16x32_bf16 v[136:139], a[120:123], a[24:27], v[136:139]\n" \
  "ds_read_b64_tr_b16 a[166:167], v213 offset:8448\n" \
  "v_bfe_u32 v252, v253, 3, 2\n" \
  "v_mfma_f32_16x16x32_bf16 v[140:143], a[120:123], a[56:59], v[140:143]\n" \
  "ds_read_b128 a[80:83], v208 offset:256\n" \
  "v_xor_b32_e64 v249, 0, v252\n" \
  "v_mfma_f32_16x16x32_bf16 v[136:139], a[124:127], a[28:31], v[136:139]\n" \
  "ds_read_b64_tr_b16 a[168:169], v214 offset:256\n" \
  "v_lshl_add_u32 v232, v249, 5, v251\n" \
  "v_mfma_f32_16x16x32_bf16 v[140:143], a[124:127], a[60:63], v[140:143]\n" \
  "s_cbranch_vccnz L_rare_p61_%=\n" \
  "L_back_p61_%=:\n" \
  "v_mfma_f32_16x16x32_bf16 v[0:3], a[128:131], v[184:187], v[0:3]\n" \
  "ds_read_b64_tr_b16 a[170:171], v214 offset:8448\n" \
  "ds_read_b128 a[84:87], v209 offset:256\n" \
  "v_mfma_f32_16x16x32_bf16 v[4:7], a[128:131], v[188:191], v[4:7]\n" \
  "v_exp_f32 v192, v128\n" \
  "v_mfma_f32_16x16x32_bf16 v[8:11], a[132:135], v[184:187], v[8:11]\n" \
  "ds_read_b64_tr_b16 a[172:173], v215 offset:256\n" \
  "v_add_u32 v228, 0x10000, v232\n" \
  "v_mfma_f32_16x16x32_bf16 v[12:15], a[132:135], v[188:191], v[12:15]\n" \
  "ds_read_b64_tr_b16 a[174:175], v215 offset:8448\n" \
  "v_xor_b32_e64 v249, 1, v252\n" \
  "v_mfma_f32_16x16x32_bf16 v[16:19], a[136:139], v[184:187], v[16:19]\n" \
  "ds_read_b128 a[88:91], v210 offset:256\n" \
  "v_lshl_add_u32 v233, v249, 5, v251\n" \
  "v_mfma_f32_16x16x32_bf16 v[20:23], a[136:139], v[188:191], v[20:23]\n" \
  "s_waitcnt lgkmcnt(5)\n" \
  "v_exp_f32 v193, v129\n" \
  "v_mfma_f32_16x16x32_bf16 v[24:27], a[140:143], v[184:187], v[24:27]\n" \
  "ds_read_b64_tr_b16 a[176:177], v216 offset:256\n" \
  "v_cvt_pk_bf16_f32 v176, v192, v193\n" \
  "v_mfma_f32_16x16x32_bf16 v[28:31], a[140:143], v[188:191], v[28:31]\n" \
  "buffer_load_dwordx4 v239, s[52:55], s86 offen offset:3072 lds\n" \
  "v_mfma_f32_16x16x32_bf16 v[32:35], a[144:147], v[184:187], v[32:35]\n" \
  "ds_read_b64_tr_b16 a[178:179], v216 offset:8448\n" \
  "ds_read_b128 a[92:95], v211 offset:256\n" \
  "v_mfma_f32_16x16x32_bf16 v[36:39], a[144:147], v[188:191], v[36:39]\n" \
  "s_add_u32 m0, s75, 0xc000\n" \
  "v_exp_f32_e64 v194, v130\n" \
  "v_mfma_f32_16x16x32_bf16 v[40:43], a[148:151], v[184:187], v[40:43]\n" \
  "ds_read_b64_tr_b16 a[180:181], v217 offset:256\n" \
  "v_add_u32 v229, 0x10000, v233\n" \
  "v_mfma_f32_16x16x32_bf16 v[44:47], a[148:151], v[188:191], v[44:47]\n" \
  "ds_read_b64_tr_b16 a[182:183], v217 offset:8448\n" \
  "v_xor_b32_e64 v249, 2, v252\n" \
  "v_mfma_f32_16x16x32_bf16 v[48:51], a[152:155], v[184:187], v[48:51]\n" \
  "ds_read_b128 a[96:99], v208 offset:8192\n" \
  "v_lshl_add_u32 v234, v249, 5, v251\n" \
  "v_mfma_f32_16x16x32_bf16 v[52:55], a[152:155], v[188:191], v[52:55]\n" \
  "v_exp_f32_e64 v195, v131\n" \
  "v_mfma_f32_16x16x32_bf16 v[56:59], a[156:159], v[184:187], v[56:59]\n" \
  "ds_read_b64_tr_b16 a[184:185], v218 offset:256\n" \
  "v_cvt_pk_bf16_f32 v177, v194, v195\n" \
  "v_mfma_f32_16x16x32_bf16 v[60:63], a[156:159], v[188:191], v[60:63]\n" \
  "buffer_load_dwordx4 v236, s[52:55], s87 offen lds\n" \
  "v_mfma_f32_16x16x32_bf16 v[64:67], a[160:163], v[184:187], v[64:67]\n" \
  "ds_read_b64_tr_b16 a[186:187], v218 offset:8448\n" \
  "ds_read_b128 a[100:103], v209 offset:8192\n" \
  "v_mfma_f32_16x16x32_bf16 v[68:71], a[160:163], v[188:191], v[68:71]\n" \
  "s_waitcnt lgkmcnt(4)\n" \
  "v_exp_f32 v196, v132\n" \
  "v_mfma_f32_16x16x32_bf16 v[72:75], a[164:167], v[184:187], v[72:75]\n" \
  "ds_read_b64_tr_b16 a[188:189], v219 offset:256\n" \
  "v_add_u32 v230, 0x10000, v234\n" \
  "v_mfma_f32_16x16x32_bf16 v[76:79], a[164:167], v[188:191], v[76:79]\n" \
  "ds_read_b64_tr_b16 a[190:191], v219 offset:8448\n" \
  "v_xor_b32_e64 v249, 3, v252\n" \
  "v_mfma_f32_16x16x32_bf16 v[80:83], a[168:171], v[184:187], v[80:83]\n" \
  "ds_read_b128 a[104:107], v210 offset:8192\n" \
  "v_lshl_add_u32 v235, v249, 5, v251\n" \
  "v_mfma_f32_16x16x32_bf16 v[84:87], a[168:171], v[188:191], v[84:87]\n" \
  "v_exp_f32_e64 v197, v133\n" \
  "v_mfma_f32_16x16x32_bf16 v[88:91], a[172:175], v[184:187], v[88:91]\n" \
  "ds_read_b128 a[108:111], v211 offset:8192\n" \
  "v_cvt_pk_bf16_f32 v180, v196, v197\n" \
  "v_mfma_f32_16x16x32_bf16 v[92:95], a[172:175], v[188:191], v[92:95]\n" \
  "buffer_load_dwordx4 v237, s[52:55], s87 offen offset:1024 lds\n" \
  "v_mfma_f32_16x16x32_bf16 v[96:99], a[176:179], v[184:187], v[96:99]\n" \
  "ds_read_b128 a[112:115], v208 offset:8448\n" \
  "ds_read_b128 a[116:119], v209 offset:8448\n" \
  "v_mfma_f32_16x16x32_bf16 v[100:103], a[176:179], v[188:191], v[100:103]\n" \
  "ds_read_b128 a[120:123], v210 offset:8448\n" \
  "v_add_u32 v231, 0x10000, v235\n" \
  "v_mfma_f32_16x16x32_bf16 v[104:107], a[180:183], v[184:187], v[104:107]\n" \
  "ds_read_b128 a[124:127], v211 offset:8448\n" \
  "s_waitcnt lgkmcnt(5)\n" \
  "v_mfma_f32_16x16x32_bf16 v[108:111], a[180:183], v[188:191], v[108:111]\n" \
  "buffer_load_dwordx4 v238, s[52:55], s87 offen offset:2048 lds\n" \
  "v_mfma_f32_16x16x32_bf16 v[112:115], a[184:187], v[184:187], v[112:115]\n" \
  "v_exp_f32 v198, v134\n" \
  "v_mfma_f32_16x16x32_bf16 v[116:119], a[184:187], v[188:191], v[116:119]\n" \
  "buffer_load_dwordx4 v239, s[52:55], s87 offen offset:3072 lds\n" \
  "v_mfma_f32_16x16x32_bf16 v[120:123], a[188:191], v[184:187], v[120:123]\n" \
  "v_exp_f32_e64 v199, v135\n" \
  "v_mfma_f32_16x16x32_bf16 v[124:127], a[188:191], v[188:191], v[124:127]\n" \
  "v_exp_f32_e64 v192, v136\n" \
  "buffer_load_dwordx4 a[236:239], v223, s[60:63], s88 offen offset:1024 sc1\n" \
  "v_mfma_f32_16x16x32_bf16 v[200:203], v[172:175], v[184:187], v[200:203]\n" \
  "v_exp_f32_e64 v193, v137\n" \
  "v_mfma_f32_16x16x32_bf16 v[204:207], v[172:175], v[188:191], v[204:207]\n" \
  "s_waitcnt vmcnt(28) lgkmcnt(0)\n" \
  "s_barrier\n" \
  "; attn phase 62\n" \
  "v_mfma_f32_16x16x32_bf16 v[144:147], a[64:67], a[0:3], v[160:163]\n" \
  "ds_read_b64_tr_b16 a[128:129], v212 offset:16384\n" \
  "ds_read_b64_tr_b16 a[130:131], v212 offset:24576\n" \
  "v_mfma_f32_16x16x32_bf16 v[148:151], a[64:67], a[32:35], v[164:167]\n" \
  "ds_read_b64_tr_b16 a[132:133], v213 offset:16384\n" \
  "v_cvt_pk_bf16_f32 v181, v198, v199\n" \
  "v_mfma_f32_16x16x32_bf16 v[144:147], a[68:71], a[4:7], v[144:147]\n" \
  "ds_read_b64_tr_b16 a[134:135], v213 offset:24576\n" \
  "s_mov_b32 s86, 0x100\n" \
  "s_add_u32 m0, s74, 0\n" \
  "v_cvt_pk_bf16_f32 v178, v192, v193\n" \
  "v_mfma_f32_16x16x32_bf16 v[148:151], a[68:71], a[36:39], v[148:151]\n" \
  "buffer_load_dwordx4 v236, s[52:55], s86 offen lds\n" \
  "v_mfma_f32_16x16x32_bf16 v[144:147], a[72:75], a[8:11], v[144:147]\n" \
  "ds_read_b64_tr_b16 a[136:137], v214 offset:16384\n" \
  "ds_read_b64_tr_b16 a[138:139], v214 offset:24576\n" \
  "v_mfma_f32_16x16x32_bf16 v[148:151], a[72:75], a[40:43], v[148:151]\n" \
  "ds_read_b64_tr_b16 a[140:141], v215 offset:16384\n" \
  "v_or3_b32 v248, v176, v177, v180\n" \
  "v_mfma_f32_16x16x32_bf16 v[144:147], a[76:79], a[12:15], v[144:147]\n" \
  "ds_read_b64_tr_b16 a[142:143], v215 offset:24576\n" \
  "v_or3_b32 v248, v248, v181, v178\n" \
  "v_mfma_f32_16x16x32_bf16 v[148:151], a[76:79], a[44:47], v[148:151]\n" \
  "ds_read_b64_tr_b16 a[144:145], v216 offset:16384\n" \
  "s_mov_b32 s87, 0x1c100\n" \
  "v_mfma_f32_16x16x32_bf16 v[144:147], a[80:83], a[16:19], v[144:147]\n" \
  "ds_read_b64_tr_b16 a[146:147], v216 offset:24576\n" \
  "ds_read_b64_tr_b16 a[148:149], v217 offset:16384\n" \
  "v_mfma_f32_16x16x32_bf16 v[148:151], a[80:83], a[48:51], v[148:151]\n" \
  "buffer_load_dwordx4 v237, s[52:55], s86 offen offset:1024 lds\n" \
  "v_exp_f32 v194, v138\n" \
  "v_mfma_f32_16x16x32_bf16 v[144:147], a[84:87], a[20:23], v[144:147]\n" \
  "ds_read_b64_tr_b16 a[150:151], v217 offset:24576\n" \
  "s_mov_b32 s88, 0x3000\n" \
  "v_mfma_f32_16x16x32_bf16 v[148:151], a[84:87], a[52:55], v[148:151]\n" \
  "ds_read_b64_tr_b16 a[152:153], v218 offset:16384\n" \
  "v_exp_f32_e64 v195, v139\n" \
  "v_mfma_f32_16x16x32_bf16 v[144:147], a[88:91], a[24:27], v[144:147]\n" \
  "ds_read_b64_tr_b16 a[154:155], v218 offset:24576\n" \
  "v_cvt_pk_bf16_f32 v179, v194, v195\n" \
  "v_mfma_f32_16x16x32_bf16 v[148:151], a[88:91], a[56:59], v[148:151]\n" \
  "ds_read_b64_tr_b16 a[156:157], v219 offset:16384\n" \
  "ds_read_b64_tr_b16 a[158:159], v219 offset:24576\n" \
  "v_mfma_f32_16x16x32_bf16 v[144:147], a[92:95], a[28:31], v[144:147]\n" \
  "buffer_load_dwordx4 v238, s[52:55], s86 offen offset:2048 lds\n" \
  "v_exp_f32_e64 v196, v140\n" \
  "v_mfma_f32_16x16x32_bf16 v[148:151], a[92:95], a[60:63], v[148:151]\n" \
  "ds_read_b64_tr_b16 a[160:161], v212 offset:16640\n" \
  "v_mfma_f32_16x16x32_bf16 v[152:155], a[96:99], a[0:3], v[160:163]\n" \
  "ds_read_b64_tr_b16 a[162:163], v212 offset:24832\n" \
  "v_exp_f32_e64 v197, v141\n" \
  "v_mfma_f32_16x16x32_bf16 v[156:159], a[96:99], a[32:35], v[164:167]\n" \
  "ds_read_b64_tr_b16 a[164:165], v213 offset:16640\n" \
  "v_cvt_pk_bf16_f32 v182, v196, v197\n" \
  "v_mfma_f32_16x16x32_bf16 v[152:155], a[100:103], a[4:7], v[152:155]\n" \
  "ds_read_b64_tr_b16 a[166:167], v213 offset:24832\n" \
  "ds_read_b64_tr_b16 a[168:169], v214 offset:16640\n" \
  "v_mfma_f32_16x16x32_bf16 v[156:159], a[100:103], a[36:39], v[156:159]\n" \
  "ds_read_b64_tr_b16 a[170:171], v214 offset:24832\n" \
  "v_or3_b32 v248, v248, v179, v182\n" \
  "v_mfma_f32_16x16x32_bf16 v[152:155], a[104:107], a[8:11], v[152:155]\n" \
  "buffer_load_dwordx4 v239, s[52:55], s86 offen offset:3072 lds\n" \
  "v_mfma_f32_16x16x32_bf16 v[156:159], a[104:107], a[40:43], v[156:159]\n" \
  "ds_read_b64_tr_b16 a[172:173], v215 offset:16640\n" \
  "s_add_u32 m0, s75, 0\n" \
  "v_mfma_f32_16x16x32_bf16 v[152:155], a[108:111], a[12:15], v[152:155]\n" \
  "ds_read_b64_tr_b16 a[174:175], v215 offset:24832\n" \
  "v_exp_f32 v198, v142\n" \
  "v_mfma_f32_16x16x32_bf16 v[156:159], a[108:111], a[44:47], v[156:159]\n" \
  "ds_read_b64_tr_b16 a[176:177], v216 offset:16640\n" \
  "ds_read_b64_tr_b16 a[178:179], v216 offset:24832\n" \
  "v_mfma_f32_16x16x32_bf16 v[152:155], a[112:115], a[16:19], v[152:155]\n" \
  "ds_read_b64_tr_b16 a[180:181], v217 offset:16640\n" \
  "v_exp_f32_e64 v199, v143\n" \
  "v_mfma_f32_16x16x32_bf16 v[156:159], a[112:115], a[48:51], v[156:159]\n" \
  "buffer_load_dwordx4 v236, s[52:55], s87 offen lds\n" \
  "v_mfma_f32_16x16x32_bf16 v[152:155], a[116:119], a[20:23], v[152:155]\n" \
  "ds_read_b64_tr_b16 a[182:183], v217 offset:24832\n" \
  "v_cvt_pk_bf16_f32 v183, v198, v199\n" \
  "v_mfma_f32_16x16x32_bf16 v[156:159], a[116:119], a[52:55], v[156:159]\n" \
  "ds_read_b64_tr_b16 a[184:185], v218 offset:16640\n" \
  "v_bitop3_b32 v248, v248, v183, s76 bitop3:0xa8\n" \
  "v_mfma_f32_16x16x32_bf16 v[152:155], a[120:123], a[24:27], v[152:155]\n" \
  "ds_read_b64_tr_b16 a[186:187], v218 offset:24832\n" \
  "ds_read_b64_tr_b16 a[188:189], v219 offset:16640\n" \
  "v_mfma_f32_16x16x32_bf16 v[156:159], a[120:123], a[56:59], v[156:159]\n" \
  "ds_read_b64_tr_b16 a[190:191], v219 offset:24832\n" \
  "v_cmp_ne_u32_e64 vcc, 0, v248\n" \
  "v_mfma_f32_16x16x32_bf16 v[152:155], a[124:127], a[28:31], v[152:155]\n" \
  "v_subrev_u32 v212, 0x10000, v212\n" \
  "v_subrev_u32 v213, 0x10000, v213\n" \
  "v_subrev_u32 v214, 0x10000, v214\n" \
  "v_subrev_u32 v215, 0x10000, v215\n" \
  "v_subrev_u32 v216, 0x10000, v216\n" \
  "v_subrev_u32 v217, 0x10000, v217\n" \
  "v_subrev_u32 v218, 0x10000, v218\n" \
  "v_subrev_u32 v219, 0x10000, v219\n" \
  "ds_read_b64_tr_b16 a[64:65], v212 offset:16384\n" \
  "s_waitcnt lgkmcnt(6)\n" \
  "v_mfma_f32_16x16x32_bf16 v[156:159], a[124:127], a[60:63], v[156:159]\n" \
  "buffer_load_dwordx4 v237, s[52:55], s87 offen offset:1024 lds\n" \
  "s_cbranch_vccnz L_rare_p62_%=\n" \
  "L_back_p62_%=:\n" \
  "v_mfma_f32_16x16x32_bf16 v[0:3], a[128:131], v[176:179], v[0:3]\n" \
  "ds_read_b64_tr_b16 a[66:67], v212 offset:24576\n" \
  "v_mfma_f32_16x16x32_bf16 v[4:7], a[128:131], v[180:183], v[4:7]\n" \
  "ds_read_b64_tr_b16 a[68:69], v213 offset:16384\n" \
  "ds_read_b64_tr_b16 a[70:71], v213 offset:24576\n" \
  "v_mfma_f32_16x16x32_bf16 v[8:11], a[132:135], v[176:179], v[8:11]\n" \
  "ds_read_b64_tr_b16 a[72:73], v214 offset:16384\n" \
  "v_mfma_f32_16x16x32_bf16 v[12:15], a[132:135], v[180:183], v[12:15]\n" \
  "ds_read_b64_tr_b16 a[74:75], v214 offset:24576\n" \
  "s_waitcnt lgkmcnt(6)\n" \
  "v_mfma_f32_16x16x32_bf16 v[16:19], a[136:139], v[176:179], v[16:19]\n" \
  "buffer_load_dwordx4 v238, s[52:55], s87 offen offset:2048 lds\n" \
  "v_mfma_f32_16x16x32_bf16 v[20:23], a[136:139], v[180:183], v[20:23]\n" \
  "ds_read_b64_tr_b16 a[76:77], v215 offset:16384\n" \
  "v_exp_f32 v192, v144\n" \
  "v_mfma_f32_16x16x32_bf16 v[24:27], a[140:143], v[176:179], v[24:27]\n" \
  "ds_read_b64_tr_b16 a[78:79], v215 offset:24576\n" \
  "ds_read_b64_tr_b16 a[80:81], v216 offset:16384\n" \
  "v_exp_f32_e64 v193, v145\n" \
  "v_mfma_f32_16x16x32_bf16 v[28:31], a[140:143], v[180:183], v[28:31]\n" \
  "ds_read_b64_tr_b16 a[82:83], v216 offset:24576\n" \
  "v_cvt_pk_bf16_f32 v184, v192, v193\n" \
  "v_mfma_f32_16x16x32_bf16 v[32:35], a[144:147], v[176:179], v[32:35]\n" \
  "ds_read_b64_tr_b16 a[84:85], v217 offset:16384\n" \
  "v_exp_f32_e64 v194, v146\n" \
  "v_mfma_f32_16x16x32_bf16 v[36:39], a[144:147], v[180:183], v[36:39]\n" \
  "ds_read_b64_tr_b16 a[86:87], v217 offset:24576\n" \
  "v_exp_f32_e64 v195, v147\n" \
  "v_mfma_f32_16x16x32_bf16 v[40:43], a[148:151], v[176:179], v[40:43]\n" \
  "buffer_load_dwordx4 v239, s[52:55], s87 offen offset:3072 lds\n" \
  "v_exp_f32_e64 v196, v148\n" \
  "v_mfma_f32_16x16x32_bf16 v[44:47], a[148:151], v[180:183], v[44:47]\n" \
  "ds_read_b64_tr_b16 a[88:89], v218 offset:16384\n" \
  "ds_read_b64_tr_b16 a[90:91], v218 offset:24576\n" \
  "v_exp_f32_e64 v197, v149\n" \
  "v_mfma_f32_16x16x32_bf16 v[48:51], a[152:155], v[176:179], v[48:51]\n" \
  "ds_read_b64_tr_b16 a[92:93], v219 offset:16384\n" \
  "v_cvt_pk_bf16_f32 v185, v194, v195\n" \
  "v_mfma_f32_16x16x32_bf16 v[52:55], a[152:155], v[180:183], v[52:55]\n" \
  "ds_read_b64_tr_b16 a[94:95], v219 offset:24576\n" \
  "v_cvt_pk_bf16_f32 v188, v196, v197\n" \
  "v_mfma_f32_16x16x32_bf16 v[56:59], a[156:159], v[176:179], v[56:59]\n" \
  "ds_read_b64_tr_b16 a[96:97], v212 offset:16640\n" \
  "v_or3_b32 v248, v184, v185, v188\n" \
  "v_mfma_f32_16x16x32_bf16 v[60:63], a[156:159], v[180:183], v[60:63]\n" \
  "buffer_load_dwordx4 a[240:243], v222, s[60:63], s88 offen sc1\n" \
  "v_exp_f32_e64 v198, v150\n" \
  "v_mfma_f32_16x16x32_bf16 v[64:67], a[160:163], v[176:179], v[64:67]\n" \
  "ds_read_b64_tr_b16 a[98:99], v212 offset:24832\n" \
  "ds_read_b64_tr_b16 a[100:101], v213 offset:16640\n" \
  "v_exp_f32_e64 v199, v151\n" \
  "v_mfma_f32_16x16x32_bf16 v[68:71], a[160:163], v[180:183], v[68:71]\n" \
  "ds_read_b64_tr_b16 a[102:103], v213 offset:24832\n" \
  "v_cvt_pk_bf16_f32 v189, v198, v199\n" \
  "v_mfma_f32_16x16x32_bf16 v[72:75], a[164:167], v[176:179], v[72:75]\n" \
  "ds_read_b64_tr_b16 a[104:105], v214 offset:16640\n" \
  "v_exp_f32_e64 v192, v152\n" \
  "v_mfma_f32_16x16x32_bf16 v[76:79], a[164:167], v[180:183], v[76:79]\n" \
  "ds_read_b64_tr_b16 a[106:107], v214 offset:24832\n" \
  "v_exp_f32_e64 v193, v153\n" \
  "v_mfma_f32_16x16x32_bf16 v[80:83], a[168:171], v[176:179], v[80:83]\n" \
  "ds_read_b64_tr_b16 a[108:109], v215 offset:16640\n" \
  "ds_read_b64_tr_b16 a[110:111], v215 offset:24832\n" \
  "v_exp_f32_e64 v194, v154\n" \
  "v_mfma_f32_16x16x32_bf16 v[84:87], a[168:171], v[180:183], v[84:87]\n" \
  "buffer_load_dwordx4 a[244:247], v223, s[60:63], s88 offen sc1\n" \
  "v_exp_f32_e64 v195, v155\n" \
  "v_mfma_f32_16x16x32_bf16 v[88:91], a[172:175], v[176:179], v[88:91]\n" \
  "ds_read_b64_tr_b16 a[112:113], v216 offset:16640\n" \
  "v_cvt_pk_bf16_f32 v186, v192, v193\n" \
  "v_mfma_f32_16x16x32_bf16 v[92:95], a[172:175], v[180:183], v[92:95]\n" \
  "ds_read_b64_tr_b16 a[114:115], v216 offset:24832\n" \
  "v_cvt_pk_bf16_f32 v187, v194, v195\n" \
  "v_mfma_f32_16x16x32_bf16 v[96:99], a[176:179], v[176:179], v[96:99]\n" \
  "ds_read_b64_tr_b16 a[116:117], v217 offset:16640\n" \
  "v_or3_b32 v248, v248, v189, v186\n" \
  "v_mfma_f32_16x16x32_bf16 v[100:103], a[176:179], v[180:183], v[100:103]\n" \
  "ds_read_b64_tr_b16 a[118:119], v217 offset:24832\n" \
  "ds_read_b64_tr_b16 a[120:121], v218 offset:16640\n" \
  "v_exp_f32_e64 v196, v156\n" \
  "v_mfma_f32_16x16x32_bf16 v[104:107], a[180:183], v[176:179], v[104:107]\n" \
  "buffer_load_dwordx4 a[248:251], v222, s[60:63], s88 offen offset:1024 sc1\n" \
  "v_exp_f32_e64 v197, v157\n" \
  "v_mfma_f32_16x16x32_bf16 v[108:111], a[180:183], v[180:183], v[108:111]\n" \
  "ds_read_b64_tr_b16 a[122:123], v218 offset:24832\n" \
  "v_cvt_pk_bf16_f32 v190, v196, v197\n" \
  "v_mfma_f32_16x16x32_bf16 v[112:115], a[184:187], v[176:179], v[112:115]\n" \
  "ds_read_b64_tr_b16 a[124:125], v219 offset:16640\n" \
  "v_or3_b32 v248, v248, v187, v190\n" \
  "v_mfma_f32_16x16x32_bf16 v[116:119], a[184:187], v[180:183], v[116:119]\n" \
  "ds_read_b64_tr_b16 a[126:127], v219 offset:24832\n" \
  "v_exp_f32_e64 v198, v158\n" \
  "v_mfma_f32_16x16x32_bf16 v[120:123], a[188:191], v[176:179], v[120:123]\n" \
  "v_exp_f32_e64 v199, v159\n" \
  "v_mfma_f32_16x16x32_bf16 v[124:127], a[188:191], v[180:183], v[124:127]\n" \
  "v_cvt_pk_bf16_f32 v191, v198, v199\n" \
  "v_bitop3_b32 v248, v248, v191, s76 bitop3:0xa8\n" \
  "v_mfma_f32_16x16x32_bf16 v[200:203], v[172:175], v[176:179], v[200:203]\n" \
  "buffer_load_dwordx4 a[252:255], v223, s[60:63], s88 offen offset:1024 sc1\n" \
  "v_cmp_ne_u32_e64 vcc, 0, v248\n" \
  "v_mfma_f32_16x16x32_bf16 v[204:207], v[172:175], v[180:183], v[204:207]\n" \
  "s_waitcnt vmcnt(27) lgkmcnt(0)\n" \
  "s_barrier\n" \
  "s_cbranch_vccnz L_rare_p63_%=\n" \
  "L_back_p63_%=:\n" \
  "; HP [155, 156] ('V', 63)\n" \
  "v_mfma_f32_16x16x32_bf16 v[0:3], a[64:67], v[184:187], v[0:3]\n" \
  "ds_read_b64_tr_b16 a[128:129], v228 offset:32768\n" \
  "ds_read_b64_tr_b16 a[130:131], v228 offset:34816\n" \
  "v_mfma_f32_16x16x32_bf16 v[4:7], a[64:67], v[188:191], v[4:7]\n" \
  "ds_read_b64_tr_b16 a[132:133], v229 offset:32768\n" \
  "s_mov_b32 s86, 0x180\n" \
  "s_add_u32 m0, s74, 0x4000\n" \
  "v_mfma_f32_16x16x32_bf16 v[8:11], a[68:71], v[184:187], v[8:11]\n" \
  "buffer_load_dwordx4 v236, s[52:55], s86 offen lds\n" \
  "v_mfma_f32_16x16x32_bf16 v[12:15], a[68:71], v[188:191], v[12:15]\n" \
  "ds_read_b64_tr_b16 a[134:135], v229 offset:34816\n" \
  "ds_read_b64_tr_b16 a[136:137], v230 offset:32768\n" \
  "v_mfma_f32_16x16x32_bf16 v[16:19], a[72:75], v[184:187], v[16:19]\n" \
  "ds_read_b64_tr_b16 a[138:139], v230 offset:34816\n" \
  "s_mov_b32 s87, 0x1c180\n" \
  "v_mfma_f32_16x16x32_bf16 v[20:23], a[72:75], v[188:191], v[20:23]\n" \
  "buffer_load_dwordx4 v237, s[52:55], s86 offen offset:1024 lds\n" \
  "v_mfma_f32_16x16x32_bf16 v[24:27], a[76:79], v[184:187], v[24:27]\n" \
  "ds_read_b64_tr_b16 a[140:141], v231 offset:32768\n" \
  "ds_read_b64_tr_b16 a[142:143], v231 offset:34816\n" \
  "v_cvt_pk_bf16_f32 v128, v0, v1\n" \
  "v_mfma_f32_16x16x32_bf16 v[28:31], a[76:79], v[188:191], v[28:31]\n" \
  "ds_read_b64_tr_b16 a[144:145], v228 offset:36864\n" \
  "v_cvt_pk_bf16_f32 v129, v2, v3\n" \
  "v_mfma_f32_16x16x32_bf16 v[32:35], a[80:83], v[184:187], v[32:35]\n" \
  "buffer_load_dwordx4 v238, s[52:55], s86 offen offset:2048 lds\n" \
  "v_cvt_pk_bf16_f32 v130, v8, v9\n" \
  "v_mfma_f32_16x16x32_bf16 v[36:39], a[80:83], v[188:191], v[36:39]\n" \
  "ds_read_b64_tr_b16 a[146:147], v228 offset:38912\n" \
  "ds_read_b64_tr_b16 a[148:149], v229 offset:36864\n" \
  "v_cvt_pk_bf16_f32 v131, v10, v11\n" \
  "v_mfma_f32_16x16x32_bf16 v[40:43], a[84:87], v[184:187], v[40:43]\n" \
  "ds_read_b64_tr_b16 a[150:151], v229 offset:38912\n" \
  "v_cvt_pk_bf16_f32 v132, v4, v5\n" \
  "v_cvt_pk_bf16_f32 v133, v6, v7\n" \
  "v_mfma_f32_16x16x32_bf16 v[44:47], a[84:87], v[188:191], v[44:47]\n" \
  "ds_read_b64_tr_b16 a[152:153], v230 offset:36864\n" \
  "ds_read_b64_tr_b16 a[154:155], v230 offset:38912\n" \
  "v_cvt_pk_bf16_f32 v134, v12, v13\n" \
  "v_mfma_f32_16x16x32_bf16 v[48:51], a[88:91], v[184:187], v[48:51]\n" \
  "buffer_load_dwordx4 v239, s[52:55], s86 offen offset:3072 lds\n" \
  "v_cvt_pk_bf16_f32 v135, v14, v15\n" \
  "v_mfma_f32_16x16x32_bf16 v[52:55], a[88:91], v[188:191], v[52:55]\n" \
  "ds_read_b64_tr_b16 a[156:157], v231 offset:36864\n" \
  "v_cvt_pk_bf16_f32 v136, v16, v17\n" \
  "v_mfma_f32_16x16x32_bf16 v[56:59], a[92:95], v[184:187], v[56:59]\n" \
  "ds_read_b64_tr_b16 a[158:159], v231 offset:38912\n" \
  "ds_read_b64_tr_b16 a[160:161], v228 offset:40960\n" \
  "s_add_u32 m0, s75, 0x4000\n" \
  "v_cvt_pk_bf16_f32 v137, v18, v19\n" \
  "v_cvt_pk_bf16_f32 v138, v24, v25\n" \
  "v_mfma_f32_16x16x32_bf16 v[60:63], a[92:95], v[188:191], v[60:63]\n" \
  "buffer_load_dwordx4 v236, s[52:55], s87 offen lds\n" \
  "v_cvt_pk_bf16_f32 v139, v26, v27\n" \
  "v_mfma_f32_16x16x32_bf16 v[64:67], a[96:99], v[184:187], v[64:67]\n" \
  "ds_read_b64_tr_b16 a[162:163], v228 offset:43008\n" \
  "v_cvt_pk_bf16_f32 v140, v20, v21\n" \
  "v_mfma_f32_16x16x32_bf16 v[68:71], a[96:99], v[188:191], v[68:71]\n" \
  "ds_read_b64_tr_b16 a[164:165], v229 offset:40960\n" \
  "ds_read_b64_tr_b16 a[166:167], v229 offset:43008\n" \
  "v_cvt_pk_bf16_f32 v141, v22, v23\n" \
  "v_mfma_f32_16x16x32_bf16 v[72:75], a[100:103], v[184:187], v[72:75]\n" \
  "buffer_load_dwordx4 v237, s[52:55], s87 offen offset:1024 lds\n" \
  "v_cvt_pk_bf16_f32 v142, v28, v29\n" \
  "v_cvt_pk_bf16_f32 v143, v30, v31\n" \
  "v_mfma_f32_16x16x32_bf16 v[76:79], a[100:103], v[188:191], v[76:79]\n" \
  "ds_read_b64_tr_b16 a[168:169], v230 offset:40960\n" \
  "v_cvt_pk_bf16_f32 v144, v32, v33\n" \
  "v_mfma_f32_16x16x32_bf16 v[80:83], a[104:107], v[184:187], v[80:83]\n" \
  "ds_read_b64_tr_b16 a[170:171], v230 offset:43008\n" \
  "ds_read_b64_tr_b16 a[172:173], v231 offset:40960\n" \
  "v_cvt_pk_bf16_f32 v145, v34, v35\n" \
  "v_mfma_f32_16x16x32_bf16 v[84:87], a[104:107], v[188:191], v[84:87]\n" \
  "ds_read_b64_tr_b16 a[174:175], v231 offset:43008\n" \
  "v_cvt_pk_bf16_f32 v146, v40, v41\n" \
  "v_mfma_f32_16x16x32_bf16 v[88:91], a[108:111], v[184:187], v[88:91]\n" \
  "buffer_load_dwordx4 v238, s[52:55], s87 offen offset:2048 lds\n" \
  "v_cvt_pk_bf16_f32 v147, v42, v43\n" \
  "v_mfma_f32_16x16x32_bf16 v[92:95], a[108:111], v[188:191], v[92:95]\n" \
  "ds_read_b64_tr_b16 a[176:177], v228 offset:45056\n" \
  "ds_read_b64_tr_b16 a[178:179], v228 offset:47104\n" \
  "v_cvt_pk_bf16_f32 v148, v36, v37\n" \
  "v_cvt_pk_bf16_f32 v149, v38, v39\n" \
  "v_cvt_pk_bf16_f32 v160, v64, v65\n" \
  "v_mfma_f32_16x16x32_bf16 v[96:99], a[112:115], v[184:187], v[96:99]\n" \
  "ds_read_b64_tr_b16 a[180:181], v229 offset:45056\n" \
  "v_cvt_pk_bf16_f32 v150, v44, v45\n" \
  "v_cvt_pk_bf16_f32 v161, v66, v67\n" \
  "v_mfma_f32_16x16x32_bf16 v[100:103], a[112:115], v[188:191], v[100:103]\n" \
  "buffer_load_dwordx4 v239, s[52:55], s87 offen offset:3072 lds\n" \
  "v_cvt_pk_bf16_f32 v151, v46, v47\n" \
  "v_cvt_pk_bf16_f32 v162, v72, v73\n" \
  "v_mfma_f32_16x16x32_bf16 v[104:107], a[116:119], v[184:187], v[104:107]\n" \
  "ds_read_b64_tr_b16 a[182:183], v229 offset:47104\n" \
  "ds_read_b64_tr_b16 a[184:185], v230 offset:45056\n" \
  "v_cvt_pk_bf16_f32 v152, v48, v49\n" \
  "v_mfma_f32_16x16x32_bf16 v[108:111], a[116:119], v[188:191], v[108:111]\n" \
  "ds_read_b64_tr_b16 a[186:187], v230 offset:47104\n" \
  "s_mov_b32 s88, 0x4000\n" \
  "v_cvt_pk_bf16_f32 v153, v50, v51\n" \
  "v_cvt_pk_bf16_f32 v154, v56, v57\n" \
  "v_cvt_pk_bf16_f32 v163, v74, v75\n" \
  "v_mfma_f32_16x16x32_bf16 v[112:115], a[120:123], v[184:187], v[112:115]\n" \
  "buffer_load_dwordx4 a[0:3], v222, s[60:63], s88 offen sc1\n" \
  "v_cvt_pk_bf16_f32 v155, v58, v59\n" \
  "v_cvt_pk_bf16_f32 v164, v68, v69\n" \
  "v_mfma_f32_16x16x32_bf16 v[116:119], a[120:123], v[188:191], v[116:119]\n" \
  "ds_read_b64_tr_b16 a[188:189], v231 offset:45056\n" \
  "ds_read_b64_tr_b16 a[190:191], v231 offset:47104\n" \
  "v_cvt_pk_bf16_f32 v156, v52, v53\n" \
  "v_cvt_pk_bf16_f32 v165, v70, v71\n" \
  "v_mfma_f32_16x16x32_bf16 v[120:123], a[124:127], v[184:187], v[120:123]\n" \
  "ds_read_b64_tr_b16 a[64:65], v232 offset:32768\n" \
  "v_cvt_pk_bf16_f32 v157, v54, v55\n" \
  "v_cvt_pk_bf16_f32 v166, v76, v77\n" \
  "v_mfma_f32_16x16x32_bf16 v[124:127], a[124:127], v[188:191], v[124:127]\n" \
  "ds_read_b64_tr_b16 a[66:67], v232 offset:34816\n" \
  "ds_read_b64_tr_b16 a[68:69], v233 offset:32768\n" \
  "v_cvt_pk_bf16_f32 v158, v60, v61\n" \
  "v_cvt_pk_bf16_f32 v159, v62, v63\n" \
  "v_mfma_f32_16x16x32_bf16 v[200:203], v[172:175], v[184:187], v[200:203]\n" \
  "buffer_load_dwordx4 a[4:7], v223, s[60:63], s88 offen sc1\n" \
  "v_cvt_pk_bf16_f32 v167, v78, v79\n" \
  "v_mfma_f32_16x16x32_bf16 v[204:207], v[172:175], v[188:191], v[204:207]\n" \
  "ds_read_b64_tr_b16 a[70:71], v233 offset:34816\n" \
  "s_waitcnt lgkmcnt(6)\n" \
  "v_cvt_pk_bf16_f32 v168, v80, v81\n" \
  "v_mfma_f32_16x16x32_bf16 v[0:3], a[128:131], v[128:131], 0\n" \
  "ds_read_b64_tr_b16 a[72:73], v234 offset:32768\n" \
  "ds_read_b64_tr_b16 a[74:75], v234 offset:34816\n" \
  "v_cvt_pk_bf16_f32 v169, v82, v83\n" \
  "v_mfma_f32_16x16x32_bf16 v[4:7], a[128:131], v[132:135], 0\n" \
  "buffer_load_dwordx4 a[8:11], v222, s[60:63], s88 offen offset:1024 sc1\n" \
  "v_mfma_f32_16x16x32_bf16 v[8:11], a[132:135], v[128:131], 0\n" \
  "ds_read_b64_tr_b16 a[76:77], v235 offset:32768\n" \
  "v_cvt_pk_bf16_f32 v170, v88, v89\n" \
  "v_mfma_f32_16x16x32_bf16 v[12:15], a[132:135], v[132:135], 0\n" \
  "ds_read_b64_tr_b16 a[78:79], v235 offset:34816\n" \
  "ds_read_b64_tr_b16 a[80:81], v232 offset:36864\n" \
  "v_cvt_pk_bf16_f32 v171, v90, v91\n" \
  "v_mfma_f32_16x16x32_bf16 v[16:19], a[136:139], v[128:131], 0\n" \
  "buffer_load_dwordx4 a[12:15], v223, s[60:63], s88 offen offset:1024 sc1\n" \
  "v_cvt_pk_bf16_f32 v172, v84, v85\n" \
  "v_mfma_f32_16x16x32_bf16 v[20:23], a[136:139], v[132:135], 0\n" \
  "ds_read_b64_tr_b16 a[82:83], v232 offset:38912\n" \
  "v_cvt_pk_bf16_f32 v173, v86, v87\n" \
  "v_mfma_f32_16x16x32_bf16 v[24:27], a[140:143], v[128:131], 0\n" \
  "ds_read_b64_tr_b16 a[84:85], v233 offset:36864\n" \
  "ds_read_b64_tr_b16 a[86:87], v233 offset:38912\n" \
  "s_mov_b32 s88, 0x5000\n" \
  "v_mfma_f32_16x16x32_bf16 v[28:31], a[140:143], v[132:135], 0\n" \
  "buffer_load_dwordx4 a[16:19], v222, s[60:63], s88 offen sc1\n" \
  "v_cvt_pk_bf16_f32 v174, v92, v93\n" \
  "v_mfma_f32_16x16x32_bf16 v[0:3], a[144:147], v[136:139], v[0:3]\n" \
  "ds_read_b64_tr_b16 a[88:89], v234 offset:36864\n" \
  "v_cvt_pk_bf16_f32 v175, v94, v95\n" \
  "v_mfma_f32_16x16x32_bf16 v[4:7], a[144:147], v[140:143], v[4:7]\n" \
  "ds_read_b64_tr_b16 a[90:91], v234 offset:38912\n" \
  "ds_read_b64_tr_b16 a[92:93], v235 offset:36864\n" \
  "v_cvt_pk_bf16_f32 v176, v96, v97\n" \
  "v_mfma_f32_16x16x32_bf16 v[8:11], a[148:151], v[136:139], v[8:11]\n" \
  "ds_read_b64_tr_b16 a[94:95], v235 offset:38912\n" \
  "v_cvt_pk_bf16_f32 v177, v98, v99\n" \
  "v_mfma_f32_16x16x32_bf16 v[12:15], a[148:151], v[140:143], v[12:15]\n" \
  "buffer_load_dwordx4 a[20:23], v223, s[60:63], s88 offen sc1\n" \
  "v_mfma_f32_16x16x32_bf16 v[16:19], a[152:155], v[136:139], v[16:19]\n" \
  "ds_read_b64_tr_b16 a[96:97], v232 offset:40960\n" \
  "ds_read_b64_tr_b16 a[98:99], v232 offset:43008\n" \
  "v_cvt_pk_bf16_f32 v178, v104, v105\n" \
  "v_mfma_f32_16x16x32_bf16 v[20:23], a[152:155], v[140:143], v[20:23]\n" \
  "ds_read_b64_tr_b16 a[100:101], v233 offset:40960\n" \
  "v_cvt_pk_bf16_f32 v179, v106, v107\n" \
  "v_mfma_f32_16x16x32_bf16 v[24:27], a[156:159], v[136:139], v[24:27]\n" \
  "buffer_load_dwordx4 a[24:27], v222, s[60:63], s88 offen offset:1024 sc1\n" \
  "v_cvt_pk_bf16_f32 v180, v100, v101\n" \
  "v_mfma_f32_16x16x32_bf16 v[28:31], a[156:159], v[140:143], v[28:31]\n" \
  "ds_read_b64_tr_b16 a[102:103], v233 offset:43008\n" \
  "ds_read_b64_tr_b16 a[104:105], v234 offset:40960\n" \
  "v_mfma_f32_16x16x32_bf16 v[0:3], a[160:163], v[144:147], v[0:3]\n" \
  "ds_read_b64_tr_b16 a[106:107], v234 offset:43008\n" \
  "v_cvt_pk_bf16_f32 v181, v102, v103\n" \
  "v_mfma_f32_16x16x32_bf16 v[4:7], a[160:163], v[148:151], v[4:7]\n" \
  "buffer_load_dwordx4 a[28:31], v223, s[60:63], s88 offen offset:1024 sc1\n" \
  "v_cvt_pk_bf16_f32 v182, v108, v109\n" \
  "v_rcp_f32_e64 v200, v200\n" \
  "v_mfma_f32_16x16x32_bf16 v[8:11], a[164:167], v[144:147], v[8:11]\n" \
  "ds_read_b64_tr_b16 a[108:109], v235 offset:40960\n" \
  "ds_read_b64_tr_b16 a[110:111], v235 offset:43008\n" \
  "v_cvt_pk_bf16_f32 v183, v110, v111\n" \
  "v_mfma_f32_16x16x32_bf16 v[12:15], a[164:167], v[148:151], v[12:15]\n" \
  "ds_read_b64_tr_b16 a[112:113], v232 offset:45056\n" \
  "v_cvt_pk_bf16_f32 v184, v112, v113\n" \
  "v_mfma_f32_16x16x32_bf16 v[16:19], a[168:171], v[144:147], v[16:19]\n" \
  "ds_read_b64_tr_b16 a[114:115], v232 offset:47104\n" \
  "ds_read_b64_tr_b16 a[116:117], v233 offset:45056\n" \
  "s_mov_b32 s88, 0x6000\n" \
  "v_mfma_f32_16x16x32_bf16 v[20:23], a[168:171], v[148:151], v[20:23]\n" \
  "buffer_load_dwordx4 a[32:35], v222, s[60:63], s88 offen sc1\n" \
  "v_cvt_pk_bf16_f32 v185, v114, v115\n" \
  "v_mfma_f32_16x16x32_bf16 v[24:27], a[172:175], v[144:147], v[24:27]\n" \
  "ds_read_b64_tr_b16 a[118:119], v233 offset:47104\n" \
  "v_cvt_pk_bf16_f32 v186, v120, v121\n" \
  "v_mfma_f32_16x16x32_bf16 v[28:31], a[172:175], v[148:151], v[28:31]\n" \
  "ds_read_b64_tr_b16 a[120:121], v234 offset:45056\n" \
  "ds_read_b64_tr_b16 a[122:123], v234 offset:47104\n" \
  "v_cvt_pk_bf16_f32 v187, v122, v123\n" \
  "v_mfma_f32_16x16x32_bf16 v[0:3], a[176:179], v[152:155], v[0:3]\n" \
  "buffer_load_dwordx4 a[36:39], v223, s[60:63], s88 offen sc1\n" \
  "v_mfma_f32_16x16x32_bf16 v[4:7], a[176:179], v[156:159], v[4:7]\n" \
  "ds_read_b64_tr_b16 a[124:125], v235 offset:45056\n" \
  "v_cvt_pk_bf16_f32 v188, v116, v117\n" \
  "v_mfma_f32_16x16x32_bf16 v[8:11], a[180:183], v[152:155], v[8:11]\n" \
  "ds_read_b64_tr_b16 a[126:127], v235 offset:47104\n" \
  "v_cvt_pk_bf16_f32 v189, v118, v119\n" \
  "v_mfma_f32_16x16x32_bf16 v[12:15], a[180:183], v[156:159], v[12:15]\n" \
  "buffer_load_dwordx4 a[40:43], v222, s[60:63], s88 offen offset:1024 sc1\n" \
  "v_cvt_pk_bf16_f32 v190, v124, v125\n" \
  "v_mfma_f32_16x16x32_bf16 v[16:19], a[184:187], v[152:155], v[16:19]\n" \
  "v_cvt_pk_bf16_f32 v191, v126, v127\n" \
  "v_rcp_f32_e64 v204, v204\n" \
  "v_mfma_f32_16x16x32_bf16 v[20:23], a[184:187], v[156:159], v[20:23]\n" \
  "s_waitcnt lgkmcnt(2)\n" \
  "v_mfma_f32_16x16x32_bf16 v[24:27], a[188:191], v[152:155], v[24:27]\n" \
  "v_mfma_f32_16x16x32_bf16 v[28:31], a[188:191], v[156:159], v[28:31]\n" \
  "buffer_load_dwordx4 a[44:47], v223, s[60:63], s88 offen offset:1024 sc1\n" \
  "s_waitcnt vmcnt(33) lgkmcnt(0)\n" \
  "s_barrier\n" \
  "s_or_b32 s94, %3, %4\n" \
  "s_or_b32 s94, s94, %1\n" \
  "s_lshl_b32 s94, s94, 24\n" \
  "s_add_u32 s94, s94, 0x80\n" \
  "v_lshrrev_b32 v96, 4, v253\n" \
  "v_lshlrev_b32 v96, 13, v96\n" \
  "v_and_b32_e64 v104, 15, v253\n" \
  "v_lshl_add_u32 v104, v104, 7, v96\n" \
  "v_add_u32_e64 v104, s94, v104\n" \
  "; HP [157, 158] ('O', 0, 1)\n" \
  "v_mfma_f32_16x16x32_bf16 v[0:3], a[64:67], v[160:163], v[0:3]\n" \
  "ds_read_b64_tr_b16 a[128:129], v228 offset:49152\n" \
  "ds_read_b64_tr_b16 a[130:131], v228 offset:51200\n" \
  "v_mfma_f32_16x16x32_bf16 v[4:7], a[64:67], v[164:167], v[4:7]\n" \
  "ds_read_b64_tr_b16 a[132:133], v229 offset:49152\n" \
  "s_mov_b32 s88, 0x7000\n" \
  "v_mfma_f32_16x16x32_bf16 v[8:11], a[68:71], v[160:163], v[8:11]\n" \
  "buffer_load_dwordx4 a[48:51], v222, s[60:63], s88 offen sc1\n" \
  "v_mfma_f32_16x16x32_bf16 v[12:15], a[68:71], v[164:167], v[12:15]\n" \
  "ds_read_b64_tr_b16 a[134:135], v229 offset:51200\n" \
  "ds_read_b64_tr_b16 a[136:137], v230 offset:49152\n" \
  "v_mfma_f32_16x16x32_bf16 v[16:19], a[72:75], v[160:163], v[16:19]\n" \
  "ds_read_b64_tr_b16 a[138:139], v230 offset:51200\n" \
  "s_mov_b32 s86, 0x200\n" \
  "v_mfma_f32_16x16x32_bf16 v[20:23], a[72:75], v[164:167], v[20:23]\n" \
  "ds_read_b64_tr_b16 a[140:141], v231 offset:49152\n" \
  "ds_read_b64_tr_b16 a[142:143], v231 offset:51200\n" \
  "v_mfma_f32_16x16x32_bf16 v[24:27], a[76:79], v[160:163], v[24:27]\n" \
  "buffer_load_dwordx4 a[52:55], v223, s[60:63], s88 offen sc1\n" \
  "v_mfma_f32_16x16x32_bf16 v[28:31], a[76:79], v[164:167], v[28:31]\n" \
  "ds_read_b64_tr_b16 a[144:145], v228 offset:53248\n" \
  "s_add_u32 m0, s74, 0x8000\n" \
  "v_mfma_f32_16x16x32_bf16 v[0:3], a[80:83], v[168:171], v[0:3]\n" \
  "ds_read_b64_tr_b16 a[146:147], v228 offset:55296\n" \
  "s_mov_b32 s87, 0x1c200\n" \
  "v_mfma_f32_16x16x32_bf16 v[4:7], a[80:83], v[172:175], v[4:7]\n" \
  "ds_read_b64_tr_b16 a[148:149], v229 offset:53248\n" \
  "ds_read_b64_tr_b16 a[150:151], v229 offset:55296\n" \
  "v_mfma_f32_16x16x32_bf16 v[8:11], a[84:87], v[168:171], v[8:11]\n" \
  "buffer_load_dwordx4 a[56:59], v222, s[60:63], s88 offen offset:1024 sc1\n" \
  "v_mfma_f32_16x16x32_bf16 v[12:15], a[84:87], v[172:175], v[12:15]\n" \
  "ds_read_b64_tr_b16 a[152:153], v230 offset:53248\n" \
  "s_mov_b32 s94, 0\n" \
  "v_mfma_f32_16x16x32_bf16 v[16:19], a[88:91], v[168:171], v[16:19]\n" \
  "ds_read_b64_tr_b16 a[154:155], v230 offset:55296\n" \
  "ds_read_b64_tr_b16 a[156:157], v231 offset:53248\n" \
  "v_mfma_f32_16x16x32_bf16 v[20:23], a[88:91], v[172:175], v[20:23]\n" \
  "ds_read_b64_tr_b16 a[158:159], v231 offset:55296\n" \
  "v_mfma_f32_16x16x32_bf16 v[24:27], a[92:95], v[168:171], v[24:27]\n" \
  "buffer_load_dwordx4 a[60:63], v223, s[60:63], s88 offen offset:1024 sc1\n" \
  "v_mfma_f32_16x16x32_bf16 v[28:31], a[92:95], v[172:175], v[28:31]\n" \
  "ds_read_b64_tr_b16 a[160:161], v228 offset:57344\n" \
  "ds_read_b64_tr_b16 a[162:163], v228 offset:59392\n" \
  "v_mfma_f32_16x16x32_bf16 v[0:3], a[96:99], v[176:179], v[0:3]\n" \
  "ds_read_b64_tr_b16 a[164:165], v229 offset:57344\n" \
  "v_mfma_f32_16x16x32_bf16 v[4:7], a[96:99], v[180:183], v[4:7]\n" \
  "ds_read_b64_tr_b16 a[166:167], v229 offset:59392\n" \
  "v_mfma_f32_16x16x32_bf16 v[8:11], a[100:103], v[176:179], v[8:11]\n" \
  "buffer_load_dwordx4 v236, s[52:55], s86 offen lds\n" \
  "v_mfma_f32_16x16x32_bf16 v[12:15], a[100:103], v[180:183], v[12:15]\n" \
  "ds_read_b64_tr_b16 a[168:169], v230 offset:57344\n" \
  "ds_read_b64_tr_b16 a[170:171], v230 offset:59392\n" \
  "v_mfma_f32_16x16x32_bf16 v[16:19], a[104:107], v[176:179], v[16:19]\n" \
  "ds_read_b64_tr_b16 a[172:173], v231 offset:57344\n" \
  "v_mfma_f32_16x16x32_bf16 v[20:23], a[104:107], v[180:183], v[20:23]\n" \
  "ds_read_b64_tr_b16 a[174:175], v231 offset:59392\n" \
  "ds_read_b64_tr_b16 a[176:177], v228 offset:61440\n" \
  "v_mfma_f32_16x16x32_bf16 v[24:27], a[108:111], v[176:179], v[24:27]\n" \
  "buffer_load_dwordx4 v237, s[52:55], s86 offen offset:1024 lds\n" \
  "v_mfma_f32_16x16x32_bf16 v[28:31], a[108:111], v[180:183], v[28:31]\n" \
  "ds_read_b64_tr_b16 a[178:179], v228 offset:63488\n" \
  "v_mfma_f32_16x16x32_bf16 v[0:3], a[112:115], v[184:187], v[0:3]\n" \
  "ds_read_b64_tr_b16 a[180:181], v229 offset:61440\n" \
  "ds_read_b64_tr_b16 a[182:183], v229 offset:63488\n" \
  "v_mfma_f32_16x16x32_bf16 v[4:7], a[112:115], v[188:191], v[4:7]\n" \
  "ds_read_b64_tr_b16 a[184:185], v230 offset:61440\n" \
  "v_mfma_f32_16x16x32_bf16 v[8:11], a[116:119], v[184:187], v[8:11]\n" \
  "buffer_load_dwordx4 v238, s[52:55], s86 offen offset:2048 lds\n" \
  "v_mfma_f32_16x16x32_bf16 v[12:15], a[116:119], v[188:191], v[12:15]\n" \
  "ds_read_b64_tr_b16 a[186:187], v230 offset:63488\n" \
  "v_mfma_f32_16x16x32_bf16 v[16:19], a[120:123], v[184:187], v[16:19]\n" \
  "ds_read_b64_tr_b16 a[188:189], v231 offset:61440\n" \
  "ds_read_b64_tr_b16 a[190:191], v231 offset:63488\n" \
  "v_mfma_f32_16x16x32_bf16 v[20:23], a[120:123], v[188:191], v[20:23]\n" \
  "ds_read_b64_tr_b16 a[64:65], v232 offset:49152\n" \
  "s_waitcnt lgkmcnt(7)\n" \
  "v_mfma_f32_16x16x32_bf16 v[24:27], a[124:127], v[184:187], v[24:27]\n" \
  "buffer_load_dwordx4 v239, s[52:55], s86 offen offset:3072 lds\n" \
  "v_mfma_f32_16x16x32_bf16 v[28:31], a[124:127], v[188:191], v[28:31]\n" \
  "ds_read_b64_tr_b16 a[66:67], v232 offset:51200\n" \
  "ds_read_b64_tr_b16 a[68:69], v233 offset:49152\n" \
  "v_mfma_f32_16x16x32_bf16 v[32:35], a[128:131], v[128:131], 0\n" \
  "ds_read_b64_tr_b16 a[70:71], v233 offset:51200\n" \
  "s_add_u32 m0, s75, 0x8000\n" \
  "v_mfma_f32_16x16x32_bf16 v[36:39], a[128:131], v[132:135], 0\n" \
  "ds_read_b64_tr_b16 a[72:73], v234 offset:49152\n" \
  "v_mfma_f32_16x16x32_bf16 v[40:43], a[132:135], v[128:131], 0\n" \
  "buffer_load_dwordx4 v236, s[52:55], s87 offen lds\n" \
  "v_mfma_f32_16x16x32_bf16 v[44:47], a[132:135], v[132:135], 0\n" \
  "ds_read_b64_tr_b16 a[74:75], v234 offset:51200\n" \
  "ds_read_b64_tr_b16 a[76:77], v235 offset:49152\n" \
  "v_mfma_f32_16x16x32_bf16 v[48:51], a[136:139], v[128:131], 0\n" \
  "ds_read_b64_tr_b16 a[78:79], v235 offset:51200\n" \
  "s_waitcnt lgkmcnt(7)\n" \
  "v_mfma_f32_16x16x32_bf16 v[52:55], a[136:139], v[132:135], 0\n" \
  "ds_read_b64_tr_b16 a[80:81], v232 offset:53248\n" \
  "ds_read_b64_tr_b16 a[82:83], v232 offset:55296\n" \
  "v_mul_f32 v0, v0, v200\n" \
  "v_mfma_f32_16x16x32_bf16 v[56:59], a[140:143], v[128:131], 0\n" \
  "buffer_load_dwordx4 v237, s[52:55], s87 offen offset:1024 lds\n" \
  "v_mul_f32_e64 v1, v1, v200\n" \
  "v_mfma_f32_16x16x32_bf16 v[60:63], a[140:143], v[132:135], 0\n" \
  "ds_read_b64_tr_b16 a[84:85], v233 offset:53248\n" \
  "v_mul_f32 v2, v2, v200\n" \
  "v_mul_f32 v3, v3, v200\n" \
  "v_mfma_f32_16x16x32_bf16 v[32:35], a[144:147], v[136:139], v[32:35]\n" \
  "ds_read_b64_tr_b16 a[86:87], v233 offset:55296\n" \
  "ds_read_b64_tr_b16 a[88:89], v234 offset:53248\n" \
  "v_mul_f32_e64 v8, v8, v200\n" \
  "v_mfma_f32_16x16x32_bf16 v[36:39], a[144:147], v[140:143], v[36:39]\n" \
  "ds_read_b64_tr_b16 a[90:91], v234 offset:55296\n" \
  "v_mul_f32_e64 v9, v9, v200\n" \
  "v_mfma_f32_16x16x32_bf16 v[40:43], a[148:151], v[136:139], v[40:43]\n" \
  "buffer_load_dwordx4 v238, s[52:55], s87 offen offset:2048 lds\n" \
  "v_mul_f32 v10, v10, v200\n" \
  "v_mul_f32 v11, v11, v200\n" \
  "v_mfma_f32_16x16x32_bf16 v[44:47], a[148:151], v[140:143], v[44:47]\n" \
  "ds_read_b64_tr_b16 a[92:93], v235 offset:53248\n" \
  "v_cvt_pk_bf16_f32 v0, v0, v1\n" \
  "v_mfma_f32_16x16x32_bf16 v[48:51], a[152:155], v[136:139], v[48:51]\n" \
  "ds_read_b64_tr_b16 a[94:95], v235 offset:55296\n" \
  "ds_read_b64_tr_b16 a[96:97], v232 offset:57344\n" \
  "v_cvt_pk_bf16_f32 v1, v2, v3\n" \
  "v_mfma_f32_16x16x32_bf16 v[52:55], a[152:155], v[140:143], v[52:55]\n" \
  "ds_read_b64_tr_b16 a[98:99], v232 offset:59392\n" \
  "v_cvt_pk_bf16_f32 v2, v8, v9\n" \
  "v_cvt_pk_bf16_f32 v3, v10, v11\n" \
  "v_mfma_f32_16x16x32_bf16 v[56:59], a[156:159], v[136:139], v[56:59]\n" \
  "buffer_load_dwordx4 v239, s[52:55], s87 offen offset:3072 lds\n" \
  "buffer_store_dwordx4 v[0:3], v244, s[64:67], 0 offen sc1\n" \
  "v_mfma_f32_16x16x32_bf16 v[60:63], a[156:159], v[140:143], v[60:63]\n" \
  "ds_read_b64_tr_b16 a[100:101], v233 offset:57344\n" \
  "ds_read_b64_tr_b16 a[102:103], v233 offset:59392\n" \
  "v_mul_f32 v16, v16, v200\n" \
  "v_mul_f32 v17, v17, v200\n" \
  "v_mfma_f32_16x16x32_bf16 v[32:35], a[160:163], v[144:147], v[32:35]\n" \
  "ds_read_b64_tr_b16 a[104:105], v234 offset:57344\n" \
  "v_mul_f32_e64 v18, v18, v200\n" \
  "v_mfma_f32_16x16x32_bf16 v[36:39], a[160:163], v[148:151], v[36:39]\n" \
  "ds_read_b64_tr_b16 a[106:107], v234 offset:59392\n" \
  "ds_read_b64_tr_b16 a[108:109], v235 offset:57344\n" \
  "v_mul_f32_e64 v19, v19, v200\n" \
  "v_mfma_f32_16x16x32_bf16 v[40:43], a[164:167], v[144:147], v[40:43]\n" \
  "buffer_load_dwordx2 v[96:97], v104, s[68:71], s94 offen sc1\n" \
  "v_mul_f32 v24, v24, v200\n" \
  "v_mul_f32 v25, v25, v200\n" \
  "v_mfma_f32_16x16x32_bf16 v[44:47], a[164:167], v[148:151], v[44:47]\n" \
  "ds_read_b64_tr_b16 a[110:111], v235 offset:59392\n" \
  "v_mul_f32_e64 v26, v26, v200\n" \
  "v_mfma_f32_16x16x32_bf16 v[48:51], a[168:171], v[144:147], v[48:51]\n" \
  "ds_read_b64_tr_b16 a[112:113], v232 offset:61440\n" \
  "v_mul_f32_e64 v27, v27, v200\n" \
  "v_mfma_f32_16x16x32_bf16 v[52:55], a[168:171], v[148:151], v[52:55]\n" \
  "ds_read_b64_tr_b16 a[114:115], v232 offset:63488\n" \
  "ds_read_b64_tr_b16 a[116:117], v233 offset:61440\n" \
  "s_mov_b32 s94, 0x8000\n" \
  "v_cvt_pk_bf16_f32 v16, v16, v17\n" \
  "v_cvt_pk_bf16_f32 v17, v18, v19\n" \
  "v_mfma_f32_16x16x32_bf16 v[56:59], a[172:175], v[144:147], v[56:59]\n" \
  "buffer_load_dwordx2 v[98:99], v104, s[68:71], s94 offen sc1\n" \
  "v_cvt_pk_bf16_f32 v18, v24, v25\n" \
  "v_mfma_f32_16x16x32_bf16 v[60:63], a[172:175], v[148:151], v[60:63]\n" \
  "ds_read_b64_tr_b16 a[118:119], v233 offset:63488\n" \
  "v_cvt_pk_bf16_f32 v19, v26, v27\n" \
  "v_mul_f32_e64 v4, v4, v204\n" \
  "v_mfma_f32_16x16x32_bf16 v[32:35], a[176:179], v[152:155], v[32:35]\n" \
  "buffer_store_dwordx4 v[16:19], v244, s[64:67], 0 offen offset:1024 sc1\n" \
  "v_mfma_f32_16x16x32_bf16 v[36:39], a[176:179], v[156:159], v[36:39]\n" \
  "ds_read_b64_tr_b16 a[120:121], v234 offset:61440\n" \
  "ds_read_b64_tr_b16 a[122:123], v234 offset:63488\n" \
  "s_mov_b32 s94, 0x10000\n" \
  "v_mul_f32_e64 v5, v5, v204\n" \
  "v_mfma_f32_16x16x32_bf16 v[40:43], a[180:183], v[152:155], v[40:43]\n" \
  "buffer_load_dwordx2 v[100:101], v104, s[68:71], s94 offen sc1\n" \
  "v_mul_f32 v6, v6, v204\n" \
  "v_mul_f32 v7, v7, v204\n" \
  "v_mfma_f32_16x16x32_bf16 v[44:47], a[180:183], v[156:159], v[44:47]\n" \
  "ds_read_b64_tr_b16 a[124:125], v235 offset:61440\n" \
  "ds_read_b64_tr_b16 a[126:127], v235 offset:63488\n" \
  "v_mul_f32_e64 v12, v12, v204\n" \
  "v_mfma_f32_16x16x32_bf16 v[48:51], a[184:187], v[152:155], v[48:51]\n" \
  "s_mov_b32 s94, 0x18000\n" \
  "v_mul_f32 v13, v13, v204\n" \
  "v_mul_f32 v14, v14, v204\n" \
  "v_mfma_f32_16x16x32_bf16 v[52:55], a[184:187], v[156:159], v[52:55]\n" \
  "v_mul_f32_e64 v15, v15, v204\n" \
  "v_cvt_pk_bf16_f32 v4, v4, v5\n" \
  "v_mfma_f32_16x16x32_bf16 v[56:59], a[188:191], v[152:155], v[56:59]\n" \
  "buffer_load_dwordx2 v[102:103], v104, s[68:71], s94 offen sc1\n" \
  "v_mfma_f32_16x16x32_bf16 v[60:63], a[188:191], v[156:159], v[60:63]\n" \
  "v_cvt_pk_bf16_f32 v5, v6, v7\n" \
  "v_cvt_pk_bf16_f32 v6, v12, v13\n" \
  "s_waitcnt vmcnt(42) lgkmcnt(0)\n" \
  "s_barrier\n" \
  "; HP [159, 160] ('O', 1, 1)\n" \
  "v_mfma_f32_16x16x32_bf16 v[32:35], a[64:67], v[160:163], v[32:35]\n" \
  "ds_read_b64_tr_b16 a[128:129], v228\n" \
  "ds_read_b64_tr_b16 a[130:131], v228 offset:2048\n" \
  "v_cvt_pk_bf16_f32 v7, v14, v15\n" \
  "v_mfma_f32_16x16x32_bf16 v[36:39], a[64:67], v[164:167], v[36:39]\n" \
  "buffer_store_dwordx4 v[4:7], v245, s[64:67], 0 offen sc1\n" \
  "v_mfma_f32_16x16x32_bf16 v[40:43], a[68:71], v[160:163], v[40:43]\n" \
  "ds_read_b64_tr_b16 a[132:133], v229\n" \
  "ds_read_b64_tr_b16 a[134:135], v229 offset:2048\n" \
  "v_mul_f32 v20, v20, v204\n" \
  "v_mul_f32 v21, v21, v204\n" \
  "v_mfma_f32_16x16x32_bf16 v[44:47], a[68:71], v[164:167], v[44:47]\n" \
  "ds_read_b64_tr_b16 a[136:137], v230\n" \
  "v_mul_f32_e64 v22, v22, v204\n" \
  "v_mfma_f32_16x16x32_bf16 v[48:51], a[72:75], v[160:163], v[48:51]\n" \
  "ds_read_b64_tr_b16 a[138:139], v230 offset:2048\n" \
  "ds_read_b64_tr_b16 a[140:141], v231\n" \
  "s_mov_b32 s86, 0x280\n" \
  "s_add_u32 m0, s74, 0xc000\n" \
  "v_mul_f32_e64 v23, v23, v204\n" \
  "v_mfma_f32_16x16x32_bf16 v[52:55], a[72:75], v[164:167], v[52:55]\n" \
  "buffer_load_dwordx4 v236, s[52:55], s86 offen lds\n" \
  "v_mul_f32 v28, v28, v204\n" \
  "v_mul_f32 v29, v29, v204\n" \
  "v_mfma_f32_16x16x32_bf16 v[56:59], a[76:79], v[160:163], v[56:59]\n" \
  "ds_read_b64_tr_b16 a[142:143], v231 offset:2048\n" \
  "v_mul_f32_e64 v30, v30, v204\n" \
  "v_mfma_f32_16x16x32_bf16 v[60:63], a[76:79], v[164:167], v[60:63]\n" \
  "ds_read_b64_tr_b16 a[144:145], v228 offset:4096\n" \
  "v_mul_f32_e64 v31, v31, v204\n" \
  "v_mfma_f32_16x16x32_bf16 v[32:35], a[80:83], v[168:171], v[32:35]\n" \
  "ds_read_b64_tr_b16 a[146:147], v228 offset:6144\n" \
  "v_cvt_pk_bf16_f32 v20, v20, v21\n" \
  "v_cvt_pk_bf16_f32 v21, v22, v23\n" \
  "v_mfma_f32_16x16x32_bf16 v[36:39], a[80:83], v[172:175], v[36:39]\n" \
  "ds_read_b64_tr_b16 a[148:149], v229 offset:4096\n" \
  "v_cvt_pk_bf16_f32 v22, v28, v29\n" \
  "v_mfma_f32_16x16x32_bf16 v[40:43], a[84:87], v[168:171], v[40:43]\n" \
  "ds_read_b64_tr_b16 a[150:151], v229 offset:6144\n" \
  "ds_read_b64_tr_b16 a[152:153], v230 offset:4096\n" \
  "v_cvt_pk_bf16_f32 v23, v30, v31\n" \
  "buffer_store_dwordx4 v[20:23], v245, s[64:67], 0 offen offset:1024 sc1\n" \
  "v_mfma_f32_16x16x32_bf16 v[44:47], a[84:87], v[172:175], v[44:47]\n" \
  "ds_read_b64_tr_b16 a[154:155], v230 offset:6144\n" \
  "s_mov_b32 s87, 0x1c280\n" \
  "v_mfma_f32_16x16x32_bf16 v[48:51], a[88:91], v[168:171], v[48:51]\n" \
  "ds_read_b64_tr_b16 a[156:157], v231 offset:4096\n" \
  "v_mfma_f32_16x16x32_bf16 v[52:55], a[88:91], v[172:175], v[52:55]\n" \
  "buffer_load_dwordx4 v237, s[52:55], s86 offen offset:1024 lds\n" \
  "v_mfma_f32_16x16x32_bf16 v[56:59], a[92:95], v[168:171], v[56:59]\n" \
  "ds_read_b64_tr_b16 a[158:159], v231 offset:6144\n" \
  "v_mfma_f32_16x16x32_bf16 v[60:63], a[92:95], v[172:175], v[60:63]\n" \
  "ds_read_b64_tr_b16 a[160:161], v228 offset:8192\n" \
  "v_mfma_f32_16x16x32_bf16 v[32:35], a[96:99], v[176:179], v[32:35]\n" \
  "ds_read_b64_tr_b16 a[162:163], v228 offset:10240\n" \
  "ds_read_b64_tr_b16 a[164:165], v229 offset:8192\n" \
  "v_mfma_f32_16x16x32_bf16 v[36:39], a[96:99], v[180:183], v[36:39]\n" \
  "ds_read_b64_tr_b16 a[166:167], v229 offset:10240\n" \
  "v_mfma_f32_16x16x32_bf16 v[40:43], a[100:103], v[176:179], v[40:43]\n" \
  "ds_read_b64_tr_b16 a[168:169], v230 offset:8192\n" \
  "v_mfma_f32_16x16x32_bf16 v[44:47], a[100:103], v[180:183], v[44:47]\n" \
  "ds_read_b64_tr_b16 a[170:171], v230 offset:10240\n" \
  "v_mfma_f32_16x16x32_bf16 v[48:51], a[104:107], v[176:179], v[48:51]\n" \
  "ds_read_b64_tr_b16 a[172:173], v231 offset:8192\n" \
  "v_mfma_f32_16x16x32_bf16 v[52:55], a[104:107], v[180:183], v[52:55]\n" \
  "buffer_load_dwordx4 v238, s[52:55], s86 offen offset:2048 lds\n" \
  "v_mfma_f32_16x16x32_bf16 v[56:59], a[108:111], v[176:179], v[56:59]\n" \
  "ds_read_b64_tr_b16 a[174:175], v231 offset:10240\n" \
  "ds_read_b64_tr_b16 a[176:177], v228 offset:12288\n" \
  "v_mfma_f32_16x16x32_bf16 v[60:63], a[108:111], v[180:183], v[60:63]\n" \
  "ds_read_b64_tr_b16 a[178:179], v228 offset:14336\n" \
  "v_mfma_f32_16x16x32_bf16 v[32:35], a[112:115], v[184:187], v[32:35]\n" \
  "ds_read_b64_tr_b16 a[180:181], v229 offset:12288\n" \
  "v_mfma_f32_16x16x32_bf16 v[36:39], a[112:115], v[188:191], v[36:39]\n" \
  "ds_read_b64_tr_b16 a[182:183], v229 offset:14336\n" \
  "v_mfma_f32_16x16x32_bf16 v[40:43], a[116:119], v[184:187], v[40:43]\n" \
  "ds_read_b64_tr_b16 a[184:185], v230 offset:12288\n" \
  "v_mfma_f32_16x16x32_bf16 v[44:47], a[116:119], v[188:191], v[44:47]\n" \
  "ds_read_b64_tr_b16 a[186:187], v230 offset:14336\n" \
  "ds_read_b64_tr_b16 a[188:189], v231 offset:12288\n" \
  "v_mfma_f32_16x16x32_bf16 v[48:51], a[120:123], v[184:187], v[48:51]\n" \
  "ds_read_b64_tr_b16 a[190:191], v231 offset:14336\n" \
  "v_mfma_f32_16x16x32_bf16 v[52:55], a[120:123], v[188:191], v[52:55]\n" \
  "buffer_load_dwordx4 v239, s[52:55], s86 offen offset:3072 lds\n" \
  "v_mfma_f32_16x16x32_bf16 v[56:59], a[124:127], v[184:187], v[56:59]\n" \
  "ds_read_b64_tr_b16 a[64:65], v232\n" \
  "s_add_u32 m0, s75, 0xc000\n" \
  "v_mfma_f32_16x16x32_bf16 v[60:63], a[124:127], v[188:191], v[60:63]\n" \
  "ds_read_b64_tr_b16 a[66:67], v232 offset:2048\n" \
  "s_waitcnt lgkmcnt(6)\n" \
  "v_mfma_f32_16x16x32_bf16 v[0:3], a[128:131], v[128:131], 0\n" \
  "ds_read_b64_tr_b16 a[68:69], v233\n" \
  "ds_read_b64_tr_b16 a[70:71], v233 offset:2048\n" \
  "v_mfma_f32_16x16x32_bf16 v[4:7], a[128:131], v[132:135], 0\n" \
  "ds_read_b64_tr_b16 a[72:73], v234\n" \
  "v_mfma_f32_16x16x32_bf16 v[8:11], a[132:135], v[128:131], 0\n" \
  "ds_read_b64_tr_b16 a[74:75], v234 offset:2048\n" \
  "v_mfma_f32_16x16x32_bf16 v[12:15], a[132:135], v[132:135], 0\n" \
  "ds_read_b64_tr_b16 a[76:77], v235\n" \
  "v_mfma_f32_16x16x32_bf16 v[16:19], a[136:139], v[128:131], 0\n" \
  "ds_read_b64_tr_b16 a[78:79], v235 offset:2048\n" \
  "s_waitcnt lgkmcnt(7)\n" \
  "v_mfma_f32_16x16x32_bf16 v[20:23], a[136:139], v[132:135], 0\n" \
  "buffer_load_dwordx4 v236, s[52:55], s87 offen lds\n" \
  "v_mul_f32_e64 v32, v32, v200\n" \
  "v_mfma_f32_16x16x32_bf16 v[24:27], a[140:143], v[128:131], 0\n" \
  "ds_read_b64_tr_b16 a[80:81], v232 offset:4096\n" \
  "ds_read_b64_tr_b16 a[82:83], v232 offset:6144\n" \
  "v_mul_f32_e64 v33, v33, v200\n" \
  "v_mfma_f32_16x16x32_bf16 v[28:31], a[140:143], v[132:135], 0\n" \
  "ds_read_b64_tr_b16 a[84:85], v233 offset:4096\n" \
  "v_mul_f32 v34, v34, v200\n" \
  "v_mul_f32 v35, v35, v200\n" \
  "v_mfma_f32_16x16x32_bf16 v[0:3], a[144:147], v[136:139], v[0:3]\n" \
  "ds_read_b64_tr_b16 a[86:87], v233 offset:6144\n" \
  "v_mul_f32_e64 v40, v40, v200\n" \
  "v_mfma_f32_16x16x32_bf16 v[4:7], a[144:147], v[140:143], v[4:7]\n" \
  "ds_read_b64_tr_b16 a[88:89], v234 offset:4096\n" \
  "v_mul_f32 v41, v41, v200\n" \
  "v_mul_f32 v42, v42, v200\n" \
  "v_mfma_f32_16x16x32_bf16 v[8:11], a[148:151], v[136:139], v[8:11]\n" \
  "ds_read_b64_tr_b16 a[90:91], v234 offset:6144\n" \
  "v_mul_f32_e64 v43, v43, v200\n" \
  "v_mfma_f32_16x16x32_bf16 v[12:15], a[148:151], v[140:143], v[12:15]\n" \
  "ds_read_b64_tr_b16 a[92:93], v235 offset:4096\n" \
  "ds_read_b64_tr_b16 a[94:95], v235 offset:6144\n" \
  "v_cvt_pk_bf16_f32 v32, v32, v33\n" \
  "v_mfma_f32_16x16x32_bf16 v[16:19], a[152:155], v[136:139], v[16:19]\n" \
  "ds_read_b64_tr_b16 a[96:97], v232 offset:8192\n" \
  "v_cvt_pk_bf16_f32 v33, v34, v35\n" \
  "v_cvt_pk_bf16_f32 v34, v40, v41\n" \
  "v_mfma_f32_16x16x32_bf16 v[20:23], a[152:155], v[140:143], v[20:23]\n" \
  "buffer_load_dwordx4 v237, s[52:55], s87 offen offset:1024 lds\n" \
  "v_cvt_pk_bf16_f32 v35, v42, v43\n" \
  "v_mfma_f32_16x16x32_bf16 v[24:27], a[156:159], v[136:139], v[24:27]\n" \
  "buffer_store_dwordx4 v[32:35], v244, s[64:67], 0 offen offset:2048 sc1\n" \
  "v_mul_f32_e64 v48, v48, v200\n" \
  "v_mfma_f32_16x16x32_bf16 v[28:31], a[156:159], v[140:143], v[28:31]\n" \
  "ds_read_b64_tr_b16 a[98:99], v232 offset:10240\n" \
  "ds_read_b64_tr_b16 a[100:101], v233 offset:8192\n" \
  "v_mul_f32_e64 v49, v49, v200\n" \
  "v_mfma_f32_16x16x32_bf16 v[0:3], a[160:163], v[144:147], v[0:3]\n" \
  "ds_read_b64_tr_b16 a[102:103], v233 offset:10240\n" \
  "v_mul_f32 v50, v50, v200\n" \
  "v_mul_f32 v51, v51, v200\n" \
  "v_mfma_f32_16x16x32_bf16 v[4:7], a[160:163], v[148:151], v[4:7]\n" \
  "ds_read_b64_tr_b16 a[104:105], v234 offset:8192\n" \
  "ds_read_b64_tr_b16 a[106:107], v234 offset:10240\n" \
  "v_mul_f32_e64 v56, v56, v200\n" \
  "v_mfma_f32_16x16x32_bf16 v[8:11], a[164:167], v[144:147], v[8:11]\n" \
  "ds_read_b64_tr_b16 a[108:109], v235 offset:8192\n" \
  "v_mul_f32_e64 v57, v57, v200\n" \
  "v_mfma_f32_16x16x32_bf16 v[12:15], a[164:167], v[148:151], v[12:15]\n" \
  "ds_read_b64_tr_b16 a[110:111], v235 offset:10240\n" \
  "v_mul_f32 v58, v58, v200\n" \
  "v_mul_f32 v59, v59, v200\n" \
  "v_mfma_f32_16x16x32_bf16 v[16:19], a[168:171], v[144:147], v[16:19]\n" \
  "ds_read_b64_tr_b16 a[112:113], v232 offset:12288\n" \
  "v_cvt_pk_bf16_f32 v48, v48, v49\n" \
  "v_mfma_f32_16x16x32_bf16 v[20:23], a[168:171], v[148:151], v[20:23]\n" \
  "buffer_load_dwordx4 v238, s[52:55], s87 offen offset:2048 lds\n" \
  "v_cvt_pk_bf16_f32 v49, v50, v51\n" \
  "v_cvt_pk_bf16_f32 v50, v56, v57\n" \
  "v_mfma_f32_16x16x32_bf16 v[24:27], a[172:175], v[144:147], v[24:27]\n" \
  "ds_read_b64_tr_b16 a[114:115], v232 offset:14336\n" \
  "v_cvt_pk_bf16_f32 v51, v58, v59\n" \
  "v_mfma_f32_16x16x32_bf16 v[28:31], a[172:175], v[148:151], v[28:31]\n" \
  "buffer_store_dwordx4 v[48:51], v244, s[64:67], 0 offen offset:3072 sc1\n" \
  "v_add_u32 v244, 0x1000, v244\n" \
  "v_mfma_f32_16x16x32_bf16 v[0:3], a[176:179], v[152:155], v[0:3]\n" \
  "ds_read_b64_tr_b16 a[116:117], v233 offset:12288\n" \
  "ds_read_b64_tr_b16 a[118:119], v233 offset:14336\n" \
  "v_mul_f32_e64 v36, v36, v204\n" \
  "v_mfma_f32_16x16x32_bf16 v[4:7], a[176:179], v[156:159], v[4:7]\n" \
  "ds_read_b64_tr_b16 a[120:121], v234 offset:12288\n" \
  "ds_read_b64_tr_b16 a[122:123], v234 offset:14336\n" \
  "v_mul_f32_e64 v37, v37, v204\n" \
  "v_mfma_f32_16x16x32_bf16 v[8:11], a[180:183], v[152:155], v[8:11]\n" \
  "ds_read_b64_tr_b16 a[124:125], v235 offset:12288\n" \
  "v_mul_f32 v38, v38, v204\n" \
  "v_mul_f32 v39, v39, v204\n" \
  "v_mfma_f32_16x16x32_bf16 v[12:15], a[180:183], v[156:159], v[12:15]\n" \
  "ds_read_b64_tr_b16 a[126:127], v235 offset:14336\n" \
  "v_mul_f32_e64 v44, v44, v204\n" \
  "v_mfma_f32_16x16x32_bf16 v[16:19], a[184:187], v[152:155], v[16:19]\n" \
  "v_mul_f32 v45, v45, v204\n" \
  "v_mul_f32 v46, v46, v204\n" \
  "v_mfma_f32_16x16x32_bf16 v[20:23], a[184:187], v[156:159], v[20:23]\n" \
  "buffer_load_dwordx4 v239, s[52:55], s87 offen offset:3072 lds\n" \
  "v_mul_f32_e64 v47, v47, v204\n" \
  "v_mfma_f32_16x16x32_bf16 v[24:27], a[188:191], v[152:155], v[24:27]\n" \
  "v_cvt_pk_bf16_f32 v36, v36, v37\n" \
  "v_cvt_pk_bf16_f32 v37, v38, v39\n" \
  "v_mfma_f32_16x16x32_bf16 v[28:31], a[188:191], v[156:159], v[28:31]\n" \
  "v_cvt_pk_bf16_f32 v38, v44, v45\n" \
  "v_cvt_pk_bf16_f32 v39, v46, v47\n" \
  "s_waitcnt vmcnt(9) lgkmcnt(0)\n" \
  "s_barrier\n" \
  "v_add_u32_e64 v247, 1, v247\n" \
  "buffer_store_dwordx2 v[246:247], v254, s[68:71], s78 offen sc1\n" \
  "s_or_b32 s94, %3, %4\n" \
  "s_or_b32 s94, s94, %1\n" \
  "s_cbranch_scc0 L_bump_%=\n" \
  "L_bumpb_%=:\n" \
  "; HP [161, 162] ('O', 2, 1)\n" \
  "v_mfma_f32_16x16x32_bf16 v[0:3], a[64:67], v[160:163], v[0:3]\n" \
  "buffer_store_dwordx4 v[36:39], v245, s[64:67], 0 offen offset:2048 sc1\n" \
  "v_mfma_f32_16x16x32_bf16 v[4:7], a[64:67], v[164:167], v[4:7]\n" \
  "ds_read_b64_tr_b16 a[128:129], v228 offset:16384\n" \
  "ds_read_b64_tr_b16 a[130:131], v228 offset:18432\n" \
  "v_mul_f32 v52, v52, v204\n" \
  "v_mfma_f32_16x16x32_bf16 v[8:11], a[68:71], v[160:163], v[8:11]\n" \
  "ds_read_b64_tr_b16 a[132:133], v229 offset:16384\n" \
  "ds_read_b64_tr_b16 a[134:135], v229 offset:18432\n" \
  "v_mul_f32 v53, v53, v204\n" \
  "v_mul_f32 v54, v54, v204\n" \
  "v_mfma_f32_16x16x32_bf16 v[12:15], a[68:71], v[164:167], v[12:15]\n" \
  "ds_read_b64_tr_b16 a[136:137], v230 offset:16384\n" \
  "v_mul_f32_e64 v55, v55, v204\n" \
  "v_mfma_f32_16x16x32_bf16 v[16:19], a[72:75], v[160:163], v[16:19]\n" \
  "ds_read_b64_tr_b16 a[138:139], v230 offset:18432\n" \
  "ds_read_b64_tr_b16 a[140:141], v231 offset:16384\n" \
  "s_mov_b32 s86, 0x300\n" \
  "s_add_u32 m0, s74, 0\n" \
  "v_mul_f32 v60, v60, v204\n" \
  "v_mfma_f32_16x16x32_bf16 v[20:23], a[72:75], v[164:167], v[20:23]\n" \
  "buffer_load_dwordx4 v236, s[52:55], s86 offen lds\n" \
  "v_mul_f32 v61, v61, v204\n" \
  "v_mul_f32 v62, v62, v204\n" \
  "v_mfma_f32_16x16x32_bf16 v[24:27], a[76:79], v[160:163], v[24:27]\n" \
  "ds_read_b64_tr_b16 a[142:143], v231 offset:18432\n" \
  "v_mul_f32_e64 v63, v63, v204\n" \
  "v_mfma_f32_16x16x32_bf16 v[28:31], a[76:79], v[164:167], v[28:31]\n" \
  "ds_read_b64_tr_b16 a[144:145], v228 offset:20480\n" \
  "v_cvt_pk_bf16_f32 v52, v52, v53\n" \
  "v_mfma_f32_16x16x32_bf16 v[0:3], a[80:83], v[168:171], v[0:3]\n" \
  "ds_read_b64_tr_b16 a[146:147], v228 offset:22528\n" \
  "v_cvt_pk_bf16_f32 v53, v54, v55\n" \
  "v_cvt_pk_bf16_f32 v54, v60, v61\n" \
  "v_mfma_f32_16x16x32_bf16 v[4:7], a[80:83], v[172:175], v[4:7]\n" \
  "ds_read_b64_tr_b16 a[148:149], v229 offset:20480\n" \
  "v_cvt_pk_bf16_f32 v55, v62, v63\n" \
  "v_mfma_f32_16x16x32_bf16 v[8:11], a[84:87], v[168:171], v[8:11]\n" \
  "buffer_store_dwordx4 v[52:55], v245, s[64:67], 0 offen offset:3072 sc1\n" \
  "v_add_u32 v245, 0x1000, v245\n" \
  "v_mfma_f32_16x16x32_bf16 v[12:15], a[84:87], v[172:175], v[12:15]\n" \
  "ds_read_b64_tr_b16 a[150:151], v229 offset:22528\n" \
  "ds_read_b64_tr_b16 a[152:153], v230 offset:20480\n" \
  "v_mfma_f32_16x16x32_bf16 v[16:19], a[88:91], v[168:171], v[16:19]\n" \
  "ds_read_b64_tr_b16 a[154:155], v230 offset:22528\n" \
  "ds_read_b64_tr_b16 a[156:157], v231 offset:20480\n" \
  "v_mfma_f32_16x16x32_bf16 v[20:23], a[88:91], v[172:175], v[20:23]\n" \
  "buffer_load_dwordx4 v237, s[52:55], s86 offen offset:1024 lds\n" \
  "v_mfma_f32_16x16x32_bf16 v[24:27], a[92:95], v[168:171], v[24:27]\n" \
  "ds_read_b64_tr_b16 a[158:159], v231 offset:22528\n" \
  "s_mov_b32 s87, 0x1c300\n" \
  "v_mfma_f32_16x16x32_bf16 v[28:31], a[92:95], v[172:175], v[28:31]\n" \
  "ds_read_b64_tr_b16 a[160:161], v228 offset:24576\n" \
  "v_mfma_f32_16x16x32_bf16 v[0:3], a[96:99], v[176:179], v[0:3]\n" \
  "ds_read_b64_tr_b16 a[162:163], v228 offset:26624\n" \
  "ds_read_b64_tr_b16 a[164:165], v229 offset:24576\n" \
  "v_mfma_f32_16x16x32_bf16 v[4:7], a[96:99], v[180:183], v[4:7]\n" \
  "ds_read_b64_tr_b16 a[166:167], v229 offset:26624\n" \
  "v_mfma_f32_16x16x32_bf16 v[8:11], a[100:103], v[176:179], v[8:11]\n" \
  "ds_read_b64_tr_b16 a[168:169], v230 offset:24576\n" \
  "v_mfma_f32_16x16x32_bf16 v[12:15], a[100:103], v[180:183], v[12:15]\n" \
  "ds_read_b64_tr_b16 a[170:171], v230 offset:26624\n" \
  "v_mfma_f32_16x16x32_bf16 v[16:19], a[104:107], v[176:179], v[16:19]\n" \
  "ds_read_b64_tr_b16 a[172:173], v231 offset:24576\n" \
  "v_mfma_f32_16x16x32_bf16 v[20:23], a[104:107], v[180:183], v[20:23]\n" \
  "buffer_load_dwordx4 v238, s[52:55], s86 offen offset:2048 lds\n" \
  "v_mfma_f32_16x16x32_bf16 v[24:27], a[108:111], v[176:179], v[24:27]\n" \
  "ds_read_b64_tr_b16 a[174:175], v231 offset:26624\n" \
  "ds_read_b64_tr_b16 a[176:177], v228 offset:28672\n" \
  "v_mfma_f32_16x16x32_bf16 v[28:31], a[108:111], v[180:183], v[28:31]\n" \
  "ds_read_b64_tr_b16 a[178:179], v228 offset:30720\n" \
  "v_mfma_f32_16x16x32_bf16 v[0:3], a[112:115], v[184:187], v[0:3]\n" \
  "ds_read_b64_tr_b16 a[180:181], v229 offset:28672\n" \
  "v_mfma_f32_16x16x32_bf16 v[4:7], a[112:115], v[188:191], v[4:7]\n" \
  "ds_read_b64_tr_b16 a[182:183], v229 offset:30720\n" \
  "v_mfma_f32_16x16x32_bf16 v[8:11], a[116:119], v[184:187], v[8:11]\n" \
  "ds_read_b64_tr_b16 a[184:185], v230 offset:28672\n" \
  "v_mfma_f32_16x16x32_bf16 v[12:15], a[116:119], v[188:191], v[12:15]\n" \
  "ds_read_b64_tr_b16 a[186:187], v230 offset:30720\n" \
  "ds_read_b64_tr_b16 a[188:189], v231 offset:28672\n" \
  "v_mfma_f32_16x16x32_bf16 v[16:19], a[120:123], v[184:187], v[16:19]\n" \
  "ds_read_b64_tr_b16 a[190:191], v231 offset:30720\n" \
  "v_mfma_f32_16x16x32_bf16 v[20:23], a[120:123], v[188:191], v[20:23]\n" \
  "buffer_load_dwordx4 v239, s[52:55], s86 offen offset:3072 lds\n" \
  "v_mfma_f32_16x16x32_bf16 v[24:27], a[124:127], v[184:187], v[24:27]\n" \
  "ds_read_b64_tr_b16 a[64:65], v232 offset:16384\n" \
  "s_add_u32 m0, s75, 0\n" \
  "v_mfma_f32_16x16x32_bf16 v[28:31], a[124:127], v[188:191], v[28:31]\n" \
  "ds_read_b64_tr_b16 a[66:67], v232 offset:18432\n" \
  "s_waitcnt lgkmcnt(6)\n" \
  "v_mfma_f32_16x16x32_bf16 v[32:35], a[128:131], v[128:131], 0\n" \
  "ds_read_b64_tr_b16 a[68:69], v233 offset:16384\n" \
  "ds_read_b64_tr_b16 a[70:71], v233 offset:18432\n" \
  "v_mfma_f32_16x16x32_bf16 v[36:39], a[128:131], v[132:135], 0\n" \
  "ds_read_b64_tr_b16 a[72:73], v234 offset:16384\n" \
  "v_mfma_f32_16x16x32_bf16 v[40:43], a[132:135], v[128:131], 0\n" \
  "ds_read_b64_tr_b16 a[74:75], v234 offset:18432\n" \
  "v_mfma_f32_16x16x32_bf16 v[44:47], a[132:135], v[132:135], 0\n" \
  "ds_read_b64_tr_b16 a[76:77], v235 offset:16384\n" \
  "v_mfma_f32_16x16x32_bf16 v[48:51], a[136:139], v[128:131], 0\n" \
  "ds_read_b64_tr_b16 a[78:79], v235 offset:18432\n" \
  "s_waitcnt lgkmcnt(7)\n" \
  "v_mfma_f32_16x16x32_bf16 v[52:55], a[136:139], v[132:135], 0\n" \
  "buffer_load_dwordx4 v236, s[52:55], s87 offen lds\n" \
  "v_mul_f32 v0, v0, v200\n" \
  "v_mfma_f32_16x16x32_bf16 v[56:59], a[140:143], v[128:131], 0\n" \
  "ds_read_b64_tr_b16 a[80:81], v232 offset:20480\n" \
  "ds_read_b64_tr_b16 a[82:83], v232 offset:22528\n" \
  "v_mul_f32_e64 v1, v1, v200\n" \
  "v_mfma_f32_16x16x32_bf16 v[60:63], a[140:143], v[132:135], 0\n" \
  "ds_read_b64_tr_b16 a[84:85], v233 offset:20480\n" \
  "v_mul_f32 v2, v2, v200\n" \
  "v_mul_f32 v3, v3, v200\n" \
  "v_mfma_f32_16x16x32_bf16 v[32:35], a[144:147], v[136:139], v[32:35]\n" \
  "ds_read_b64_tr_b16 a[86:87], v233 offset:22528\n" \
  "v_mul_f32_e64 v8, v8, v200\n" \
  "v_mfma_f32_16x16x32_bf16 v[36:39], a[144:147], v[140:143], v[36:39]\n" \
  "ds_read_b64_tr_b16 a[88:89], v234 offset:20480\n" \
  "v_mul_f32_e64 v9, v9, v200\n" \
  "v_mfma_f32_16x16x32_bf16 v[40:43], a[148:151], v[136:139], v[40:43]\n" \
  "ds_read_b64_tr_b16 a[90:91], v234 offset:22528\n" \
  "v_mul_f32 v10, v10, v200\n" \
  "v_mul_f32 v11, v11, v200\n" \
  "v_mfma_f32_16x16x32_bf16 v[44:47], a[148:151], v[140:143], v[44:47]\n" \
  "ds_read_b64_tr_b16 a[92:93], v235 offset:20480\n" \
  "ds_read_b64_tr_b16 a[94:95], v235 offset:22528\n" \
  "v_cvt_pk_bf16_f32 v0, v0, v1\n" \
  "v_mfma_f32_16x16x32_bf16 v[48:51], a[152:155], v[136:139], v[48:51]\n" \
  "ds_read_b64_tr_b16 a[96:97], v232 offset:24576\n" \
  "v_cvt_pk_bf16_f32 v1, v2, v3\n" \
  "v_mfma_f32_16x16x32_bf16 v[52:55], a[152:155], v[140:143], v[52:55]\n" \
  "buffer_load_dwordx4 v237, s[52:55], s87 offen offset:1024 lds\n" \
  "v_cvt_pk_bf16_f32 v2, v8, v9\n" \
  "v_cvt_pk_bf16_f32 v3, v10, v11\n" \
  "v_mfma_f32_16x16x32_bf16 v[56:59], a[156:159], v[136:139], v[56:59]\n" \
  "buffer_store_dwordx4 v[0:3], v244, s[64:67], 0 offen sc1\n" \
  "v_mfma_f32_16x16x32_bf16 v[60:63], a[156:159], v[140:143], v[60:63]\n" \
  "ds_read_b64_tr_b16 a[98:99], v232 offset:26624\n" \
  "ds_read_b64_tr_b16 a[100:101], v233 offset:24576\n" \
  "v_mul_f32 v16, v16, v200\n" \
  "v_mul_f32 v17, v17, v200\n" \
  "v_mfma_f32_16x16x32_bf16 v[32:35], a[160:163], v[144:147], v[32:35]\n" \
  "ds_read_b64_tr_b16 a[102:103], v233 offset:26624\n" \
  "v_mul_f32_e64 v18, v18, v200\n" \
  "v_mfma_f32_16x16x32_bf16 v[36:39], a[160:163], v[148:151], v[36:39]\n" \
  "ds_read_b64_tr_b16 a[104:105], v234 offset:24576\n" \
  "ds_read_b64_tr_b16 a[106:107], v234 offset:26624\n" \
  "v_mul_f32_e64 v19, v19, v200\n" \
  "v_mfma_f32_16x16x32_bf16 v[40:43], a[164:167], v[144:147], v[40:43]\n" \
  "ds_read_b64_tr_b16 a[108:109], v235 offset:24576\n" \
  "v_mul_f32 v24, v24, v200\n" \
  "v_mul_f32 v25, v25, v200\n" \
  "v_mfma_f32_16x16x32_bf16 v[44:47], a[164:167], v[148:151], v[44:47]\n" \
  "ds_read_b64_tr_b16 a[110:111], v235 offset:26624\n" \
  "v_mul_f32_e64 v26, v26, v200\n" \
  "v_mfma_f32_16x16x32_bf16 v[48:51], a[168:171], v[144:147], v[48:51]\n" \
  "ds_read_b64_tr_b16 a[112:113], v232 offset:28672\n" \
  "v_mul_f32_e64 v27, v27, v200\n" \
  "v_mfma_f32_16x16x32_bf16 v[52:55], a[168:171], v[148:151], v[52:55]\n" \
  "buffer_load_dwordx4 v238, s[52:55], s87 offen offset:2048 lds\n" \
  "v_cvt_pk_bf16_f32 v16, v16, v17\n" \
  "v_cvt_pk_bf16_f32 v17, v18, v19\n" \
  "v_mfma_f32_16x16x32_bf16 v[56:59], a[172:175], v[144:147], v[56:59]\n" \
  "ds_read_b64_tr_b16 a[114:115], v232 offset:30720\n" \
  "v_cvt_pk_bf16_f32 v18, v24, v25\n" \
  "v_mfma_f32_16x16x32_bf16 v[60:63], a[172:175], v[148:151], v[60:63]\n" \
  "ds_read_b64_tr_b16 a[116:117], v233 offset:28672\n" \
  "ds_read_b64_tr_b16 a[118:119], v233 offset:30720\n" \
  "v_cvt_pk_bf16_f32 v19, v26, v27\n" \
  "v_mul_f32_e64 v4, v4, v204\n" \
  "v_mfma_f32_16x16x32_bf16 v[32:35], a[176:179], v[152:155], v[32:35]\n" \
  "buffer_store_dwordx4 v[16:19], v244, s[64:67], 0 offen offset:1024 sc1\n" \
  "v_mfma_f32_16x16x32_bf16 v[36:39], a[176:179], v[156:159], v[36:39]\n" \
  "ds_read_b64_tr_b16 a[120:121], v234 offset:28672\n" \
  "ds_read_b64_tr_b16 a[122:123], v234 offset:30720\n" \
  "v_mul_f32_e64 v5, v5, v204\n" \
  "v_mfma_f32_16x16x32_bf16 v[40:43], a[180:183], v[152:155], v[40:43]\n" \
  "ds_read_b64_tr_b16 a[124:125], v235 offset:28672\n" \
  "v_mul_f32 v6, v6, v204\n" \
  "v_mul_f32 v7, v7, v204\n" \
  "v_mfma_f32_16x16x32_bf16 v[44:47], a[180:183], v[156:159], v[44:47]\n" \
  "ds_read_b64_tr_b16 a[126:127], v235 offset:30720\n" \
  "v_mul_f32_e64 v12, v12, v204\n" \
  "v_mfma_f32_16x16x32_bf16 v[48:51], a[184:187], v[152:155], v[48:51]\n" \
  "v_mul_f32 v13, v13, v204\n" \
  "v_mul_f32 v14, v14, v204\n" \
  "v_mfma_f32_16x16x32_bf16 v[52:55], a[184:187], v[156:159], v[52:55]\n" \
  "buffer_load_dwordx4 v239, s[52:55], s87 offen offset:3072 lds\n" \
  "v_mul_f32_e64 v15, v15, v204\n" \
  "v_mfma_f32_16x16x32_bf16 v[56:59], a[188:191], v[152:155], v[56:59]\n" \
  "v_cvt_pk_bf16_f32 v4, v4, v5\n" \
  "v_cvt_pk_bf16_f32 v5, v6, v7\n" \
  "v_mfma_f32_16x16x32_bf16 v[60:63], a[188:191], v[156:159], v[60:63]\n" \
  "v_cvt_pk_bf16_f32 v6, v12, v13\n" \
  "v_cvt_pk_bf16_f32 v7, v14, v15\n" \
  "s_waitcnt vmcnt(9) lgkmcnt(0)\n" \
  "s_barrier\n" \
  "v_add_u32_e64 v247, 1, v247\n" \
  "buffer_store_dwordx2 v[246:247], v254, s[68:71], s78 offen sc1\n" \
  "; HP [163, 164] ('O', 3, 1)\n" \
  "v_mfma_f32_16x16x32_bf16 v[32:35], a[64:67], v[160:163], v[32:35]\n" \
  "buffer_store_dwordx4 v[4:7], v245, s[64:67], 0 offen sc1\n" \
  "v_mfma_f32_16x16x32_bf16 v[36:39], a[64:67], v[164:167], v[36:39]\n" \
  "ds_read_b64_tr_b16 a[128:129], v228 offset:32768\n" \
  "ds_read_b64_tr_b16 a[130:131], v228 offset:34816\n" \
  "v_mul_f32_e64 v20, v20, v204\n" \
  "v_mfma_f32_16x16x32_bf16 v[40:43], a[68:71], v[160:163], v[40:43]\n" \
  "ds_read_b64_tr_b16 a[132:133], v229 offset:32768\n" \
  "ds_read_b64_tr_b16 a[134:135], v229 offset:34816\n" \
  "v_mul_f32_e64 v21, v21, v204\n" \
  "v_mfma_f32_16x16x32_bf16 v[44:47], a[68:71], v[164:167], v[44:47]\n" \
  "ds_read_b64_tr_b16 a[136:137], v230 offset:32768\n" \
  "v_mul_f32 v22, v22, v204\n" \
  "v_mul_f32 v23, v23, v204\n" \
  "v_mfma_f32_16x16x32_bf16 v[48:51], a[72:75], v[160:163], v[48:51]\n" \
  "ds_read_b64_tr_b16 a[138:139], v230 offset:34816\n" \
  "ds_read_b64_tr_b16 a[140:141], v231 offset:32768\n" \
  "s_mov_b32 s86, 0\n" \
  "s_add_u32 m0, s74, 0x4000\n" \
  "v_mul_f32 v28, v28, v204\n" \
  "v_mfma_f32_16x16x32_bf16 v[52:55], a[72:75], v[164:167], v[52:55]\n" \
  "buffer_load_dwordx4 v236, s[36:39], s86 offen lds\n" \
  "v_mul_f32_e64 v29, v29, v204\n" \
  "v_mfma_f32_16x16x32_bf16 v[56:59], a[76:79], v[160:163], v[56:59]\n" \
  "ds_read_b64_tr_b16 a[142:143], v231 offset:34816\n" \
  "v_mul_f32_e64 v30, v30, v204\n" \
  "v_mfma_f32_16x16x32_bf16 v[60:63], a[76:79], v[164:167], v[60:63]\n" \
  "ds_read_b64_tr_b16 a[144:145], v228 offset:36864\n" \
  "v_mul_f32_e64 v31, v31, v204\n" \
  "v_cvt_pk_bf16_f32 v20, v20, v21\n" \
  "v_mfma_f32_16x16x32_bf16 v[32:35], a[80:83], v[168:171], v[32:35]\n" \
  "ds_read_b64_tr_b16 a[146:147], v228 offset:38912\n" \
  "v_cvt_pk_bf16_f32 v21, v22, v23\n" \
  "v_mfma_f32_16x16x32_bf16 v[36:39], a[80:83], v[172:175], v[36:39]\n" \
  "ds_read_b64_tr_b16 a[148:149], v229 offset:36864\n" \
  "v_cvt_pk_bf16_f32 v22, v28, v29\n" \
  "v_mfma_f32_16x16x32_bf16 v[40:43], a[84:87], v[168:171], v[40:43]\n" \
  "ds_read_b64_tr_b16 a[150:151], v229 offset:38912\n" \
  "ds_read_b64_tr_b16 a[152:153], v230 offset:36864\n" \
  "v_cvt_pk_bf16_f32 v23, v30, v31\n" \
  "buffer_store_dwordx4 v[20:23], v245, s[64:67], 0 offen offset:1024 sc1\n" \
  "v_mfma_f32_16x16x32_bf16 v[44:47], a[84:87], v[172:175], v[44:47]\n" \
  "ds_read_b64_tr_b16 a[154:155], v230 offset:38912\n" \
  "s_mov_b32 s87, 0x1c000\n" \
  "v_mfma_f32_16x16x32_bf16 v[48:51], a[88:91], v[168:171], v[48:51]\n" \
  "ds_read_b64_tr_b16 a[156:157], v231 offset:36864\n" \
  "v_mfma_f32_16x16x32_bf16 v[52:55], a[88:91], v[172:175], v[52:55]\n" \
  "buffer_load_dwordx4 v237, s[36:39], s86 offen offset:1024 lds\n" \
  "v_mfma_f32_16x16x32_bf16 v[56:59], a[92:95], v[168:171], v[56:59]\n" \
  "ds_read_b64_tr_b16 a[158:159], v231 offset:38912\n" \
  "v_mfma_f32_16x16x32_bf16 v[60:63], a[92:95], v[172:175], v[60:63]\n" \
  "ds_read_b64_tr_b16 a[160:161], v228 offset:40960\n" \
  "v_mfma_f32_16x16x32_bf16 v[32:35], a[96:99], v[176:179], v[32:35]\n" \
  "ds_read_b64_tr_b16 a[162:163], v228 offset:43008\n" \
  "ds_read_b64_tr_b16 a[164:165], v229 offset:40960\n" \
  "v_mfma_f32_16x16x32_bf16 v[36:39], a[96:99], v[180:183], v[36:39]\n" \
  "ds_read_b64_tr_b16 a[166:167], v229 offset:43008\n" \
  "v_mfma_f32_16x16x32_bf16 v[40:43], a[100:103], v[176:179], v[40:43]\n" \
  "ds_read_b64_tr_b16 a[168:169], v230 offset:40960\n" \
  "v_mfma_f32_16x16x32_bf16 v[44:47], a[100:103], v[180:183], v[44:47]\n" \
  "ds_read_b64_tr_b16 a[170:171], v230 offset:43008\n" \
  "v_mfma_f32_16x16x32_bf16 v[48:51], a[104:107], v[176:179], v[48:51]\n" \
  "ds_read_b64_tr_b16 a[172:173], v231 offset:40960\n" \
  "v_mfma_f32_16x16x32_bf16 v[52:55], a[104:107], v[180:183], v[52:55]\n" \
  "buffer_load_dwordx4 v238, s[36:39], s86 offen offset:2048 lds\n" \
  "v_mfma_f32_16x16x32_bf16 v[56:59], a[108:111], v[176:179], v[56:59]\n" \
  "ds_read_b64_tr_b16 a[174:175], v231 offset:43008\n" \
  "ds_read_b64_tr_b16 a[176:177], v228 offset:45056\n" \
  "v_mfma_f32_16x16x32_bf16 v[60:63], a[108:111], v[180:183], v[60:63]\n" \
  "ds_read_b64_tr_b16 a[178:179], v228 offset:47104\n" \
  "v_mfma_f32_16x16x32_bf16 v[32:35], a[112:115], v[184:187], v[32:35]\n" \
  "ds_read_b64_tr_b16 a[180:181], v229 offset:45056\n" \
  "v_mfma_f32_16x16x32_bf16 v[36:39], a[112:115], v[188:191], v[36:39]\n" \
  "ds_read_b64_tr_b16 a[182:183], v229 offset:47104\n" \
  "v_mfma_f32_16x16x32_bf16 v[40:43], a[116:119], v[184:187], v[40:43]\n" \
  "ds_read_b64_tr_b16 a[184:185], v230 offset:45056\n" \
  "v_mfma_f32_16x16x32_bf16 v[44:47], a[116:119], v[188:191], v[44:47]\n" \
  "ds_read_b64_tr_b16 a[186:187], v230 offset:47104\n" \
  "ds_read_b64_tr_b16 a[188:189], v231 offset:45056\n" \
  "v_mfma_f32_16x16x32_bf16 v[48:51], a[120:123], v[184:187], v[48:51]\n" \
  "ds_read_b64_tr_b16 a[190:191], v231 offset:47104\n" \
  "v_mfma_f32_16x16x32_bf16 v[52:55], a[120:123], v[188:191], v[52:55]\n" \
  "buffer_load_dwordx4 v239, s[36:39], s86 offen offset:3072 lds\n" \
  "v_mfma_f32_16x16x32_bf16 v[56:59], a[124:127], v[184:187], v[56:59]\n" \
  "ds_read_b64_tr_b16 a[64:65], v232 offset:32768\n" \
  "s_add_u32 m0, s75, 0x4000\n" \
  "v_mfma_f32_16x16x32_bf16 v[60:63], a[124:127], v[188:191], v[60:63]\n" \
  "ds_read_b64_tr_b16 a[66:67], v232 offset:34816\n" \
  "s_waitcnt lgkmcnt(6)\n" \
  "v_mfma_f32_16x16x32_bf16 v[0:3], a[128:131], v[128:131], 0\n" \
  "ds_read_b64_tr_b16 a[68:69], v233 offset:32768\n" \
  "ds_read_b64_tr_b16 a[70:71], v233 offset:34816\n" \
  "v_mfma_f32_16x16x32_bf16 v[4:7], a[128:131], v[132:135], 0\n" \
  "ds_read_b64_tr_b16 a[72:73], v234 offset:32768\n" \
  "v_mfma_f32_16x16x32_bf16 v[8:11], a[132:135], v[128:131], 0\n" \
  "ds_read_b64_tr_b16 a[74:75], v234 offset:34816\n" \
  "v_mfma_f32_16x16x32_bf16 v[12:15], a[132:135], v[132:135], 0\n" \
  "ds_read_b64_tr_b16 a[76:77], v235 offset:32768\n" \
  "v_mfma_f32_16x16x32_bf16 v[16:19], a[136:139], v[128:131], 0\n" \
  "ds_read_b64_tr_b16 a[78:79], v235 offset:34816\n" \
  "s_waitcnt lgkmcnt(7)\n" \
  "v_mfma_f32_16x16x32_bf16 v[20:23], a[136:139], v[132:135], 0\n" \
  "buffer_load_dwordx4 v236, s[36:39], s87 offen lds\n" \
  "v_mul_f32_e64 v32, v32, v200\n" \
  "v_mfma_f32_16x16x32_bf16 v[24:27], a[140:143], v[128:131], 0\n" \
  "ds_read_b64_tr_b16 a[80:81], v232 offset:36864\n" \
  "ds_read_b64_tr_b16 a[82:83], v232 offset:38912\n" \
  "v_mul_f32_e64 v33, v33, v200\n" \
  "v_mfma_f32_16x16x32_bf16 v[28:31], a[140:143], v[132:135], 0\n" \
  "ds_read_b64_tr_b16 a[84:85], v233 offset:36864\n" \
  "v_mul_f32 v34, v34, v200\n" \
  "v_mul_f32 v35, v35, v200\n" \
  "v_mfma_f32_16x16x32_bf16 v[0:3], a[144:147], v[136:139], v[0:3]\n" \
  "ds_read_b64_tr_b16 a[86:87], v233 offset:38912\n" \
  "v_mul_f32_e64 v40, v40, v200\n" \
  "v_mfma_f32_16x16x32_bf16 v[4:7], a[144:147], v[140:143], v[4:7]\n" \
  "ds_read_b64_tr_b16 a[88:89], v234 offset:36864\n" \
  "v_mul_f32 v41, v41, v200\n" \
  "v_mul_f32 v42, v42, v200\n" \
  "v_mfma_f32_16x16x32_bf16 v[8:11], a[148:151], v[136:139], v[8:11]\n" \
  "ds_read_b64_tr_b16 a[90:91], v234 offset:38912\n" \
  "v_mul_f32_e64 v43, v43, v200\n" \
  "v_mfma_f32_16x16x32_bf16 v[12:15], a[148:151], v[140:143], v[12:15]\n" \
  "ds_read_b64_tr_b16 a[92:93], v235 offset:36864\n" \
  "ds_read_b64_tr_b16 a[94:95], v235 offset:38912\n" \
  "v_cvt_pk_bf16_f32 v32, v32, v33\n" \
  "v_mfma_f32_16x16x32_bf16 v[16:19], a[152:155], v[136:139], v[16:19]\n" \
  "ds_read_b64_tr_b16 a[96:97], v232 offset:40960\n" \
  "v_cvt_pk_bf16_f32 v33, v34, v35\n" \
  "v_cvt_pk_bf16_f32 v34, v40, v41\n" \
  "v_mfma_f32_16x16x32_bf16 v[20:23], a[152:155], v[140:143], v[20:23]\n" \
  "buffer_load_dwordx4 v237, s[36:39], s87 offen offset:1024 lds\n" \
  "v_cvt_pk_bf16_f32 v35, v42, v43\n" \
  "v_mfma_f32_16x16x32_bf16 v[24:27], a[156:159], v[136:139], v[24:27]\n" \
  "buffer_store_dwordx4 v[32:35], v244, s[64:67], 0 offen offset:2048 sc1\n" \
  "v_mul_f32_e64 v48, v48, v200\n" \
  "v_mfma_f32_16x16x32_bf16 v[28:31], a[156:159], v[140:143], v[28:31]\n" \
  "ds_read_b64_tr_b16 a[98:99], v232 offset:43008\n" \
  "ds_read_b64_tr_b16 a[100:101], v233 offset:40960\n" \
  "v_mul_f32_e64 v49, v49, v200\n" \
  "v_mfma_f32_16x16x32_bf16 v[0:3], a[160:163], v[144:147], v[0:3]\n" \
  "ds_read_b64_tr_b16 a[102:103], v233 offset:43008\n" \
  "v_mul_f32 v50, v50, v200\n" \
  "v_mul_f32 v51, v51, v200\n" \
  "v_mfma_f32_16x16x32_bf16 v[4:7], a[160:163], v[148:151], v[4:7]\n" \
  "ds_read_b64_tr_b16 a[104:105], v234 offset:40960\n" \
  "ds_read_b64_tr_b16 a[106:107], v234 offset:43008\n" \
  "v_mul_f32_e64 v56, v56, v200\n" \
  "v_mfma_f32_16x16x32_bf16 v[8:11], a[164:167], v[144:147], v[8:11]\n" \
  "ds_read_b64_tr_b16 a[108:109], v235 offset:40960\n" \
  "v_mul_f32_e64 v57, v57, v200\n" \
  "v_mfma_f32_16x16x32_bf16 v[12:15], a[164:167], v[148:151], v[12:15]\n" \
  "ds_read_b64_tr_b16 a[110:111], v235 offset:43008\n" \
  "v_mul_f32 v58, v58, v200\n" \
  "v_mul_f32 v59, v59, v200\n" \
  "v_mfma_f32_16x16x32_bf16 v[16:19], a[168:171], v[144:147], v[16:19]\n" \
  "ds_read_b64_tr_b16 a[112:113], v232 offset:45056\n" \
  "v_cvt_pk_bf16_f32 v48, v48, v49\n" \
  "v_mfma_f32_16x16x32_bf16 v[20:23], a[168:171], v[148:151], v[20:23]\n" \
  "buffer_load_dwordx4 v238, s[36:39], s87 offen offset:2048 lds\n" \
  "v_cvt_pk_bf16_f32 v49, v50, v51\n" \
  "v_cvt_pk_bf16_f32 v50, v56, v57\n" \
  "v_mfma_f32_16x16x32_bf16 v[24:27], a[172:175], v[144:147], v[24:27]\n" \
  "ds_read_b64_tr_b16 a[114:115], v232 offset:47104\n" \
  "v_cvt_pk_bf16_f32 v51, v58, v59\n" \
  "v_mfma_f32_16x16x32_bf16 v[28:31], a[172:175], v[148:151], v[28:31]\n" \
  "buffer_store_dwordx4 v[48:51], v244, s[64:67], 0 offen offset:3072 sc1\n" \
  "v_add_u32 v244, 0x1000, v244\n" \
  "v_mfma_f32_16x16x32_bf16 v[0:3], a[176:179], v[152:155], v[0:3]\n" \
  "ds_read_b64_tr_b16 a[116:117], v233 offset:45056\n" \
  "ds_read_b64_tr_b16 a[118:119], v233 offset:47104\n" \
  "v_mul_f32_e64 v36, v36, v204\n" \
  "v_mfma_f32_16x16x32_bf16 v[4:7], a[176:179], v[156:159], v[4:7]\n" \
  "ds_read_b64_tr_b16 a[120:121], v234 offset:45056\n" \
  "ds_read_b64_tr_b16 a[122:123], v234 offset:47104\n" \
  "v_mul_f32_e64 v37, v37, v204\n" \
  "v_mfma_f32_16x16x32_bf16 v[8:11], a[180:183], v[152:155], v[8:11]\n" \
  "ds_read_b64_tr_b16 a[124:125], v235 offset:45056\n" \
  "v_mul_f32 v38, v38, v204\n" \
  "v_mul_f32 v39, v39, v204\n" \
  "v_mfma_f32_16x16x32_bf16 v[12:15], a[180:183], v[156:159], v[12:15]\n" \
  "ds_read_b64_tr_b16 a[126:127], v235 offset:47104\n" \
  "v_mul_f32_e64 v44, v44, v204\n" \
  "v_mfma_f32_16x16x32_bf16 v[16:19], a[184:187], v[152:155], v[16:19]\n" \
  "v_mul_f32 v45, v45, v204\n" \
  "v_mul_f32 v46, v46, v204\n" \
  "v_mfma_f32_16x16x32_bf16 v[20:23], a[184:187], v[156:159], v[20:23]\n" \
  "buffer_load_dwordx4 v239, s[36:39], s87 offen offset:3072 lds\n" \
  "v_mul_f32_e64 v47, v47, v204\n" \
  "v_mfma_f32_16x16x32_bf16 v[24:27], a[188:191], v[152:155], v[24:27]\n" \
  "v_cvt_pk_bf16_f32 v36, v36, v37\n" \
  "v_cvt_pk_bf16_f32 v37, v38, v39\n" \
  "v_mfma_f32_16x16x32_bf16 v[28:31], a[188:191], v[156:159], v[28:31]\n" \
  "v_cvt_pk_bf16_f32 v38, v44, v45\n" \
  "v_cvt_pk_bf16_f32 v39, v46, v47\n" \
  "s_waitcnt vmcnt(9) lgkmcnt(0)\n" \
  "s_barrier\n" \
  "v_add_u32_e64 v247, 1, v247\n" \
  "buffer_store_dwordx2 v[246:247], v254, s[68:71], s78 offen sc1\n" \
  "; HP [165, 166] ('O', 4, 1)\n" \
  "v_mfma_f32_16x16x32_bf16 v[0:3], a[64:67], v[160:163], v[0:3]\n" \
  "buffer_store_dwordx4 v[36:39], v245, s[64:67], 0 offen offset:2048 sc1\n" \
  "v_mfma_f32_16x16x32_bf16 v[4:7], a[64:67], v[164:167], v[4:7]\n" \
  "ds_read_b64_tr_b16 a[128:129], v228 offset:49152\n" \
  "ds_read_b64_tr_b16 a[130:131], v228 offset:51200\n" \
  "v_mul_f32_e64 v52, v52, v204\n" \
  "v_mfma_f32_16x16x32_bf16 v[8:11], a[68:71], v[160:163], v[8:11]\n" \
  "ds_read_b64_tr_b16 a[132:133], v229 offset:49152\n" \
  "ds_read_b64_tr_b16 a[134:135], v229 offset:51200\n" \
  "v_mul_f32 v53, v53, v204\n" \
  "v_mul_f32 v54, v54, v204\n" \
  "v_mfma_f32_16x16x32_bf16 v[12:15], a[68:71], v[164:167], v[12:15]\n" \
  "ds_read_b64_tr_b16 a[136:137], v230 offset:49152\n" \
  "v_mul_f32_e64 v55, v55, v204\n" \
  "v_mfma_f32_16x16x32_bf16 v[16:19], a[72:75], v[160:163], v[16:19]\n" \
  "ds_read_b64_tr_b16 a[138:139], v230 offset:51200\n" \
  "ds_read_b64_tr_b16 a[140:141], v231 offset:49152\n" \
  "s_mov_b32 s86, 0x38000\n" \
  "s_add_u32 m0, s74, 0x8000\n" \
  "v_mul_f32_e64 v60, v60, v204\n" \
  "v_mfma_f32_16x16x32_bf16 v[20:23], a[72:75], v[164:167], v[20:23]\n" \
  "buffer_load_dwordx4 v236, s[36:39], s86 offen lds\n" \
  "v_mul_f32 v61, v61, v204\n" \
  "v_mul_f32 v62, v62, v204\n" \
  "v_mfma_f32_16x16x32_bf16 v[24:27], a[76:79], v[160:163], v[24:27]\n" \
  "ds_read_b64_tr_b16 a[142:143], v231 offset:51200\n" \
  "v_mul_f32_e64 v63, v63, v204\n" \
  "v_mfma_f32_16x16x32_bf16 v[28:31], a[76:79], v[164:167], v[28:31]\n" \
  "ds_read_b64_tr_b16 a[144:145], v228 offset:53248\n" \
  "v_cvt_pk_bf16_f32 v52, v52, v53\n" \
  "v_mfma_f32_16x16x32_bf16 v[0:3], a[80:83], v[168:171], v[0:3]\n" \
  "ds_read_b64_tr_b16 a[146:147], v228 offset:55296\n" \
  "v_cvt_pk_bf16_f32 v53, v54, v55\n" \
  "v_cvt_pk_bf16_f32 v54, v60, v61\n" \
  "v_mfma_f32_16x16x32_bf16 v[4:7], a[80:83], v[172:175], v[4:7]\n" \
  "ds_read_b64_tr_b16 a[148:149], v229 offset:53248\n" \
  "v_cvt_pk_bf16_f32 v55, v62, v63\n" \
  "v_mfma_f32_16x16x32_bf16 v[8:11], a[84:87], v[168:171], v[8:11]\n" \
  "buffer_store_dwordx4 v[52:55], v245, s[64:67], 0 offen offset:3072 sc1\n" \
  "v_add_u32 v245, 0x1000, v245\n" \
  "v_mfma_f32_16x16x32_bf16 v[12:15], a[84:87], v[172:175], v[12:15]\n" \
  "ds_read_b64_tr_b16 a[150:151], v229 offset:55296\n" \
  "ds_read_b64_tr_b16 a[152:153], v230 offset:53248\n" \
  "v_mfma_f32_16x16x32_bf16 v[16:19], a[88:91], v[168:171], v[16:19]\n" \
  "ds_read_b64_tr_b16 a[154:155], v230 offset:55296\n" \
  "ds_read_b64_tr_b16 a[156:157], v231 offset:53248\n" \
  "v_mfma_f32_16x16x32_bf16 v[20:23], a[88:91], v[172:175], v[20:23]\n" \
  "buffer_load_dwordx4 v237, s[36:39], s86 offen offset:1024 lds\n" \
  "v_mfma_f32_16x16x32_bf16 v[24:27], a[92:95], v[168:171], v[24:27]\n" \
  "ds_read_b64_tr_b16 a[158:159], v231 offset:55296\n" \
  "s_mov_b32 s87, 0x54000\n" \
  "v_mfma_f32_16x16x32_bf16 v[28:31], a[92:95], v[172:175], v[28:31]\n" \
  "ds_read_b64_tr_b16 a[160:161], v228 offset:57344\n" \
  "v_mfma_f32_16x16x32_bf16 v[0:3], a[96:99], v[176:179], v[0:3]\n" \
  "ds_read_b64_tr_b16 a[162:163], v228 offset:59392\n" \
  "ds_read_b64_tr_b16 a[164:165], v229 offset:57344\n" \
  "v_mfma_f32_16x16x32_bf16 v[4:7], a[96:99], v[180:183], v[4:7]\n" \
  "ds_read_b64_tr_b16 a[166:167], v229 offset:59392\n" \
  "v_mfma_f32_16x16x32_bf16 v[8:11], a[100:103], v[176:179], v[8:11]\n" \
  "ds_read_b64_tr_b16 a[168:169], v230 offset:57344\n" \
  "v_mfma_f32_16x16x32_bf16 v[12:15], a[100:103], v[180:183], v[12:15]\n" \
  "ds_read_b64_tr_b16 a[170:171], v230 offset:59392\n" \
  "v_mfma_f32_16x16x32_bf16 v[16:19], a[104:107], v[176:179], v[16:19]\n" \
  "ds_read_b64_tr_b16 a[172:173], v231 offset:57344\n" \
  "v_mfma_f32_16x16x32_bf16 v[20:23], a[104:107], v[180:183], v[20:23]\n" \
  "buffer_load_dwordx4 v238, s[36:39], s86 offen offset:2048 lds\n" \
  "v_mfma_f32_16x16x32_bf16 v[24:27], a[108:111], v[176:179], v[24:27]\n" \
  "ds_read_b64_tr_b16 a[174:175], v231 offset:59392\n" \
  "ds_read_b64_tr_b16 a[176:177], v228 offset:61440\n" \
  "v_mfma_f32_16x16x32_bf16 v[28:31], a[108:111], v[180:183], v[28:31]\n" \
  "ds_read_b64_tr_b16 a[178:179], v228 offset:63488\n" \
  "v_mfma_f32_16x16x32_bf16 v[0:3], a[112:115], v[184:187], v[0:3]\n" \
  "ds_read_b64_tr_b16 a[180:181], v229 offset:61440\n" \
  "v_mfma_f32_16x16x32_bf16 v[4:7], a[112:115], v[188:191], v[4:7]\n" \
  "ds_read_b64_tr_b16 a[182:183], v229 offset:63488\n" \
  "v_mfma_f32_16x16x32_bf16 v[8:11], a[116:119], v[184:187], v[8:11]\n" \
  "ds_read_b64_tr_b16 a[184:185], v230 offset:61440\n" \
  "v_mfma_f32_16x16x32_bf16 v[12:15], a[116:119], v[188:191], v[12:15]\n" \
  "ds_read_b64_tr_b16 a[186:187], v230 offset:63488\n" \
  "ds_read_b64_tr_b16 a[188:189], v231 offset:61440\n" \
  "v_mfma_f32_16x16x32_bf16 v[16:19], a[120:123], v[184:187], v[16:19]\n" \
  "ds_read_b64_tr_b16 a[190:191], v231 offset:63488\n" \
  "v_mfma_f32_16x16x32_bf16 v[20:23], a[120:123], v[188:191], v[20:23]\n" \
  "buffer_load_dwordx4 v239, s[36:39], s86 offen offset:3072 lds\n" \
  "v_mfma_f32_16x16x32_bf16 v[24:27], a[124:127], v[184:187], v[24:27]\n" \
  "ds_read_b64_tr_b16 a[64:65], v232 offset:49152\n" \
  "s_add_u32 m0, s75, 0x8000\n" \
  "v_mfma_f32_16x16x32_bf16 v[28:31], a[124:127], v[188:191], v[28:31]\n" \
  "ds_read_b64_tr_b16 a[66:67], v232 offset:51200\n" \
  "s_waitcnt lgkmcnt(6)\n" \
  "v_mfma_f32_16x16x32_bf16 v[32:35], a[128:131], v[128:131], 0\n" \
  "ds_read_b64_tr_b16 a[68:69], v233 offset:49152\n" \
  "ds_read_b64_tr_b16 a[70:71], v233 offset:51200\n" \
  "v_mfma_f32_16x16x32_bf16 v[36:39], a[128:131], v[132:135], 0\n" \
  "ds_read_b64_tr_b16 a[72:73], v234 offset:49152\n" \
  "v_mfma_f32_16x16x32_bf16 v[40:43], a[132:135], v[128:131], 0\n" \
  "ds_read_b64_tr_b16 a[74:75], v234 offset:51200\n" \
  "v_mfma_f32_16x16x32_bf16 v[44:47], a[132:135], v[132:135], 0\n" \
  "ds_read_b64_tr_b16 a[76:77], v235 offset:49152\n" \
  "v_mfma_f32_16x16x32_bf16 v[48:51], a[136:139], v[128:131], 0\n" \
  "ds_read_b64_tr_b16 a[78:79], v235 offset:51200\n" \
  "s_waitcnt lgkmcnt(7)\n" \
  "v_mfma_f32_16x16x32_bf16 v[52:55], a[136:139], v[132:135], 0\n" \
  "buffer_load_dwordx4 v236, s[36:39], s87 offen lds\n" \
  "v_mul_f32_e64 v0, v0, v200\n" \
  "v_mfma_f32_16x16x32_bf16 v[56:59], a[140:143], v[128:131], 0\n" \
  "ds_read_b64_tr_b16 a[80:81], v232 offset:53248\n" \
  "ds_read_b64_tr_b16 a[82:83], v232 offset:55296\n" \
  "v_mul_f32_e64 v1, v1, v200\n" \
  "v_mfma_f32_16x16x32_bf16 v[60:63], a[140:143], v[132:135], 0\n" \
  "ds_read_b64_tr_b16 a[84:85], v233 offset:53248\n" \
  "v_mul_f32 v2, v2, v200\n" \
  "v_mul_f32 v3, v3, v200\n" \
  "v_mfma_f32_16x16x32_bf16 v[32:35], a[144:147], v[136:139], v[32:35]\n" \
  "ds_read_b64_tr_b16 a[86:87], v233 offset:55296\n" \
  "v_mul_f32_e64 v8, v8, v200\n" \
  "v_mfma_f32_16x16x32_bf16 v[36:39], a[144:147], v[140:143], v[36:39]\n" \
  "ds_read_b64_tr_b16 a[88:89], v234 offset:53248\n" \
  "v_mul_f32_e64 v9, v9, v200\n" \
  "v_mfma_f32_16x16x32_bf16 v[40:43], a[148:151], v[136:139], v[40:43]\n" \
  "ds_read_b64_tr_b16 a[90:91], v234 offset:55296\n" \
  "v_mul_f32 v10, v10, v200\n" \
  "v_mul_f32 v11, v11, v200\n" \
  "v_mfma_f32_16x16x32_bf16 v[44:47], a[148:151], v[140:143], v[44:47]\n" \
  "ds_read_b64_tr_b16 a[92:93], v235 offset:53248\n" \
  "ds_read_b64_tr_b16 a[94:95], v235 offset:55296\n" \
  "v_cvt_pk_bf16_f32 v0, v0, v1\n" \
  "v_mfma_f32_16x16x32_bf16 v[48:51], a[152:155], v[136:139], v[48:51]\n" \
  "ds_read_b64_tr_b16 a[96:97], v232 offset:57344\n" \
  "v_cvt_pk_bf16_f32 v1, v2, v3\n" \
  "v_mfma_f32_16x16x32_bf16 v[52:55], a[152:155], v[140:143], v[52:55]\n" \
  "buffer_load_dwordx4 v237, s[36:39], s87 offen offset:1024 lds\n" \
  "v_cvt_pk_bf16_f32 v2, v8, v9\n" \
  "v_cvt_pk_bf16_f32 v3, v10, v11\n" \
  "v_mfma_f32_16x16x32_bf16 v[56:59], a[156:159], v[136:139], v[56:59]\n" \
  "buffer_store_dwordx4 v[0:3], v244, s[64:67], 0 offen sc1\n" \
  "v_mfma_f32_16x16x32_bf16 v[60:63], a[156:159], v[140:143], v[60:63]\n" \
  "ds_read_b64_tr_b16 a[98:99], v232 offset:59392\n" \
  "ds_read_b64_tr_b16 a[100:101], v233 offset:57344\n" \
  "v_mul_f32 v16, v16, v200\n" \
  "v_mul_f32 v17, v17, v200\n" \
  "v_mfma_f32_16x16x32_bf16 v[32:35], a[160:163], v[144:147], v[32:35]\n" \
  "ds_read_b64_tr_b16 a[102:103], v233 offset:59392\n" \
  "v_mul_f32_e64 v18, v18, v200\n" \
  "v_mfma_f32_16x16x32_bf16 v[36:39], a[160:163], v[148:151], v[36:39]\n" \
  "ds_read_b64_tr_b16 a[104:105], v234 offset:57344\n" \
  "ds_read_b64_tr_b16 a[106:107], v234 offset:59392\n" \
  "v_mul_f32_e64 v19, v19, v200\n" \
  "v_mfma_f32_16x16x32_bf16 v[40:43], a[164:167], v[144:147], v[40:43]\n" \
  "ds_read_b64_tr_b16 a[108:109], v235 offset:57344\n" \
  "v_mul_f32 v24, v24, v200\n" \
  "v_mul_f32 v25, v25, v200\n" \
  "v_mfma_f32_16x16x32_bf16 v[44:47], a[164:167], v[148:151], v[44:47]\n" \
  "ds_read_b64_tr_b16 a[110:111], v235 offset:59392\n" \
  "v_mul_f32_e64 v26, v26, v200\n" \
  "v_mfma_f32_16x16x32_bf16 v[48:51], a[168:171], v[144:147], v[48:51]\n" \
  "ds_read_b64_tr_b16 a[112:113], v232 offset:61440\n" \
  "v_mul_f32_e64 v27, v27, v200\n" \
  "v_mfma_f32_16x16x32_bf16 v[52:55], a[168:171], v[148:151], v[52:55]\n" \
  "buffer_load_dwordx4 v238, s[36:39], s87 offen offset:2048 lds\n" \
  "v_cvt_pk_bf16_f32 v16, v16, v17\n" \
  "v_cvt_pk_bf16_f32 v17, v18, v19\n" \
  "v_mfma_f32_16x16x32_bf16 v[56:59], a[172:175], v[144:147], v[56:59]\n" \
  "ds_read_b64_tr_b16 a[114:115], v232 offset:63488\n" \
  "v_cvt_pk_bf16_f32 v18, v24, v25\n" \
  "v_mfma_f32_16x16x32_bf16 v[60:63], a[172:175], v[148:151], v[60:63]\n" \
  "ds_read_b64_tr_b16 a[116:117], v233 offset:61440\n" \
  "ds_read_b64_tr_b16 a[118:119], v233 offset:63488\n" \
  "v_cvt_pk_bf16_f32 v19, v26, v27\n" \
  "v_mul_f32_e64 v4, v4, v204\n" \
  "v_mfma_f32_16x16x32_bf16 v[32:35], a[176:179], v[152:155], v[32:35]\n" \
  "buffer_store_dwordx4 v[16:19], v244, s[64:67], 0 offen offset:1024 sc1\n" \
  "v_mfma_f32_16x16x32_bf16 v[36:39], a[176:179], v[156:159], v[36:39]\n" \
  "ds_read_b64_tr_b16 a[120:121], v234 offset:61440\n" \
  "ds_read_b64_tr_b16 a[122:123], v234 offset:63488\n" \
  "v_mul_f32_e64 v5, v5, v204\n" \
  "v_mfma_f32_16x16x32_bf16 v[40:43], a[180:183], v[152:155], v[40:43]\n" \
  "ds_read_b64_tr_b16 a[124:125], v235 offset:61440\n" \
  "v_mul_f32 v6, v6, v204\n" \
  "v_mul_f32 v7, v7, v204\n" \
  "v_mfma_f32_16x16x32_bf16 v[44:47], a[180:183], v[156:159], v[44:47]\n" \
  "ds_read_b64_tr_b16 a[126:127], v235 offset:63488\n" \
  "v_mul_f32_e64 v12, v12, v204\n" \
  "v_mfma_f32_16x16x32_bf16 v[48:51], a[184:187], v[152:155], v[48:51]\n" \
  "v_mul_f32 v13, v13, v204\n" \
  "v_mul_f32 v14, v14, v204\n" \
  "v_mfma_f32_16x16x32_bf16 v[52:55], a[184:187], v[156:159], v[52:55]\n" \
  "buffer_load_dwordx4 v239, s[36:39], s87 offen offset:3072 lds\n" \
  "v_mul_f32_e64 v15, v15, v204\n" \
  "v_mfma_f32_16x16x32_bf16 v[56:59], a[188:191], v[152:155], v[56:59]\n" \
  "v_cvt_pk_bf16_f32 v4, v4, v5\n" \
  "v_cvt_pk_bf16_f32 v5, v6, v7\n" \
  "v_mfma_f32_16x16x32_bf16 v[60:63], a[188:191], v[156:159], v[60:63]\n" \
  "v_cvt_pk_bf16_f32 v6, v12, v13\n" \
  "v_cvt_pk_bf16_f32 v7, v14, v15\n" \
  "s_waitcnt vmcnt(9) lgkmcnt(0)\n" \
  "s_barrier\n" \
  "v_add_u32_e64 v247, 1, v247\n" \
  "buffer_store_dwordx2 v[246:247], v254, s[68:71], s78 offen sc1\n" \
  "; HP [167, 168] ('O', 5, 1)\n" \
  "v_mfma_f32_16x16x32_bf16 v[32:35], a[64:67], v[160:163], v[32:35]\n" \
  "buffer_store_dwordx4 v[4:7], v245, s[64:67], 0 offen sc1\n" \
  "v_mfma_f32_16x16x32_bf16 v[36:39], a[64:67], v[164:167], v[36:39]\n" \
  "ds_read_b64_tr_b16 a[128:129], v228\n" \
  "ds_read_b64_tr_b16 a[130:131], v228 offset:2048\n" \
  "v_mul_f32_e64 v20, v20, v204\n" \
  "v_mfma_f32_16x16x32_bf16 v[40:43], a[68:71], v[160:163], v[40:43]\n" \
  "ds_read_b64_tr_b16 a[132:133], v229\n" \
  "ds_read_b64_tr_b16 a[134:135], v229 offset:2048\n" \
  "s_mov_b32 s86, 0x70000\n" \
  "s_add_u32 m0, s74, 0xc000\n" \
  "v_mul_f32_e64 v21, v21, v204\n" \
  "v_mfma_f32_16x16x32_bf16 v[44:47], a[68:71], v[164:167], v[44:47]\n" \
  "buffer_load_dwordx4 v236, s[36:39], s86 offen lds\n" \
  "v_mul_f32 v22, v22, v204\n" \
  "v_mul_f32 v23, v23, v204\n" \
  "v_mfma_f32_16x16x32_bf16 v[48:51], a[72:75], v[160:163], v[48:51]\n" \
  "ds_read_b64_tr_b16 a[136:137], v230\n" \
  "ds_read_b64_tr_b16 a[138:139], v230 offset:2048\n" \
  "v_mul_f32_e64 v28, v28, v204\n" \
  "v_mfma_f32_16x16x32_bf16 v[52:55], a[72:75], v[164:167], v[52:55]\n" \
  "ds_read_b64_tr_b16 a[140:141], v231\n" \
  "v_mul_f32_e64 v29, v29, v204\n" \
  "v_mfma_f32_16x16x32_bf16 v[56:59], a[76:79], v[160:163], v[56:59]\n" \
  "ds_read_b64_tr_b16 a[142:143], v231 offset:2048\n" \
  "v_mul_f32_e64 v30, v30, v204\n" \
  "v_mfma_f32_16x16x32_bf16 v[60:63], a[76:79], v[164:167], v[60:63]\n" \
  "ds_read_b64_tr_b16 a[144:145], v228 offset:4096\n" \
  "ds_read_b64_tr_b16 a[146:147], v228 offset:6144\n" \
  "v_mul_f32_e64 v31, v31, v204\n" \
  "v_cvt_pk_bf16_f32 v20, v20, v21\n" \
  "v_mfma_f32_16x16x32_bf16 v[32:35], a[80:83], v[168:171], v[32:35]\n" \
  "ds_read_b64_tr_b16 a[148:149], v229 offset:4096\n" \
  "v_cvt_pk_bf16_f32 v21, v22, v23\n" \
  "v_mfma_f32_16x16x32_bf16 v[36:39], a[80:83], v[172:175], v[36:39]\n" \
  "buffer_load_dwordx4 v237, s[36:39], s86 offen offset:1024 lds\n" \
  "v_cvt_pk_bf16_f32 v22, v28, v29\n" \
  "v_mfma_f32_16x16x32_bf16 v[40:43], a[84:87], v[168:171], v[40:43]\n" \
  "ds_read_b64_tr_b16 a[150:151], v229 offset:6144\n" \
  "v_cvt_pk_bf16_f32 v23, v30, v31\n" \
  "buffer_store_dwordx4 v[20:23], v245, s[64:67], 0 offen offset:1024 sc1\n" \
  "v_mfma_f32_16x16x32_bf16 v[44:47], a[84:87], v[172:175], v[44:47]\n" \
  "ds_read_b64_tr_b16 a[152:153], v230 offset:4096\n" \
  "ds_read_b64_tr_b16 a[154:155], v230 offset:6144\n" \
  "v_mfma_f32_16x16x32_bf16 v[48:51], a[88:91], v[168:171], v[48:51]\n" \
  "ds_read_b64_tr_b16 a[156:157], v231 offset:4096\n" \
  "s_mov_b32 s87, 0x8c000\n" \
  "v_mfma_f32_16x16x32_bf16 v[52:55], a[88:91], v[172:175], v[52:55]\n" \
  "ds_read_b64_tr_b16 a[158:159], v231 offset:6144\n" \
  "v_mfma_f32_16x16x32_bf16 v[56:59], a[92:95], v[168:171], v[56:59]\n" \
  "buffer_load_dwordx4 v238, s[36:39], s86 offen offset:2048 lds\n" \
  "v_mfma_f32_16x16x32_bf16 v[60:63], a[92:95], v[172:175], v[60:63]\n" \
  "ds_read_b64_tr_b16 a[160:161], v228 offset:8192\n" \
  "v_mfma_f32_16x16x32_bf16 v[32:35], a[96:99], v[176:179], v[32:35]\n" \
  "ds_read_b64_tr_b16 a[162:163], v228 offset:10240\n" \
  "ds_read_b64_tr_b16 a[164:165], v229 offset:8192\n" \
  "v_mfma_f32_16x16x32_bf16 v[36:39], a[96:99], v[180:183], v[36:39]\n" \
  "ds_read_b64_tr_b16 a[166:167], v229 offset:10240\n" \
  "v_mfma_f32_16x16x32_bf16 v[40:43], a[100:103], v[176:179], v[40:43]\n" \
  "ds_read_b64_tr_b16 a[168:169], v230 offset:8192\n" \
  "v_mfma_f32_16x16x32_bf16 v[44:47], a[100:103], v[180:183], v[44:47]\n" \
  "buffer_load_dwordx4 v239, s[36:39], s86 offen offset:3072 lds\n" \
  "v_mfma_f32_16x16x32_bf16 v[48:51], a[104:107], v[176:179], v[48:51]\n" \
  "ds_read_b64_tr_b16 a[170:171], v230 offset:10240\n" \
  "ds_read_b64_tr_b16 a[172:173], v231 offset:8192\n" \
  "v_mfma_f32_16x16x32_bf16 v[52:55], a[104:107], v[180:183], v[52:55]\n" \
  "ds_read_b64_tr_b16 a[174:175], v231 offset:10240\n" \
  "s_add_u32 m0, s75, 0xc000\n" \
  "v_mfma_f32_16x16x32_bf16 v[56:59], a[108:111], v[176:179], v[56:59]\n" \
  "ds_read_b64_tr_b16 a[176:177], v228 offset:12288\n" \
  "v_mfma_f32_16x16x32_bf16 v[60:63], a[108:111], v[180:183], v[60:63]\n" \
  "ds_read_b64_tr_b16 a[178:179], v228 offset:14336\n" \
  "ds_read_b64_tr_b16 a[180:181], v229 offset:12288\n" \
  "v_mfma_f32_16x16x32_bf16 v[32:35], a[112:115], v[184:187], v[32:35]\n" \
  "ds_read_b64_tr_b16 a[182:183], v229 offset:14336\n" \
  "v_mfma_f32_16x16x32_bf16 v[36:39], a[112:115], v[188:191], v[36:39]\n" \
  "buffer_load_dwordx4 v236, s[36:39], s87 offen lds\n" \
  "v_mfma_f32_16x16x32_bf16 v[40:43], a[116:119], v[184:187], v[40:43]\n" \
  "ds_read_b64_tr_b16 a[184:185], v230 offset:12288\n" \
  "v_mfma_f32_16x16x32_bf16 v[44:47], a[116:119], v[188:191], v[44:47]\n" \
  "ds_read_b64_tr_b16 a[186:187], v230 offset:14336\n" \
  "ds_read_b64_tr_b16 a[188:189], v231 offset:12288\n" \
  "v_mfma_f32_16x16x32_bf16 v[48:51], a[120:123], v[184:187], v[48:51]\n" \
  "ds_read_b64_tr_b16 a[190:191], v231 offset:14336\n" \
  "v_mfma_f32_16x16x32_bf16 v[52:55], a[120:123], v[188:191], v[52:55]\n" \
  "ds_read_b64_tr_b16 a[64:65], v232\n" \
  "v_mfma_f32_16x16x32_bf16 v[56:59], a[124:127], v[184:187], v[56:59]\n" \
  "buffer_load_dwordx4 v237, s[36:39], s87 offen offset:1024 lds\n" \
  "v_mfma_f32_16x16x32_bf16 v[60:63], a[124:127], v[188:191], v[60:63]\n" \
  "ds_read_b64_tr_b16 a[66:67], v232 offset:2048\n" \
  "s_waitcnt lgkmcnt(6)\n" \
  "v_mfma_f32_16x16x32_bf16 v[0:3], a[128:131], v[128:131], 0\n" \
  "ds_read_b64_tr_b16 a[68:69], v233\n" \
  "ds_read_b64_tr_b16 a[70:71], v233 offset:2048\n" \
  "v_mfma_f32_16x16x32_bf16 v[4:7], a[128:131], v[132:135], 0\n" \
  "ds_read_b64_tr_b16 a[72:73], v234\n" \
  "v_mfma_f32_16x16x32_bf16 v[8:11], a[132:135], v[128:131], 0\n" \
  "ds_read_b64_tr_b16 a[74:75], v234 offset:2048\n" \
  "s_waitcnt lgkmcnt(6)\n" \
  "v_mfma_f32_16x16x32_bf16 v[12:15], a[132:135], v[132:135], 0\n" \
  "buffer_load_dwordx4 v238, s[36:39], s87 offen offset:2048 lds\n" \
  "v_mfma_f32_16x16x32_bf16 v[16:19], a[136:139], v[128:131], 0\n" \
  "ds_read_b64_tr_b16 a[76:77], v235\n" \
  "ds_read_b64_tr_b16 a[78:79], v235 offset:2048\n" \
  "v_mfma_f32_16x16x32_bf16 v[20:23], a[136:139], v[132:135], 0\n" \
  "ds_read_b64_tr_b16 a[80:81], v232 offset:4096\n" \
  "v_mul_f32_e64 v32, v32, v200\n" \
  "v_mfma_f32_16x16x32_bf16 v[24:27], a[140:143], v[128:131], 0\n" \
  "ds_read_b64_tr_b16 a[82:83], v232 offset:6144\n" \
  "v_mul_f32_e64 v33, v33, v200\n" \
  "v_mfma_f32_16x16x32_bf16 v[28:31], a[140:143], v[132:135], 0\n" \
  "ds_read_b64_tr_b16 a[84:85], v233 offset:4096\n" \
  "ds_read_b64_tr_b16 a[86:87], v233 offset:6144\n" \
  "v_mul_f32 v34, v34, v200\n" \
  "v_mul_f32 v35, v35, v200\n" \
  "v_mfma_f32_16x16x32_bf16 v[0:3], a[144:147], v[136:139], v[0:3]\n" \
  "ds_read_b64_tr_b16 a[88:89], v234 offset:4096\n" \
  "v_mul_f32_e64 v40, v40, v200\n" \
  "v_mfma_f32_16x16x32_bf16 v[4:7], a[144:147], v[140:143], v[4:7]\n" \
  "buffer_load_dwordx4 v239, s[36:39], s87 offen offset:3072 lds\n" \
  "v_mul_f32 v41, v41, v200\n" \
  "v_mul_f32 v42, v42, v200\n" \
  "v_mfma_f32_16x16x32_bf16 v[8:11], a[148:151], v[136:139], v[8:11]\n" \
  "ds_read_b64_tr_b16 a[90:91], v234 offset:6144\n" \
  "v_mul_f32_e64 v43, v43, v200\n" \
  "v_mfma_f32_16x16x32_bf16 v[12:15], a[148:151], v[140:143], v[12:15]\n" \
  "ds_read_b64_tr_b16 a[92:93], v235 offset:4096\n" \
  "ds_read_b64_tr_b16 a[94:95], v235 offset:6144\n" \
  "v_cvt_pk_bf16_f32 v32, v32, v33\n" \
  "v_mfma_f32_16x16x32_bf16 v[16:19], a[152:155], v[136:139], v[16:19]\n" \
  "ds_read_b64_tr_b16 a[96:97], v232 offset:8192\n" \
  "v_cvt_pk_bf16_f32 v33, v34, v35\n" \
  "v_cvt_pk_bf16_f32 v34, v40, v41\n" \
  "v_mfma_f32_16x16x32_bf16 v[20:23], a[152:155], v[140:143], v[20:23]\n" \
  "ds_read_b64_tr_b16 a[98:99], v232 offset:10240\n" \
  "v_cvt_pk_bf16_f32 v35, v42, v43\n" \
  "v_mfma_f32_16x16x32_bf16 v[24:27], a[156:159], v[136:139], v[24:27]\n" \
  "buffer_load_dwordx2 v[192:193], v243, s[56:59], 0 offen\n" \
  "buffer_store_dwordx4 v[32:35], v244, s[64:67], 0 offen offset:2048 sc1\n" \
  "v_mul_f32_e64 v48, v48, v200\n" \
  "v_mfma_f32_16x16x32_bf16 v[28:31], a[156:159], v[140:143], v[28:31]\n" \
  "ds_read_b64_tr_b16 a[100:101], v233 offset:8192\n" \
  "v_mul_f32_e64 v49, v49, v200\n" \
  "v_mfma_f32_16x16x32_bf16 v[0:3], a[160:163], v[144:147], v[0:3]\n" \
  "ds_read_b64_tr_b16 a[102:103], v233 offset:10240\n" \
  "ds_read_b64_tr_b16 a[104:105], v234 offset:8192\n" \
  "v_mul_f32 v50, v50, v200\n" \
  "v_mul_f32 v51, v51, v200\n" \
  "v_mfma_f32_16x16x32_bf16 v[4:7], a[160:163], v[148:151], v[4:7]\n" \
  "ds_read_b64_tr_b16 a[106:107], v234 offset:10240\n" \
  "v_mul_f32_e64 v56, v56, v200\n" \
  "v_mfma_f32_16x16x32_bf16 v[8:11], a[164:167], v[144:147], v[8:11]\n" \
  "ds_read_b64_tr_b16 a[108:109], v235 offset:8192\n" \
  "v_mul_f32_e64 v57, v57, v200\n" \
  "v_mfma_f32_16x16x32_bf16 v[12:15], a[164:167], v[148:151], v[12:15]\n" \
  "buffer_load_dwordx2 v[194:195], v243, s[56:59], 0 offen offset:32\n" \
  "v_mul_f32 v58, v58, v200\n" \
  "v_mul_f32 v59, v59, v200\n" \
  "v_mfma_f32_16x16x32_bf16 v[16:19], a[168:171], v[144:147], v[16:19]\n" \
  "ds_read_b64_tr_b16 a[110:111], v235 offset:10240\n" \
  "ds_read_b64_tr_b16 a[112:113], v232 offset:12288\n" \
  "v_cvt_pk_bf16_f32 v48, v48, v49\n" \
  "v_mfma_f32_16x16x32_bf16 v[20:23], a[168:171], v[148:151], v[20:23]\n" \
  "ds_read_b64_tr_b16 a[114:115], v232 offset:14336\n" \
  "v_cvt_pk_bf16_f32 v49, v50, v51\n" \
  "v_cvt_pk_bf16_f32 v50, v56, v57\n" \
  "v_mfma_f32_16x16x32_bf16 v[24:27], a[172:175], v[144:147], v[24:27]\n" \
  "ds_read_b64_tr_b16 a[116:117], v233 offset:12288\n" \
  "v_cvt_pk_bf16_f32 v51, v58, v59\n" \
  "v_mfma_f32_16x16x32_bf16 v[28:31], a[172:175], v[148:151], v[28:31]\n" \
  "buffer_store_dwordx4 v[48:51], v244, s[64:67], 0 offen offset:3072 sc1\n" \
  "v_add_u32 v244, 0x1000, v244\n" \
  "v_mfma_f32_16x16x32_bf16 v[0:3], a[176:179], v[152:155], v[0:3]\n" \
  "ds_read_b64_tr_b16 a[118:119], v233 offset:14336\n" \
  "ds_read_b64_tr_b16 a[120:121], v234 offset:12288\n" \
  "v_mul_f32_e64 v36, v36, v204\n" \
  "v_mfma_f32_16x16x32_bf16 v[4:7], a[176:179], v[156:159], v[4:7]\n" \
  "buffer_load_dwordx2 v[196:197], v243, s[56:59], 0 offen offset:64\n" \
  "v_mul_f32_e64 v37, v37, v204\n" \
  "v_mfma_f32_16x16x32_bf16 v[8:11], a[180:183], v[152:155], v[8:11]\n" \
  "ds_read_b64_tr_b16 a[122:123], v234 offset:14336\n" \
  "ds_read_b64_tr_b16 a[124:125], v235 offset:12288\n" \
  "v_mul_f32 v38, v38, v204\n" \
  "v_mul_f32 v39, v39, v204\n" \
  "v_mfma_f32_16x16x32_bf16 v[12:15], a[180:183], v[156:159], v[12:15]\n" \
  "ds_read_b64_tr_b16 a[126:127], v235 offset:14336\n" \
  "v_mul_f32_e64 v44, v44, v204\n" \
  "v_mfma_f32_16x16x32_bf16 v[16:19], a[184:187], v[152:155], v[16:19]\n" \
  "v_mul_f32 v45, v45, v204\n" \
  "v_mul_f32 v46, v46, v204\n" \
  "v_mfma_f32_16x16x32_bf16 v[20:23], a[184:187], v[156:159], v[20:23]\n" \
  "v_mul_f32_e64 v47, v47, v204\n" \
  "v_cvt_pk_bf16_f32 v36, v36, v37\n" \
  "v_mfma_f32_16x16x32_bf16 v[24:27], a[188:191], v[152:155], v[24:27]\n" \
  "buffer_load_dwordx2 v[198:199], v243, s[56:59], 0 offen offset:96\n" \
  "v_mfma_f32_16x16x32_bf16 v[28:31], a[188:191], v[156:159], v[28:31]\n" \
  "v_cvt_pk_bf16_f32 v37, v38, v39\n" \
  "v_cvt_pk_bf16_f32 v38, v44, v45\n" \
  "s_waitcnt vmcnt(12) lgkmcnt(0)\n" \
  "s_barrier\n" \
  "v_add_u32_e64 v247, 1, v247\n" \
  "buffer_store_dwordx2 v[246:247], v254, s[68:71], s78 offen sc1\n" \
  "; HP [169, 170] ('O', 6, 1)\n" \
  "v_mfma_f32_16x16x32_bf16 v[0:3], a[64:67], v[160:163], v[0:3]\n" \
  "s_and_b32 s95, %1, 1\n" \
  "s_lshl_b32 s95, s95, 13\n" \
  "v_add_u32 v228, s95, v228\n" \
  "v_add_u32 v229, s95, v229\n" \
  "v_add_u32 v230, s95, v230\n" \
  "v_add_u32 v231, s95, v231\n" \
  "v_add_u32 v232, s95, v232\n" \
  "v_add_u32 v233, s95, v233\n" \
  "v_add_u32 v234, s95, v234\n" \
  "v_add_u32 v235, s95, v235\n" \
  "ds_read_b64_tr_b16 a[128:129], v228 offset:16384\n" \
  "ds_read_b64_tr_b16 a[130:131], v228 offset:18432\n" \
  "v_cvt_pk_bf16_f32 v39, v46, v47\n" \
  "v_mfma_f32_16x16x32_bf16 v[4:7], a[64:67], v[164:167], v[4:7]\n" \
  "s_mov_b32 s86, 0xa8000\n" \
  "s_add_u32 m0, s74, 0\n" \
  "buffer_store_dwordx4 v[36:39], v245, s[64:67], 0 offen offset:2048 sc1\n" \
  "v_mfma_f32_16x16x32_bf16 v[8:11], a[68:71], v[160:163], v[8:11]\n" \
  "buffer_load_dwordx4 v236, s[36:39], s86 offen lds\n" \
  "v_mul_f32 v52, v52, v204\n" \
  "v_mul_f32_e64 v53, v53, v204\n" \
  "v_mfma_f32_16x16x32_bf16 v[12:15], a[68:71], v[164:167], v[12:15]\n" \
  "ds_read_b64_tr_b16 a[132:133], v229 offset:16384\n" \
  "ds_read_b64_tr_b16 a[134:135], v229 offset:18432\n" \
  "v_mul_f32_e64 v54, v54, v204\n" \
  "v_mfma_f32_16x16x32_bf16 v[16:19], a[72:75], v[160:163], v[16:19]\n" \
  "ds_read_b64_tr_b16 a[136:137], v230 offset:16384\n" \
  "v_mul_f32 v55, v55, v204\n" \
  "v_mul_f32 v60, v60, v204\n" \
  "v_mfma_f32_16x16x32_bf16 v[20:23], a[72:75], v[164:167], v[20:23]\n" \
  "buffer_load_dwordx4 v237, s[36:39], s86 offen offset:1024 lds\n" \
  "v_mul_f32_e64 v61, v61, v204\n" \
  "v_mfma_f32_16x16x32_bf16 v[24:27], a[76:79], v[160:163], v[24:27]\n" \
  "ds_read_b64_tr_b16 a[138:139], v230 offset:18432\n" \
  "v_mul_f32 v62, v62, v204\n" \
  "v_mul_f32 v63, v63, v204\n" \
  "v_mfma_f32_16x16x32_bf16 v[28:31], a[76:79], v[164:167], v[28:31]\n" \
  "ds_read_b64_tr_b16 a[140:141], v231 offset:16384\n" \
  "v_cvt_pk_bf16_f32 v52, v52, v53\n" \
  "v_mfma_f32_16x16x32_bf16 v[0:3], a[80:83], v[168:171], v[0:3]\n" \
  "buffer_load_dwordx4 v238, s[36:39], s86 offen offset:2048 lds\n" \
  "v_cvt_pk_bf16_f32 v53, v54, v55\n" \
  "v_cvt_pk_bf16_f32 v54, v60, v61\n" \
  "v_mfma_f32_16x16x32_bf16 v[4:7], a[80:83], v[172:175], v[4:7]\n" \
  "ds_read_b64_tr_b16 a[142:143], v231 offset:18432\n" \
  "v_cvt_pk_bf16_f32 v55, v62, v63\n" \
  "v_mfma_f32_16x16x32_bf16 v[8:11], a[84:87], v[168:171], v[8:11]\n" \
  "buffer_store_dwordx4 v[52:55], v245, s[64:67], 0 offen offset:3072 sc1\n" \
  "v_add_u32 v245, 0x1000, v245\n" \
  "v_mfma_f32_16x16x32_bf16 v[12:15], a[84:87], v[172:175], v[12:15]\n" \
  "buffer_load_dwordx4 v239, s[36:39], s86 offen offset:3072 lds\n" \
  "v_mfma_f32_16x16x32_bf16 v[16:19], a[88:91], v[168:171], v[16:19]\n" \
  "ds_read_b64_tr_b16 a[144:145], v228 offset:20480\n" \
  "ds_read_b64_tr_b16 a[146:147], v228 offset:22528\n" \
  "v_mfma_f32_16x16x32_bf16 v[20:23], a[88:91], v[172:175], v[20:23]\n" \
  "ds_read_b64_tr_b16 a[148:149], v229 offset:20480\n" \
  "s_mov_b32 s87, 0xc4000\n" \
  "s_add_u32 m0, s75, 0\n" \
  "v_mfma_f32_16x16x32_bf16 v[24:27], a[92:95], v[168:171], v[24:27]\n" \
  "buffer_load_dwordx4 v236, s[36:39], s87 offen lds\n" \
  "v_mfma_f32_16x16x32_bf16 v[28:31], a[92:95], v[172:175], v[28:31]\n" \
  "ds_read_b64_tr_b16 a[150:151], v229 offset:22528\n" \
  "s_mov_b32 s88, 0x8000\n" \
  "v_mfma_f32_16x16x32_bf16 v[0:3], a[96:99], v[176:179], v[0:3]\n" \
  "ds_read_b64_tr_b16 a[152:153], v230 offset:20480\n" \
  "v_mfma_f32_16x16x32_bf16 v[4:7], a[96:99], v[180:183], v[4:7]\n" \
  "buffer_load_dwordx4 v237, s[36:39], s87 offen offset:1024 lds\n" \
  "v_mfma_f32_16x16x32_bf16 v[8:11], a[100:103], v[176:179], v[8:11]\n" \
  "ds_read_b64_tr_b16 a[154:155], v230 offset:22528\n" \
  "v_mfma_f32_16x16x32_bf16 v[12:15], a[100:103], v[180:183], v[12:15]\n" \
  "ds_read_b64_tr_b16 a[156:157], v231 offset:20480\n" \
  "v_mfma_f32_16x16x32_bf16 v[16:19], a[104:107], v[176:179], v[16:19]\n" \
  "buffer_load_dwordx4 v238, s[36:39], s87 offen offset:2048 lds\n" \
  "v_mfma_f32_16x16x32_bf16 v[20:23], a[104:107], v[180:183], v[20:23]\n" \
  "ds_read_b64_tr_b16 a[158:159], v231 offset:22528\n" \
  "v_mfma_f32_16x16x32_bf16 v[24:27], a[108:111], v[176:179], v[24:27]\n" \
  "ds_read_b64_tr_b16 a[64:65], v232 offset:16384\n" \
  "ds_read_b64_tr_b16 a[66:67], v232 offset:18432\n" \
  "v_mfma_f32_16x16x32_bf16 v[28:31], a[108:111], v[180:183], v[28:31]\n" \
  "buffer_load_dwordx4 v239, s[36:39], s87 offen offset:3072 lds\n" \
  "v_mfma_f32_16x16x32_bf16 v[0:3], a[112:115], v[184:187], v[0:3]\n" \
  "ds_read_b64_tr_b16 a[68:69], v233 offset:16384\n" \
  "v_mfma_f32_16x16x32_bf16 v[4:7], a[112:115], v[188:191], v[4:7]\n" \
  "ds_read_b64_tr_b16 a[70:71], v233 offset:18432\n" \
  "v_mfma_f32_16x16x32_bf16 v[8:11], a[116:119], v[184:187], v[8:11]\n" \
  "buffer_load_dwordx4 a[160:163], v222, s[60:63], s88 offen sc1\n" \
  "v_mfma_f32_16x16x32_bf16 v[12:15], a[116:119], v[188:191], v[12:15]\n" \
  "ds_read_b64_tr_b16 a[72:73], v234 offset:16384\n" \
  "v_mfma_f32_16x16x32_bf16 v[16:19], a[120:123], v[184:187], v[16:19]\n" \
  "ds_read_b64_tr_b16 a[74:75], v234 offset:18432\n" \
  "v_mfma_f32_16x16x32_bf16 v[20:23], a[120:123], v[188:191], v[20:23]\n" \
  "buffer_load_dwordx4 a[164:167], v223, s[60:63], s88 offen sc1\n" \
  "v_mfma_f32_16x16x32_bf16 v[24:27], a[124:127], v[184:187], v[24:27]\n" \
  "ds_read_b64_tr_b16 a[76:77], v235 offset:16384\n" \
  "v_mfma_f32_16x16x32_bf16 v[28:31], a[124:127], v[188:191], v[28:31]\n" \
  "ds_read_b64_tr_b16 a[78:79], v235 offset:18432\n" \
  "s_waitcnt lgkmcnt(4)\n" \
  "v_mfma_f32_16x16x32_bf16 v[64:67], a[128:131], a[192:195], 0\n" \
  "buffer_load_dwordx4 a[168:171], v222, s[60:63], s88 offen offset:1024 sc1\n" \
  "v_mfma_f32_16x16x32_bf16 v[80:83], a[128:131], a[196:199], 0\n" \
  "ds_read_b64_tr_b16 a[80:81], v232 offset:20480\n" \
  "v_mfma_f32_16x16x32_bf16 v[68:71], a[132:135], a[192:195], 0\n" \
  "ds_read_b64_tr_b16 a[82:83], v232 offset:22528\n" \
  "v_mfma_f32_16x16x32_bf16 v[84:87], a[132:135], a[196:199], 0\n" \
  "buffer_load_dwordx4 a[172:175], v223, s[60:63], s88 offen offset:1024 sc1\n" \
  "v_mfma_f32_16x16x32_bf16 v[72:75], a[136:139], a[192:195], 0\n" \
  "ds_read_b64_tr_b16 a[84:85], v233 offset:20480\n" \
  "s_mov_b32 s88, 0x9000\n" \
  "v_mfma_f32_16x16x32_bf16 v[88:91], a[136:139], a[196:199], 0\n" \
  "ds_read_b64_tr_b16 a[86:87], v233 offset:22528\n" \
  "v_mul_f32_e64 v0, v0, v200\n" \
  "v_mfma_f32_16x16x32_bf16 v[76:79], a[140:143], a[192:195], 0\n" \
  "buffer_load_dwordx4 a[176:179], v222, s[60:63], s88 offen sc1\n" \
  "v_mul_f32 v1, v1, v200\n" \
  "v_mul_f32 v2, v2, v200\n" \
  "v_mfma_f32_16x16x32_bf16 v[92:95], a[140:143], a[196:199], 0\n" \
  "ds_read_b64_tr_b16 a[88:89], v234 offset:20480\n" \
  "v_mul_f32_e64 v3, v3, v200\n" \
  "v_mfma_f32_16x16x32_bf16 v[64:67], a[144:147], a[200:203], v[64:67]\n" \
  "ds_read_b64_tr_b16 a[90:91], v234 offset:22528\n" \
  "v_mul_f32 v8, v8, v200\n" \
  "v_mul_f32 v9, v9, v200\n" \
  "v_mfma_f32_16x16x32_bf16 v[80:83], a[144:147], a[204:207], v[80:83]\n" \
  "buffer_load_dwordx4 a[180:183], v223, s[60:63], s88 offen sc1\n" \
  "v_mul_f32 v10, v10, v200\n" \
  "v_mul_f32 v11, v11, v200\n" \
  "v_mfma_f32_16x16x32_bf16 v[68:71], a[148:151], a[200:203], v[68:71]\n" \
  "ds_read_b64_tr_b16 a[92:93], v235 offset:20480\n" \
  "v_cvt_pk_bf16_f32 v0, v0, v1\n" \
  "v_mfma_f32_16x16x32_bf16 v[84:87], a[148:151], a[204:207], v[84:87]\n" \
  "ds_read_b64_tr_b16 a[94:95], v235 offset:22528\n" \
  "v_cvt_pk_bf16_f32 v1, v2, v3\n" \
  "v_cvt_pk_bf16_f32 v2, v8, v9\n" \
  "v_mfma_f32_16x16x32_bf16 v[72:75], a[152:155], a[200:203], v[72:75]\n" \
  "buffer_load_dwordx4 a[184:187], v222, s[60:63], s88 offen offset:1024 sc1\n" \
  "v_cvt_pk_bf16_f32 v3, v10, v11\n" \
  "v_mul_f32_e64 v16, v16, v200\n" \
  "v_mfma_f32_16x16x32_bf16 v[88:91], a[152:155], a[204:207], v[88:91]\n" \
  "buffer_store_dwordx4 v[0:3], v244, s[64:67], 0 offen sc1\n" \
  "v_mfma_f32_16x16x32_bf16 v[76:79], a[156:159], a[200:203], v[76:79]\n" \
  "v_mul_f32 v17, v17, v200\n" \
  "v_mul_f32 v18, v18, v200\n" \
  "v_mfma_f32_16x16x32_bf16 v[92:95], a[156:159], a[204:207], v[92:95]\n" \
  "buffer_load_dwordx4 a[188:191], v223, s[60:63], s88 offen offset:1024 sc1\n" \
  "v_mul_f32 v19, v19, v200\n" \
  "s_waitcnt vmcnt(14) lgkmcnt(0)\n" \
  "s_barrier\n" \
  "v_add_u32 v247, 1, v247\n" \
  "buffer_store_dwordx2 v[246:247], v254, s[68:71], s78 offen sc1\n" \
  "s_lshl_b32 s95, %1, 12\n" \
  "s_add_u32 s95, s95, 0x20000\n" \
  "s_add_u32 s95, s95, s84\n" \
  "v_add_u32_e64 v208, s95, v250\n" \
  "v_xor_b32 v209, 0x1000, v208\n" \
  "; HP [171, 172] ('G', 1)\n" \
  "v_mfma_f32_16x16x32_bf16 v[64:67], a[64:67], a[208:211], v[64:67]\n" \
  "ds_read_b64_tr_b16 a[128:129], v228 offset:32768\n" \
  "ds_read_b64_tr_b16 a[130:131], v228 offset:34816\n" \
  "s_mov_b32 s86, 0xe0000\n" \
  "s_add_u32 m0, s74, 0x4000\n" \
  "v_mul_f32_e64 v24, v24, v200\n" \
  "v_mfma_f32_16x16x32_bf16 v[80:83], a[64:67], a[212:215], v[80:83]\n" \
  "buffer_load_dwordx4 v236, s[36:39], s86 offen lds\n" \
  "v_mul_f32 v25, v25, v200\n" \
  "v_mul_f32 v26, v26, v200\n" \
  "v_mfma_f32_16x16x32_bf16 v[68:71], a[68:71], a[208:211], v[68:71]\n" \
  "ds_read_b64_tr_b16 a[132:133], v229 offset:32768\n" \
  "ds_read_b64_tr_b16 a[134:135], v229 offset:34816\n" \
  "v_mul_f32_e64 v27, v27, v200\n" \
  "v_cvt_pk_bf16_f32 v16, v16, v17\n" \
  "v_mfma_f32_16x16x32_bf16 v[84:87], a[68:71], a[212:215], v[84:87]\n" \
  "buffer_load_dwordx4 v237, s[36:39], s86 offen offset:1024 lds\n" \
  "v_cvt_pk_bf16_f32 v17, v18, v19\n" \
  "v_mfma_f32_16x16x32_bf16 v[72:75], a[72:75], a[208:211], v[72:75]\n" \
  "ds_read_b64_tr_b16 a[136:137], v230 offset:32768\n" \
  "ds_read_b64_tr_b16 a[138:139], v230 offset:34816\n" \
  "v_cvt_pk_bf16_f32 v18, v24, v25\n" \
  "v_cvt_pk_bf16_f32 v19, v26, v27\n" \
  "v_mfma_f32_16x16x32_bf16 v[88:91], a[72:75], a[212:215], v[88:91]\n" \
  "buffer_load_dwordx4 v238, s[36:39], s86 offen offset:2048 lds\n" \
  "buffer_store_dwordx4 v[16:19], v244, s[64:67], 0 offen offset:1024 sc1\n" \
  "v_mul_f32_e64 v4, v4, v204\n" \
  "v_mfma_f32_16x16x32_bf16 v[76:79], a[76:79], a[208:211], v[76:79]\n" \
  "ds_read_b64_tr_b16 a[140:141], v231 offset:32768\n" \
  "ds_read_b64_tr_b16 a[142:143], v231 offset:34816\n" \
  "v_mul_f32_e64 v5, v5, v204\n" \
  "v_mfma_f32_16x16x32_bf16 v[92:95], a[76:79], a[212:215], v[92:95]\n" \
  "buffer_load_dwordx4 v239, s[36:39], s86 offen offset:3072 lds\n" \
  "v_mul_f32 v6, v6, v204\n" \
  "v_mul_f32 v7, v7, v204\n" \
  "v_mfma_f32_16x16x32_bf16 v[64:67], a[80:83], a[216:219], v[64:67]\n" \
  "ds_read_b64_tr_b16 a[144:145], v228 offset:36864\n" \
  "ds_read_b64_tr_b16 a[146:147], v228 offset:38912\n" \
  "s_mov_b32 s87, 0xfc000\n" \
  "s_add_u32 m0, s75, 0x4000\n" \
  "v_mul_f32 v12, v12, v204\n" \
  "v_mul_f32 v13, v13, v204\n" \
  "v_mfma_f32_16x16x32_bf16 v[80:83], a[80:83], a[220:223], v[80:83]\n" \
  "buffer_load_dwordx4 v236, s[36:39], s87 offen lds\n" \
  "v_mul_f32_e64 v14, v14, v204\n" \
  "v_mfma_f32_16x16x32_bf16 v[68:71], a[84:87], a[216:219], v[68:71]\n" \
  "ds_read_b64_tr_b16 a[148:149], v229 offset:36864\n" \
  "ds_read_b64_tr_b16 a[150:151], v229 offset:38912\n" \
  "v_mul_f32_e64 v15, v15, v204\n" \
  "v_cvt_pk_bf16_f32 v4, v4, v5\n" \
  "v_mfma_f32_16x16x32_bf16 v[84:87], a[84:87], a[220:223], v[84:87]\n" \
  "buffer_load_dwordx4 v237, s[36:39], s87 offen offset:1024 lds\n" \
  "v_cvt_pk_bf16_f32 v5, v6, v7\n" \
  "v_cvt_pk_bf16_f32 v6, v12, v13\n" \
  "v_mfma_f32_16x16x32_bf16 v[72:75], a[88:91], a[216:219], v[72:75]\n" \
  "ds_read_b64_tr_b16 a[152:153], v230 offset:36864\n" \
  "ds_read_b64_tr_b16 a[154:155], v230 offset:38912\n" \
  "v_cvt_pk_bf16_f32 v7, v14, v15\n" \
  "v_mfma_f32_16x16x32_bf16 v[88:91], a[88:91], a[220:223], v[88:91]\n" \
  "buffer_load_dwordx4 v238, s[36:39], s87 offen offset:2048 lds\n" \
  "buffer_store_dwordx4 v[4:7], v245, s[64:67], 0 offen sc1\n" \
  "v_mul_f32_e64 v20, v20, v204\n" \
  "v_mfma_f32_16x16x32_bf16 v[76:79], a[92:95], a[216:219], v[76:79]\n" \
  "ds_read_b64_tr_b16 a[156:157], v231 offset:36864\n" \
  "ds_read_b64_tr_b16 a[158:159], v231 offset:38912\n" \
  "v_mul_f32 v21, v21, v204\n" \
  "v_mul_f32 v22, v22, v204\n" \
  "v_mfma_f32_16x16x32_bf16 v[92:95], a[92:95], a[220:223], v[92:95]\n" \
  "buffer_load_dwordx4 v239, s[36:39], s87 offen offset:3072 lds\n" \
  "s_waitcnt lgkmcnt(6)\n" \
  "v_mul_f32 v23, v23, v204\n" \
  "v_mfma_f32_16x16x32_bf16 v[64:67], a[128:131], a[224:227], v[64:67]\n" \
  "ds_read_b64_tr_b16 a[64:65], v232 offset:32768\n" \
  "ds_read_b64_tr_b16 a[66:67], v232 offset:34816\n" \
  "s_mov_b32 s88, 0xa000\n" \
  "v_mul_f32 v28, v28, v204\n" \
  "v_mul_f32 v29, v29, v204\n" \
  "v_mfma_f32_16x16x32_bf16 v[80:83], a[128:131], a[228:231], v[80:83]\n" \
  "buffer_load_dwordx4 a[96:99], v222, s[60:63], s88 offen sc1\n" \
  "v_mul_f32 v30, v30, v204\n" \
  "v_mul_f32 v31, v31, v204\n" \
  "v_mfma_f32_16x16x32_bf16 v[68:71], a[132:135], a[224:227], v[68:71]\n" \
  "ds_read_b64_tr_b16 a[68:69], v233 offset:32768\n" \
  "ds_read_b64_tr_b16 a[70:71], v233 offset:34816\n" \
  "v_cvt_pk_bf16_f32 v20, v20, v21\n" \
  "v_mfma_f32_16x16x32_bf16 v[84:87], a[132:135], a[228:231], v[84:87]\n" \
  "buffer_load_dwordx4 a[100:103], v223, s[60:63], s88 offen sc1\n" \
  "v_cvt_pk_bf16_f32 v21, v22, v23\n" \
  "v_cvt_pk_bf16_f32 v22, v28, v29\n" \
  "v_mfma_f32_16x16x32_bf16 v[72:75], a[136:139], a[224:227], v[72:75]\n" \
  "ds_read_b64_tr_b16 a[72:73], v234 offset:32768\n" \
  "ds_read_b64_tr_b16 a[74:75], v234 offset:34816\n" \
  "v_cvt_pk_bf16_f32 v23, v30, v31\n" \
  "buffer_store_dwordx4 v[20:23], v245, s[64:67], 0 offen offset:1024 sc1\n" \
  "v_mfma_f32_16x16x32_bf16 v[88:91], a[136:139], a[228:231], v[88:91]\n" \
  "buffer_load_dwordx4 a[104:107], v222, s[60:63], s88 offen offset:1024 sc1\n" \
  "v_mfma_f32_16x16x32_bf16 v[76:79], a[140:143], a[224:227], v[76:79]\n" \
  "ds_read_b64_tr_b16 a[76:77], v235 offset:32768\n" \
  "ds_read_b64_tr_b16 a[78:79], v235 offset:34816\n" \
  "v_mfma_f32_16x16x32_bf16 v[92:95], a[140:143], a[228:231], v[92:95]\n" \
  "buffer_load_dwordx4 a[108:111], v223, s[60:63], s88 offen offset:1024 sc1\n" \
  "v_mfma_f32_16x16x32_bf16 v[64:67], a[144:147], a[232:235], v[64:67]\n" \
  "ds_read_b64_tr_b16 a[80:81], v232 offset:36864\n" \
  "ds_read_b64_tr_b16 a[82:83], v232 offset:38912\n" \
  "s_mov_b32 s88, 0xb000\n" \
  "v_mfma_f32_16x16x32_bf16 v[80:83], a[144:147], a[236:239], v[80:83]\n" \
  "buffer_load_dwordx4 a[112:115], v222, s[60:63], s88 offen sc1\n" \
  "s_waitcnt lgkmcnt(6)\n" \
  "v_mfma_f32_16x16x32_bf16 v[68:71], a[148:151], a[232:235], v[68:71]\n" \
  "ds_read_b64_tr_b16 a[84:85], v233 offset:36864\n" \
  "ds_read_b64_tr_b16 a[86:87], v233 offset:38912\n" \
  "v_mfma_f32_16x16x32_bf16 v[84:87], a[148:151], a[236:239], v[84:87]\n" \
  "buffer_load_dwordx4 a[116:119], v223, s[60:63], s88 offen sc1\n" \
  "v_mfma_f32_16x16x32_bf16 v[72:75], a[152:155], a[232:235], v[72:75]\n" \
  "v_mfma_f32_16x16x32_bf16 v[88:91], a[152:155], a[236:239], v[88:91]\n" \
  "buffer_load_dwordx4 a[120:123], v222, s[60:63], s88 offen offset:1024 sc1\n" \
  "v_mfma_f32_16x16x32_bf16 v[76:79], a[156:159], a[232:235], v[76:79]\n" \
  "v_mfma_f32_16x16x32_bf16 v[92:95], a[156:159], a[236:239], v[92:95]\n" \
  "buffer_load_dwordx4 a[124:127], v223, s[60:63], s88 offen offset:1024 sc1\n" \
  "ds_read_b64_tr_b16 a[88:89], v234 offset:36864\n" \
  "ds_read_b64_tr_b16 a[90:91], v234 offset:38912\n" \
  "ds_read_b64_tr_b16 a[92:93], v235 offset:36864\n" \
  "ds_read_b64_tr_b16 a[94:95], v235 offset:38912\n" \
  "s_waitcnt vmcnt(34) lgkmcnt(0)\n" \
  "s_barrier\n" \
  "; HP [173, 174] ('G', 3)\n" \
  "v_mfma_f32_16x16x32_bf16 v[64:67], a[64:67], a[240:243], v[64:67]\n" \
  "ds_read_b64_tr_b16 a[128:129], v228 offset:49152\n" \
  "ds_read_b64_tr_b16 a[130:131], v228 offset:51200\n" \
  "s_mov_b32 s86, 0x118000\n" \
  "s_add_u32 m0, s74, 0x8000\n" \
  "v_mfma_f32_16x16x32_bf16 v[80:83], a[64:67], a[244:247], v[80:83]\n" \
  "buffer_load_dwordx4 v236, s[36:39], s86 offen lds\n" \
  "v_mfma_f32_16x16x32_bf16 v[68:71], a[68:71], a[240:243], v[68:71]\n" \
  "ds_read_b64_tr_b16 a[132:133], v229 offset:49152\n" \
  "ds_read_b64_tr_b16 a[134:135], v229 offset:51200\n" \
  "v_mfma_f32_16x16x32_bf16 v[84:87], a[68:71], a[244:247], v[84:87]\n" \
  "buffer_load_dwordx4 v237, s[36:39], s86 offen offset:1024 lds\n" \
  "v_mfma_f32_16x16x32_bf16 v[72:75], a[72:75], a[240:243], v[72:75]\n" \
  "ds_read_b64_tr_b16 a[136:137], v230 offset:49152\n" \
  "ds_read_b64_tr_b16 a[138:139], v230 offset:51200\n" \
  "v_mfma_f32_16x16x32_bf16 v[88:91], a[72:75], a[244:247], v[88:91]\n" \
  "buffer_load_dwordx4 v238, s[36:39], s86 offen offset:2048 lds\n" \
  "v_mfma_f32_16x16x32_bf16 v[76:79], a[76:79], a[240:243], v[76:79]\n" \
  "buffer_load_dwordx4 v239, s[36:39], s86 offen offset:3072 lds\n" \
  "v_mfma_f32_16x16x32_bf16 v[92:95], a[76:79], a[244:247], v[92:95]\n" \
  "ds_read_b64_tr_b16 a[140:141], v231 offset:49152\n" \
  "ds_read_b64_tr_b16 a[142:143], v231 offset:51200\n" \
  "s_mov_b32 s87, 0x134000\n" \
  "s_add_u32 m0, s75, 0x8000\n" \
  "v_mfma_f32_16x16x32_bf16 v[64:67], a[80:83], a[248:251], v[64:67]\n" \
  "buffer_load_dwordx4 v236, s[36:39], s87 offen lds\n" \
  "v_mfma_f32_16x16x32_bf16 v[80:83], a[80:83], a[252:255], v[80:83]\n" \
  "ds_read_b64_tr_b16 a[144:145], v228 offset:53248\n" \
  "ds_read_b64_tr_b16 a[146:147], v228 offset:55296\n" \
  "v_mfma_f32_16x16x32_bf16 v[68:71], a[84:87], a[248:251], v[68:71]\n" \
  "buffer_load_dwordx4 v237, s[36:39], s87 offen offset:1024 lds\n" \
  "v_mfma_f32_16x16x32_bf16 v[84:87], a[84:87], a[252:255], v[84:87]\n" \
  "ds_read_b64_tr_b16 a[148:149], v229 offset:53248\n" \
  "ds_read_b64_tr_b16 a[150:151], v229 offset:55296\n" \
  "v_mfma_f32_16x16x32_bf16 v[72:75], a[88:91], a[248:251], v[72:75]\n" \
  "buffer_load_dwordx4 v238, s[36:39], s87 offen offset:2048 lds\n" \
  "v_mfma_f32_16x16x32_bf16 v[88:91], a[88:91], a[252:255], v[88:91]\n" \
  "ds_read_b64_tr_b16 a[152:153], v230 offset:53248\n" \
  "ds_read_b64_tr_b16 a[154:155], v230 offset:55296\n" \
  "v_mfma_f32_16x16x32_bf16 v[76:79], a[92:95], a[248:251], v[76:79]\n" \
  "buffer_load_dwordx4 v239, s[36:39], s87 offen offset:3072 lds\n" \
  "v_mfma_f32_16x16x32_bf16 v[92:95], a[92:95], a[252:255], v[92:95]\n" \
  "ds_read_b64_tr_b16 a[156:157], v231 offset:53248\n" \
  "ds_read_b64_tr_b16 a[158:159], v231 offset:55296\n" \
  "s_mov_b32 s88, 0xc000\n" \
  "s_waitcnt lgkmcnt(6)\n" \
  "v_mfma_f32_16x16x32_bf16 v[64:67], a[128:131], a[0:3], v[64:67]\n" \
  "buffer_load_dwordx4 a[192:195], v222, s[60:63], s88 offen sc1\n" \
  "v_mfma_f32_16x16x32_bf16 v[80:83], a[128:131], a[4:7], v[80:83]\n" \
  "ds_read_b64_tr_b16 a[64:65], v232 offset:49152\n" \
  "ds_read_b64_tr_b16 a[66:67], v232 offset:51200\n" \
  "v_mfma_f32_16x16x32_bf16 v[68:71], a[132:135], a[0:3], v[68:71]\n" \
  "buffer_load_dwordx4 a[196:199], v223, s[60:63], s88 offen sc1\n" \
  "v_mfma_f32_16x16x32_bf16 v[84:87], a[132:135], a[4:7], v[84:87]\n" \
  "ds_read_b64_tr_b16 a[68:69], v233 offset:49152\n" \
  "ds_read_b64_tr_b16 a[70:71], v233 offset:51200\n" \
  "v_mfma_f32_16x16x32_bf16 v[72:75], a[136:139], a[0:3], v[72:75]\n" \
  "buffer_load_dwordx4 a[200:203], v222, s[60:63], s88 offen offset:1024 sc1\n" \
  "v_mfma_f32_16x16x32_bf16 v[88:91], a[136:139], a[4:7], v[88:91]\n" \
  "ds_read_b64_tr_b16 a[72:73], v234 offset:49152\n" \
  "ds_read_b64_tr_b16 a[74:75], v234 offset:51200\n" \
  "v_mfma_f32_16x16x32_bf16 v[76:79], a[140:143], a[0:3], v[76:79]\n" \
  "buffer_load_dwordx4 a[204:207], v223, s[60:63], s88 offen offset:1024 sc1\n" \
  "s_mov_b32 s88, 0xd000\n" \
  "v_mfma_f32_16x16x32_bf16 v[92:95], a[140:143], a[4:7], v[92:95]\n" \
  "buffer_load_dwordx4 a[208:211], v222, s[60:63], s88 offen sc1\n" \
  "v_mfma_f32_16x16x32_bf16 v[64:67], a[144:147], a[8:11], v[64:67]\n" \
  "ds_read_b64_tr_b16 a[76:77], v235 offset:49152\n" \
  "ds_read_b64_tr_b16 a[78:79], v235 offset:51200\n" \
  "v_mfma_f32_16x16x32_bf16 v[80:83], a[144:147], a[12:15], v[80:83]\n" \
  "buffer_load_dwordx4 a[212:215], v223, s[60:63], s88 offen sc1\n" \
  "s_waitcnt lgkmcnt(4)\n" \
  "v_mfma_f32_16x16x32_bf16 v[68:71], a[148:151], a[8:11], v[68:71]\n" \
  "ds_read_b64_tr_b16 a[80:81], v232 offset:53248\n" \
  "ds_read_b64_tr_b16 a[82:83], v232 offset:55296\n" \
  "v_mfma_f32_16x16x32_bf16 v[84:87], a[148:151], a[12:15], v[84:87]\n" \
  "buffer_load_dwordx4 a[216:219], v222, s[60:63], s88 offen offset:1024 sc1\n" \
  "v_mfma_f32_16x16x32_bf16 v[72:75], a[152:155], a[8:11], v[72:75]\n" \
  "v_mfma_f32_16x16x32_bf16 v[88:91], a[152:155], a[12:15], v[88:91]\n" \
  "buffer_load_dwordx4 a[220:223], v223, s[60:63], s88 offen offset:1024 sc1\n" \
  "v_mfma_f32_16x16x32_bf16 v[76:79], a[156:159], a[8:11], v[76:79]\n" \
  "v_mfma_f32_16x16x32_bf16 v[92:95], a[156:159], a[12:15], v[92:95]\n" \
  "buffer_load_dwordx2 v[240:241], v242, s[68:71], 0 offen sc1\n" \
  "ds_read_b64_tr_b16 a[84:85], v233 offset:53248\n" \
  "ds_read_b64_tr_b16 a[86:87], v233 offset:55296\n" \
  "ds_read_b64_tr_b16 a[88:89], v234 offset:53248\n" \
  "ds_read_b64_tr_b16 a[90:91], v234 offset:55296\n" \
  "ds_read_b64_tr_b16 a[92:93], v235 offset:53248\n" \
  "ds_read_b64_tr_b16 a[94:95], v235 offset:55296\n" \
  "s_waitcnt vmcnt(23) lgkmcnt(0)\n" \
  "s_barrier\n" \
  "v_add_u32 v247, 1, v247\n" \
  "buffer_store_dwordx2 v[246:247], v254, s[68:71], s78 offen sc1\n" \
  "; HP [175, 176] ('G', 5)\n" \
  "v_mfma_f32_16x16x32_bf16 v[64:67], a[64:67], a[16:19], v[64:67]\n" \
  "ds_read_b64_tr_b16 a[128:129], v228\n" \
  "ds_read_b64_tr_b16 a[130:131], v228 offset:2048\n" \
  "v_mfma_f32_16x16x32_bf16 v[80:83], a[64:67], a[20:23], v[80:83]\n" \
  "ds_read_b64_tr_b16 a[132:133], v229\n" \
  "ds_read_b64_tr_b16 a[134:135], v229 offset:2048\n" \
  "s_mov_b32 s86, 0x150000\n" \
  "s_add_u32 m0, s74, 0xc000\n" \
  "v_mfma_f32_16x16x32_bf16 v[68:71], a[68:71], a[16:19], v[68:71]\n" \
  "buffer_load_dwordx4 v236, s[36:39], s86 offen lds\n" \
  "v_mfma_f32_16x16x32_bf16 v[84:87], a[68:71], a[20:23], v[84:87]\n" \
  "ds_read_b64_tr_b16 a[136:137], v230\n" \
  "s_mov_b32 s87, 0x16c000\n" \
  "v_mfma_f32_16x16x32_bf16 v[72:75], a[72:75], a[16:19], v[72:75]\n" \
  "ds_read_b64_tr_b16 a[138:139], v230 offset:2048\n" \
  "ds_read_b64_tr_b16 a[140:141], v231\n" \
  "v_mfma_f32_16x16x32_bf16 v[88:91], a[72:75], a[20:23], v[88:91]\n" \
  "ds_read_b64_tr_b16 a[142:143], v231 offset:2048\n" \
  "v_mfma_f32_16x16x32_bf16 v[76:79], a[76:79], a[16:19], v[76:79]\n" \
  "buffer_load_dwordx4 v237, s[36:39], s86 offen offset:1024 lds\n" \
  "v_mfma_f32_16x16x32_bf16 v[92:95], a[76:79], a[20:23], v[92:95]\n" \
  "ds_read_b64_tr_b16 a[144:145], v228 offset:4096\n" \
  "ds_read_b64_tr_b16 a[146:147], v228 offset:6144\n" \
  "v_mfma_f32_16x16x32_bf16 v[64:67], a[80:83], a[24:27], v[64:67]\n" \
  "ds_read_b64_tr_b16 a[148:149], v229 offset:4096\n" \
  "v_mfma_f32_16x16x32_bf16 v[80:83], a[80:83], a[28:31], v[80:83]\n" \
  "ds_read_b64_tr_b16 a[150:151], v229 offset:6144\n" \
  "ds_read_b64_tr_b16 a[152:153], v230 offset:4096\n" \
  "v_mfma_f32_16x16x32_bf16 v[68:71], a[84:87], a[24:27], v[68:71]\n" \
  "buffer_load_dwordx4 v238, s[36:39], s86 offen offset:2048 lds\n" \
  "v_mfma_f32_16x16x32_bf16 v[84:87], a[84:87], a[28:31], v[84:87]\n" \
  "ds_read_b64_tr_b16 a[154:155], v230 offset:6144\n" \
  "v_mfma_f32_16x16x32_bf16 v[72:75], a[88:91], a[24:27], v[72:75]\n" \
  "ds_read_b64_tr_b16 a[156:157], v231 offset:4096\n" \
  "ds_read_b64_tr_b16 a[158:159], v231 offset:6144\n" \
  "v_mfma_f32_16x16x32_bf16 v[88:91], a[88:91], a[28:31], v[88:91]\n" \
  "ds_read_b64_tr_b16 a[64:65], v232\n" \
  "s_waitcnt lgkmcnt(7)\n" \
  "v_mfma_f32_16x16x32_bf16 v[76:79], a[92:95], a[24:27], v[76:79]\n" \
  "buffer_load_dwordx4 v239, s[36:39], s86 offen offset:3072 lds\n" \
  "v_mfma_f32_16x16x32_bf16 v[92:95], a[92:95], a[28:31], v[92:95]\n" \
  "ds_read_b64_tr_b16 a[66:67], v232 offset:2048\n" \
  "ds_read_b64_tr_b16 a[68:69], v233\n" \
  "v_mfma_f32_16x16x32_bf16 v[64:67], a[128:131], a[32:35], v[64:67]\n" \
  "ds_read_b64_tr_b16 a[70:71], v233 offset:2048\n" \
  "s_add_u32 m0, s75, 0xc000\n" \
  "v_mfma_f32_16x16x32_bf16 v[80:83], a[128:131], a[36:39], v[80:83]\n" \
  "ds_read_b64_tr_b16 a[72:73], v234\n" \
  "ds_read_b64_tr_b16 a[74:75], v234 offset:2048\n" \
  "v_mfma_f32_16x16x32_bf16 v[68:71], a[132:135], a[32:35], v[68:71]\n" \
  "buffer_load_dwordx4 v236, s[36:39], s87 offen lds\n" \
  "v_mfma_f32_16x16x32_bf16 v[84:87], a[132:135], a[36:39], v[84:87]\n" \
  "ds_read_b64_tr_b16 a[76:77], v235\n" \
  "v_mfma_f32_16x16x32_bf16 v[72:75], a[136:139], a[32:35], v[72:75]\n" \
  "ds_read_b64_tr_b16 a[78:79], v235 offset:2048\n" \
  "ds_read_b64_tr_b16 a[80:81], v232 offset:4096\n" \
  "v_mfma_f32_16x16x32_bf16 v[88:91], a[136:139], a[36:39], v[88:91]\n" \
  "ds_read_b64_tr_b16 a[82:83], v232 offset:6144\n" \
  "v_mfma_f32_16x16x32_bf16 v[76:79], a[140:143], a[32:35], v[76:79]\n" \
  "buffer_load_dwordx4 v237, s[36:39], s87 offen offset:1024 lds\n" \
  "v_mfma_f32_16x16x32_bf16 v[92:95], a[140:143], a[36:39], v[92:95]\n" \
  "ds_read_b64_tr_b16 a[84:85], v233 offset:4096\n" \
  "ds_read_b64_tr_b16 a[86:87], v233 offset:6144\n" \
  "v_mfma_f32_16x16x32_bf16 v[64:67], a[144:147], a[40:43], v[64:67]\n" \
  "ds_read_b64_tr_b16 a[88:89], v234 offset:4096\n" \
  "s_waitcnt lgkmcnt(7)\n" \
  "v_mfma_f32_16x16x32_bf16 v[80:83], a[144:147], a[44:47], v[80:83]\n" \
  "ds_read_b64_tr_b16 a[90:91], v234 offset:6144\n" \
  "ds_read_b64_tr_b16 a[92:93], v235 offset:4096\n" \
  "v_mfma_f32_16x16x32_bf16 v[68:71], a[148:151], a[40:43], v[68:71]\n" \
  "buffer_load_dwordx4 v238, s[36:39], s87 offen offset:2048 lds\n" \
  "v_mfma_f32_16x16x32_bf16 v[84:87], a[148:151], a[44:47], v[84:87]\n" \
  "ds_read_b64_tr_b16 a[94:95], v235 offset:6144\n" \
  "v_mfma_f32_16x16x32_bf16 v[72:75], a[152:155], a[40:43], v[72:75]\n" \
  "v_mfma_f32_16x16x32_bf16 v[88:91], a[152:155], a[44:47], v[88:91]\n" \
  "v_mfma_f32_16x16x32_bf16 v[76:79], a[156:159], a[40:43], v[76:79]\n" \
  "buffer_load_dwordx4 v239, s[36:39], s87 offen offset:3072 lds\n" \
  "v_mfma_f32_16x16x32_bf16 v[92:95], a[156:159], a[44:47], v[92:95]\n" \
  "s_waitcnt vmcnt(9) lgkmcnt(0)\n" \
  "s_barrier\n" \
  "s_and_b32 s90, %6, 7\n" \
  "s_add_u32 s90, s90, s80\n" \
  "s_add_u32 s90, s90, 2\n" \
  "s_mov_b32 s72, 0x3f80\n" \
  "s_mov_b32 s73, 0x3f800000\n" \
  "v_subrev_u32_e64 v251, s90, v241\n" \
  "v_cmp_lt_u32 s[92:93], 8, v251\n" \
  "v_cmp_ne_u32_e64 vcc, s79, v240\n" \
  "s_or_b64 vcc, vcc, s[92:93]\n" \
  "s_cmp_eq_u64 vcc, 0\n" \
  "s_cselect_b32 s82, 1, 0\n" \
  "s_cselect_b32 s46, s46, 0\n" \
  "; HP [177, 178] ('G', 7)\n" \
  "v_mfma_f32_16x16x32_bf16 v[64:67], a[64:67], a[48:51], v[64:67]\n" \
  "ds_read_b64_tr_b16 a[128:129], v228 offset:16384\n" \
  "ds_read_b64_tr_b16 a[130:131], v228 offset:18432\n" \
  "v_mfma_f32_16x16x32_bf16 v[80:83], a[64:67], a[52:55], v[80:83]\n" \
  "ds_read_b64_tr_b16 a[132:133], v229 offset:16384\n" \
  "ds_read_b64_tr_b16 a[134:135], v229 offset:18432\n" \
  "s_mov_b32 s94, 0\n" \
  "v_mfma_f32_16x16x32_bf16 v[68:71], a[68:71], a[48:51], v[68:71]\n" \
  "buffer_load_dwordx4 v[128:131], v250, s[44:47], s94 offen sc1\n" \
  "v_mfma_f32_16x16x32_bf16 v[84:87], a[68:71], a[52:55], v[84:87]\n" \
  "ds_read_b64_tr_b16 a[136:137], v230 offset:16384\n" \
  "v_mfma_f32_16x16x32_bf16 v[72:75], a[72:75], a[48:51], v[72:75]\n" \
  "ds_read_b64_tr_b16 a[138:139], v230 offset:18432\n" \
  "ds_read_b64_tr_b16 a[140:141], v231 offset:16384\n" \
  "v_mfma_f32_16x16x32_bf16 v[88:91], a[72:75], a[52:55], v[88:91]\n" \
  "ds_read_b64_tr_b16 a[142:143], v231 offset:18432\n" \
  "v_mfma_f32_16x16x32_bf16 v[76:79], a[76:79], a[48:51], v[76:79]\n" \
  "buffer_load_dwordx4 v[0:3], v250, s[44:47], s94 offen offset:1024 sc1\n" \
  "v_mfma_f32_16x16x32_bf16 v[92:95], a[76:79], a[52:55], v[92:95]\n" \
  "ds_read_b64_tr_b16 a[144:145], v228 offset:20480\n" \
  "ds_read_b64_tr_b16 a[146:147], v228 offset:22528\n" \
  "v_mfma_f32_16x16x32_bf16 v[64:67], a[80:83], a[56:59], v[64:67]\n" \
  "ds_read_b64_tr_b16 a[148:149], v229 offset:20480\n" \
  "s_mov_b32 s94, 0x3800\n" \
  "v_mfma_f32_16x16x32_bf16 v[80:83], a[80:83], a[60:63], v[80:83]\n" \
  "ds_read_b64_tr_b16 a[150:151], v229 offset:22528\n" \
  "ds_read_b64_tr_b16 a[152:153], v230 offset:20480\n" \
  "v_mfma_f32_16x16x32_bf16 v[68:71], a[84:87], a[56:59], v[68:71]\n" \
  "buffer_load_dwordx4 v[132:135], v250, s[44:47], s94 offen sc1\n" \
  "v_mfma_f32_16x16x32_bf16 v[84:87], a[84:87], a[60:63], v[84:87]\n" \
  "ds_read_b64_tr_b16 a[154:155], v230 offset:22528\n" \
  "v_mfma_f32_16x16x32_bf16 v[72:75], a[88:91], a[56:59], v[72:75]\n" \
  "ds_read_b64_tr_b16 a[156:157], v231 offset:20480\n" \
  "ds_read_b64_tr_b16 a[158:159], v231 offset:22528\n" \
  "v_mfma_f32_16x16x32_bf16 v[88:91], a[88:91], a[60:63], v[88:91]\n" \
  "ds_read_b64_tr_b16 a[64:65], v232 offset:16384\n" \
  "s_waitcnt lgkmcnt(7)\n" \
  "v_mfma_f32_16x16x32_bf16 v[76:79], a[92:95], a[56:59], v[76:79]\n" \
  "buffer_load_dwordx4 v[4:7], v250, s[44:47], s94 offen offset:1024 sc1\n" \
  "v_mfma_f32_16x16x32_bf16 v[92:95], a[92:95], a[60:63], v[92:95]\n" \
  "ds_read_b64_tr_b16 a[66:67], v232 offset:18432\n" \
  "ds_read_b64_tr_b16 a[68:69], v233 offset:16384\n" \
  "v_mfma_f32_16x16x32_bf16 v[64:67], a[128:131], a[160:163], v[64:67]\n" \
  "ds_read_b64_tr_b16 a[70:71], v233 offset:18432\n" \
  "s_mov_b32 s94, 0x7000\n" \
  "v_mfma_f32_16x16x32_bf16 v[80:83], a[128:131], a[164:167], v[80:83]\n" \
  "ds_read_b64_tr_b16 a[72:73], v234 offset:16384\n" \
  "ds_read_b64_tr_b16 a[74:75], v234 offset:18432\n" \
  "v_mfma_f32_16x16x32_bf16 v[68:71], a[132:135], a[160:163], v[68:71]\n" \
  "buffer_load_dwordx4 v[136:139], v250, s[44:47], s94 offen sc1\n" \
  "v_mfma_f32_16x16x32_bf16 v[84:87], a[132:135], a[164:167], v[84:87]\n" \
  "ds_read_b64_tr_b16 a[76:77], v235 offset:16384\n" \
  "v_mfma_f32_16x16x32_bf16 v[72:75], a[136:139], a[160:163], v[72:75]\n" \
  "ds_read_b64_tr_b16 a[78:79], v235 offset:18432\n" \
  "ds_read_b64_tr_b16 a[80:81], v232 offset:20480\n" \
  "v_mfma_f32_16x16x32_bf16 v[88:91], a[136:139], a[164:167], v[88:91]\n" \
  "ds_read_b64_tr_b16 a[82:83], v232 offset:22528\n" \
  "s_waitcnt lgkmcnt(7)\n" \
  "v_mfma_f32_16x16x32_bf16 v[76:79], a[140:143], a[160:163], v[76:79]\n" \
  "buffer_load_dwordx4 v[8:11], v250, s[44:47], s94 offen offset:1024 sc1\n" \
  "v_mfma_f32_16x16x32_bf16 v[92:95], a[140:143], a[164:167], v[92:95]\n" \
  "ds_read_b64_tr_b16 a[84:85], v233 offset:20480\n" \
  "ds_read_b64_tr_b16 a[86:87], v233 offset:22528\n" \
  "v_mfma_f32_16x16x32_bf16 v[64:67], a[144:147], a[168:171], v[64:67]\n" \
  "ds_read_b64_tr_b16 a[88:89], v234 offset:20480\n" \
  "s_mov_b32 s94, 0xa800\n" \
  "v_mfma_f32_16x16x32_bf16 v[80:83], a[144:147], a[172:175], v[80:83]\n" \
  "ds_read_b64_tr_b16 a[90:91], v234 offset:22528\n" \
  "ds_read_b64_tr_b16 a[92:93], v235 offset:20480\n" \
  "v_mfma_f32_16x16x32_bf16 v[68:71], a[148:151], a[168:171], v[68:71]\n" \
  "buffer_load_dwordx4 v[140:143], v250, s[44:47], s94 offen sc1\n" \
  "v_mfma_f32_16x16x32_bf16 v[84:87], a[148:151], a[172:175], v[84:87]\n" \
  "ds_read_b64_tr_b16 a[94:95], v235 offset:22528\n" \
  "v_mfma_f32_16x16x32_bf16 v[72:75], a[152:155], a[168:171], v[72:75]\n" \
  "v_mfma_f32_16x16x32_bf16 v[88:91], a[152:155], a[172:175], v[88:91]\n" \
  "v_mfma_f32_16x16x32_bf16 v[76:79], a[156:159], a[168:171], v[76:79]\n" \
  "buffer_load_dwordx4 v[12:15], v250, s[44:47], s94 offen offset:1024 sc1\n" \
  "v_mfma_f32_16x16x32_bf16 v[92:95], a[156:159], a[172:175], v[92:95]\n" \
  "s_waitcnt vmcnt(17) lgkmcnt(0)\n" \
  "s_barrier\n" \
  "; HP [179, 180] ('G', 9)\n" \
  "v_mfma_f32_16x16x32_bf16 v[64:67], a[64:67], a[176:179], v[64:67]\n" \
  "ds_read_b64_tr_b16 a[128:129], v228 offset:32768\n" \
  "ds_read_b64_tr_b16 a[130:131], v228 offset:34816\n" \
  "v_mfma_f32_16x16x32_bf16 v[80:83], a[64:67], a[180:183], v[80:83]\n" \
  "ds_read_b64_tr_b16 a[132:133], v229 offset:32768\n" \
  "ds_read_b64_tr_b16 a[134:135], v229 offset:34816\n" \
  "s_mov_b32 s94, 0xe000\n" \
  "v_mfma_f32_16x16x32_bf16 v[68:71], a[68:71], a[176:179], v[68:71]\n" \
  "buffer_load_dwordx4 v[144:147], v250, s[44:47], s94 offen sc1\n" \
  "v_mfma_f32_16x16x32_bf16 v[84:87], a[68:71], a[180:183], v[84:87]\n" \
  "ds_read_b64_tr_b16 a[136:137], v230 offset:32768\n" \
  "v_mfma_f32_16x16x32_bf16 v[72:75], a[72:75], a[176:179], v[72:75]\n" \
  "ds_read_b64_tr_b16 a[138:139], v230 offset:34816\n" \
  "ds_read_b64_tr_b16 a[140:141], v231 offset:32768\n" \
  "v_mfma_f32_16x16x32_bf16 v[88:91], a[72:75], a[180:183], v[88:91]\n" \
  "ds_read_b64_tr_b16 a[142:143], v231 offset:34816\n" \
  "v_mfma_f32_16x16x32_bf16 v[76:79], a[76:79], a[176:179], v[76:79]\n" \
  "buffer_load_dwordx4 v[16:19], v250, s[44:47], s94 offen offset:1024 sc1\n" \
  "v_mfma_f32_16x16x32_bf16 v[92:95], a[76:79], a[180:183], v[92:95]\n" \
  "ds_read_b64_tr_b16 a[144:145], v228 offset:36864\n" \
  "ds_read_b64_tr_b16 a[146:147], v228 offset:38912\n" \
  "v_mfma_f32_16x16x32_bf16 v[64:67], a[80:83], a[184:187], v[64:67]\n" \
  "ds_read_b64_tr_b16 a[148:149], v229 offset:36864\n" \
  "s_mov_b32 s94, 0x11800\n" \
  "v_mfma_f32_16x16x32_bf16 v[80:83], a[80:83], a[188:191], v[80:83]\n" \
  "ds_read_b64_tr_b16 a[150:151], v229 offset:38912\n" \
  "ds_read_b64_tr_b16 a[152:153], v230 offset:36864\n" \
  "v_mfma_f32_16x16x32_bf16 v[68:71], a[84:87], a[184:187], v[68:71]\n" \
  "buffer_load_dwordx4 v[148:151], v250, s[44:47], s94 offen sc1\n" \
  "v_mfma_f32_16x16x32_bf16 v[84:87], a[84:87], a[188:191], v[84:87]\n" \
  "ds_read_b64_tr_b16 a[154:155], v230 offset:38912\n" \
  "v_mfma_f32_16x16x32_bf16 v[72:75], a[88:91], a[184:187], v[72:75]\n" \
  "ds_read_b64_tr_b16 a[156:157], v231 offset:36864\n" \
  "ds_read_b64_tr_b16 a[158:159], v231 offset:38912\n" \
  "v_mfma_f32_16x16x32_bf16 v[88:91], a[88:91], a[188:191], v[88:91]\n" \
  "ds_read_b64_tr_b16 a[64:65], v232 offset:32768\n" \
  "s_waitcnt lgkmcnt(7)\n" \
  "v_mfma_f32_16x16x32_bf16 v[76:79], a[92:95], a[184:187], v[76:79]\n" \
  "buffer_load_dwordx4 v[20:23], v250, s[44:47], s94 offen offset:1024 sc1\n" \
  "v_mfma_f32_16x16x32_bf16 v[92:95], a[92:95], a[188:191], v[92:95]\n" \
  "ds_read_b64_tr_b16 a[66:67], v232 offset:34816\n" \
  "ds_read_b64_tr_b16 a[68:69], v233 offset:32768\n" \
  "v_mfma_f32_16x16x32_bf16 v[64:67], a[128:131], a[96:99], v[64:67]\n" \
  "ds_read_b64_tr_b16 a[70:71], v233 offset:34816\n" \
  "s_mov_b32 s94, 0x15000\n" \
  "v_mfma_f32_16x16x32_bf16 v[80:83], a[128:131], a[100:103], v[80:83]\n" \
  "ds_read_b64_tr_b16 a[72:73], v234 offset:32768\n" \
  "ds_read_b64_tr_b16 a[74:75], v234 offset:34816\n" \
  "v_mfma_f32_16x16x32_bf16 v[68:71], a[132:135], a[96:99], v[68:71]\n" \
  "buffer_load_dwordx4 v[152:155], v250, s[44:47], s94 offen sc1\n" \
  "v_mfma_f32_16x16x32_bf16 v[84:87], a[132:135], a[100:103], v[84:87]\n" \
  "ds_read_b64_tr_b16 a[76:77], v235 offset:32768\n" \
  "v_mfma_f32_16x16x32_bf16 v[72:75], a[136:139], a[96:99], v[72:75]\n" \
  "ds_read_b64_tr_b16 a[78:79], v235 offset:34816\n" \
  "ds_read_b64_tr_b16 a[80:81], v232 offset:36864\n" \
  "v_mfma_f32_16x16x32_bf16 v[88:91], a[136:139], a[100:103], v[88:91]\n" \
  "ds_read_b64_tr_b16 a[82:83], v232 offset:38912\n" \
  "s_waitcnt lgkmcnt(7)\n" \
  "v_mfma_f32_16x16x32_bf16 v[76:79], a[140:143], a[96:99], v[76:79]\n" \
  "buffer_load_dwordx4 v[24:27], v250, s[44:47], s94 offen offset:1024 sc1\n" \
  "v_mfma_f32_16x16x32_bf16 v[92:95], a[140:143], a[100:103], v[92:95]\n" \
  "ds_read_b64_tr_b16 a[84:85], v233 offset:36864\n" \
  "ds_read_b64_tr_b16 a[86:87], v233 offset:38912\n" \
  "v_mfma_f32_16x16x32_bf16 v[64:67], a[144:147], a[104:107], v[64:67]\n" \
  "ds_read_b64_tr_b16 a[88:89], v234 offset:36864\n" \
  "s_mov_b32 s94, 0x18800\n" \
  "v_mfma_f32_16x16x32_bf16 v[80:83], a[144:147], a[108:111], v[80:83]\n" \
  "ds_read_b64_tr_b16 a[90:91], v234 offset:38912\n" \
  "ds_read_b64_tr_b16 a[92:93], v235 offset:36864\n" \
  "v_mfma_f32_16x16x32_bf16 v[68:71], a[148:151], a[104:107], v[68:71]\n" \
  "buffer_load_dwordx4 v[156:159], v250, s[44:47], s94 offen sc1\n" \
  "v_mfma_f32_16x16x32_bf16 v[84:87], a[148:151], a[108:111], v[84:87]\n" \
  "ds_read_b64_tr_b16 a[94:95], v235 offset:38912\n" \
  "v_mfma_f32_16x16x32_bf16 v[72:75], a[152:155], a[104:107], v[72:75]\n" \
  "v_mfma_f32_16x16x32_bf16 v[88:91], a[152:155], a[108:111], v[88:91]\n" \
  "v_mfma_f32_16x16x32_bf16 v[76:79], a[156:159], a[104:107], v[76:79]\n" \
  "buffer_load_dwordx4 v[28:31], v250, s[44:47], s94 offen offset:1024 sc1\n" \
  "v_mfma_f32_16x16x32_bf16 v[92:95], a[156:159], a[108:111], v[92:95]\n" \
  "s_waitcnt vmcnt(16) lgkmcnt(0)\n" \
  "s_barrier\n" \
  "; HP [181, 182] ('G', 11)\n" \
  "v_mfma_f32_16x16x32_bf16 v[64:67], a[64:67], a[112:115], v[64:67]\n" \
  "ds_read_b64_tr_b16 a[128:129], v228 offset:49152\n" \
  "ds_read_b64_tr_b16 a[130:131], v228 offset:51200\n" \
  "v_mfma_f32_16x16x32_bf16 v[80:83], a[64:67], a[116:119], v[80:83]\n" \
  "ds_read_b64_tr_b16 a[132:133], v229 offset:49152\n" \
  "ds_read_b64_tr_b16 a[134:135], v229 offset:51200\n" \
  "s_mov_b32 s94, 0x1c000\n" \
  "v_mfma_f32_16x16x32_bf16 v[68:71], a[68:71], a[112:115], v[68:71]\n" \
  "buffer_load_dwordx4 v[160:163], v250, s[44:47], s94 offen sc1\n" \
  "v_mfma_f32_16x16x32_bf16 v[84:87], a[68:71], a[116:119], v[84:87]\n" \
  "ds_read_b64_tr_b16 a[136:137], v230 offset:49152\n" \
  "v_mfma_f32_16x16x32_bf16 v[72:75], a[72:75], a[112:115], v[72:75]\n" \
  "ds_read_b64_tr_b16 a[138:139], v230 offset:51200\n" \
  "ds_read_b64_tr_b16 a[140:141], v231 offset:49152\n" \
  "v_mfma_f32_16x16x32_bf16 v[88:91], a[72:75], a[116:119], v[88:91]\n" \
  "ds_read_b64_tr_b16 a[142:143], v231 offset:51200\n" \
  "v_mfma_f32_16x16x32_bf16 v[76:79], a[76:79], a[112:115], v[76:79]\n" \
  "buffer_load_dwordx4 v[32:35], v250, s[44:47], s94 offen offset:1024 sc1\n" \
  "v_mfma_f32_16x16x32_bf16 v[92:95], a[76:79], a[116:119], v[92:95]\n" \
  "ds_read_b64_tr_b16 a[144:145], v228 offset:53248\n" \
  "ds_read_b64_tr_b16 a[146:147], v228 offset:55296\n" \
  "v_mfma_f32_16x16x32_bf16 v[64:67], a[80:83], a[120:123], v[64:67]\n" \
  "ds_read_b64_tr_b16 a[148:149], v229 offset:53248\n" \
  "s_mov_b32 s94, 0x1f800\n" \
  "v_mfma_f32_16x16x32_bf16 v[80:83], a[80:83], a[124:127], v[80:83]\n" \
  "ds_read_b64_tr_b16 a[150:151], v229 offset:55296\n" \
  "ds_read_b64_tr_b16 a[152:153], v230 offset:53248\n" \
  "v_mfma_f32_16x16x32_bf16 v[68:71], a[84:87], a[120:123], v[68:71]\n" \
  "buffer_load_dwordx4 v[164:167], v250, s[44:47], s94 offen sc1\n" \
  "v_mfma_f32_16x16x32_bf16 v[84:87], a[84:87], a[124:127], v[84:87]\n" \
  "ds_read_b64_tr_b16 a[154:155], v230 offset:55296\n" \
  "v_mfma_f32_16x16x32_bf16 v[72:75], a[88:91], a[120:123], v[72:75]\n" \
  "ds_read_b64_tr_b16 a[156:157], v231 offset:53248\n" \
  "ds_read_b64_tr_b16 a[158:159], v231 offset:55296\n" \
  "v_mfma_f32_16x16x32_bf16 v[88:91], a[88:91], a[124:127], v[88:91]\n" \
  "ds_read_b64_tr_b16 a[64:65], v232 offset:49152\n" \
  "s_waitcnt lgkmcnt(7)\n" \
  "v_mfma_f32_16x16x32_bf16 v[76:79], a[92:95], a[120:123], v[76:79]\n" \
  "buffer_load_dwordx4 v[36:39], v250, s[44:47], s94 offen offset:1024 sc1\n" \
  "v_mfma_f32_16x16x32_bf16 v[92:95], a[92:95], a[124:127], v[92:95]\n" \
  "ds_read_b64_tr_b16 a[66:67], v232 offset:51200\n" \
  "ds_read_b64_tr_b16 a[68:69], v233 offset:49152\n" \
  "v_mfma_f32_16x16x32_bf16 v[64:67], a[128:131], a[192:195], v[64:67]\n" \
  "ds_read_b64_tr_b16 a[70:71], v233 offset:51200\n" \
  "s_mov_b32 s94, 0x23000\n" \
  "v_mfma_f32_16x16x32_bf16 v[80:83], a[128:131], a[196:199], v[80:83]\n" \
  "ds_read_b64_tr_b16 a[72:73], v234 offset:49152\n" \
  "ds_read_b64_tr_b16 a[74:75], v234 offset:51200\n" \
  "v_mfma_f32_16x16x32_bf16 v[68:71], a[132:135], a[192:195], v[68:71]\n" \
  "buffer_load_dwordx4 v[168:171], v250, s[44:47], s94 offen sc1\n" \
  "v_mfma_f32_16x16x32_bf16 v[84:87], a[132:135], a[196:199], v[84:87]\n" \
  "ds_read_b64_tr_b16 a[76:77], v235 offset:49152\n" \
  "v_mfma_f32_16x16x32_bf16 v[72:75], a[136:139], a[192:195], v[72:75]\n" \
  "ds_read_b64_tr_b16 a[78:79], v235 offset:51200\n" \
  "ds_read_b64_tr_b16 a[80:81], v232 offset:53248\n" \
  "v_mfma_f32_16x16x32_bf16 v[88:91], a[136:139], a[196:199], v[88:91]\n" \
  "ds_read_b64_tr_b16 a[82:83], v232 offset:55296\n" \
  "s_waitcnt lgkmcnt(7)\n" \
  "v_mfma_f32_16x16x32_bf16 v[76:79], a[140:143], a[192:195], v[76:79]\n" \
  "buffer_load_dwordx4 v[40:43], v250, s[44:47], s94 offen offset:1024 sc1\n" \
  "v_mfma_f32_16x16x32_bf16 v[92:95], a[140:143], a[196:199], v[92:95]\n" \
  "ds_read_b64_tr_b16 a[84:85], v233 offset:53248\n" \
  "ds_read_b64_tr_b16 a[86:87], v233 offset:55296\n" \
  "v_mfma_f32_16x16x32_bf16 v[64:67], a[144:147], a[200:203], v[64:67]\n" \
  "ds_read_b64_tr_b16 a[88:89], v234 offset:53248\n" \
  "s_mov_b32 s94, 0x26800\n" \
  "v_mfma_f32_16x16x32_bf16 v[80:83], a[144:147], a[204:207], v[80:83]\n" \
  "ds_read_b64_tr_b16 a[90:91], v234 offset:55296\n" \
  "ds_read_b64_tr_b16 a[92:93], v235 offset:53248\n" \
  "v_mfma_f32_16x16x32_bf16 v[68:71], a[148:151], a[200:203], v[68:71]\n" \
  "buffer_load_dwordx4 v[172:175], v250, s[44:47], s94 offen sc1\n" \
  "v_mfma_f32_16x16x32_bf16 v[84:87], a[148:151], a[204:207], v[84:87]\n" \
  "ds_read_b64_tr_b16 a[94:95], v235 offset:55296\n" \
  "v_mfma_f32_16x16x32_bf16 v[72:75], a[152:155], a[200:203], v[72:75]\n" \
  "v_mfma_f32_16x16x32_bf16 v[88:91], a[152:155], a[204:207], v[88:91]\n" \
  "v_mfma_f32_16x16x32_bf16 v[76:79], a[156:159], a[200:203], v[76:79]\n" \
  "buffer_load_dwordx4 v[44:47], v250, s[44:47], s94 offen offset:1024 sc1\n" \
  "v_mfma_f32_16x16x32_bf16 v[92:95], a[156:159], a[204:207], v[92:95]\n" \
  "s_waitcnt vmcnt(24) lgkmcnt(0)\n" \
  "; HP [183] ('G', 13)\n" \
  "v_mfma_f32_16x16x32_bf16 v[64:67], a[64:67], a[208:211], v[64:67]\n" \
  "s_mov_b32 s94, 0x2a000\n" \
  "v_mfma_f32_16x16x32_bf16 v[80:83], a[64:67], a[212:215], v[80:83]\n" \
  "buffer_load_dwordx4 v[176:179], v250, s[44:47], s94 offen sc1\n" \
  "v_mfma_f32_16x16x32_bf16 v[68:71], a[68:71], a[208:211], v[68:71]\n" \
  "v_mfma_f32_16x16x32_bf16 v[84:87], a[68:71], a[212:215], v[84:87]\n" \
  "buffer_load_dwordx4 v[48:51], v250, s[44:47], s94 offen offset:1024 sc1\n" \
  "v_mfma_f32_16x16x32_bf16 v[72:75], a[72:75], a[208:211], v[72:75]\n" \
  "s_mov_b32 s94, 0x2d800\n" \
  "v_mfma_f32_16x16x32_bf16 v[88:91], a[72:75], a[212:215], v[88:91]\n" \
  "buffer_load_dwordx4 v[180:183], v250, s[44:47], s94 offen sc1\n" \
  "v_mfma_f32_16x16x32_bf16 v[76:79], a[76:79], a[208:211], v[76:79]\n" \
  "v_mfma_f32_16x16x32_bf16 v[92:95], a[76:79], a[212:215], v[92:95]\n" \
  "buffer_load_dwordx4 v[52:55], v250, s[44:47], s94 offen offset:1024 sc1\n" \
  "v_mfma_f32_16x16x32_bf16 v[64:67], a[80:83], a[216:219], v[64:67]\n" \
  "s_mov_b32 s94, 0x31000\n" \
  "v_mfma_f32_16x16x32_bf16 v[80:83], a[80:83], a[220:223], v[80:83]\n" \
  "buffer_load_dwordx4 v[184:187], v250, s[44:47], s94 offen sc1\n" \
  "v_mfma_f32_16x16x32_bf16 v[68:71], a[84:87], a[216:219], v[68:71]\n" \
  "v_mfma_f32_16x16x32_bf16 v[84:87], a[84:87], a[220:223], v[84:87]\n" \
  "buffer_load_dwordx4 v[56:59], v250, s[44:47], s94 offen offset:1024 sc1\n" \
  "v_mfma_f32_16x16x32_bf16 v[72:75], a[88:91], a[216:219], v[72:75]\n" \
  "s_mov_b32 s94, 0x34800\n" \
  "v_mfma_f32_16x16x32_bf16 v[88:91], a[88:91], a[220:223], v[88:91]\n" \
  "buffer_load_dwordx4 v[188:191], v250, s[44:47], s94 offen sc1\n" \
  "v_mfma_f32_16x16x32_bf16 v[76:79], a[92:95], a[216:219], v[76:79]\n" \
  "v_mfma_f32_16x16x32_bf16 v[92:95], a[92:95], a[220:223], v[92:95]\n" \
  "buffer_load_dwordx4 v[60:63], v250, s[44:47], s94 offen offset:1024 sc1\n" \
  "s_waitcnt vmcnt(32) lgkmcnt(0)\n" \
  "s_nop 7\n" \
  "ds_write_b128 v208, v[80:83]\n" \
  "ds_write_b128 v208, v[84:87] offset:1024\n" \
  "ds_write_b128 v208, v[88:91] offset:2048\n" \
  "ds_write_b128 v208, v[92:95] offset:3072\n" \
  "v_readlane_b32 s46, v255, 45\n" \
  "s_waitcnt lgkmcnt(0)\n" \
  "s_barrier\n" \
  "ds_read_b128 v[80:83], v209\n" \
  "ds_read_b128 v[84:87], v209 offset:1024\n" \
  "ds_read_b128 v[88:91], v209 offset:2048\n" \
  "ds_read_b128 v[92:95], v209 offset:3072\n" \
  "s_cmp_eq_u32 s82, 0\n" \
  "s_cbranch_scc1 L_late_%=\n" \
  "L_lateb_%=:\n" \
  "s_waitcnt lgkmcnt(0)\n" \
  "v_add_f32 v64, v64, v80\n" \
  "v_add_f32 v65, v65, v81\n" \
  "v_add_f32 v66, v66, v82\n" \
  "v_add_f32 v67, v67, v83\n" \
  "v_add_f32 v68, v68, v84\n" \
  "v_add_f32 v69, v69, v85\n" \
  "v_add_f32 v70, v70, v86\n" \
  "v_add_f32 v71, v71, v87\n" \
  "v_add_f32 v72, v72, v88\n" \
  "v_add_f32 v73, v73, v89\n" \
  "v_add_f32 v74, v74, v90\n" \
  "v_add_f32 v75, v75, v91\n" \
  "v_add_f32 v76, v76, v92\n" \
  "v_add_f32 v77, v77, v93\n" \
  "v_add_f32 v78, v78, v94\n" \
  "v_add_f32_e64 v79, v79, v95\n" \
  "v_mul_f32 v112, 0xbfb8aa3b, v64\n" \
  "v_mul_f32 v113, 0xbfb8aa3b, v65\n" \
  "v_mul_f32 v114, 0xbfb8aa3b, v66\n" \
  "v_mul_f32 v115, 0xbfb8aa3b, v67\n" \
  "v_mul_f32 v116, 0xbfb8aa3b, v68\n" \
  "v_mul_f32 v117, 0xbfb8aa3b, v69\n" \
  "v_mul_f32 v118, 0xbfb8aa3b, v70\n" \
  "v_mul_f32 v119, 0xbfb8aa3b, v71\n" \
  "v_mul_f32 v120, 0xbfb8aa3b, v72\n" \
  "v_mul_f32 v121, 0xbfb8aa3b, v73\n" \
  "v_mul_f32 v122, 0xbfb8aa3b, v74\n" \
  "v_mul_f32 v123, 0xbfb8aa3b, v75\n" \
  "v_mul_f32 v124, 0xbfb8aa3b, v76\n" \
  "v_mul_f32 v125, 0xbfb8aa3b, v77\n" \
  "v_mul_f32 v126, 0xbfb8aa3b, v78\n" \
  "v_mul_f32 v127, 0xbfb8aa3b, v79\n" \
  "v_exp_f32 v112, v112\n" \
  "v_exp_f32 v113, v113\n" \
  "v_exp_f32 v114, v114\n" \
  "v_exp_f32 v115, v115\n" \
  "v_exp_f32 v116, v116\n" \
  "v_exp_f32 v117, v117\n" \
  "v_exp_f32 v118, v118\n" \
  "v_exp_f32 v119, v119\n" \
  "v_exp_f32 v120, v120\n" \
  "v_exp_f32 v121, v121\n" \
  "v_exp_f32 v122, v122\n" \
  "v_exp_f32 v123, v123\n" \
  "v_exp_f32 v124, v124\n" \
  "v_exp_f32 v125, v125\n" \
  "v_exp_f32 v126, v126\n" \
  "v_exp_f32 v127, v127\n" \
  "v_add_f32 v112, 1.0, v112\n" \
  "v_add_f32 v113, 1.0, v113\n" \
  "v_add_f32 v114, 1.0, v114\n" \
  "v_add_f32 v115, 1.0, v115\n" \
  "v_add_f32 v116, 1.0, v116\n" \
  "v_add_f32 v117, 1.0, v117\n" \
  "v_add_f32 v118, 1.0, v118\n" \
  "v_add_f32 v119, 1.0, v119\n" \
  "v_add_f32 v120, 1.0, v120\n" \
  "v_add_f32 v121, 1.0, v121\n" \
  "v_add_f32 v122, 1.0, v122\n" \
  "v_add_f32 v123, 1.0, v123\n" \
  "v_add_f32 v124, 1.0, v124\n" \
  "v_add_f32 v125, 1.0, v125\n" \
  "v_add_f32 v126, 1.0, v126\n" \
  "v_add_f32 v127, 1.0, v127\n" \
  "v_rcp_f32 v112, v112\n" \
  "v_rcp_f32 v113, v113\n" \
  "v_rcp_f32 v114, v114\n" \
  "v_rcp_f32 v115, v115\n" \
  "v_rcp_f32 v116, v116\n" \
  "v_rcp_f32 v117, v117\n" \
  "v_rcp_f32 v118, v118\n" \
  "v_rcp_f32 v119, v119\n" \
  "v_rcp_f32 v120, v120\n" \
  "v_rcp_f32 v121, v121\n" \
  "v_rcp_f32 v122, v122\n" \
  "v_rcp_f32 v123, v123\n" \
  "v_rcp_f32 v124, v124\n" \
  "v_rcp_f32 v125, v125\n" \
  "v_rcp_f32 v126, v126\n" \
  "v_rcp_f32 v127, v127\n" \
  "v_mul_f32 v64, v64, v112\n" \
  "v_mul_f32 v65, v65, v113\n" \
  "v_mul_f32 v66, v66, v114\n" \
  "v_mul_f32 v67, v67, v115\n" \
  "v_mul_f32 v68, v68, v116\n" \
  "v_mul_f32 v69, v69, v117\n" \
  "v_mul_f32 v70, v70, v118\n" \
  "v_mul_f32 v71, v71, v119\n" \
  "v_mul_f32 v72, v72, v120\n" \
  "v_mul_f32 v73, v73, v121\n" \
  "v_mul_f32 v74, v74, v122\n" \
  "v_mul_f32 v75, v75, v123\n" \
  "v_mul_f32 v76, v76, v124\n" \
  "v_mul_f32 v77, v77, v125\n" \
  "v_mul_f32 v78, v78, v126\n" \
  "v_mul_f32 v79, v79, v127\n" \
  "v_lshlrev_b32_e64 v112, 16, v192\n" \
  "v_and_b32 v113, 0xffff0000, v192\n" \
  "v_lshlrev_b32_e64 v114, 16, v193\n" \
  "v_and_b32 v115, 0xffff0000, v193\n" \
  "v_lshlrev_b32_e64 v116, 16, v194\n" \
  "v_and_b32 v117, 0xffff0000, v194\n" \
  "v_lshlrev_b32_e64 v118, 16, v195\n" \
  "v_and_b32 v119, 0xffff0000, v195\n" \
  "v_lshlrev_b32_e64 v120, 16, v196\n" \
  "v_and_b32 v121, 0xffff0000, v196\n" \
  "v_lshlrev_b32_e64 v122, 16, v197\n" \
  "v_and_b32 v123, 0xffff0000, v197\n" \
  "v_lshlrev_b32_e64 v124, 16, v198\n" \
  "v_and_b32 v125, 0xffff0000, v198\n" \
  "v_lshlrev_b32_e64 v126, 16, v199\n" \
  "v_and_b32 v127, 0xffff0000, v199\n" \
  "s_waitcnt vmcnt(28)\n" \
  "v_dot2_f32_bf16 v96, v128, s72, 0\n" \
  "v_dot2_f32_bf16 v97, v128, s73, 0\n" \
  "v_dot2_f32_bf16 v98, v129, s72, 0\n" \
  "v_dot2_f32_bf16 v99, v129, s73, 0\n" \
  "v_dot2_f32_bf16 v100, v130, s72, 0\n" \
  "v_dot2_f32_bf16 v101, v130, s73, 0\n" \
  "v_dot2_f32_bf16 v102, v131, s72, 0\n" \
  "v_dot2_f32_bf16 v103, v131, s73, 0\n" \
  "v_dot2_f32_bf16 v104, v0, s72, 0\n" \
  "v_dot2_f32_bf16 v105, v0, s73, 0\n" \
  "v_dot2_f32_bf16 v106, v1, s72, 0\n" \
  "v_dot2_f32_bf16 v107, v1, s73, 0\n" \
  "v_dot2_f32_bf16 v108, v2, s72, 0\n" \
  "v_dot2_f32_bf16 v109, v2, s73, 0\n" \
  "v_dot2_f32_bf16 v110, v3, s72, 0\n" \
  "v_dot2_f32_bf16 v111, v3, s73, 0\n" \
  "v_dot2_f32_bf16 v96, v132, s72, v96\n" \
  "v_dot2_f32_bf16 v97, v132, s73, v97\n" \
  "v_dot2_f32_bf16 v98, v133, s72, v98\n" \
  "v_dot2_f32_bf16 v99, v133, s73, v99\n" \
  "v_dot2_f32_bf16 v100, v134, s72, v100\n" \
  "v_dot2_f32_bf16 v101, v134, s73, v101\n" \
  "v_dot2_f32_bf16 v102, v135, s72, v102\n" \
  "v_dot2_f32_bf16 v103, v135, s73, v103\n" \
  "v_dot2_f32_bf16 v104, v4, s72, v104\n" \
  "v_dot2_f32_bf16 v105, v4, s73, v105\n" \
  "v_dot2_f32_bf16 v106, v5, s72, v106\n" \
  "v_dot2_f32_bf16 v107, v5, s73, v107\n" \
  "v_dot2_f32_bf16 v108, v6, s72, v108\n" \
  "v_dot2_f32_bf16 v109, v6, s73, v109\n" \
  "v_dot2_f32_bf16 v110, v7, s72, v110\n" \
  "v_dot2_f32_bf16 v111, v7, s73, v111\n" \
  "s_waitcnt vmcnt(24)\n" \
  "v_dot2_f32_bf16 v96, v136, s72, v96\n" \
  "v_dot2_f32_bf16 v97, v136, s73, v97\n" \
  "v_dot2_f32_bf16 v98, v137, s72, v98\n" \
  "v_dot2_f32_bf16 v99, v137, s73, v99\n" \
  "v_dot2_f32_bf16 v100, v138, s72, v100\n" \
  "v_dot2_f32_bf16 v101, v138, s73, v101\n" \
  "v_dot2_f32_bf16 v102, v139, s72, v102\n" \
  "v_dot2_f32_bf16 v103, v139, s73, v103\n" \
  "v_dot2_f32_bf16 v104, v8, s72, v104\n" \
  "v_dot2_f32_bf16 v105, v8, s73, v105\n" \
  "v_dot2_f32_bf16 v106, v9, s72, v106\n" \
  "v_dot2_f32_bf16 v107, v9, s73, v107\n" \
  "v_dot2_f32_bf16 v108, v10, s72, v108\n" \
  "v_dot2_f32_bf16 v109, v10, s73, v109\n" \
  "v_dot2_f32_bf16 v110, v11, s72, v110\n" \
  "v_dot2_f32_bf16 v111, v11, s73, v111\n" \
  "v_dot2_f32_bf16 v96, v140, s72, v96\n" \
  "v_dot2_f32_bf16 v97, v140, s73, v97\n" \
  "v_dot2_f32_bf16 v98, v141, s72, v98\n" \
  "v_dot2_f32_bf16 v99, v141, s73, v99\n" \
  "v_dot2_f32_bf16 v100, v142, s72, v100\n" \
  "v_dot2_f32_bf16 v101, v142, s73, v101\n" \
  "v_dot2_f32_bf16 v102, v143, s72, v102\n" \
  "v_dot2_f32_bf16 v103, v143, s73, v103\n" \
  "v_dot2_f32_bf16 v104, v12, s72, v104\n" \
  "v_dot2_f32_bf16 v105, v12, s73, v105\n" \
  "v_dot2_f32_bf16 v106, v13, s72, v106\n" \
  "v_dot2_f32_bf16 v107, v13, s73, v107\n" \
  "v_dot2_f32_bf16 v108, v14, s72, v108\n" \
  "v_dot2_f32_bf16 v109, v14, s73, v109\n" \
  "v_dot2_f32_bf16 v110, v15, s72, v110\n" \
  "v_dot2_f32_bf16 v111, v15, s73, v111\n" \
  "s_waitcnt vmcnt(20)\n" \
  "v_dot2_f32_bf16 v96, v144, s72, v96\n" \
  "v_dot2_f32_bf16 v97, v144, s73, v97\n" \
  "v_dot2_f32_bf16 v98, v145, s72, v98\n" \
  "v_dot2_f32_bf16 v99, v145, s73, v99\n" \
  "v_dot2_f32_bf16 v100, v146, s72, v100\n" \
  "v_dot2_f32_bf16 v101, v146, s73, v101\n" \
  "v_dot2_f32_bf16 v102, v147, s72, v102\n" \
  "v_dot2_f32_bf16 v103, v147, s73, v103\n" \
  "v_dot2_f32_bf16 v104, v16, s72, v104\n" \
  "v_dot2_f32_bf16 v105, v16, s73, v105\n" \
  "v_dot2_f32_bf16 v106, v17, s72, v106\n" \
  "v_dot2_f32_bf16 v107, v17, s73, v107\n" \
  "v_dot2_f32_bf16 v108, v18, s72, v108\n" \
  "v_dot2_f32_bf16 v109, v18, s73, v109\n" \
  "v_dot2_f32_bf16 v110, v19, s72, v110\n" \
  "v_dot2_f32_bf16 v111, v19, s73, v111\n" \
  "v_dot2_f32_bf16 v96, v148, s72, v96\n" \
  "v_dot2_f32_bf16 v97, v148, s73, v97\n" \
  "v_dot2_f32_bf16 v98, v149, s72, v98\n" \
  "v_dot2_f32_bf16 v99, v149, s73, v99\n" \
  "v_dot2_f32_bf16 v100, v150, s72, v100\n" \
  "v_dot2_f32_bf16 v101, v150, s73, v101\n" \
  "v_dot2_f32_bf16 v102, v151, s72, v102\n" \
  "v_dot2_f32_bf16 v103, v151, s73, v103\n" \
  "v_dot2_f32_bf16 v104, v20, s72, v104\n" \
  "v_dot2_f32_bf16 v105, v20, s73, v105\n" \
  "v_dot2_f32_bf16 v106, v21, s72, v106\n" \
  "v_dot2_f32_bf16 v107, v21, s73, v107\n" \
  "v_dot2_f32_bf16 v108, v22, s72, v108\n" \
  "v_dot2_f32_bf16 v109, v22, s73, v109\n" \
  "v_dot2_f32_bf16 v110, v23, s72, v110\n" \
  "v_dot2_f32_bf16 v111, v23, s73, v111\n" \
  "s_waitcnt vmcnt(16)\n" \
  "v_dot2_f32_bf16 v96, v152, s72, v96\n" \
  "v_dot2_f32_bf16 v97, v152, s73, v97\n" \
  "v_dot2_f32_bf16 v98, v153, s72, v98\n" \
  "v_dot2_f32_bf16 v99, v153, s73, v99\n" \
  "v_dot2_f32_bf16 v100, v154, s72, v100\n" \
  "v_dot2_f32_bf16 v101, v154, s73, v101\n" \
  "v_dot2_f32_bf16 v102, v155, s72, v102\n" \
  "v_dot2_f32_bf16 v103, v155, s73, v103\n" \
  "v_dot2_f32_bf16 v104, v24, s72, v104\n" \
  "v_dot2_f32_bf16 v105, v24, s73, v105\n" \
  "v_dot2_f32_bf16 v106, v25, s72, v106\n" \
  "v_dot2_f32_bf16 v107, v25, s73, v107\n" \
  "v_dot2_f32_bf16 v108, v26, s72, v108\n" \
  "v_dot2_f32_bf16 v109, v26, s73, v109\n" \
  "v_dot2_f32_bf16 v110, v27, s72, v110\n" \
  "v_dot2_f32_bf16 v111, v27, s73, v111\n" \
  "v_dot2_f32_bf16 v96, v156, s72, v96\n" \
  "v_dot2_f32_bf16 v97, v156, s73, v97\n" \
  "v_dot2_f32_bf16 v98, v157, s72, v98\n" \
  "v_dot2_f32_bf16 v99, v157, s73, v99\n" \
  "v_dot2_f32_bf16 v100, v158, s72, v100\n" \
  "v_dot2_f32_bf16 v101, v158, s73, v101\n" \
  "v_dot2_f32_bf16 v102, v159, s72, v102\n" \
  "v_dot2_f32_bf16 v103, v159, s73, v103\n" \
  "v_dot2_f32_bf16 v104, v28, s72, v104\n" \
  "v_dot2_f32_bf16 v105, v28, s73, v105\n" \
  "v_dot2_f32_bf16 v106, v29, s72, v106\n" \
  "v_dot2_f32_bf16 v107, v29, s73, v107\n" \
  "v_dot2_f32_bf16 v108, v30, s72, v108\n" \
  "v_dot2_f32_bf16 v109, v30, s73, v109\n" \
  "v_dot2_f32_bf16 v110, v31, s72, v110\n" \
  "v_dot2_f32_bf16 v111, v31, s73, v111\n" \
  "s_waitcnt vmcnt(12)\n" \
  "v_dot2_f32_bf16 v96, v160, s72, v96\n" \
  "v_dot2_f32_bf16 v97, v160, s73, v97\n" \
  "v_dot2_f32_bf16 v98, v161, s72, v98\n" \
  "v_dot2_f32_bf16 v99, v161, s73, v99\n" \
  "v_dot2_f32_bf16 v100, v162, s72, v100\n" \
  "v_dot2_f32_bf16 v101, v162, s73, v101\n" \
  "v_dot2_f32_bf16 v102, v163, s72, v102\n" \
  "v_dot2_f32_bf16 v103, v163, s73, v103\n" \
  "v_dot2_f32_bf16 v104, v32, s72, v104\n" \
  "v_dot2_f32_bf16 v105, v32, s73, v105\n" \
  "v_dot2_f32_bf16 v106, v33, s72, v106\n" \
  "v_dot2_f32_bf16 v107, v33, s73, v107\n" \
  "v_dot2_f32_bf16 v108, v34, s72, v108\n" \
  "v_dot2_f32_bf16 v109, v34, s73, v109\n" \
  "v_dot2_f32_bf16 v110, v35, s72, v110\n" \
  "v_dot2_f32_bf16 v111, v35, s73, v111\n" \
  "v_dot2_f32_bf16 v96, v164, s72, v96\n" \
  "v_dot2_f32_bf16 v97, v164, s73, v97\n" \
  "v_dot2_f32_bf16 v98, v165, s72, v98\n" \
  "v_dot2_f32_bf16 v99, v165, s73, v99\n" \
  "v_dot2_f32_bf16 v100, v166, s72, v100\n" \
  "v_dot2_f32_bf16 v101, v166, s73, v101\n" \
  "v_dot2_f32_bf16 v102, v167, s72, v102\n" \
  "v_dot2_f32_bf16 v103, v167, s73, v103\n" \
  "v_dot2_f32_bf16 v104, v36, s72, v104\n" \
  "v_dot2_f32_bf16 v105, v36, s73, v105\n" \
  "v_dot2_f32_bf16 v106, v37, s72, v106\n" \
  "v_dot2_f32_bf16 v107, v37, s73, v107\n" \
  "v_dot2_f32_bf16 v108, v38, s72, v108\n" \
  "v_dot2_f32_bf16 v109, v38, s73, v109\n" \
  "v_dot2_f32_bf16 v110, v39, s72, v110\n" \
  "v_dot2_f32_bf16 v111, v39, s73, v111\n" \
  "s_waitcnt vmcnt(8)\n" \
  "v_dot2_f32_bf16 v96, v168, s72, v96\n" \
  "v_dot2_f32_bf16 v97, v168, s73, v97\n" \
  "v_dot2_f32_bf16 v98, v169, s72, v98\n" \
  "v_dot2_f32_bf16 v99, v169, s73, v99\n" \
  "v_dot2_f32_bf16 v100, v170, s72, v100\n" \
  "v_dot2_f32_bf16 v101, v170, s73, v101\n" \
  "v_dot2_f32_bf16 v102, v171, s72, v102\n" \
  "v_dot2_f32_bf16 v103, v171, s73, v103\n" \
  "v_dot2_f32_bf16 v104, v40, s72, v104\n" \
  "v_dot2_f32_bf16 v105, v40, s73, v105\n" \
  "v_dot2_f32_bf16 v106, v41, s72, v106\n" \
  "v_dot2_f32_bf16 v107, v41, s73, v107\n" \
  "v_dot2_f32_bf16 v108, v42, s72, v108\n" \
  "v_dot2_f32_bf16 v109, v42, s73, v109\n" \
  "v_dot2_f32_bf16 v110, v43, s72, v110\n" \
  "v_dot2_f32_bf16 v111, v43, s73, v111\n" \
  "v_dot2_f32_bf16 v96, v172, s72, v96\n" \
  "v_dot2_f32_bf16 v97, v172, s73, v97\n" \
  "v_dot2_f32_bf16 v98, v173, s72, v98\n" \
  "v_dot2_f32_bf16 v99, v173, s73, v99\n" \
  "v_dot2_f32_bf16 v100, v174, s72, v100\n" \
  "v_dot2_f32_bf16 v101, v174, s73, v101\n" \
  "v_dot2_f32_bf16 v102, v175, s72, v102\n" \
  "v_dot2_f32_bf16 v103, v175, s73, v103\n" \
  "v_dot2_f32_bf16 v104, v44, s72, v104\n" \
  "v_dot2_f32_bf16 v105, v44, s73, v105\n" \
  "v_dot2_f32_bf16 v106, v45, s72, v106\n" \
  "v_dot2_f32_bf16 v107, v45, s73, v107\n" \
  "v_dot2_f32_bf16 v108, v46, s72, v108\n" \
  "v_dot2_f32_bf16 v109, v46, s73, v109\n" \
  "v_dot2_f32_bf16 v110, v47, s72, v110\n" \
  "v_dot2_f32_bf16 v111, v47, s73, v111\n" \
  "s_waitcnt vmcnt(4)\n" \
  "v_dot2_f32_bf16 v96, v176, s72, v96\n" \
  "v_dot2_f32_bf16 v97, v176, s73, v97\n" \
  "v_dot2_f32_bf16 v98, v177, s72, v98\n" \
  "v_dot2_f32_bf16 v99, v177, s73, v99\n" \
  "v_dot2_f32_bf16 v100, v178, s72, v100\n" \
  "v_dot2_f32_bf16 v101, v178, s73, v101\n" \
  "v_dot2_f32_bf16 v102, v179, s72, v102\n" \
  "v_dot2_f32_bf16 v103, v179, s73, v103\n" \
  "v_dot2_f32_bf16 v104, v48, s72, v104\n" \
  "v_dot2_f32_bf16 v105, v48, s73, v105\n" \
  "v_dot2_f32_bf16 v106, v49, s72, v106\n" \
  "v_dot2_f32_bf16 v107, v49, s73, v107\n" \
  "v_dot2_f32_bf16 v108, v50, s72, v108\n" \
  "v_dot2_f32_bf16 v109, v50, s73, v109\n" \
  "v_dot2_f32_bf16 v110, v51, s72, v110\n" \
  "v_dot2_f32_bf16 v111, v51, s73, v111\n" \
  "v_dot2_f32_bf16 v96, v180, s72, v96\n" \
  "v_dot2_f32_bf16 v97, v180, s73, v97\n" \
  "v_dot2_f32_bf16 v98, v181, s72, v98\n" \
  "v_dot2_f32_bf16 v99, v181, s73, v99\n" \
  "v_dot2_f32_bf16 v100, v182, s72, v100\n" \
  "v_dot2_f32_bf16 v101, v182, s73, v101\n" \
  "v_dot2_f32_bf16 v102, v183, s72, v102\n" \
  "v_dot2_f32_bf16 v103, v183, s73, v103\n" \
  "v_dot2_f32_bf16 v104, v52, s72, v104\n" \
  "v_dot2_f32_bf16 v105, v52, s73, v105\n" \
  "v_dot2_f32_bf16 v106, v53, s72, v106\n" \
  "v_dot2_f32_bf16 v107, v53, s73, v107\n" \
  "v_dot2_f32_bf16 v108, v54, s72, v108\n" \
  "v_dot2_f32_bf16 v109, v54, s73, v109\n" \
  "v_dot2_f32_bf16 v110, v55, s72, v110\n" \
  "v_dot2_f32_bf16 v111, v55, s73, v111\n" \
  "s_waitcnt vmcnt(0)\n" \
  "v_dot2_f32_bf16 v96, v184, s72, v96\n" \
  "v_dot2_f32_bf16 v97, v184, s73, v97\n" \
  "v_dot2_f32_bf16 v98, v185, s72, v98\n" \
  "v_dot2_f32_bf16 v99, v185, s73, v99\n" \
  "v_dot2_f32_bf16 v100, v186, s72, v100\n" \
  "v_dot2_f32_bf16 v101, v186, s73, v101\n" \
  "v_dot2_f32_bf16 v102, v187, s72, v102\n" \
  "v_dot2_f32_bf16 v103, v187, s73, v103\n" \
  "v_dot2_f32_bf16 v104, v56, s72, v104\n" \
  "v_dot2_f32_bf16 v105, v56, s73, v105\n" \
  "v_dot2_f32_bf16 v106, v57, s72, v106\n" \
  "v_dot2_f32_bf16 v107, v57, s73, v107\n" \
  "v_dot2_f32_bf16 v108, v58, s72, v108\n" \
  "v_dot2_f32_bf16 v109, v58, s73, v109\n" \
  "v_dot2_f32_bf16 v110, v59, s72, v110\n" \
  "v_dot2_f32_bf16 v111, v59, s73, v111\n" \
  "v_dot2_f32_bf16 v96, v188, s72, v96\n" \
  "v_dot2_f32_bf16 v97, v188, s73, v97\n" \
  "v_dot2_f32_bf16 v98, v189, s72, v98\n" \
  "v_dot2_f32_bf16 v99, v189, s73, v99\n" \
  "v_dot2_f32_bf16 v100, v190, s72, v100\n" \
  "v_dot2_f32_bf16 v101, v190, s73, v101\n" \
  "v_dot2_f32_bf16 v102, v191, s72, v102\n" \
  "v_dot2_f32_bf16 v103, v191, s73, v103\n" \
  "v_dot2_f32_bf16 v104, v60, s72, v104\n" \
  "v_dot2_f32_bf16 v105, v60, s73, v105\n" \
  "v_dot2_f32_bf16 v106, v61, s72, v106\n" \
  "v_dot2_f32_bf16 v107, v61, s73, v107\n" \
  "v_dot2_f32_bf16 v108, v62, s72, v108\n" \
  "v_dot2_f32_bf16 v109, v62, s73, v109\n" \
  "v_dot2_f32_bf16 v110, v63, s72, v110\n" \
  "v_dot2_f32_bf16 v111, v63, s73, v111\n" \
  "v_fmac_f32 v112, v96, v64\n" \
  "v_fmac_f32 v113, v97, v65\n" \
  "v_fmac_f32 v114, v98, v66\n" \
  "v_fmac_f32 v115, v99, v67\n" \
  "v_fmac_f32 v116, v100, v68\n" \
  "v_fmac_f32 v117, v101, v69\n" \
  "v_fmac_f32 v118, v102, v70\n" \
  "v_fmac_f32 v119, v103, v71\n" \
  "v_fmac_f32 v120, v104, v72\n" \
  "v_fmac_f32 v121, v105, v73\n" \
  "v_fmac_f32 v122, v106, v74\n" \
  "v_fmac_f32 v123, v107, v75\n" \
  "v_fmac_f32 v124, v108, v76\n" \
  "v_fmac_f32 v125, v109, v77\n" \
  "v_fmac_f32 v126, v110, v78\n" \
  "v_fmac_f32 v127, v111, v79\n" \
  "v_cvt_pk_bf16_f32 v192, v112, v113\n" \
  "v_cvt_pk_bf16_f32 v193, v114, v115\n" \
  "v_cvt_pk_bf16_f32 v194, v116, v117\n" \
  "v_cvt_pk_bf16_f32 v195, v118, v119\n" \
  "buffer_store_dwordx2 v[192:193], v243, s[48:51], 0 offen\n" \
  "buffer_store_dwordx2 v[194:195], v243, s[48:51], 0 offen offset:32\n" \
  "v_cvt_pk_bf16_f32 v196, v120, v121\n" \
  "v_cvt_pk_bf16_f32 v197, v122, v123\n" \
  "v_cvt_pk_bf16_f32 v198, v124, v125\n" \
  "v_cvt_pk_bf16_f32 v199, v126, v127\n" \
  "buffer_store_dwordx2 v[196:197], v243, s[48:51], 0 offen offset:64\n" \
  "buffer_store_dwordx2 v[198:199], v243, s[48:51], 0 offen offset:96\n" \
  "s_branch L_end_%=\n" \
  "L_slowh_%=:\n" \
  "s_mov_b32 s81, 0x40000\n" \
  "L_spin_h_%=:\n" \
  "buffer_load_dwordx2 v[240:241], v242, s[68:71], 0 offen sc1\n" \
  "s_waitcnt vmcnt(0)\n" \
  "v_subrev_u32_e64 v251, s95, v241\n" \
  "v_cmp_lt_u32 s[92:93], 8, v251\n" \
  "v_cmp_ne_u32 vcc, s79, v240\n" \
  "s_or_b64 vcc, vcc, s[92:93]\n" \
  "s_cbranch_vccz L_ok_h_%=\n" \
  "s_sleep 1\n" \
  "s_sub_u32 s81, s81, 1\n" \
  "s_cmp_lg_u32 s81, 0\n" \
  "s_cbranch_scc1 L_spin_h_%=\n" \
  "L_ok_h_%=:\n" \
  "s_branch L_sbackh_%=\n" \
  "L_bump_%=:\n" \
  "s_add_u32 s95, s80, 1\n" \
  "s_mov_b64 s[34:35], 0\n" \
  "v_subrev_u32_e64 v251, s95, v97\n" \
  "v_cmp_lt_u32 s[92:93], 8, v251\n" \
  "v_cmp_ne_u32 vcc, s79, v96\n" \
  "s_or_b64 s[34:35], s[34:35], s[92:93]\n" \
  "s_or_b64 s[34:35], s[34:35], vcc\n" \
  "v_subrev_u32 v251, s95, v99\n" \
  "v_cmp_lt_u32 s[92:93], 8, v251\n" \
  "v_cmp_ne_u32 vcc, s79, v98\n" \
  "s_or_b64 s[34:35], s[34:35], s[92:93]\n" \
  "s_or_b64 s[34:35], s[34:35], vcc\n" \
  "v_subrev_u32 v251, s95, v101\n" \
  "v_cmp_lt_u32 s[92:93], 8, v251\n" \
  "v_cmp_ne_u32 vcc, s79, v100\n" \
  "s_or_b64 s[34:35], s[34:35], s[92:93]\n" \
  "s_or_b64 s[34:35], s[34:35], vcc\n" \
  "v_subrev_u32 v251, s95, v103\n" \
  "v_cmp_lt_u32 s[92:93], 8, v251\n" \
  "v_cmp_ne_u32 vcc, s79, v102\n" \
  "s_or_b64 s[34:35], s[34:35], s[92:93]\n" \
  "s_or_b64 s[34:35], s[34:35], vcc\n" \
  "s_cmp_eq_u64 s[34:35], 0\n" \
  "s_cbranch_scc1 L_bumpok_%=\n" \
  "v_mov_b32 v98, v242\n" \
  "v_mov_b32 v100, v240\n" \
  "v_mov_b32 v101, v241\n" \
  "v_mov_b32_e64 v242, v104\n" \
  "s_mov_b32 s81, 0x40000\n" \
  "L_spin_e0_%=:\n" \
  "buffer_load_dwordx2 v[240:241], v242, s[68:71], 0 offen sc1\n" \
  "s_waitcnt vmcnt(0)\n" \
  "v_subrev_u32 v251, s95, v241\n" \
  "v_cmp_lt_u32 s[92:93], 8, v251\n" \
  "v_cmp_ne_u32_e64 vcc, s79, v240\n" \
  "s_or_b64 vcc, vcc, s[92:93]\n" \
  "s_cbranch_vccz L_ok_e0_%=\n" \
  "s_sleep 1\n" \
  "s_sub_u32 s81, s81, 1\n" \
  "s_cmp_lg_u32 s81, 0\n" \
  "s_cbranch_scc1 L_spin_e0_%=\n" \
  "L_ok_e0_%=:\n" \
  "v_add_u32 v242, 0x8000, v242\n" \
  "s_mov_b32 s81, 0x40000\n" \
  "L_spin_e1_%=:\n" \
  "buffer_load_dwordx2 v[240:241], v242, s[68:71], 0 offen sc1\n" \
  "s_waitcnt vmcnt(0)\n" \
  "v_subrev_u32 v251, s95, v241\n" \
  "v_cmp_lt_u32 s[92:93], 8, v251\n" \
  "v_cmp_ne_u32_e64 vcc, s79, v240\n" \
  "s_or_b64 vcc, vcc, s[92:93]\n" \
  "s_cbranch_vccz L_ok_e1_%=\n" \
  "s_sleep 1\n" \
  "s_sub_u32 s81, s81, 1\n" \
  "s_cmp_lg_u32 s81, 0\n" \
  "s_cbranch_scc1 L_spin_e1_%=\n" \
  "L_ok_e1_%=:\n" \
  "v_add_u32 v242, 0x8000, v242\n" \
  "s_mov_b32 s81, 0x40000\n" \
  "L_spin_e2_%=:\n" \
  "buffer_load_dwordx2 v[240:241], v242, s[68:71], 0 offen sc1\n" \
  "s_waitcnt vmcnt(0)\n" \
  "v_subrev_u32 v251, s95, v241\n" \
  "v_cmp_lt_u32 s[92:93], 8, v251\n" \
  "v_cmp_ne_u32_e64 vcc, s79, v240\n" \
  "s_or_b64 vcc, vcc, s[92:93]\n" \
  "s_cbranch_vccz L_ok_e2_%=\n" \
  "s_sleep 1\n" \
  "s_sub_u32 s81, s81, 1\n" \
  "s_cmp_lg_u32 s81, 0\n" \
  "s_cbranch_scc1 L_spin_e2_%=\n" \
  "L_ok_e2_%=:\n" \
  "v_add_u32 v242, 0x8000, v242\n" \
  "s_mov_b32 s81, 0x40000\n" \
  "L_spin_e3_%=:\n" \
  "buffer_load_dwordx2 v[240:241], v242, s[68:71], 0 offen sc1\n" \
  "s_waitcnt vmcnt(0)\n" \
  "v_subrev_u32 v251, s95, v241\n" \
  "v_cmp_lt_u32 s[92:93], 8, v251\n" \
  "v_cmp_ne_u32_e64 vcc, s79, v240\n" \
  "s_or_b64 vcc, vcc, s[92:93]\n" \
  "s_cbranch_vccz L_ok_e3_%=\n" \
  "s_sleep 1\n" \
  "s_sub_u32 s81, s81, 1\n" \
  "s_cmp_lg_u32 s81, 0\n" \
  "s_cbranch_scc1 L_spin_e3_%=\n" \
  "L_ok_e3_%=:\n" \
  "v_add_u32 v242, 0x8000, v242\n" \
  "v_mov_b32 v242, v98\n" \
  "v_mov_b32 v240, v100\n" \
  "v_mov_b32 v241, v101\n" \
  "L_bumpok_%=:\n" \
  "v_mov_b32 v96, s79\n" \
  "v_mov_b32_e64 v97, s80\n" \
  "buffer_store_dwordx2 v[96:97], v254, s[68:71], 0 offen sc1\n" \
  "s_branch L_bumpb_%=\n" \
  "L_late_%=:\n" \
  "s_waitcnt vmcnt(0)\n" \
  "s_mov_b32 s81, 0x40000\n" \
  "L_spin_a_%=:\n" \
  "buffer_load_dwordx2 v[240:241], v242, s[68:71], 0 offen sc1\n" \
  "s_waitcnt vmcnt(0)\n" \
  "v_subrev_u32 v251, s90, v241\n" \
  "v_cmp_lt_u32 s[92:93], 8, v251\n" \
  "v_cmp_ne_u32 vcc, s79, v240\n" \
  "s_or_b64 vcc, vcc, s[92:93]\n" \
  "s_cbranch_vccz L_ok_a_%=\n" \
  "s_sleep 1\n" \
  "s_sub_u32 s81, s81, 1\n" \
  "s_cmp_lg_u32 s81, 0\n" \
  "s_cbranch_scc1 L_spin_a_%=\n" \
  "L_ok_a_%=:\n" \
  "s_mov_b32 s94, 0\n" \
  "buffer_load_dwordx4 v[128:131], v250, s[44:47], s94 offen sc1\n" \
  "buffer_load_dwordx4 v[0:3], v250, s[44:47], s94 offen offset:1024 sc1\n" \
  "s_mov_b32 s94, 0x3800\n" \
  "buffer_load_dwordx4 v[132:135], v250, s[44:47], s94 offen sc1\n" \
  "buffer_load_dwordx4 v[4:7], v250, s[44:47], s94 offen offset:1024 sc1\n" \
  "s_mov_b32 s94, 0x7000\n" \
  "buffer_load_dwordx4 v[136:139], v250, s[44:47], s94 offen sc1\n" \
  "buffer_load_dwordx4 v[8:11], v250, s[44:47], s94 offen offset:1024 sc1\n" \
  "s_mov_b32 s94, 0xa800\n" \
  "buffer_load_dwordx4 v[140:143], v250, s[44:47], s94 offen sc1\n" \
  "buffer_load_dwordx4 v[12:15], v250, s[44:47], s94 offen offset:1024 sc1\n" \
  "s_mov_b32 s94, 0xe000\n" \
  "buffer_load_dwordx4 v[144:147], v250, s[44:47], s94 offen sc1\n" \
  "buffer_load_dwordx4 v[16:19], v250, s[44:47], s94 offen offset:1024 sc1\n" \
  "s_mov_b32 s94, 0x11800\n" \
  "buffer_load_dwordx4 v[148:151], v250, s[44:47], s94 offen sc1\n" \
  "buffer_load_dwordx4 v[20:23], v250, s[44:47], s94 offen offset:1024 sc1\n" \
  "s_mov_b32 s94, 0x15000\n" \
  "buffer_load_dwordx4 v[152:155], v250, s[44:47], s94 offen sc1\n" \
  "buffer_load_dwordx4 v[24:27], v250, s[44:47], s94 offen offset:1024 sc1\n" \
  "s_mov_b32 s94, 0x18800\n" \
  "buffer_load_dwordx4 v[156:159], v250, s[44:47], s94 offen sc1\n" \
  "buffer_load_dwordx4 v[28:31], v250, s[44:47], s94 offen offset:1024 sc1\n" \
  "s_mov_b32 s94, 0x1c000\n" \
  "buffer_load_dwordx4 v[160:163], v250, s[44:47], s94 offen sc1\n" \
  "buffer_load_dwordx4 v[32:35], v250, s[44:47], s94 offen offset:1024 sc1\n" \
  "s_mov_b32 s94, 0x1f800\n" \
  "buffer_load_dwordx4 v[164:167], v250, s[44:47], s94 offen sc1\n" \
  "buffer_load_dwordx4 v[36:39], v250, s[44:47], s94 offen offset:1024 sc1\n" \
  "s_mov_b32 s94, 0x23000\n" \
  "buffer_load_dwordx4 v[168:171], v250, s[44:47], s94 offen sc1\n" \
  "buffer_load_dwordx4 v[40:43], v250, s[44:47], s94 offen offset:1024 sc1\n" \
  "s_mov_b32 s94, 0x26800\n" \
  "buffer_load_dwordx4 v[172:175], v250, s[44:47], s94 offen sc1\n" \
  "buffer_load_dwordx4 v[44:47], v250, s[44:47], s94 offen offset:1024 sc1\n" \
  "s_mov_b32 s94, 0x2a000\n" \
  "buffer_load_dwordx4 v[176:179], v250, s[44:47], s94 offen sc1\n" \
  "buffer_load_dwordx4 v[48:51], v250, s[44:47], s94 offen offset:1024 sc1\n" \
  "s_mov_b32 s94, 0x2d800\n" \
  "buffer_load_dwordx4 v[180:183], v250, s[44:47], s94 offen sc1\n" \
  "buffer_load_dwordx4 v[52:55], v250, s[44:47], s94 offen offset:1024 sc1\n" \
  "s_mov_b32 s94, 0x31000\n" \
  "buffer_load_dwordx4 v[184:187], v250, s[44:47], s94 offen sc1\n" \
  "buffer_load_dwordx4 v[56:59], v250, s[44:47], s94 offen offset:1024 sc1\n" \
  "s_mov_b32 s94, 0x34800\n" \
  "buffer_load_dwordx4 v[188:191], v250, s[44:47], s94 offen sc1\n" \
  "buffer_load_dwordx4 v[60:63], v250, s[44:47], s94 offen offset:1024 sc1\n" \
  "s_branch L_lateb_%=\n" \
  "L_rare0_%=:\n" \
  "L_rare_p1_%=:\n" \
  "s_nop 15\n" \
  "v_max3_f32 v192, v144, v145, v146\n" \
  "v_max3_f32 v192, v192, v147, v152\n" \
  "v_max3_f32 v192, v192, v153, v154\n" \
  "v_max_f32 v192, v192, v155\n" \
  "v_mov_b32 v193, v192\n" \
  "s_nop 1\n" \
  "v_permlane32_swap_b32 v192, v193\n" \
  "v_max_f32 v192, v192, v193\n" \
  "v_mov_b32 v193, v192\n" \
  "s_nop 1\n" \
  "v_permlane16_swap_b32 v192, v193\n" \
  "v_max_f32_e64 v192, v192, v193\n" \
  "v_add_f32 v192, 0x41a00000, v192\n" \
  "v_max_f32_e64 v192, 0, v192\n" \
  "v_exp_f32 v194, -v192\n" \
  "v_sub_f32 v160, v160, v192\n" \
  "v_sub_f32 v161, v161, v192\n" \
  "v_sub_f32 v162, v162, v192\n" \
  "v_sub_f32 v163, v163, v192\n" \
  "v_sub_f32 v128, v128, v192\n" \
  "v_sub_f32 v144, v144, v192\n" \
  "v_sub_f32 v129, v129, v192\n" \
  "v_sub_f32 v145, v145, v192\n" \
  "v_sub_f32 v130, v130, v192\n" \
  "v_sub_f32 v146, v146, v192\n" \
  "v_sub_f32 v131, v131, v192\n" \
  "v_sub_f32 v147, v147, v192\n" \
  "v_sub_f32 v136, v136, v192\n" \
  "v_sub_f32 v152, v152, v192\n" \
  "v_sub_f32 v137, v137, v192\n" \
  "v_sub_f32 v153, v153, v192\n" \
  "v_sub_f32 v138, v138, v192\n" \
  "v_sub_f32 v154, v154, v192\n" \
  "v_sub_f32 v139, v139, v192\n" \
  "v_sub_f32 v155, v155, v192\n" \
  "v_mul_f32 v0, v0, v194\n" \
  "v_mul_f32 v1, v1, v194\n" \
  "v_mul_f32 v2, v2, v194\n" \
  "v_mul_f32 v3, v3, v194\n" \
  "v_mul_f32 v8, v8, v194\n" \
  "v_mul_f32 v9, v9, v194\n" \
  "v_mul_f32 v10, v10, v194\n" \
  "v_mul_f32 v11, v11, v194\n" \
  "v_mul_f32 v16, v16, v194\n" \
  "v_mul_f32 v17, v17, v194\n" \
  "v_mul_f32 v18, v18, v194\n" \
  "v_mul_f32 v19, v19, v194\n" \
  "v_mul_f32 v24, v24, v194\n" \
  "v_mul_f32 v25, v25, v194\n" \
  "v_mul_f32 v26, v26, v194\n" \
  "v_mul_f32 v27, v27, v194\n" \
  "v_mul_f32 v32, v32, v194\n" \
  "v_mul_f32 v33, v33, v194\n" \
  "v_mul_f32 v34, v34, v194\n" \
  "v_mul_f32 v35, v35, v194\n" \
  "v_mul_f32 v40, v40, v194\n" \
  "v_mul_f32 v41, v41, v194\n" \
  "v_mul_f32 v42, v42, v194\n" \
  "v_mul_f32 v43, v43, v194\n" \
  "v_mul_f32 v48, v48, v194\n" \
  "v_mul_f32 v49, v49, v194\n" \
  "v_mul_f32 v50, v50, v194\n" \
  "v_mul_f32 v51, v51, v194\n" \
  "v_mul_f32 v56, v56, v194\n" \
  "v_mul_f32 v57, v57, v194\n" \
  "v_mul_f32 v58, v58, v194\n" \
  "v_mul_f32 v59, v59, v194\n" \
  "v_mul_f32 v64, v64, v194\n" \
  "v_mul_f32 v65, v65, v194\n" \
  "v_mul_f32 v66, v66, v194\n" \
  "v_mul_f32 v67, v67, v194\n" \
  "v_mul_f32 v72, v72, v194\n" \
  "v_mul_f32 v73, v73, v194\n" \
  "v_mul_f32 v74, v74, v194\n" \
  "v_mul_f32 v75, v75, v194\n" \
  "v_mul_f32 v80, v80, v194\n" \
  "v_mul_f32 v81, v81, v194\n" \
  "v_mul_f32 v82, v82, v194\n" \
  "v_mul_f32 v83, v83, v194\n" \
  "v_mul_f32 v88, v88, v194\n" \
  "v_mul_f32 v89, v89, v194\n" \
  "v_mul_f32 v90, v90, v194\n" \
  "v_mul_f32 v91, v91, v194\n" \
  "v_mul_f32 v96, v96, v194\n" \
  "v_mul_f32 v97, v97, v194\n" \
  "v_mul_f32 v98, v98, v194\n" \
  "v_mul_f32 v99, v99, v194\n" \
  "v_mul_f32 v104, v104, v194\n" \
  "v_mul_f32 v105, v105, v194\n" \
  "v_mul_f32 v106, v106, v194\n" \
  "v_mul_f32 v107, v107, v194\n" \
  "v_mul_f32 v112, v112, v194\n" \
  "v_mul_f32 v113, v113, v194\n" \
  "v_mul_f32 v114, v114, v194\n" \
  "v_mul_f32 v115, v115, v194\n" \
  "v_mul_f32 v120, v120, v194\n" \
  "v_mul_f32 v121, v121, v194\n" \
  "v_mul_f32 v122, v122, v194\n" \
  "v_mul_f32 v123, v123, v194\n" \
  "v_mul_f32 v200, v200, v194\n" \
  "v_mul_f32 v201, v201, v194\n" \
  "v_mul_f32 v202, v202, v194\n" \
  "v_mul_f32 v203, v203, v194\n" \
  "v_max3_f32 v192, v148, v149, v150\n" \
  "v_max3_f32 v192, v192, v151, v156\n" \
  "v_max3_f32 v192, v192, v157, v158\n" \
  "v_max_f32 v192, v192, v159\n" \
  "v_mov_b32 v193, v192\n" \
  "s_nop 1\n" \
  "v_permlane32_swap_b32 v192, v193\n" \
  "v_max_f32 v192, v192, v193\n" \
  "v_mov_b32 v193, v192\n" \
  "s_nop 1\n" \
  "v_permlane16_swap_b32 v192, v193\n" \
  "v_max_f32_e64 v192, v192, v193\n" \
  "v_add_f32 v192, 0x41a00000, v192\n" \
  "v_max_f32_e64 v192, 0, v192\n" \
  "v_exp_f32 v194, -v192\n" \
  "v_sub_f32 v164, v164, v192\n" \
  "v_sub_f32 v165, v165, v192\n" \
  "v_sub_f32 v166, v166, v192\n" \
  "v_sub_f32 v167, v167, v192\n" \
  "v_sub_f32 v132, v132, v192\n" \
  "v_sub_f32 v148, v148, v192\n" \
  "v_sub_f32 v133, v133, v192\n" \
  "v_sub_f32 v149, v149, v192\n" \
  "v_sub_f32 v134, v134, v192\n" \
  "v_sub_f32 v150, v150, v192\n" \
  "v_sub_f32 v135, v135, v192\n" \
  "v_sub_f32 v151, v151, v192\n" \
  "v_sub_f32 v140, v140, v192\n" \
  "v_sub_f32 v156, v156, v192\n" \
  "v_sub_f32 v141, v141, v192\n" \
  "v_sub_f32 v157, v157, v192\n" \
  "v_sub_f32 v142, v142, v192\n" \
  "v_sub_f32 v158, v158, v192\n" \
  "v_sub_f32 v143, v143, v192\n" \
  "v_sub_f32 v159, v159, v192\n" \
  "v_mul_f32 v4, v4, v194\n" \
  "v_mul_f32 v5, v5, v194\n" \
  "v_mul_f32 v6, v6, v194\n" \
  "v_mul_f32 v7, v7, v194\n" \
  "v_mul_f32 v12, v12, v194\n" \
  "v_mul_f32 v13, v13, v194\n" \
  "v_mul_f32 v14, v14, v194\n" \
  "v_mul_f32 v15, v15, v194\n" \
  "v_mul_f32 v20, v20, v194\n" \
  "v_mul_f32 v21, v21, v194\n" \
  "v_mul_f32 v22, v22, v194\n" \
  "v_mul_f32 v23, v23, v194\n" \
  "v_mul_f32 v28, v28, v194\n" \
  "v_mul_f32 v29, v29, v194\n" \
  "v_mul_f32 v30, v30, v194\n" \
  "v_mul_f32 v31, v31, v194\n" \
  "v_mul_f32 v36, v36, v194\n" \
  "v_mul_f32 v37, v37, v194\n" \
  "v_mul_f32 v38, v38, v194\n" \
  "v_mul_f32 v39, v39, v194\n" \
  "v_mul_f32 v44, v44, v194\n" \
  "v_mul_f32 v45, v45, v194\n" \
  "v_mul_f32 v46, v46, v194\n" \
  "v_mul_f32 v47, v47, v194\n" \
  "v_mul_f32 v52, v52, v194\n" \
  "v_mul_f32 v53, v53, v194\n" \
  "v_mul_f32 v54, v54, v194\n" \
  "v_mul_f32 v55, v55, v194\n" \
  "v_mul_f32 v60, v60, v194\n" \
  "v_mul_f32 v61, v61, v194\n" \
  "v_mul_f32 v62, v62, v194\n" \
  "v_mul_f32 v63, v63, v194\n" \
  "v_mul_f32 v68, v68, v194\n" \
  "v_mul_f32 v69, v69, v194\n" \
  "v_mul_f32 v70, v70, v194\n" \
  "v_mul_f32 v71, v71, v194\n" \
  "v_mul_f32 v76, v76, v194\n" \
  "v_mul_f32 v77, v77, v194\n" \
  "v_mul_f32 v78, v78, v194\n" \
  "v_mul_f32 v79, v79, v194\n" \
  "v_mul_f32 v84, v84, v194\n" \
  "v_mul_f32 v85, v85, v194\n" \
  "v_mul_f32 v86, v86, v194\n" \
  "v_mul_f32 v87, v87, v194\n" \
  "v_mul_f32 v92, v92, v194\n" \
  "v_mul_f32 v93, v93, v194\n" \
  "v_mul_f32 v94, v94, v194\n" \
  "v_mul_f32 v95, v95, v194\n" \
  "v_mul_f32 v100, v100, v194\n" \
  "v_mul_f32 v101, v101, v194\n" \
  "v_mul_f32 v102, v102, v194\n" \
  "v_mul_f32 v103, v103, v194\n" \
  "v_mul_f32 v108, v108, v194\n" \
  "v_mul_f32 v109, v109, v194\n" \
  "v_mul_f32 v110, v110, v194\n" \
  "v_mul_f32 v111, v111, v194\n" \
  "v_mul_f32 v116, v116, v194\n" \
  "v_mul_f32 v117, v117, v194\n" \
  "v_mul_f32 v118, v118, v194\n" \
  "v_mul_f32 v119, v119, v194\n" \
  "v_mul_f32 v124, v124, v194\n" \
  "v_mul_f32 v125, v125, v194\n" \
  "v_mul_f32 v126, v126, v194\n" \
  "v_mul_f32 v127, v127, v194\n" \
  "v_mul_f32 v204, v204, v194\n" \
  "v_mul_f32 v205, v205, v194\n" \
  "v_mul_f32 v206, v206, v194\n" \
  "v_mul_f32 v207, v207, v194\n" \
  "v_exp_f32 v192, v144\n" \
  "v_exp_f32 v193, v145\n" \
  "v_exp_f32 v194, v146\n" \
  "v_exp_f32 v195, v147\n" \
  "v_cvt_pk_bf16_f32 v184, v192, v193\n" \
  "v_exp_f32 v196, v148\n" \
  "v_exp_f32 v197, v149\n" \
  "v_cvt_pk_bf16_f32 v185, v194, v195\n" \
  "v_exp_f32 v198, v150\n" \
  "v_exp_f32 v199, v151\n" \
  "v_cvt_pk_bf16_f32 v188, v196, v197\n" \
  "v_cvt_pk_bf16_f32 v189, v198, v199\n" \
  "v_exp_f32 v192, v152\n" \
  "v_exp_f32 v193, v153\n" \
  "v_exp_f32 v194, v154\n" \
  "v_exp_f32 v195, v155\n" \
  "v_cvt_pk_bf16_f32 v186, v192, v193\n" \
  "v_exp_f32 v196, v156\n" \
  "v_exp_f32 v197, v157\n" \
  "v_cvt_pk_bf16_f32 v187, v194, v195\n" \
  "v_exp_f32 v198, v158\n" \
  "v_exp_f32 v199, v159\n" \
  "v_cvt_pk_bf16_f32 v190, v196, v197\n" \
  "v_cvt_pk_bf16_f32 v191, v198, v199\n" \
  "s_nop 1\n" \
  "s_branch L_back_p1_%=\n" \
  "L_rare_2_%=:\n" \
  "s_nop 15\n" \
  "v_max3_f32 v192, v128, v129, v130\n" \
  "v_max3_f32 v192, v192, v131, v136\n" \
  "v_max3_f32 v192, v192, v137, v138\n" \
  "v_max_f32 v192, v192, v139\n" \
  "v_mov_b32 v193, v192\n" \
  "s_nop 1\n" \
  "v_permlane32_swap_b32 v192, v193\n" \
  "v_max_f32 v192, v192, v193\n" \
  "v_mov_b32 v193, v192\n" \
  "s_nop 1\n" \
  "v_permlane16_swap_b32 v192, v193\n" \
  "v_max_f32 v192, v192, v193\n" \
  "v_add_f32 v192, 0x41a00000, v192\n" \
  "v_max_f32_e64 v192, 0, v192\n" \
  "v_exp_f32 v194, -v192\n" \
  "v_sub_f32 v160, v160, v192\n" \
  "v_sub_f32 v161, v161, v192\n" \
  "v_sub_f32 v162, v162, v192\n" \
  "v_sub_f32 v163, v163, v192\n" \
  "v_sub_f32 v144, v144, v192\n" \
  "v_sub_f32 v128, v128, v192\n" \
  "v_sub_f32 v145, v145, v192\n" \
  "v_sub_f32 v129, v129, v192\n" \
  "v_sub_f32 v146, v146, v192\n" \
  "v_sub_f32 v130, v130, v192\n" \
  "v_sub_f32 v147, v147, v192\n" \
  "v_sub_f32 v131, v131, v192\n" \
  "v_sub_f32 v152, v152, v192\n" \
  "v_sub_f32 v136, v136, v192\n" \
  "v_sub_f32 v153, v153, v192\n" \
  "v_sub_f32 v137, v137, v192\n" \
  "v_sub_f32 v154, v154, v192\n" \
  "v_sub_f32 v138, v138, v192\n" \
  "v_sub_f32 v155, v155, v192\n" \
  "v_sub_f32 v139, v139, v192\n" \
  "v_mul_f32 v0, v0, v194\n" \
  "v_mul_f32 v1, v1, v194\n" \
  "v_mul_f32 v2, v2, v194\n" \
  "v_mul_f32 v3, v3, v194\n" \
  "v_mul_f32 v8, v8, v194\n" \
  "v_mul_f32 v9, v9, v194\n" \
  "v_mul_f32 v10, v10, v194\n" \
  "v_mul_f32 v11, v11, v194\n" \
  "v_mul_f32 v16, v16, v194\n" \
  "v_mul_f32 v17, v17, v194\n" \
  "v_mul_f32 v18, v18, v194\n" \
  "v_mul_f32 v19, v19, v194\n" \
  "v_mul_f32 v24, v24, v194\n" \
  "v_mul_f32 v25, v25, v194\n" \
  "v_mul_f32 v26, v26, v194\n" \
  "v_mul_f32 v27, v27, v194\n" \
  "v_mul_f32 v32, v32, v194\n" \
  "v_mul_f32 v33, v33, v194\n" \
  "v_mul_f32 v34, v34, v194\n" \
  "v_mul_f32 v35, v35, v194\n" \
  "v_mul_f32 v40, v40, v194\n" \
  "v_mul_f32 v41, v41, v194\n" \
  "v_mul_f32 v42, v42, v194\n" \
  "v_mul_f32 v43, v43, v194\n" \
  "v_mul_f32 v48, v48, v194\n" \
  "v_mul_f32 v49, v49, v194\n" \
  "v_mul_f32 v50, v50, v194\n" \
  "v_mul_f32 v51, v51, v194\n" \
  "v_mul_f32 v56, v56, v194\n" \
  "v_mul_f32 v57, v57, v194\n" \
  "v_mul_f32 v58, v58, v194\n" \
  "v_mul_f32 v59, v59, v194\n" \
  "v_mul_f32 v64, v64, v194\n" \
  "v_mul_f32 v65, v65, v194\n" \
  "v_mul_f32 v66, v66, v194\n" \
  "v_mul_f32 v67, v67, v194\n" \
  "v_mul_f32 v72, v72, v194\n" \
  "v_mul_f32 v73, v73, v194\n" \
  "v_mul_f32 v74, v74, v194\n" \
  "v_mul_f32 v75, v75, v194\n" \
  "v_mul_f32 v80, v80, v194\n" \
  "v_mul_f32 v81, v81, v194\n" \
  "v_mul_f32 v82, v82, v194\n" \
  "v_mul_f32 v83, v83, v194\n" \
  "v_mul_f32 v88, v88, v194\n" \
  "v_mul_f32 v89, v89, v194\n" \
  "v_mul_f32 v90, v90, v194\n" \
  "v_mul_f32 v91, v91, v194\n" \
  "v_mul_f32 v96, v96, v194\n" \
  "v_mul_f32 v97, v97, v194\n" \
  "v_mul_f32 v98, v98, v194\n" \
  "v_mul_f32 v99, v99, v194\n" \
  "v_mul_f32 v104, v104, v194\n" \
  "v_mul_f32 v105, v105, v194\n" \
  "v_mul_f32 v106, v106, v194\n" \
  "v_mul_f32 v107, v107, v194\n" \
  "v_mul_f32 v112, v112, v194\n" \
  "v_mul_f32 v113, v113, v194\n" \
  "v_mul_f32 v114, v114, v194\n" \
  "v_mul_f32 v115, v115, v194\n" \
  "v_mul_f32 v120, v120, v194\n" \
  "v_mul_f32 v121, v121, v194\n" \
  "v_mul_f32 v122, v122, v194\n" \
  "v_mul_f32 v123, v123, v194\n" \
  "v_mul_f32 v200, v200, v194\n" \
  "v_mul_f32 v201, v201, v194\n" \
  "v_mul_f32 v202, v202, v194\n" \
  "v_mul_f32 v203, v203, v194\n" \
  "v_max3_f32 v192, v132, v133, v134\n" \
  "v_max3_f32 v192, v192, v135, v140\n" \
  "v_max3_f32 v192, v192, v141, v142\n" \
  "v_max_f32 v192, v192, v143\n" \
  "v_mov_b32 v193, v192\n" \
  "s_nop 1\n" \
  "v_permlane32_swap_b32 v192, v193\n" \
  "v_max_f32 v192, v192, v193\n" \
  "v_mov_b32 v193, v192\n" \
  "s_nop 1\n" \
  "v_permlane16_swap_b32 v192, v193\n" \
  "v_max_f32_e64 v192, v192, v193\n" \
  "v_add_f32 v192, 0x41a00000, v192\n" \
  "v_max_f32_e64 v192, 0, v192\n" \
  "v_exp_f32 v194, -v192\n" \
  "v_sub_f32 v164, v164, v192\n" \
  "v_sub_f32 v165, v165, v192\n" \
  "v_sub_f32 v166, v166, v192\n" \
  "v_sub_f32 v167, v167, v192\n" \
  "v_sub_f32 v148, v148, v192\n" \
  "v_sub_f32 v132, v132, v192\n" \
  "v_sub_f32 v149, v149, v192\n" \
  "v_sub_f32 v133, v133, v192\n" \
  "v_sub_f32 v150, v150, v192\n" \
  "v_sub_f32 v134, v134, v192\n" \
  "v_sub_f32 v151, v151, v192\n" \
  "v_sub_f32 v135, v135, v192\n" \
  "v_sub_f32 v156, v156, v192\n" \
  "v_sub_f32 v140, v140, v192\n" \
  "v_sub_f32 v157, v157, v192\n" \
  "v_sub_f32 v141, v141, v192\n" \
  "v_sub_f32 v158, v158, v192\n" \
  "v_sub_f32 v142, v142, v192\n" \
  "v_sub_f32 v159, v159, v192\n" \
  "v_sub_f32 v143, v143, v192\n" \
  "v_mul_f32 v4, v4, v194\n" \
  "v_mul_f32 v5, v5, v194\n" \
  "v_mul_f32 v6, v6, v194\n" \
  "v_mul_f32 v7, v7, v194\n" \
  "v_mul_f32 v12, v12, v194\n" \
  "v_mul_f32 v13, v13, v194\n" \
  "v_mul_f32 v14, v14, v194\n" \
  "v_mul_f32 v15, v15, v194\n" \
  "v_mul_f32 v20, v20, v194\n" \
  "v_mul_f32 v21, v21, v194\n" \
  "v_mul_f32 v22, v22, v194\n" \
  "v_mul_f32 v23, v23, v194\n" \
  "v_mul_f32 v28, v28, v194\n" \
  "v_mul_f32 v29, v29, v194\n" \
  "v_mul_f32 v30, v30, v194\n" \
  "v_mul_f32 v31, v31, v194\n" \
  "v_mul_f32 v36, v36, v194\n" \
  "v_mul_f32 v37, v37, v194\n" \
  "v_mul_f32 v38, v38, v194\n" \
  "v_mul_f32 v39, v39, v194\n" \
  "v_mul_f32 v44, v44, v194\n" \
  "v_mul_f32 v45, v45, v194\n" \
  "v_mul_f32 v46, v46, v194\n" \
  "v_mul_f32 v47, v47, v194\n" \
  "v_mul_f32 v52, v52, v194\n" \
  "v_mul_f32 v53, v53, v194\n" \
  "v_mul_f32 v54, v54, v194\n" \
  "v_mul_f32 v55, v55, v194\n" \
  "v_mul_f32 v60, v60, v194\n" \
  "v_mul_f32 v61, v61, v194\n" \
  "v_mul_f32 v62, v62, v194\n" \
  "v_mul_f32 v63, v63, v194\n" \
  "v_mul_f32 v68, v68, v194\n" \
  "v_mul_f32 v69, v69, v194\n" \
  "v_mul_f32 v70, v70, v194\n" \
  "v_mul_f32 v71, v71, v194\n" \
  "v_mul_f32 v76, v76, v194\n" \
  "v_mul_f32 v77, v77, v194\n" \
  "v_mul_f32 v78, v78, v194\n" \
  "v_mul_f32 v79, v79, v194\n" \
  "v_mul_f32 v84, v84, v194\n" \
  "v_mul_f32 v85, v85, v194\n" \
  "v_mul_f32 v86, v86, v194\n" \
  "v_mul_f32 v87, v87, v194\n" \
  "v_mul_f32 v92, v92, v194\n" \
  "v_mul_f32 v93, v93, v194\n" \
  "v_mul_f32 v94, v94, v194\n" \
  "v_mul_f32 v95, v95, v194\n" \
  "v_mul_f32 v100, v100, v194\n" \
  "v_mul_f32 v101, v101, v194\n" \
  "v_mul_f32 v102, v102, v194\n" \
  "v_mul_f32 v103, v103, v194\n" \
  "v_mul_f32 v108, v108, v194\n" \
  "v_mul_f32 v109, v109, v194\n" \
  "v_mul_f32 v110, v110, v194\n" \
  "v_mul_f32 v111, v111, v194\n" \
  "v_mul_f32 v116, v116, v194\n" \
  "v_mul_f32 v117, v117, v194\n" \
  "v_mul_f32 v118, v118, v194\n" \
  "v_mul_f32 v119, v119, v194\n" \
  "v_mul_f32 v124, v124, v194\n" \
  "v_mul_f32 v125, v125, v194\n" \
  "v_mul_f32 v126, v126, v194\n" \
  "v_mul_f32 v127, v127, v194\n" \
  "v_mul_f32 v204, v204, v194\n" \
  "v_mul_f32 v205, v205, v194\n" \
  "v_mul_f32 v206, v206, v194\n" \
  "v_mul_f32 v207, v207, v194\n" \
  "v_exp_f32 v192, v128\n" \
  "v_exp_f32 v193, v129\n" \
  "v_exp_f32 v194, v130\n" \
  "v_exp_f32 v195, v131\n" \
  "v_cvt_pk_bf16_f32 v176, v192, v193\n" \
  "v_exp_f32 v196, v132\n" \
  "v_exp_f32 v197, v133\n" \
  "v_cvt_pk_bf16_f32 v177, v194, v195\n" \
  "v_exp_f32 v198, v134\n" \
  "v_exp_f32 v199, v135\n" \
  "v_cvt_pk_bf16_f32 v180, v196, v197\n" \
  "v_cvt_pk_bf16_f32 v181, v198, v199\n" \
  "v_exp_f32 v192, v136\n" \
  "v_exp_f32 v193, v137\n" \
  "v_exp_f32 v194, v138\n" \
  "v_exp_f32 v195, v139\n" \
  "v_cvt_pk_bf16_f32 v178, v192, v193\n" \
  "v_exp_f32 v196, v140\n" \
  "v_exp_f32 v197, v141\n" \
  "v_cvt_pk_bf16_f32 v179, v194, v195\n" \
  "v_exp_f32 v198, v142\n" \
  "v_exp_f32 v199, v143\n" \
  "v_cvt_pk_bf16_f32 v182, v196, v197\n" \
  "v_cvt_pk_bf16_f32 v183, v198, v199\n" \
  "s_nop 1\n" \
  "s_branch L_back_2_%=\n" \
  "L_rare_3_%=:\n" \
  "s_nop 15\n" \
  "v_max3_f32 v192, v144, v145, v146\n" \
  "v_max3_f32 v192, v192, v147, v152\n" \
  "v_max3_f32 v192, v192, v153, v154\n" \
  "v_max_f32 v192, v192, v155\n" \
  "v_mov_b32 v193, v192\n" \
  "s_nop 1\n" \
  "v_permlane32_swap_b32 v192, v193\n" \
  "v_max_f32 v192, v192, v193\n" \
  "v_mov_b32 v193, v192\n" \
  "s_nop 1\n" \
  "v_permlane16_swap_b32 v192, v193\n" \
  "v_max_f32 v192, v192, v193\n" \
  "v_add_f32 v192, 0x41a00000, v192\n" \
  "v_max_f32_e64 v192, 0, v192\n" \
  "v_exp_f32 v194, -v192\n" \
  "v_sub_f32 v160, v160, v192\n" \
  "v_sub_f32 v161, v161, v192\n" \
  "v_sub_f32 v162, v162, v192\n" \
  "v_sub_f32 v163, v163, v192\n" \
  "v_sub_f32 v128, v128, v192\n" \
  "v_sub_f32 v144, v144, v192\n" \
  "v_sub_f32 v129, v129, v192\n" \
  "v_sub_f32 v145, v145, v192\n" \
  "v_sub_f32 v130, v130, v192\n" \
  "v_sub_f32 v146, v146, v192\n" \
  "v_sub_f32 v131, v131, v192\n" \
  "v_sub_f32 v147, v147, v192\n" \
  "v_sub_f32 v136, v136, v192\n" \
  "v_sub_f32 v152, v152, v192\n" \
  "v_sub_f32 v137, v137, v192\n" \
  "v_sub_f32 v153, v153, v192\n" \
  "v_sub_f32 v138, v138, v192\n" \
  "v_sub_f32 v154, v154, v192\n" \
  "v_sub_f32 v139, v139, v192\n" \
  "v_sub_f32 v155, v155, v192\n" \
  "v_mul_f32 v0, v0, v194\n" \
  "v_mul_f32 v1, v1, v194\n" \
  "v_mul_f32 v2, v2, v194\n" \
  "v_mul_f32 v3, v3, v194\n" \
  "v_mul_f32 v8, v8, v194\n" \
  "v_mul_f32 v9, v9, v194\n" \
  "v_mul_f32 v10, v10, v194\n" \
  "v_mul_f32 v11, v11, v194\n" \
  "v_mul_f32 v16, v16, v194\n" \
  "v_mul_f32 v17, v17, v194\n" \
  "v_mul_f32 v18, v18, v194\n" \
  "v_mul_f32 v19, v19, v194\n" \
  "v_mul_f32 v24, v24, v194\n" \
  "v_mul_f32 v25, v25, v194\n" \
  "v_mul_f32 v26, v26, v194\n" \
  "v_mul_f32 v27, v27, v194\n" \
  "v_mul_f32 v32, v32, v194\n" \
  "v_mul_f32 v33, v33, v194\n" \
  "v_mul_f32 v34, v34, v194\n" \
  "v_mul_f32 v35, v35, v194\n" \
  "v_mul_f32 v40, v40, v194\n" \
  "v_mul_f32 v41, v41, v194\n" \
  "v_mul_f32 v42, v42, v194\n" \
  "v_mul_f32 v43, v43, v194\n" \
  "v_mul_f32 v48, v48, v194\n" \
  "v_mul_f32 v49, v49, v194\n" \
  "v_mul_f32 v50, v50, v194\n" \
  "v_mul_f32 v51, v51, v194\n" \
  "v_mul_f32 v56, v56, v194\n" \
  "v_mul_f32 v57, v57, v194\n" \
  "v_mul_f32 v58, v58, v194\n" \
  "v_mul_f32 v59, v59, v194\n" \
  "v_mul_f32 v64, v64, v194\n" \
  "v_mul_f32 v65, v65, v194\n" \
  "v_mul_f32 v66, v66, v194\n" \
  "v_mul_f32 v67, v67, v194\n" \
  "v_mul_f32 v72, v72, v194\n" \
  "v_mul_f32 v73, v73, v194\n" \
  "v_mul_f32 v74, v74, v194\n" \
  "v_mul_f32 v75, v75, v194\n" \
  "v_mul_f32 v80, v80, v194\n" \
  "v_mul_f32 v81, v81, v194\n" \
  "v_mul_f32 v82, v82, v194\n" \
  "v_mul_f32 v83, v83, v194\n" \
  "v_mul_f32 v88, v88, v194\n" \
  "v_mul_f32 v89, v89, v194\n" \
  "v_mul_f32 v90, v90, v194\n" \
  "v_mul_f32 v91, v91, v194\n" \
  "v_mul_f32 v96, v96, v194\n" \
  "v_mul_f32 v97, v97, v194\n" \
  "v_mul_f32 v98, v98, v194\n" \
  "v_mul_f32 v99, v99, v194\n" \
  "v_mul_f32 v104, v104, v194\n" \
  "v_mul_f32 v105, v105, v194\n" \
  "v_mul_f32 v106, v106, v194\n" \
  "v_mul_f32 v107, v107, v194\n" \
  "v_mul_f32 v112, v112, v194\n" \
  "v_mul_f32 v113, v113, v194\n" \
  "v_mul_f32 v114, v114, v194\n" \
  "v_mul_f32 v115, v115, v194\n" \
  "v_mul_f32 v120, v120, v194\n" \
  "v_mul_f32 v121, v121, v194\n" \
  "v_mul_f32 v122, v122, v194\n" \
  "v_mul_f32 v123, v123, v194\n" \
  "v_mul_f32 v200, v200, v194\n" \
  "v_mul_f32 v201, v201, v194\n" \
  "v_mul_f32 v202, v202, v194\n" \
  "v_mul_f32 v203, v203, v194\n" \
  "v_max3_f32 v192, v148, v149, v150\n" \
  "v_max3_f32 v192, v192, v151, v156\n" \
  "v_max3_f32 v192, v192, v157, v158\n" \
  "v_max_f32 v192, v192, v159\n" \
  "v_mov_b32 v193, v192\n" \
  "s_nop 1\n" \
  "v_permlane32_swap_b32 v192, v193\n" \
  "v_max_f32 v192, v192, v193\n" \
  "v_mov_b32 v193, v192\n" \
  "s_nop 1\n" \
  "v_permlane16_swap_b32 v192, v193\n" \
  "v_max_f32_e64 v192, v192, v193\n" \
  "v_add_f32 v192, 0x41a00000, v192\n" \
  "v_max_f32_e64 v192, 0, v192\n" \
  "v_exp_f32 v194, -v192\n" \
  "v_sub_f32 v164, v164, v192\n" \
  "v_sub_f32 v165, v165, v192\n" \
  "v_sub_f32 v166, v166, v192\n" \
  "v_sub_f32 v167, v167, v192\n" \
  "v_sub_f32 v132, v132, v192\n" \
  "v_sub_f32 v148, v148, v192\n" \
  "v_sub_f32 v133, v133, v192\n" \
  "v_sub_f32 v149, v149, v192\n" \
  "v_sub_f32 v134, v134, v192\n" \
  "v_sub_f32 v150, v150, v192\n" \
  "v_sub_f32 v135, v135, v192\n" \
  "v_sub_f32 v151, v151, v192\n" \
  "v_sub_f32 v140, v140, v192\n" \
  "v_sub_f32 v156, v156, v192\n" \
  "v_sub_f32 v141, v141, v192\n" \
  "v_sub_f32 v157, v157, v192\n" \
  "v_sub_f32 v142, v142, v192\n" \
  "v_sub_f32 v158, v158, v192\n" \
  "v_sub_f32 v143, v143, v192\n" \
  "v_sub_f32 v159, v159, v192\n" \
  "v_mul_f32 v4, v4, v194\n" \
  "v_mul_f32 v5, v5, v194\n" \
  "v_mul_f32 v6, v6, v194\n" \
  "v_mul_f32 v7, v7, v194\n" \
  "v_mul_f32 v12, v12, v194\n" \
  "v_mul_f32 v13, v13, v194\n" \
  "v_mul_f32 v14, v14, v194\n" \
  "v_mul_f32 v15, v15, v194\n" \
  "v_mul_f32 v20, v20, v194\n" \
  "v_mul_f32 v21, v21, v194\n" \
  "v_mul_f32 v22, v22, v194\n" \
  "v_mul_f32 v23, v23, v194\n" \
  "v_mul_f32 v28, v28, v194\n" \
  "v_mul_f32 v29, v29, v194\n" \
  "v_mul_f32 v30, v30, v194\n" \
  "v_mul_f32 v31, v31, v194\n" \
  "v_mul_f32 v36, v36, v194\n" \
  "v_mul_f32 v37, v37, v194\n" \
  "v_mul_f32 v38, v38, v194\n" \
  "v_mul_f32 v39, v39, v194\n" \
  "v_mul_f32 v44, v44, v194\n" \
  "v_mul_f32 v45, v45, v194\n" \
  "v_mul_f32 v46, v46, v194\n" \
  "v_mul_f32 v47, v47, v194\n" \
  "v_mul_f32 v52, v52, v194\n" \
  "v_mul_f32 v53, v53, v194\n" \
  "v_mul_f32 v54, v54, v194\n" \
  "v_mul_f32 v55, v55, v194\n" \
  "v_mul_f32 v60, v60, v194\n" \
  "v_mul_f32 v61, v61, v194\n" \
  "v_mul_f32 v62, v62, v194\n" \
  "v_mul_f32 v63, v63, v194\n" \
  "v_mul_f32 v68, v68, v194\n" \
  "v_mul_f32 v69, v69, v194\n" \
  "v_mul_f32 v70, v70, v194\n" \
  "v_mul_f32 v71, v71, v194\n" \
  "v_mul_f32 v76, v76, v194\n" \
  "v_mul_f32 v77, v77, v194\n" \
  "v_mul_f32 v78, v78, v194\n" \
  "v_mul_f32 v79, v79, v194\n" \
  "v_mul_f32 v84, v84, v194\n" \
  "v_mul_f32 v85, v85, v194\n" \
  "v_mul_f32 v86, v86, v194\n" \
  "v_mul_f32 v87, v87, v194\n" \
  "v_mul_f32 v92, v92, v194\n" \
  "v_mul_f32 v93, v93, v194\n" \
  "v_mul_f32 v94, v94, v194\n" \
  "v_mul_f32 v95, v95, v194\n" \
  "v_mul_f32 v100, v100, v194\n" \
  "v_mul_f32 v101, v101, v194\n" \
  "v_mul_f32 v102, v102, v194\n" \
  "v_mul_f32 v103, v103, v194\n" \
  "v_mul_f32 v108, v108, v194\n" \
  "v_mul_f32 v109, v109, v194\n" \
  "v_mul_f32 v110, v110, v194\n" \
  "v_mul_f32 v111, v111, v194\n" \
  "v_mul_f32 v116, v116, v194\n" \
  "v_mul_f32 v117, v117, v194\n" \
  "v_mul_f32 v118, v118, v194\n" \
  "v_mul_f32 v119, v119, v194\n" \
  "v_mul_f32 v124, v124, v194\n" \
  "v_mul_f32 v125, v125, v194\n" \
  "v_mul_f32 v126, v126, v194\n" \
  "v_mul_f32 v127, v127, v194\n" \
  "v_mul_f32 v204, v204, v194\n" \
  "v_mul_f32 v205, v205, v194\n" \
  "v_mul_f32 v206, v206, v194\n" \
  "v_mul_f32 v207, v207, v194\n" \
  "v_exp_f32 v192, v144\n" \
  "v_exp_f32 v193, v145\n" \
  "v_exp_f32 v194, v146\n" \
  "v_exp_f32 v195, v147\n" \
  "v_cvt_pk_bf16_f32 v184, v192, v193\n" \
  "v_exp_f32 v196, v148\n" \
  "v_exp_f32 v197, v149\n" \
  "v_cvt_pk_bf16_f32 v185, v194, v195\n" \
  "v_exp_f32 v198, v150\n" \
  "v_exp_f32 v199, v151\n" \
  "v_cvt_pk_bf16_f32 v188, v196, v197\n" \
  "v_cvt_pk_bf16_f32 v189, v198, v199\n" \
  "v_exp_f32 v192, v152\n" \
  "v_exp_f32 v193, v153\n" \
  "v_exp_f32 v194, v154\n" \
  "v_exp_f32 v195, v155\n" \
  "v_cvt_pk_bf16_f32 v186, v192, v193\n" \
  "v_exp_f32 v196, v156\n" \
  "v_exp_f32 v197, v157\n" \
  "v_cvt_pk_bf16_f32 v187, v194, v195\n" \
  "v_exp_f32 v198, v158\n" \
  "v_exp_f32 v199, v159\n" \
  "v_cvt_pk_bf16_f32 v190, v196, v197\n" \
  "v_cvt_pk_bf16_f32 v191, v198, v199\n" \
  "s_nop 1\n" \
  "s_branch L_back_3_%=\n" \
  "L_rare_0_%=:\n" \
  "s_nop 15\n" \
  "v_max3_f32 v192, v128, v129, v130\n" \
  "v_max3_f32 v192, v192, v131, v136\n" \
  "v_max3_f32 v192, v192, v137, v138\n" \
  "v_max_f32 v192, v192, v139\n" \
  "v_mov_b32 v193, v192\n" \
  "s_nop 1\n" \
  "v_permlane32_swap_b32 v192, v193\n" \
  "v_max_f32 v192, v192, v193\n" \
  "v_mov_b32 v193, v192\n" \
  "s_nop 1\n" \
  "v_permlane16_swap_b32 v192, v193\n" \
  "v_max_f32 v192, v192, v193\n" \
  "v_add_f32 v192, 0x41a00000, v192\n" \
  "v_max_f32_e64 v192, 0, v192\n" \
  "v_exp_f32 v194, -v192\n" \
  "v_sub_f32 v160, v160, v192\n" \
  "v_sub_f32 v161, v161, v192\n" \
  "v_sub_f32 v162, v162, v192\n" \
  "v_sub_f32 v163, v163, v192\n" \
  "v_sub_f32 v144, v144, v192\n" \
  "v_sub_f32 v128, v128, v192\n" \
  "v_sub_f32 v145, v145, v192\n" \
  "v_sub_f32 v129, v129, v192\n" \
  "v_sub_f32 v146, v146, v192\n" \
  "v_sub_f32 v130, v130, v192\n" \
  "v_sub_f32 v147, v147, v192\n" \
  "v_sub_f32 v131, v131, v192\n" \
  "v_sub_f32 v152, v152, v192\n" \
  "v_sub_f32 v136, v136, v192\n" \
  "v_sub_f32 v153, v153, v192\n" \
  "v_sub_f32 v137, v137, v192\n" \
  "v_sub_f32 v154, v154, v192\n" \
  "v_sub_f32 v138, v138, v192\n" \
  "v_sub_f32 v155, v155, v192\n" \
  "v_sub_f32 v139, v139, v192\n" \
  "v_mul_f32 v0, v0, v194\n" \
  "v_mul_f32 v1, v1, v194\n" \
  "v_mul_f32 v2, v2, v194\n" \
  "v_mul_f32 v3, v3, v194\n" \
  "v_mul_f32 v8, v8, v194\n" \
  "v_mul_f32 v9, v9, v194\n" \
  "v_mul_f32 v10, v10, v194\n" \
  "v_mul_f32 v11, v11, v194\n" \
  "v_mul_f32 v16, v16, v194\n" \
  "v_mul_f32 v17, v17, v194\n" \
  "v_mul_f32 v18, v18, v194\n" \
  "v_mul_f32 v19, v19, v194\n" \
  "v_mul_f32 v24, v24, v194\n" \
  "v_mul_f32 v25, v25, v194\n" \
  "v_mul_f32 v26, v26, v194\n" \
  "v_mul_f32 v27, v27, v194\n" \
  "v_mul_f32 v32, v32, v194\n" \
  "v_mul_f32 v33, v33, v194\n" \
  "v_mul_f32 v34, v34, v194\n" \
  "v_mul_f32 v35, v35, v194\n" \
  "v_mul_f32 v40, v40, v194\n" \
  "v_mul_f32 v41, v41, v194\n" \
  "v_mul_f32 v42, v42, v194\n" \
  "v_mul_f32 v43, v43, v194\n" \
  "v_mul_f32 v48, v48, v194\n" \
  "v_mul_f32 v49, v49, v194\n" \
  "v_mul_f32 v50, v50, v194\n" \
  "v_mul_f32 v51, v51, v194\n" \
  "v_mul_f32 v56, v56, v194\n" \
  "v_mul_f32 v57, v57, v194\n" \
  "v_mul_f32 v58, v58, v194\n" \
  "v_mul_f32 v59, v59, v194\n" \
  "v_mul_f32 v64, v64, v194\n" \
  "v_mul_f32 v65, v65, v194\n" \
  "v_mul_f32 v66, v66, v194\n" \
  "v_mul_f32 v67, v67, v194\n" \
  "v_mul_f32 v72, v72, v194\n" \
  "v_mul_f32 v73, v73, v194\n" \
  "v_mul_f32 v74, v74, v194\n" \
  "v_mul_f32 v75, v75, v194\n" \
  "v_mul_f32 v80, v80, v194\n" \
  "v_mul_f32 v81, v81, v194\n" \
  "v_mul_f32 v82, v82, v194\n" \
  "v_mul_f32 v83, v83, v194\n" \
  "v_mul_f32 v88, v88, v194\n" \
  "v_mul_f32 v89, v89, v194\n" \
  "v_mul_f32 v90, v90, v194\n" \
  "v_mul_f32 v91, v91, v194\n" \
  "v_mul_f32 v96, v96, v194\n" \
  "v_mul_f32 v97, v97, v194\n" \
  "v_mul_f32 v98, v98, v194\n" \
  "v_mul_f32 v99, v99, v194\n" \
  "v_mul_f32 v104, v104, v194\n" \
  "v_mul_f32 v105, v105, v194\n" \
  "v_mul_f32 v106, v106, v194\n" \
  "v_mul_f32 v107, v107, v194\n" \
  "v_mul_f32 v112, v112, v194\n" \
  "v_mul_f32 v113, v113, v194\n" \
  "v_mul_f32 v114, v114, v194\n" \
  "v_mul_f32 v115, v115, v194\n" \
  "v_mul_f32 v120, v120, v194\n" \
  "v_mul_f32 v121, v121, v194\n" \
  "v_mul_f32 v122, v122, v194\n" \
  "v_mul_f32 v123, v123, v194\n" \
  "v_mul_f32 v200, v200, v194\n" \
  "v_mul_f32 v201, v201, v194\n" \
  "v_mul_f32 v202, v202, v194\n" \
  "v_mul_f32 v203, v203, v194\n" \
  "v_max3_f32 v192, v132, v133, v134\n" \
  "v_max3_f32 v192, v192, v135, v140\n" \
  "v_max3_f32 v192, v192, v141, v142\n" \
  "v_max_f32 v192, v192, v143\n" \
  "v_mov_b32 v193, v192\n" \
  "s_nop 1\n" \
  "v_permlane32_swap_b32 v192, v193\n" \
  "v_max_f32 v192, v192, v193\n" \
  "v_mov_b32 v193, v192\n" \
  "s_nop 1\n" \
  "v_permlane16_swap_b32 v192, v193\n" \
  "v_max_f32_e64 v192, v192, v193\n" \
  "v_add_f32 v192, 0x41a00000, v192\n" \
  "v_max_f32_e64 v192, 0, v192\n" \
  "v_exp_f32 v194, -v192\n" \
  "v_sub_f32 v164, v164, v192\n" \
  "v_sub_f32 v165, v165, v192\n" \
  "v_sub_f32 v166, v166, v192\n" \
  "v_sub_f32 v167, v167, v192\n" \
  "v_sub_f32 v148, v148, v192\n" \
  "v_sub_f32 v132, v132, v192\n" \
  "v_sub_f32 v149, v149, v192\n" \
  "v_sub_f32 v133, v133, v192\n" \
  "v_sub_f32 v150, v150, v192\n" \
  "v_sub_f32 v134, v134, v192\n" \
  "v_sub_f32 v151, v151, v192\n" \
  "v_sub_f32 v135, v135, v192\n" \
  "v_sub_f32 v156, v156, v192\n" \
  "v_sub_f32 v140, v140, v192\n" \
  "v_sub_f32 v157, v157, v192\n" \
  "v_sub_f32 v141, v141, v192\n" \
  "v_sub_f32 v158, v158, v192\n" \
  "v_sub_f32 v142, v142, v192\n" \
  "v_sub_f32 v159, v159, v192\n" \
  "v_sub_f32 v143, v143, v192\n" \
  "v_mul_f32 v4, v4, v194\n" \
  "v_mul_f32 v5, v5, v194\n" \
  "v_mul_f32 v6, v6, v194\n" \
  "v_mul_f32 v7, v7, v194\n" \
  "v_mul_f32 v12, v12, v194\n" \
  "v_mul_f32 v13, v13, v194\n" \
  "v_mul_f32 v14, v14, v194\n" \
  "v_mul_f32 v15, v15, v194\n" \
  "v_mul_f32 v20, v20, v194\n" \
  "v_mul_f32 v21, v21, v194\n" \
  "v_mul_f32 v22, v22, v194\n" \
  "v_mul_f32 v23, v23, v194\n" \
  "v_mul_f32 v28, v28, v194\n" \
  "v_mul_f32 v29, v29, v194\n" \
  "v_mul_f32 v30, v30, v194\n" \
  "v_mul_f32 v31, v31, v194\n" \
  "v_mul_f32 v36, v36, v194\n" \
  "v_mul_f32 v37, v37, v194\n" \
  "v_mul_f32 v38, v38, v194\n" \
  "v_mul_f32 v39, v39, v194\n" \
  "v_mul_f32 v44, v44, v194\n" \
  "v_mul_f32 v45, v45, v194\n" \
  "v_mul_f32 v46, v46, v194\n" \
  "v_mul_f32 v47, v47, v194\n" \
  "v_mul_f32 v52, v52, v194\n" \
  "v_mul_f32 v53, v53, v194\n" \
  "v_mul_f32 v54, v54, v194\n" \
  "v_mul_f32 v55, v55, v194\n" \
  "v_mul_f32 v60, v60, v194\n" \
  "v_mul_f32 v61, v61, v194\n" \
  "v_mul_f32 v62, v62, v194\n" \
  "v_mul_f32 v63, v63, v194\n" \
  "v_mul_f32 v68, v68, v194\n" \
  "v_mul_f32 v69, v69, v194\n" \
  "v_mul_f32 v70, v70, v194\n" \
  "v_mul_f32 v71, v71, v194\n" \
  "v_mul_f32 v76, v76, v194\n" \
  "v_mul_f32 v77, v77, v194\n" \
  "v_mul_f32 v78, v78, v194\n" \
  "v_mul_f32 v79, v79, v194\n" \
  "v_mul_f32 v84, v84, v194\n" \
  "v_mul_f32 v85, v85, v194\n" \
  "v_mul_f32 v86, v86, v194\n" \
  "v_mul_f32 v87, v87, v194\n" \
  "v_mul_f32 v92, v92, v194\n" \
  "v_mul_f32 v93, v93, v194\n" \
  "v_mul_f32 v94, v94, v194\n" \
  "v_mul_f32 v95, v95, v194\n" \
  "v_mul_f32 v100, v100, v194\n" \
  "v_mul_f32 v101, v101, v194\n" \
  "v_mul_f32 v102, v102, v194\n" \
  "v_mul_f32 v103, v103, v194\n" \
  "v_mul_f32 v108, v108, v194\n" \
  "v_mul_f32 v109, v109, v194\n" \
  "v_mul_f32 v110, v110, v194\n" \
  "v_mul_f32 v111, v111, v194\n" \
  "v_mul_f32 v116, v116, v194\n" \
  "v_mul_f32 v117, v117, v194\n" \
  "v_mul_f32 v118, v118, v194\n" \
  "v_mul_f32 v119, v119, v194\n" \
  "v_mul_f32 v124, v124, v194\n" \
  "v_mul_f32 v125, v125, v194\n" \
  "v_mul_f32 v126, v126, v194\n" \
  "v_mul_f32 v127, v127, v194\n" \
  "v_mul_f32 v204, v204, v194\n" \
  "v_mul_f32 v205, v205, v194\n" \
  "v_mul_f32 v206, v206, v194\n" \
  "v_mul_f32 v207, v207, v194\n" \
  "v_exp_f32 v192, v128\n" \
  "v_exp_f32 v193, v129\n" \
  "v_exp_f32 v194, v130\n" \
  "v_exp_f32 v195, v131\n" \
  "v_cvt_pk_bf16_f32 v176, v192, v193\n" \
  "v_exp_f32 v196, v132\n" \
  "v_exp_f32 v197, v133\n" \
  "v_cvt_pk_bf16_f32 v177, v194, v195\n" \
  "v_exp_f32 v198, v134\n" \
  "v_exp_f32 v199, v135\n" \
  "v_cvt_pk_bf16_f32 v180, v196, v197\n" \
  "v_cvt_pk_bf16_f32 v181, v198, v199\n" \
  "v_exp_f32 v192, v136\n" \
  "v_exp_f32 v193, v137\n" \
  "v_exp_f32 v194, v138\n" \
  "v_exp_f32 v195, v139\n" \
  "v_cvt_pk_bf16_f32 v178, v192, v193\n" \
  "v_exp_f32 v196, v140\n" \
  "v_exp_f32 v197, v141\n" \
  "v_cvt_pk_bf16_f32 v179, v194, v195\n" \
  "v_exp_f32 v198, v142\n" \
  "v_exp_f32 v199, v143\n" \
  "v_cvt_pk_bf16_f32 v182, v196, v197\n" \
  "v_cvt_pk_bf16_f32 v183, v198, v199\n" \
  "s_nop 1\n" \
  "s_branch L_back_0_%=\n" \
  "L_rare_1_%=:\n" \
  "s_nop 15\n" \
  "v_max3_f32 v192, v144, v145, v146\n" \
  "v_max3_f32 v192, v192, v147, v152\n" \
  "v_max3_f32 v192, v192, v153, v154\n" \
  "v_max_f32 v192, v192, v155\n" \
  "v_mov_b32 v193, v192\n" \
  "s_nop 1\n" \
  "v_permlane32_swap_b32 v192, v193\n" \
  "v_max_f32 v192, v192, v193\n" \
  "v_mov_b32 v193, v192\n" \
  "s_nop 1\n" \
  "v_permlane16_swap_b32 v192, v193\n" \
  "v_max_f32 v192, v192, v193\n" \
  "v_add_f32 v192, 0x41a00000, v192\n" \
  "v_max_f32_e64 v192, 0, v192\n" \
  "v_exp_f32 v194, -v192\n" \
  "v_sub_f32 v160, v160, v192\n" \
  "v_sub_f32 v161, v161, v192\n" \
  "v_sub_f32 v162, v162, v192\n" \
  "v_sub_f32 v163, v163, v192\n" \
  "v_sub_f32 v128, v128, v192\n" \
  "v_sub_f32 v144, v144, v192\n" \
  "v_sub_f32 v129, v129, v192\n" \
  "v_sub_f32 v145, v145, v192\n" \
  "v_sub_f32 v130, v130, v192\n" \
  "v_sub_f32 v146, v146, v192\n" \
  "v_sub_f32 v131, v131, v192\n" \
  "v_sub_f32 v147, v147, v192\n" \
  "v_sub_f32 v136, v136, v192\n" \
  "v_sub_f32 v152, v152, v192\n" \
  "v_sub_f32 v137, v137, v192\n" \
  "v_sub_f32 v153, v153, v192\n" \
  "v_sub_f32 v138, v138, v192\n" \
  "v_sub_f32 v154, v154, v192\n" \
  "v_sub_f32 v139, v139, v192\n" \
  "v_sub_f32 v155, v155, v192\n" \
  "v_mul_f32 v0, v0, v194\n" \
  "v_mul_f32 v1, v1, v194\n" \
  "v_mul_f32 v2, v2, v194\n" \
  "v_mul_f32 v3, v3, v194\n" \
  "v_mul_f32 v8, v8, v194\n" \
  "v_mul_f32 v9, v9, v194\n" \
  "v_mul_f32 v10, v10, v194\n" \
  "v_mul_f32 v11, v11, v194\n" \
  "v_mul_f32 v16, v16, v194\n" \
  "v_mul_f32 v17, v17, v194\n" \
  "v_mul_f32 v18, v18, v194\n" \
  "v_mul_f32 v19, v19, v194\n" \
  "v_mul_f32 v24, v24, v194\n" \
  "v_mul_f32 v25, v25, v194\n" \
  "v_mul_f32 v26, v26, v194\n" \
  "v_mul_f32 v27, v27, v194\n" \
  "v_mul_f32 v32, v32, v194\n" \
  "v_mul_f32 v33, v33, v194\n" \
  "v_mul_f32 v34, v34, v194\n" \
  "v_mul_f32 v35, v35, v194\n" \
  "v_mul_f32 v40, v40, v194\n" \
  "v_mul_f32 v41, v41, v194\n" \
  "v_mul_f32 v42, v42, v194\n" \
  "v_mul_f32 v43, v43, v194\n" \
  "v_mul_f32 v48, v48, v194\n" \
  "v_mul_f32 v49, v49, v194\n" \
  "v_mul_f32 v50, v50, v194\n" \
  "v_mul_f32 v51, v51, v194\n" \
  "v_mul_f32 v56, v56, v194\n" \
  "v_mul_f32 v57, v57, v194\n" \
  "v_mul_f32 v58, v58, v194\n" \
  "v_mul_f32 v59, v59, v194\n" \
  "v_mul_f32 v64, v64, v194\n" \
  "v_mul_f32 v65, v65, v194\n" \
  "v_mul_f32 v66, v66, v194\n" \
  "v_mul_f32 v67, v67, v194\n" \
  "v_mul_f32 v72, v72, v194\n" \
  "v_mul_f32 v73, v73, v194\n" \
  "v_mul_f32 v74, v74, v194\n" \
  "v_mul_f32 v75, v75, v194\n" \
  "v_mul_f32 v80, v80, v194\n" \
  "v_mul_f32 v81, v81, v194\n" \
  "v_mul_f32 v82, v82, v194\n" \
  "v_mul_f32 v83, v83, v194\n" \
  "v_mul_f32 v88, v88, v194\n" \
  "v_mul_f32 v89, v89, v194\n" \
  "v_mul_f32 v90, v90, v194\n" \
  "v_mul_f32 v91, v91, v194\n" \
  "v_mul_f32 v96, v96, v194\n" \
  "v_mul_f32 v97, v97, v194\n" \
  "v_mul_f32 v98, v98, v194\n" \
  "v_mul_f32 v99, v99, v194\n" \
  "v_mul_f32 v104, v104, v194\n" \
  "v_mul_f32 v105, v105, v194\n" \
  "v_mul_f32 v106, v106, v194\n" \
  "v_mul_f32 v107, v107, v194\n" \
  "v_mul_f32 v112, v112, v194\n" \
  "v_mul_f32 v113, v113, v194\n" \
  "v_mul_f32 v114, v114, v194\n" \
  "v_mul_f32 v115, v115, v194\n" \
  "v_mul_f32 v120, v120, v194\n" \
  "v_mul_f32 v121, v121, v194\n" \
  "v_mul_f32 v122, v122, v194\n" \
  "v_mul_f32 v123, v123, v194\n" \
  "v_mul_f32 v200, v200, v194\n" \
  "v_mul_f32 v201, v201, v194\n" \
  "v_mul_f32 v202, v202, v194\n" \
  "v_mul_f32 v203, v203, v194\n" \
  "v_max3_f32 v192, v148, v149, v150\n" \
  "v_max3_f32 v192, v192, v151, v156\n" \
  "v_max3_f32 v192, v192, v157, v158\n" \
  "v_max_f32 v192, v192, v159\n" \
  "v_mov_b32 v193, v192\n" \
  "s_nop 1\n" \
  "v_permlane32_swap_b32 v192, v193\n" \
  "v_max_f32 v192, v192, v193\n" \
  "v_mov_b32 v193, v192\n" \
  "s_nop 1\n" \
  "v_permlane16_swap_b32 v192, v193\n" \
  "v_max_f32_e64 v192, v192, v193\n" \
  "v_add_f32 v192, 0x41a00000, v192\n" \
  "v_max_f32_e64 v192, 0, v192\n" \
  "v_exp_f32 v194, -v192\n" \
  "v_sub_f32 v164, v164, v192\n" \
  "v_sub_f32 v165, v165, v192\n" \
  "v_sub_f32 v166, v166, v192\n" \
  "v_sub_f32 v167, v167, v192\n" \
  "v_sub_f32 v132, v132, v192\n" \
  "v_sub_f32 v148, v148, v192\n" \
  "v_sub_f32 v133, v133, v192\n" \
  "v_sub_f32 v149, v149, v192\n" \
  "v_sub_f32 v134, v134, v192\n" \
  "v_sub_f32 v150, v150, v192\n" \
  "v_sub_f32 v135, v135, v192\n" \
  "v_sub_f32 v151, v151, v192\n" \
  "v_sub_f32 v140, v140, v192\n" \
  "v_sub_f32 v156, v156, v192\n" \
  "v_sub_f32 v141, v141, v192\n" \
  "v_sub_f32 v157, v157, v192\n" \
  "v_sub_f32 v142, v142, v192\n" \
  "v_sub_f32 v158, v158, v192\n" \
  "v_sub_f32 v143, v143, v192\n" \
  "v_sub_f32 v159, v159, v192\n" \
  "v_mul_f32 v4, v4, v194\n" \
  "v_mul_f32 v5, v5, v194\n" \
  "v_mul_f32 v6, v6, v194\n" \
  "v_mul_f32 v7, v7, v194\n" \
  "v_mul_f32 v12, v12, v194\n" \
  "v_mul_f32 v13, v13, v194\n" \
  "v_mul_f32 v14, v14, v194\n" \
  "v_mul_f32 v15, v15, v194\n" \
  "v_mul_f32 v20, v20, v194\n" \
  "v_mul_f32 v21, v21, v194\n" \
  "v_mul_f32 v22, v22, v194\n" \
  "v_mul_f32 v23, v23, v194\n" \
  "v_mul_f32 v28, v28, v194\n" \
  "v_mul_f32 v29, v29, v194\n" \
  "v_mul_f32 v30, v30, v194\n" \
  "v_mul_f32 v31, v31, v194\n" \
  "v_mul_f32 v36, v36, v194\n" \
  "v_mul_f32 v37, v37, v194\n" \
  "v_mul_f32 v38, v38, v194\n" \
  "v_mul_f32 v39, v39, v194\n" \
  "v_mul_f32 v44, v44, v194\n" \
  "v_mul_f32 v45, v45, v194\n" \
  "v_mul_f32 v46, v46, v194\n" \
  "v_mul_f32 v47, v47, v194\n" \
  "v_mul_f32 v52, v52, v194\n" \
  "v_mul_f32 v53, v53, v194\n" \
  "v_mul_f32 v54, v54, v194\n" \
  "v_mul_f32 v55, v55, v194\n" \
  "v_mul_f32 v60, v60, v194\n" \
  "v_mul_f32 v61, v61, v194\n" \
  "v_mul_f32 v62, v62, v194\n" \
  "v_mul_f32 v63, v63, v194\n" \
  "v_mul_f32 v68, v68, v194\n" \
  "v_mul_f32 v69, v69, v194\n" \
  "v_mul_f32 v70, v70, v194\n" \
  "v_mul_f32 v71, v71, v194\n" \
  "v_mul_f32 v76, v76, v194\n" \
  "v_mul_f32 v77, v77, v194\n" \
  "v_mul_f32 v78, v78, v194\n" \
  "v_mul_f32 v79, v79, v194\n" \
  "v_mul_f32 v84, v84, v194\n" \
  "v_mul_f32 v85, v85, v194\n" \
  "v_mul_f32 v86, v86, v194\n" \
  "v_mul_f32 v87, v87, v194\n" \
  "v_mul_f32 v92, v92, v194\n" \
  "v_mul_f32 v93, v93, v194\n" \
  "v_mul_f32 v94, v94, v194\n" \
  "v_mul_f32 v95, v95, v194\n" \
  "v_mul_f32 v100, v100, v194\n" \
  "v_mul_f32 v101, v101, v194\n" \
  "v_mul_f32 v102, v102, v194\n" \
  "v_mul_f32 v103, v103, v194\n" \
  "v_mul_f32 v108, v108, v194\n" \
  "v_mul_f32 v109, v109, v194\n" \
  "v_mul_f32 v110, v110, v194\n" \
  "v_mul_f32 v111, v111, v194\n" \
  "v_mul_f32 v116, v116, v194\n" \
  "v_mul_f32 v117, v117, v194\n" \
  "v_mul_f32 v118, v118, v194\n" \
  "v_mul_f32 v119, v119, v194\n" \
  "v_mul_f32 v124, v124, v194\n" \
  "v_mul_f32 v125, v125, v194\n" \
  "v_mul_f32 v126, v126, v194\n" \
  "v_mul_f32 v127, v127, v194\n" \
  "v_mul_f32 v204, v204, v194\n" \
  "v_mul_f32 v205, v205, v194\n" \
  "v_mul_f32 v206, v206, v194\n" \
  "v_mul_f32 v207, v207, v194\n" \
  "v_exp_f32 v192, v144\n" \
  "v_exp_f32 v193, v145\n" \
  "v_exp_f32 v194, v146\n" \
  "v_exp_f32 v195, v147\n" \
  "v_cvt_pk_bf16_f32 v184, v192, v193\n" \
  "v_exp_f32 v196, v148\n" \
  "v_exp_f32 v197, v149\n" \
  "v_cvt_pk_bf16_f32 v185, v194, v195\n" \
  "v_exp_f32 v198, v150\n" \
  "v_exp_f32 v199, v151\n" \
  "v_cvt_pk_bf16_f32 v188, v196, v197\n" \
  "v_cvt_pk_bf16_f32 v189, v198, v199\n" \
  "v_exp_f32 v192, v152\n" \
  "v_exp_f32 v193, v153\n" \
  "v_exp_f32 v194, v154\n" \
  "v_exp_f32 v195, v155\n" \
  "v_cvt_pk_bf16_f32 v186, v192, v193\n" \
  "v_exp_f32 v196, v156\n" \
  "v_exp_f32 v197, v157\n" \
  "v_cvt_pk_bf16_f32 v187, v194, v195\n" \
  "v_exp_f32 v198, v158\n" \
  "v_exp_f32 v199, v159\n" \
  "v_cvt_pk_bf16_f32 v190, v196, v197\n" \
  "v_cvt_pk_bf16_f32 v191, v198, v199\n" \
  "s_nop 1\n" \
  "s_branch L_back_1_%=\n" \
  "L_rare_p58_%=:\n" \
  "s_nop 15\n" \
  "v_max3_f32 v192, v128, v129, v130\n" \
  "v_max3_f32 v192, v192, v131, v136\n" \
  "v_max3_f32 v192, v192, v137, v138\n" \
  "v_max_f32 v192, v192, v139\n" \
  "v_mov_b32 v193, v192\n" \
  "s_nop 1\n" \
  "v_permlane32_swap_b32 v192, v193\n" \
  "v_max_f32 v192, v192, v193\n" \
  "v_mov_b32 v193, v192\n" \
  "s_nop 1\n" \
  "v_permlane16_swap_b32 v192, v193\n" \
  "v_max_f32 v192, v192, v193\n" \
  "v_add_f32 v192, 0x41a00000, v192\n" \
  "v_max_f32_e64 v192, 0, v192\n" \
  "v_exp_f32 v194, -v192\n" \
  "v_sub_f32 v160, v160, v192\n" \
  "v_sub_f32 v161, v161, v192\n" \
  "v_sub_f32 v162, v162, v192\n" \
  "v_sub_f32 v163, v163, v192\n" \
  "v_sub_f32 v144, v144, v192\n" \
  "v_sub_f32 v128, v128, v192\n" \
  "v_sub_f32 v145, v145, v192\n" \
  "v_sub_f32 v129, v129, v192\n" \
  "v_sub_f32 v146, v146, v192\n" \
  "v_sub_f32 v130, v130, v192\n" \
  "v_sub_f32 v147, v147, v192\n" \
  "v_sub_f32 v131, v131, v192\n" \
  "v_sub_f32 v152, v152, v192\n" \
  "v_sub_f32 v136, v136, v192\n" \
  "v_sub_f32 v153, v153, v192\n" \
  "v_sub_f32 v137, v137, v192\n" \
  "v_sub_f32 v154, v154, v192\n" \
  "v_sub_f32 v138, v138, v192\n" \
  "v_sub_f32 v155, v155, v192\n" \
  "v_sub_f32 v139, v139, v192\n" \
  "v_mul_f32 v0, v0, v194\n" \
  "v_mul_f32 v1, v1, v194\n" \
  "v_mul_f32 v2, v2, v194\n" \
  "v_mul_f32 v3, v3, v194\n" \
  "v_mul_f32 v8, v8, v194\n" \
  "v_mul_f32 v9, v9, v194\n" \
  "v_mul_f32 v10, v10, v194\n" \
  "v_mul_f32 v11, v11, v194\n" \
  "v_mul_f32 v16, v16, v194\n" \
  "v_mul_f32 v17, v17, v194\n" \
  "v_mul_f32 v18, v18, v194\n" \
  "v_mul_f32 v19, v19, v194\n" \
  "v_mul_f32 v24, v24, v194\n" \
  "v_mul_f32 v25, v25, v194\n" \
  "v_mul_f32 v26, v26, v194\n" \
  "v_mul_f32 v27, v27, v194\n" \
  "v_mul_f32 v32, v32, v194\n" \
  "v_mul_f32 v33, v33, v194\n" \
  "v_mul_f32 v34, v34, v194\n" \
  "v_mul_f32 v35, v35, v194\n" \
  "v_mul_f32 v40, v40, v194\n" \
  "v_mul_f32 v41, v41, v194\n" \
  "v_mul_f32 v42, v42, v194\n" \
  "v_mul_f32 v43, v43, v194\n" \
  "v_mul_f32 v48, v48, v194\n" \
  "v_mul_f32 v49, v49, v194\n" \
  "v_mul_f32 v50, v50, v194\n" \
  "v_mul_f32 v51, v51, v194\n" \
  "v_mul_f32 v56, v56, v194\n" \
  "v_mul_f32 v57, v57, v194\n" \
  "v_mul_f32 v58, v58, v194\n" \
  "v_mul_f32 v59, v59, v194\n" \
  "v_mul_f32 v64, v64, v194\n" \
  "v_mul_f32 v65, v65, v194\n" \
  "v_mul_f32 v66, v66, v194\n" \
  "v_mul_f32 v67, v67, v194\n" \
  "v_mul_f32 v72, v72, v194\n" \
  "v_mul_f32 v73, v73, v194\n" \
  "v_mul_f32 v74, v74, v194\n" \
  "v_mul_f32 v75, v75, v194\n" \
  "v_mul_f32 v80, v80, v194\n" \
  "v_mul_f32 v81, v81, v194\n" \
  "v_mul_f32 v82, v82, v194\n" \
  "v_mul_f32 v83, v83, v194\n" \
  "v_mul_f32 v88, v88, v194\n" \
  "v_mul_f32 v89, v89, v194\n" \
  "v_mul_f32 v90, v90, v194\n" \
  "v_mul_f32 v91, v91, v194\n" \
  "v_mul_f32 v96, v96, v194\n" \
  "v_mul_f32 v97, v97, v194\n" \
  "v_mul_f32 v98, v98, v194\n" \
  "v_mul_f32 v99, v99, v194\n" \
  "v_mul_f32 v104, v104, v194\n" \
  "v_mul_f32 v105, v105, v194\n" \
  "v_mul_f32 v106, v106, v194\n" \
  "v_mul_f32 v107, v107, v194\n" \
  "v_mul_f32 v112, v112, v194\n" \
  "v_mul_f32 v113, v113, v194\n" \
  "v_mul_f32 v114, v114, v194\n" \
  "v_mul_f32 v115, v115, v194\n" \
  "v_mul_f32 v120, v120, v194\n" \
  "v_mul_f32 v121, v121, v194\n" \
  "v_mul_f32 v122, v122, v194\n" \
  "v_mul_f32 v123, v123, v194\n" \
  "v_mul_f32 v200, v200, v194\n" \
  "v_mul_f32 v201, v201, v194\n" \
  "v_mul_f32 v202, v202, v194\n" \
  "v_mul_f32 v203, v203, v194\n" \
  "v_max3_f32 v192, v132, v133, v134\n" \
  "v_max3_f32 v192, v192, v135, v140\n" \
  "v_max3_f32 v192, v192, v141, v142\n" \
  "v_max_f32 v192, v192, v143\n" \
  "v_mov_b32 v193, v192\n" \
  "s_nop 1\n" \
  "v_permlane32_swap_b32 v192, v193\n" \
  "v_max_f32 v192, v192, v193\n" \
  "v_mov_b32 v193, v192\n" \
  "s_nop 1\n" \
  "v_permlane16_swap_b32 v192, v193\n" \
  "v_max_f32_e64 v192, v192, v193\n" \
  "v_add_f32 v192, 0x41a00000, v192\n" \
  "v_max_f32_e64 v192, 0, v192\n" \
  "v_exp_f32 v194, -v192\n" \
  "v_sub_f32 v164, v164, v192\n" \
  "v_sub_f32 v165, v165, v192\n" \
  "v_sub_f32 v166, v166, v192\n" \
  "v_sub_f32 v167, v167, v192\n" \
  "v_sub_f32 v148, v148, v192\n" \
  "v_sub_f32 v132, v132, v192\n" \
  "v_sub_f32 v149, v149, v192\n" \
  "v_sub_f32 v133, v133, v192\n" \
  "v_sub_f32 v150, v150, v192\n" \
  "v_sub_f32 v134, v134, v192\n" \
  "v_sub_f32 v151, v151, v192\n" \
  "v_sub_f32 v135, v135, v192\n" \
  "v_sub_f32 v156, v156, v192\n" \
  "v_sub_f32 v140, v140, v192\n" \
  "v_sub_f32 v157, v157, v192\n" \
  "v_sub_f32 v141, v141, v192\n" \
  "v_sub_f32 v158, v158, v192\n" \
  "v_sub_f32 v142, v142, v192\n" \
  "v_sub_f32 v159, v159, v192\n" \
  "v_sub_f32 v143, v143, v192\n" \
  "v_mul_f32 v4, v4, v194\n" \
  "v_mul_f32 v5, v5, v194\n" \
  "v_mul_f32 v6, v6, v194\n" \
  "v_mul_f32 v7, v7, v194\n" \
  "v_mul_f32 v12, v12, v194\n" \
  "v_mul_f32 v13, v13, v194\n" \
  "v_mul_f32 v14, v14, v194\n" \
  "v_mul_f32 v15, v15, v194\n" \
  "v_mul_f32 v20, v20, v194\n" \
  "v_mul_f32 v21, v21, v194\n" \
  "v_mul_f32 v22, v22, v194\n" \
  "v_mul_f32 v23, v23, v194\n" \
  "v_mul_f32 v28, v28, v194\n" \
  "v_mul_f32 v29, v29, v194\n" \
  "v_mul_f32 v30, v30, v194\n" \
  "v_mul_f32 v31, v31, v194\n" \
  "v_mul_f32 v36, v36, v194\n" \
  "v_mul_f32 v37, v37, v194\n" \
  "v_mul_f32 v38, v38, v194\n" \
  "v_mul_f32 v39, v39, v194\n" \
  "v_mul_f32 v44, v44, v194\n" \
  "v_mul_f32 v45, v45, v194\n" \
  "v_mul_f32 v46, v46, v194\n" \
  "v_mul_f32 v47, v47, v194\n" \
  "v_mul_f32 v52, v52, v194\n" \
  "v_mul_f32 v53, v53, v194\n" \
  "v_mul_f32 v54, v54, v194\n" \
  "v_mul_f32 v55, v55, v194\n" \
  "v_mul_f32 v60, v60, v194\n" \
  "v_mul_f32 v61, v61, v194\n" \
  "v_mul_f32 v62, v62, v194\n" \
  "v_mul_f32 v63, v63, v194\n" \
  "v_mul_f32 v68, v68, v194\n" \
  "v_mul_f32 v69, v69, v194\n" \
  "v_mul_f32 v70, v70, v194\n" \
  "v_mul_f32 v71, v71, v194\n" \
  "v_mul_f32 v76, v76, v194\n" \
  "v_mul_f32 v77, v77, v194\n" \
  "v_mul_f32 v78, v78, v194\n" \
  "v_mul_f32 v79, v79, v194\n" \
  "v_mul_f32 v84, v84, v194\n" \
  "v_mul_f32 v85, v85, v194\n" \
  "v_mul_f32 v86, v86, v194\n" \
  "v_mul_f32 v87, v87, v194\n" \
  "v_mul_f32 v92, v92, v194\n" \
  "v_mul_f32 v93, v93, v194\n" \
  "v_mul_f32 v94, v94, v194\n" \
  "v_mul_f32 v95, v95, v194\n" \
  "v_mul_f32 v100, v100, v194\n" \
  "v_mul_f32 v101, v101, v194\n" \
  "v_mul_f32 v102, v102, v194\n" \
  "v_mul_f32 v103, v103, v194\n" \
  "v_mul_f32 v108, v108, v194\n" \
  "v_mul_f32 v109, v109, v194\n" \
  "v_mul_f32 v110, v110, v194\n" \
  "v_mul_f32 v111, v111, v194\n" \
  "v_mul_f32 v116, v116, v194\n" \
  "v_mul_f32 v117, v117, v194\n" \
  "v_mul_f32 v118, v118, v194\n" \
  "v_mul_f32 v119, v119, v194\n" \
  "v_mul_f32 v124, v124, v194\n" \
  "v_mul_f32 v125, v125, v194\n" \
  "v_mul_f32 v126, v126, v194\n" \
  "v_mul_f32 v127, v127, v194\n" \
  "v_mul_f32 v204, v204, v194\n" \
  "v_mul_f32 v205, v205, v194\n" \
  "v_mul_f32 v206, v206, v194\n" \
  "v_mul_f32 v207, v207, v194\n" \
  "v_exp_f32 v192, v128\n" \
  "v_exp_f32 v193, v129\n" \
  "v_exp_f32 v194, v130\n" \
  "v_exp_f32 v195, v131\n" \
  "v_cvt_pk_bf16_f32 v176, v192, v193\n" \
  "v_exp_f32 v196, v132\n" \
  "v_exp_f32 v197, v133\n" \
  "v_cvt_pk_bf16_f32 v177, v194, v195\n" \
  "v_exp_f32 v198, v134\n" \
  "v_exp_f32 v199, v135\n" \
  "v_cvt_pk_bf16_f32 v180, v196, v197\n" \
  "v_cvt_pk_bf16_f32 v181, v198, v199\n" \
  "v_exp_f32 v192, v136\n" \
  "v_exp_f32 v193, v137\n" \
  "v_exp_f32 v194, v138\n" \
  "v_exp_f32 v195, v139\n" \
  "v_cvt_pk_bf16_f32 v178, v192, v193\n" \
  "v_exp_f32 v196, v140\n" \
  "v_exp_f32 v197, v141\n" \
  "v_cvt_pk_bf16_f32 v179, v194, v195\n" \
  "v_exp_f32 v198, v142\n" \
  "v_exp_f32 v199, v143\n" \
  "v_cvt_pk_bf16_f32 v182, v196, v197\n" \
  "v_cvt_pk_bf16_f32 v183, v198, v199\n" \
  "s_nop 1\n" \
  "s_branch L_back_p58_%=\n" \
  "L_rare_p59_%=:\n" \
  "s_nop 15\n" \
  "v_max3_f32 v192, v144, v145, v146\n" \
  "v_max3_f32 v192, v192, v147, v152\n" \
  "v_max3_f32 v192, v192, v153, v154\n" \
  "v_max_f32 v192, v192, v155\n" \
  "v_mov_b32 v193, v192\n" \
  "s_nop 1\n" \
  "v_permlane32_swap_b32 v192, v193\n" \
  "v_max_f32 v192, v192, v193\n" \
  "v_mov_b32 v193, v192\n" \
  "s_nop 1\n" \
  "v_permlane16_swap_b32 v192, v193\n" \
  "v_max_f32 v192, v192, v193\n" \
  "v_add_f32 v192, 0x41a00000, v192\n" \
  "v_max_f32_e64 v192, 0, v192\n" \
  "v_exp_f32 v194, -v192\n" \
  "v_sub_f32 v160, v160, v192\n" \
  "v_sub_f32 v161, v161, v192\n" \
  "v_sub_f32 v162, v162, v192\n" \
  "v_sub_f32 v163, v163, v192\n" \
  "v_sub_f32 v128, v128, v192\n" \
  "v_sub_f32 v144, v144, v192\n" \
  "v_sub_f32 v129, v129, v192\n" \
  "v_sub_f32 v145, v145, v192\n" \
  "v_sub_f32 v130, v130, v192\n" \
  "v_sub_f32 v146, v146, v192\n" \
  "v_sub_f32 v131, v131, v192\n" \
  "v_sub_f32 v147, v147, v192\n" \
  "v_sub_f32 v136, v136, v192\n" \
  "v_sub_f32 v152, v152, v192\n" \
  "v_sub_f32 v137, v137, v192\n" \
  "v_sub_f32 v153, v153, v192\n" \
  "v_sub_f32 v138, v138, v192\n" \
  "v_sub_f32 v154, v154, v192\n" \
  "v_sub_f32 v139, v139, v192\n" \
  "v_sub_f32 v155, v155, v192\n" \
  "v_mul_f32 v0, v0, v194\n" \
  "v_mul_f32 v1, v1, v194\n" \
  "v_mul_f32 v2, v2, v194\n" \
  "v_mul_f32 v3, v3, v194\n" \
  "v_mul_f32 v8, v8, v194\n" \
  "v_mul_f32 v9, v9, v194\n" \
  "v_mul_f32 v10, v10, v194\n" \
  "v_mul_f32 v11, v11, v194\n" \
  "v_mul_f32 v16, v16, v194\n" \
  "v_mul_f32 v17, v17, v194\n" \
  "v_mul_f32 v18, v18, v194\n" \
  "v_mul_f32 v19, v19, v194\n" \
  "v_mul_f32 v24, v24, v194\n" \
  "v_mul_f32 v25, v25, v194\n" \
  "v_mul_f32 v26, v26, v194\n" \
  "v_mul_f32 v27, v27, v194\n" \
  "v_mul_f32 v32, v32, v194\n" \
  "v_mul_f32 v33, v33, v194\n" \
  "v_mul_f32 v34, v34, v194\n" \
  "v_mul_f32 v35, v35, v194\n" \
  "v_mul_f32 v40, v40, v194\n" \
  "v_mul_f32 v41, v41, v194\n" \
  "v_mul_f32 v42, v42, v194\n" \
  "v_mul_f32 v43, v43, v194\n" \
  "v_mul_f32 v48, v48, v194\n" \
  "v_mul_f32 v49, v49, v194\n" \
  "v_mul_f32 v50, v50, v194\n" \
  "v_mul_f32 v51, v51, v194\n" \
  "v_mul_f32 v56, v56, v194\n" \
  "v_mul_f32 v57, v57, v194\n" \
  "v_mul_f32 v58, v58, v194\n" \
  "v_mul_f32 v59, v59, v194\n" \
  "v_mul_f32 v64, v64, v194\n" \
  "v_mul_f32 v65, v65, v194\n" \
  "v_mul_f32 v66, v66, v194\n" \
  "v_mul_f32 v67, v67, v194\n" \
  "v_mul_f32 v72, v72, v194\n" \
  "v_mul_f32 v73, v73, v194\n" \
  "v_mul_f32 v74, v74, v194\n" \
  "v_mul_f32 v75, v75, v194\n" \
  "v_mul_f32 v80, v80, v194\n" \
  "v_mul_f32 v81, v81, v194\n" \
  "v_mul_f32 v82, v82, v194\n" \
  "v_mul_f32 v83, v83, v194\n" \
  "v_mul_f32 v88, v88, v194\n" \
  "v_mul_f32 v89, v89, v194\n" \
  "v_mul_f32 v90, v90, v194\n" \
  "v_mul_f32 v91, v91, v194\n" \
  "v_mul_f32 v96, v96, v194\n" \
  "v_mul_f32 v97, v97, v194\n" \
  "v_mul_f32 v98, v98, v194\n" \
  "v_mul_f32 v99, v99, v194\n" \
  "v_mul_f32 v104, v104, v194\n" \
  "v_mul_f32 v105, v105, v194\n" \
  "v_mul_f32 v106, v106, v194\n" \
  "v_mul_f32 v107, v107, v194\n" \
  "v_mul_f32 v112, v112, v194\n" \
  "v_mul_f32 v113, v113, v194\n" \
  "v_mul_f32 v114, v114, v194\n" \
  "v_mul_f32 v115, v115, v194\n" \
  "v_mul_f32 v120, v120, v194\n" \
  "v_mul_f32 v121, v121, v194\n" \
  "v_mul_f32 v122, v122, v194\n" \
  "v_mul_f32 v123, v123, v194\n" \
  "v_mul_f32 v200, v200, v194\n" \
  "v_mul_f32 v201, v201, v194\n" \
  "v_mul_f32 v202, v202, v194\n" \
  "v_mul_f32 v203, v203, v194\n" \
  "v_max3_f32 v192, v148, v149, v150\n" \
  "v_max3_f32 v192, v192, v151, v156\n" \
  "v_max3_f32 v192, v192, v157, v158\n" \
  "v_max_f32 v192, v192, v159\n" \
  "v_mov_b32 v193, v192\n" \
  "s_nop 1\n" \
  "v_permlane32_swap_b32 v192, v193\n" \
  "v_max_f32 v192, v192, v193\n" \
  "v_mov_b32 v193, v192\n" \
  "s_nop 1\n" \
  "v_permlane16_swap_b32 v192, v193\n" \
  "v_max_f32_e64 v192, v192, v193\n" \
  "v_add_f32 v192, 0x41a00000, v192\n" \
  "v_max_f32_e64 v192, 0, v192\n" \
  "v_exp_f32 v194, -v192\n" \
  "v_sub_f32 v164, v164, v192\n" \
  "v_sub_f32 v165, v165, v192\n" \
  "v_sub_f32 v166, v166, v192\n" \
  "v_sub_f32 v167, v167, v192\n" \
  "v_sub_f32 v132, v132, v192\n" \
  "v_sub_f32 v148, v148, v192\n" \
  "v_sub_f32 v133, v133, v192\n" \
  "v_sub_f32 v149, v149, v192\n" \
  "v_sub_f32 v134, v134, v192\n" \
  "v_sub_f32 v150, v150, v192\n" \
  "v_sub_f32 v135, v135, v192\n" \
  "v_sub_f32 v151, v151, v192\n" \
  "v_sub_f32 v140, v140, v192\n" \
  "v_sub_f32 v156, v156, v192\n" \
  "v_sub_f32 v141, v141, v192\n" \
  "v_sub_f32 v157, v157, v192\n" \
  "v_sub_f32 v142, v142, v192\n" \
  "v_sub_f32 v158, v158, v192\n" \
  "v_sub_f32 v143, v143, v192\n" \
  "v_sub_f32 v159, v159, v192\n" \
  "v_mul_f32 v4, v4, v194\n" \
  "v_mul_f32 v5, v5, v194\n" \
  "v_mul_f32 v6, v6, v194\n" \
  "v_mul_f32 v7, v7, v194\n" \
  "v_mul_f32 v12, v12, v194\n" \
  "v_mul_f32 v13, v13, v194\n" \
  "v_mul_f32 v14, v14, v194\n" \
  "v_mul_f32 v15, v15, v194\n" \
  "v_mul_f32 v20, v20, v194\n" \
  "v_mul_f32 v21, v21, v194\n" \
  "v_mul_f32 v22, v22, v194\n" \
  "v_mul_f32 v23, v23, v194\n" \
  "v_mul_f32 v28, v28, v194\n" \
  "v_mul_f32 v29, v29, v194\n" \
  "v_mul_f32 v30, v30, v194\n" \
  "v_mul_f32 v31, v31, v194\n" \
  "v_mul_f32 v36, v36, v194\n" \
  "v_mul_f32 v37, v37, v194\n" \
  "v_mul_f32 v38, v38, v194\n" \
  "v_mul_f32 v39, v39, v194\n" \
  "v_mul_f32 v44, v44, v194\n" \
  "v_mul_f32 v45, v45, v194\n" \
  "v_mul_f32 v46, v46, v194\n" \
  "v_mul_f32 v47, v47, v194\n" \
  "v_mul_f32 v52, v52, v194\n" \
  "v_mul_f32 v53, v53, v194\n" \
  "v_mul_f32 v54, v54, v194\n" \
  "v_mul_f32 v55, v55, v194\n" \
  "v_mul_f32 v60, v60, v194\n" \
  "v_mul_f32 v61, v61, v194\n" \
  "v_mul_f32 v62, v62, v194\n" \
  "v_mul_f32 v63, v63, v194\n" \
  "v_mul_f32 v68, v68, v194\n" \
  "v_mul_f32 v69, v69, v194\n" \
  "v_mul_f32 v70, v70, v194\n" \
  "v_mul_f32 v71, v71, v194\n" \
  "v_mul_f32 v76, v76, v194\n" \
  "v_mul_f32 v77, v77, v194\n" \
  "v_mul_f32 v78, v78, v194\n" \
  "v_mul_f32 v79, v79, v194\n" \
  "v_mul_f32 v84, v84, v194\n" \
  "v_mul_f32 v85, v85, v194\n" \
  "v_mul_f32 v86, v86, v194\n" \
  "v_mul_f32 v87, v87, v194\n" \
  "v_mul_f32 v92, v92, v194\n" \
  "v_mul_f32 v93, v93, v194\n" \
  "v_mul_f32 v94, v94, v194\n" \
  "v_mul_f32 v95, v95, v194\n" \
  "v_mul_f32 v100, v100, v194\n" \
  "v_mul_f32 v101, v101, v194\n" \
  "v_mul_f32 v102, v102, v194\n" \
  "v_mul_f32 v103, v103, v194\n" \
  "v_mul_f32 v108, v108, v194\n" \
  "v_mul_f32 v109, v109, v194\n" \
  "v_mul_f32 v110, v110, v194\n" \
  "v_mul_f32 v111, v111, v194\n" \
  "v_mul_f32 v116, v116, v194\n" \
  "v_mul_f32 v117, v117, v194\n" \
  "v_mul_f32 v118, v118, v194\n" \
  "v_mul_f32 v119, v119, v194\n" \
  "v_mul_f32 v124, v124, v194\n" \
  "v_mul_f32 v125, v125, v194\n" \
  "v_mul_f32 v126, v126, v194\n" \
  "v_mul_f32 v127, v127, v194\n" \
  "v_mul_f32 v204, v204, v194\n" \
  "v_mul_f32 v205, v205, v194\n" \
  "v_mul_f32 v206, v206, v194\n" \
  "v_mul_f32 v207, v207, v194\n" \
  "v_exp_f32 v192, v144\n" \
  "v_exp_f32 v193, v145\n" \
  "v_exp_f32 v194, v146\n" \
  "v_exp_f32 v195, v147\n" \
  "v_cvt_pk_bf16_f32 v184, v192, v193\n" \
  "v_exp_f32 v196, v148\n" \
  "v_exp_f32 v197, v149\n" \
  "v_cvt_pk_bf16_f32 v185, v194, v195\n" \
  "v_exp_f32 v198, v150\n" \
  "v_exp_f32 v199, v151\n" \
  "v_cvt_pk_bf16_f32 v188, v196, v197\n" \
  "v_cvt_pk_bf16_f32 v189, v198, v199\n" \
  "v_exp_f32 v192, v152\n" \
  "v_exp_f32 v193, v153\n" \
  "v_exp_f32 v194, v154\n" \
  "v_exp_f32 v195, v155\n" \
  "v_cvt_pk_bf16_f32 v186, v192, v193\n" \
  "v_exp_f32 v196, v156\n" \
  "v_exp_f32 v197, v157\n" \
  "v_cvt_pk_bf16_f32 v187, v194, v195\n" \
  "v_exp_f32 v198, v158\n" \
  "v_exp_f32 v199, v159\n" \
  "v_cvt_pk_bf16_f32 v190, v196, v197\n" \
  "v_cvt_pk_bf16_f32 v191, v198, v199\n" \
  "s_nop 1\n" \
  "s_branch L_back_p59_%=\n" \
  "L_rare_p60_%=:\n" \
  "s_nop 15\n" \
  "v_max3_f32 v192, v128, v129, v130\n" \
  "v_max3_f32 v192, v192, v131, v136\n" \
  "v_max3_f32 v192, v192, v137, v138\n" \
  "v_max_f32 v192, v192, v139\n" \
  "v_mov_b32 v193, v192\n" \
  "s_nop 1\n" \
  "v_permlane32_swap_b32 v192, v193\n" \
  "v_max_f32 v192, v192, v193\n" \
  "v_mov_b32 v193, v192\n" \
  "s_nop 1\n" \
  "v_permlane16_swap_b32 v192, v193\n" \
  "v_max_f32 v192, v192, v193\n" \
  "v_add_f32 v192, 0x41a00000, v192\n" \
  "v_max_f32_e64 v192, 0, v192\n" \
  "v_exp_f32 v194, -v192\n" \
  "v_sub_f32 v160, v160, v192\n" \
  "v_sub_f32 v161, v161, v192\n" \
  "v_sub_f32 v162, v162, v192\n" \
  "v_sub_f32 v163, v163, v192\n" \
  "v_sub_f32 v144, v144, v192\n" \
  "v_sub_f32 v128, v128, v192\n" \
  "v_sub_f32 v145, v145, v192\n" \
  "v_sub_f32 v129, v129, v192\n" \
  "v_sub_f32 v146, v146, v192\n" \
  "v_sub_f32 v130, v130, v192\n" \
  "v_sub_f32 v147, v147, v192\n" \
  "v_sub_f32 v131, v131, v192\n" \
  "v_sub_f32 v152, v152, v192\n" \
  "v_sub_f32 v136, v136, v192\n" \
  "v_sub_f32 v153, v153, v192\n" \
  "v_sub_f32 v137, v137, v192\n" \
  "v_sub_f32 v154, v154, v192\n" \
  "v_sub_f32 v138, v138, v192\n" \
  "v_sub_f32 v155, v155, v192\n" \
  "v_sub_f32 v139, v139, v192\n" \
  "v_mul_f32 v0, v0, v194\n" \
  "v_mul_f32 v1, v1, v194\n" \
  "v_mul_f32 v2, v2, v194\n" \
  "v_mul_f32 v3, v3, v194\n" \
  "v_mul_f32 v8, v8, v194\n" \
  "v_mul_f32 v9, v9, v194\n" \
  "v_mul_f32 v10, v10, v194\n" \
  "v_mul_f32 v11, v11, v194\n" \
  "v_mul_f32 v16, v16, v194\n" \
  "v_mul_f32 v17, v17, v194\n" \
  "v_mul_f32 v18, v18, v194\n" \
  "v_mul_f32 v19, v19, v194\n" \
  "v_mul_f32 v24, v24, v194\n" \
  "v_mul_f32 v25, v25, v194\n" \
  "v_mul_f32 v26, v26, v194\n" \
  "v_mul_f32 v27, v27, v194\n" \
  "v_mul_f32 v32, v32, v194\n" \
  "v_mul_f32 v33, v33, v194\n" \
  "v_mul_f32 v34, v34, v194\n" \
  "v_mul_f32 v35, v35, v194\n" \
  "v_mul_f32 v40, v40, v194\n" \
  "v_mul_f32 v41, v41, v194\n" \
  "v_mul_f32 v42, v42, v194\n" \
  "v_mul_f32 v43, v43, v194\n" \
  "v_mul_f32 v48, v48, v194\n" \
  "v_mul_f32 v49, v49, v194\n" \
  "v_mul_f32 v50, v50, v194\n" \
  "v_mul_f32 v51, v51, v194\n" \
  "v_mul_f32 v56, v56, v194\n" \
  "v_mul_f32 v57, v57, v194\n" \
  "v_mul_f32 v58, v58, v194\n" \
  "v_mul_f32 v59, v59, v194\n" \
  "v_mul_f32 v64, v64, v194\n" \
  "v_mul_f32 v65, v65, v194\n" \
  "v_mul_f32 v66, v66, v194\n" \
  "v_mul_f32 v67, v67, v194\n" \
  "v_mul_f32 v72, v72, v194\n" \
  "v_mul_f32 v73, v73, v194\n" \
  "v_mul_f32 v74, v74, v194\n" \
  "v_mul_f32 v75, v75, v194\n" \
  "v_mul_f32 v80, v80, v194\n" \
  "v_mul_f32 v81, v81, v194\n" \
  "v_mul_f32 v82, v82, v194\n" \
  "v_mul_f32 v83, v83, v194\n" \
  "v_mul_f32 v88, v88, v194\n" \
  "v_mul_f32 v89, v89, v194\n" \
  "v_mul_f32 v90, v90, v194\n" \
  "v_mul_f32 v91, v91, v194\n" \
  "v_mul_f32 v96, v96, v194\n" \
  "v_mul_f32 v97, v97, v194\n" \
  "v_mul_f32 v98, v98, v194\n" \
  "v_mul_f32 v99, v99, v194\n" \
  "v_mul_f32 v104, v104, v194\n" \
  "v_mul_f32 v105, v105, v194\n" \
  "v_mul_f32 v106, v106, v194\n" \
  "v_mul_f32 v107, v107, v194\n" \
  "v_mul_f32 v112, v112, v194\n" \
  "v_mul_f32 v113, v113, v194\n" \
  "v_mul_f32 v114, v114, v194\n" \
  "v_mul_f32 v115, v115, v194\n" \
  "v_mul_f32 v120, v120, v194\n" \
  "v_mul_f32 v121, v121, v194\n" \
  "v_mul_f32 v122, v122, v194\n" \
  "v_mul_f32 v123, v123, v194\n" \
  "v_mul_f32 v200, v200, v194\n" \
  "v_mul_f32 v201, v201, v194\n" \
  "v_mul_f32 v202, v202, v194\n" \
  "v_mul_f32 v203, v203, v194\n" \
  "v_max3_f32 v192, v132, v133, v134\n" \
  "v_max3_f32 v192, v192, v135, v140\n" \
  "v_max3_f32 v192, v192, v141, v142\n" \
  "v_max_f32 v192, v192, v143\n" \
  "v_mov_b32 v193, v192\n" \
  "s_nop 1\n" \
  "v_permlane32_swap_b32 v192, v193\n" \
  "v_max_f32 v192, v192, v193\n" \
  "v_mov_b32 v193, v192\n" \
  "s_nop 1\n" \
  "v_permlane16_swap_b32 v192, v193\n" \
  "v_max_f32_e64 v192, v192, v193\n" \
  "v_add_f32 v192, 0x41a00000, v192\n" \
  "v_max_f32_e64 v192, 0, v192\n" \
  "v_exp_f32 v194, -v192\n" \
  "v_sub_f32 v164, v164, v192\n" \
  "v_sub_f32 v165, v165, v192\n" \
  "v_sub_f32 v166, v166, v192\n" \
  "v_sub_f32 v167, v167, v192\n" \
  "v_sub_f32 v148, v148, v192\n" \
  "v_sub_f32 v132, v132, v192\n" \
  "v_sub_f32 v149, v149, v192\n" \
  "v_sub_f32 v133, v133, v192\n" \
  "v_sub_f32 v150, v150, v192\n" \
  "v_sub_f32 v134, v134, v192\n" \
  "v_sub_f32 v151, v151, v192\n" \
  "v_sub_f32 v135, v135, v192\n" \
  "v_sub_f32 v156, v156, v192\n" \
  "v_sub_f32 v140, v140, v192\n" \
  "v_sub_f32 v157, v157, v192\n" \
  "v_sub_f32 v141, v141, v192\n" \
  "v_sub_f32 v158, v158, v192\n" \
  "v_sub_f32 v142, v142, v192\n" \
  "v_sub_f32 v159, v159, v192\n" \
  "v_sub_f32 v143, v143, v192\n" \
  "v_mul_f32 v4, v4, v194\n" \
  "v_mul_f32 v5, v5, v194\n" \
  "v_mul_f32 v6, v6, v194\n" \
  "v_mul_f32 v7, v7, v194\n" \
  "v_mul_f32 v12, v12, v194\n" \
  "v_mul_f32 v13, v13, v194\n" \
  "v_mul_f32 v14, v14, v194\n" \
  "v_mul_f32 v15, v15, v194\n" \
  "v_mul_f32 v20, v20, v194\n" \
  "v_mul_f32 v21, v21, v194\n" \
  "v_mul_f32 v22, v22, v194\n" \
  "v_mul_f32 v23, v23, v194\n" \
  "v_mul_f32 v28, v28, v194\n" \
  "v_mul_f32 v29, v29, v194\n" \
  "v_mul_f32 v30, v30, v194\n" \
  "v_mul_f32 v31, v31, v194\n" \
  "v_mul_f32 v36, v36, v194\n" \
  "v_mul_f32 v37, v37, v194\n" \
  "v_mul_f32 v38, v38, v194\n" \
  "v_mul_f32 v39, v39, v194\n" \
  "v_mul_f32 v44, v44, v194\n" \
  "v_mul_f32 v45, v45, v194\n" \
  "v_mul_f32 v46, v46, v194\n" \
  "v_mul_f32 v47, v47, v194\n" \
  "v_mul_f32 v52, v52, v194\n" \
  "v_mul_f32 v53, v53, v194\n" \
  "v_mul_f32 v54, v54, v194\n" \
  "v_mul_f32 v55, v55, v194\n" \
  "v_mul_f32 v60, v60, v194\n" \
  "v_mul_f32 v61, v61, v194\n" \
  "v_mul_f32 v62, v62, v194\n" \
  "v_mul_f32 v63, v63, v194\n" \
  "v_mul_f32 v68, v68, v194\n" \
  "v_mul_f32 v69, v69, v194\n" \
  "v_mul_f32 v70, v70, v194\n" \
  "v_mul_f32 v71, v71, v194\n" \
  "v_mul_f32 v76, v76, v194\n" \
  "v_mul_f32 v77, v77, v194\n" \
  "v_mul_f32 v78, v78, v194\n" \
  "v_mul_f32 v79, v79, v194\n" \
  "v_mul_f32 v84, v84, v194\n" \
  "v_mul_f32 v85, v85, v194\n" \
  "v_mul_f32 v86, v86, v194\n" \
  "v_mul_f32 v87, v87, v194\n" \
  "v_mul_f32 v92, v92, v194\n" \
  "v_mul_f32 v93, v93, v194\n" \
  "v_mul_f32 v94, v94, v194\n" \
  "v_mul_f32 v95, v95, v194\n" \
  "v_mul_f32 v100, v100, v194\n" \
  "v_mul_f32 v101, v101, v194\n" \
  "v_mul_f32 v102, v102, v194\n" \
  "v_mul_f32 v103, v103, v194\n" \
  "v_mul_f32 v108, v108, v194\n" \
  "v_mul_f32 v109, v109, v194\n" \
  "v_mul_f32 v110, v110, v194\n" \
  "v_mul_f32 v111, v111, v194\n" \
  "v_mul_f32 v116, v116, v194\n" \
  "v_mul_f32 v117, v117, v194\n" \
  "v_mul_f32 v118, v118, v194\n" \
  "v_mul_f32 v119, v119, v194\n" \
  "v_mul_f32 v124, v124, v194\n" \
  "v_mul_f32 v125, v125, v194\n" \
  "v_mul_f32 v126, v126, v194\n" \
  "v_mul_f32 v127, v127, v194\n" \
  "v_mul_f32 v204, v204, v194\n" \
  "v_mul_f32 v205, v205, v194\n" \
  "v_mul_f32 v206, v206, v194\n" \
  "v_mul_f32 v207, v207, v194\n" \
  "v_exp_f32 v192, v128\n" \
  "v_exp_f32 v193, v129\n" \
  "v_exp_f32 v194, v130\n" \
  "v_exp_f32 v195, v131\n" \
  "v_cvt_pk_bf16_f32 v176, v192, v193\n" \
  "v_exp_f32 v196, v132\n" \
  "v_exp_f32 v197, v133\n" \
  "v_cvt_pk_bf16_f32 v177, v194, v195\n" \
  "v_exp_f32 v198, v134\n" \
  "v_exp_f32 v199, v135\n" \
  "v_cvt_pk_bf16_f32 v180, v196, v197\n" \
  "v_cvt_pk_bf16_f32 v181, v198, v199\n" \
  "v_exp_f32 v192, v136\n" \
  "v_exp_f32 v193, v137\n" \
  "v_exp_f32 v194, v138\n" \
  "v_exp_f32 v195, v139\n" \
  "v_cvt_pk_bf16_f32 v178, v192, v193\n" \
  "v_exp_f32 v196, v140\n" \
  "v_exp_f32 v197, v141\n" \
  "v_cvt_pk_bf16_f32 v179, v194, v195\n" \
  "v_exp_f32 v198, v142\n" \
  "v_exp_f32 v199, v143\n" \
  "v_cvt_pk_bf16_f32 v182, v196, v197\n" \
  "v_cvt_pk_bf16_f32 v183, v198, v199\n" \
  "s_nop 1\n" \
  "s_branch L_back_p60_%=\n" \
  "L_rare_p61_%=:\n" \
  "s_nop 15\n" \
  "v_max3_f32 v192, v144, v145, v146\n" \
  "v_max3_f32 v192, v192, v147, v152\n" \
  "v_max3_f32 v192, v192, v153, v154\n" \
  "v_max_f32 v192, v192, v155\n" \
  "v_mov_b32 v193, v192\n" \
  "s_nop 1\n" \
  "v_permlane32_swap_b32 v192, v193\n" \
  "v_max_f32 v192, v192, v193\n" \
  "v_mov_b32 v193, v192\n" \
  "s_nop 1\n" \
  "v_permlane16_swap_b32 v192, v193\n" \
  "v_max_f32 v192, v192, v193\n" \
  "v_add_f32 v192, 0x41a00000, v192\n" \
  "v_max_f32_e64 v192, 0, v192\n" \
  "v_exp_f32 v194, -v192\n" \
  "v_sub_f32 v160, v160, v192\n" \
  "v_sub_f32 v161, v161, v192\n" \
  "v_sub_f32 v162, v162, v192\n" \
  "v_sub_f32 v163, v163, v192\n" \
  "v_sub_f32 v128, v128, v192\n" \
  "v_sub_f32 v144, v144, v192\n" \
  "v_sub_f32 v129, v129, v192\n" \
  "v_sub_f32 v145, v145, v192\n" \
  "v_sub_f32 v130, v130, v192\n" \
  "v_sub_f32 v146, v146, v192\n" \
  "v_sub_f32 v131, v131, v192\n" \
  "v_sub_f32 v147, v147, v192\n" \
  "v_sub_f32 v136, v136, v192\n" \
  "v_sub_f32 v152, v152, v192\n" \
  "v_sub_f32 v137, v137, v192\n" \
  "v_sub_f32 v153, v153, v192\n" \
  "v_sub_f32 v138, v138, v192\n" \
  "v_sub_f32 v154, v154, v192\n" \
  "v_sub_f32 v139, v139, v192\n" \
  "v_sub_f32 v155, v155, v192\n" \
  "v_mul_f32 v0, v0, v194\n" \
  "v_mul_f32 v1, v1, v194\n" \
  "v_mul_f32 v2, v2, v194\n" \
  "v_mul_f32 v3, v3, v194\n" \
  "v_mul_f32 v8, v8, v194\n" \
  "v_mul_f32 v9, v9, v194\n" \
  "v_mul_f32 v10, v10, v194\n" \
  "v_mul_f32 v11, v11, v194\n" \
  "v_mul_f32 v16, v16, v194\n" \
  "v_mul_f32 v17, v17, v194\n" \
  "v_mul_f32 v18, v18, v194\n" \
  "v_mul_f32 v19, v19, v194\n" \
  "v_mul_f32 v24, v24, v194\n" \
  "v_mul_f32 v25, v25, v194\n" \
  "v_mul_f32 v26, v26, v194\n" \
  "v_mul_f32 v27, v27, v194\n" \
  "v_mul_f32 v32, v32, v194\n" \
  "v_mul_f32 v33, v33, v194\n" \
  "v_mul_f32 v34, v34, v194\n" \
  "v_mul_f32 v35, v35, v194\n" \
  "v_mul_f32 v40, v40, v194\n" \
  "v_mul_f32 v41, v41, v194\n" \
  "v_mul_f32 v42, v42, v194\n" \
  "v_mul_f32 v43, v43, v194\n" \
  "v_mul_f32 v48, v48, v194\n" \
  "v_mul_f32 v49, v49, v194\n" \
  "v_mul_f32 v50, v50, v194\n" \
  "v_mul_f32 v51, v51, v194\n" \
  "v_mul_f32 v56, v56, v194\n" \
  "v_mul_f32 v57, v57, v194\n" \
  "v_mul_f32 v58, v58, v194\n" \
  "v_mul_f32 v59, v59, v194\n" \
  "v_mul_f32 v64, v64, v194\n" \
  "v_mul_f32 v65, v65, v194\n" \
  "v_mul_f32 v66, v66, v194\n" \
  "v_mul_f32 v67, v67, v194\n" \
  "v_mul_f32 v72, v72, v194\n" \
  "v_mul_f32 v73, v73, v194\n" \
  "v_mul_f32 v74, v74, v194\n" \
  "v_mul_f32 v75, v75, v194\n" \
  "v_mul_f32 v80, v80, v194\n" \
  "v_mul_f32 v81, v81, v194\n" \
  "v_mul_f32 v82, v82, v194\n" \
  "v_mul_f32 v83, v83, v194\n" \
  "v_mul_f32 v88, v88, v194\n" \
  "v_mul_f32 v89, v89, v194\n" \
  "v_mul_f32 v90, v90, v194\n" \
  "v_mul_f32 v91, v91, v194\n" \
  "v_mul_f32 v96, v96, v194\n" \
  "v_mul_f32 v97, v97, v194\n" \
  "v_mul_f32 v98, v98, v194\n" \
  "v_mul_f32 v99, v99, v194\n" \
  "v_mul_f32 v104, v104, v194\n" \
  "v_mul_f32 v105, v105, v194\n" \
  "v_mul_f32 v106, v106, v194\n" \
  "v_mul_f32 v107, v107, v194\n" \
  "v_mul_f32 v112, v112, v194\n" \
  "v_mul_f32 v113, v113, v194\n" \
  "v_mul_f32 v114, v114, v194\n" \
  "v_mul_f32 v115, v115, v194\n" \
  "v_mul_f32 v120, v120, v194\n" \
  "v_mul_f32 v121, v121, v194\n" \
  "v_mul_f32 v122, v122, v194\n" \
  "v_mul_f32 v123, v123, v194\n" \
  "v_mul_f32 v200, v200, v194\n" \
  "v_mul_f32 v201, v201, v194\n" \
  "v_mul_f32 v202, v202, v194\n" \
  "v_mul_f32 v203, v203, v194\n" \
  "v_max3_f32 v192, v148, v149, v150\n" \
  "v_max3_f32 v192, v192, v151, v156\n" \
  "v_max3_f32 v192, v192, v157, v158\n" \
  "v_max_f32 v192, v192, v159\n" \
  "v_mov_b32 v193, v192\n" \
  "s_nop 1\n" \
  "v_permlane32_swap_b32 v192, v193\n" \
  "v_max_f32 v192, v192, v193\n" \
  "v_mov_b32 v193, v192\n" \
  "s_nop 1\n" \
  "v_permlane16_swap_b32 v192, v193\n" \
  "v_max_f32_e64 v192, v192, v193\n" \
  "v_add_f32 v192, 0x41a00000, v192\n" \
  "v_max_f32_e64 v192, 0, v192\n" \
  "v_exp_f32 v194, -v192\n" \
  "v_sub_f32 v164, v164, v192\n" \
  "v_sub_f32 v165, v165, v192\n" \
  "v_sub_f32 v166, v166, v192\n" \
  "v_sub_f32 v167, v167, v192\n" \
  "v_sub_f32 v132, v132, v192\n" \
  "v_sub_f32 v148, v148, v192\n" \
  "v_sub_f32 v133, v133, v192\n" \
  "v_sub_f32 v149, v149, v192\n" \
  "v_sub_f32 v134, v134, v192\n" \
  "v_sub_f32 v150, v150, v192\n" \
  "v_sub_f32 v135, v135, v192\n" \
  "v_sub_f32 v151, v151, v192\n" \
  "v_sub_f32 v140, v140, v192\n" \
  "v_sub_f32 v156, v156, v192\n" \
  "v_sub_f32 v141, v141, v192\n" \
  "v_sub_f32 v157, v157, v192\n" \
  "v_sub_f32 v142, v142, v192\n" \
  "v_sub_f32 v158, v158, v192\n" \
  "v_sub_f32 v143, v143, v192\n" \
  "v_sub_f32 v159, v159, v192\n" \
  "v_mul_f32 v4, v4, v194\n" \
  "v_mul_f32 v5, v5, v194\n" \
  "v_mul_f32 v6, v6, v194\n" \
  "v_mul_f32 v7, v7, v194\n" \
  "v_mul_f32 v12, v12, v194\n" \
  "v_mul_f32 v13, v13, v194\n" \
  "v_mul_f32 v14, v14, v194\n" \
  "v_mul_f32 v15, v15, v194\n" \
  "v_mul_f32 v20, v20, v194\n" \
  "v_mul_f32 v21, v21, v194\n" \
  "v_mul_f32 v22, v22, v194\n" \
  "v_mul_f32 v23, v23, v194\n" \
  "v_mul_f32 v28, v28, v194\n" \
  "v_mul_f32 v29, v29, v194\n" \
  "v_mul_f32 v30, v30, v194\n" \
  "v_mul_f32 v31, v31, v194\n" \
  "v_mul_f32 v36, v36, v194\n" \
  "v_mul_f32 v37, v37, v194\n" \
  "v_mul_f32 v38, v38, v194\n" \
  "v_mul_f32 v39, v39, v194\n" \
  "v_mul_f32 v44, v44, v194\n" \
  "v_mul_f32 v45, v45, v194\n" \
  "v_mul_f32 v46, v46, v194\n" \
  "v_mul_f32 v47, v47, v194\n" \
  "v_mul_f32 v52, v52, v194\n" \
  "v_mul_f32 v53, v53, v194\n" \
  "v_mul_f32 v54, v54, v194\n" \
  "v_mul_f32 v55, v55, v194\n" \
  "v_mul_f32 v60, v60, v194\n" \
  "v_mul_f32 v61, v61, v194\n" \
  "v_mul_f32 v62, v62, v194\n" \
  "v_mul_f32 v63, v63, v194\n" \
  "v_mul_f32 v68, v68, v194\n" \
  "v_mul_f32 v69, v69, v194\n" \
  "v_mul_f32 v70, v70, v194\n" \
  "v_mul_f32 v71, v71, v194\n" \
  "v_mul_f32 v76, v76, v194\n" \
  "v_mul_f32 v77, v77, v194\n" \
  "v_mul_f32 v78, v78, v194\n" \
  "v_mul_f32 v79, v79, v194\n" \
  "v_mul_f32 v84, v84, v194\n" \
  "v_mul_f32 v85, v85, v194\n" \
  "v_mul_f32 v86, v86, v194\n" \
  "v_mul_f32 v87, v87, v194\n" \
  "v_mul_f32 v92, v92, v194\n" \
  "v_mul_f32 v93, v93, v194\n" \
  "v_mul_f32 v94, v94, v194\n" \
  "v_mul_f32 v95, v95, v194\n" \
  "v_mul_f32 v100, v100, v194\n" \
  "v_mul_f32 v101, v101, v194\n" \
  "v_mul_f32 v102, v102, v194\n" \
  "v_mul_f32 v103, v103, v194\n" \
  "v_mul_f32 v108, v108, v194\n" \
  "v_mul_f32 v109, v109, v194\n" \
  "v_mul_f32 v110, v110, v194\n" \
  "v_mul_f32 v111, v111, v194\n" \
  "v_mul_f32 v116, v116, v194\n" \
  "v_mul_f32 v117, v117, v194\n" \
  "v_mul_f32 v118, v118, v194\n" \
  "v_mul_f32 v119, v119, v194\n" \
  "v_mul_f32 v124, v124, v194\n" \
  "v_mul_f32 v125, v125, v194\n" \
  "v_mul_f32 v126, v126, v194\n" \
  "v_mul_f32 v127, v127, v194\n" \
  "v_mul_f32 v204, v204, v194\n" \
  "v_mul_f32 v205, v205, v194\n" \
  "v_mul_f32 v206, v206, v194\n" \
  "v_mul_f32 v207, v207, v194\n" \
  "v_exp_f32 v192, v144\n" \
  "v_exp_f32 v193, v145\n" \
  "v_exp_f32 v194, v146\n" \
  "v_exp_f32 v195, v147\n" \
  "v_cvt_pk_bf16_f32 v184, v192, v193\n" \
  "v_exp_f32 v196, v148\n" \
  "v_exp_f32 v197, v149\n" \
  "v_cvt_pk_bf16_f32 v185, v194, v195\n" \
  "v_exp_f32 v198, v150\n" \
  "v_exp_f32 v199, v151\n" \
  "v_cvt_pk_bf16_f32 v188, v196, v197\n" \
  "v_cvt_pk_bf16_f32 v189, v198, v199\n" \
  "v_exp_f32 v192, v152\n" \
  "v_exp_f32 v193, v153\n" \
  "v_exp_f32 v194, v154\n" \
  "v_exp_f32 v195, v155\n" \
  "v_cvt_pk_bf16_f32 v186, v192, v193\n" \
  "v_exp_f32 v196, v156\n" \
  "v_exp_f32 v197, v157\n" \
  "v_cvt_pk_bf16_f32 v187, v194, v195\n" \
  "v_exp_f32 v198, v158\n" \
  "v_exp_f32 v199, v159\n" \
  "v_cvt_pk_bf16_f32 v190, v196, v197\n" \
  "v_cvt_pk_bf16_f32 v191, v198, v199\n" \
  "s_nop 1\n" \
  "s_branch L_back_p61_%=\n" \
  "L_rare_p62_%=:\n" \
  "s_nop 15\n" \
  "v_max3_f32 v192, v128, v129, v130\n" \
  "v_max3_f32 v192, v192, v131, v136\n" \
  "v_max3_f32 v192, v192, v137, v138\n" \
  "v_max_f32 v192, v192, v139\n" \
  "v_mov_b32 v193, v192\n" \
  "s_nop 1\n" \
  "v_permlane32_swap_b32 v192, v193\n" \
  "v_max_f32 v192, v192, v193\n" \
  "v_mov_b32 v193, v192\n" \
  "s_nop 1\n" \
  "v_permlane16_swap_b32 v192, v193\n" \
  "v_max_f32 v192, v192, v193\n" \
  "v_add_f32 v192, 0x41a00000, v192\n" \
  "v_max_f32_e64 v192, 0, v192\n" \
  "v_exp_f32 v194, -v192\n" \
  "v_sub_f32 v160, v160, v192\n" \
  "v_sub_f32 v161, v161, v192\n" \
  "v_sub_f32 v162, v162, v192\n" \
  "v_sub_f32 v163, v163, v192\n" \
  "v_sub_f32 v144, v144, v192\n" \
  "v_sub_f32 v128, v128, v192\n" \
  "v_sub_f32 v145, v145, v192\n" \
  "v_sub_f32 v129, v129, v192\n" \
  "v_sub_f32 v146, v146, v192\n" \
  "v_sub_f32 v130, v130, v192\n" \
  "v_sub_f32 v147, v147, v192\n" \
  "v_sub_f32 v131, v131, v192\n" \
  "v_sub_f32 v152, v152, v192\n" \
  "v_sub_f32 v136, v136, v192\n" \
  "v_sub_f32 v153, v153, v192\n" \
  "v_sub_f32 v137, v137, v192\n" \
  "v_sub_f32 v154, v154, v192\n" \
  "v_sub_f32 v138, v138, v192\n" \
  "v_sub_f32 v155, v155, v192\n" \
  "v_sub_f32 v139, v139, v192\n" \
  "v_mul_f32 v0, v0, v194\n" \
  "v_mul_f32 v1, v1, v194\n" \
  "v_mul_f32 v2, v2, v194\n" \
  "v_mul_f32 v3, v3, v194\n" \
  "v_mul_f32 v8, v8, v194\n" \
  "v_mul_f32 v9, v9, v194\n" \
  "v_mul_f32 v10, v10, v194\n" \
  "v_mul_f32 v11, v11, v194\n" \
  "v_mul_f32 v16, v16, v194\n" \
  "v_mul_f32 v17, v17, v194\n" \
  "v_mul_f32 v18, v18, v194\n" \
  "v_mul_f32 v19, v19, v194\n" \
  "v_mul_f32 v24, v24, v194\n" \
  "v_mul_f32 v25, v25, v194\n" \
  "v_mul_f32 v26, v26, v194\n" \
  "v_mul_f32 v27, v27, v194\n" \
  "v_mul_f32 v32, v32, v194\n" \
  "v_mul_f32 v33, v33, v194\n" \
  "v_mul_f32 v34, v34, v194\n" \
  "v_mul_f32 v35, v35, v194\n" \
  "v_mul_f32 v40, v40, v194\n" \
  "v_mul_f32 v41, v41, v194\n" \
  "v_mul_f32 v42, v42, v194\n" \
  "v_mul_f32 v43, v43, v194\n" \
  "v_mul_f32 v48, v48, v194\n" \
  "v_mul_f32 v49, v49, v194\n" \
  "v_mul_f32 v50, v50, v194\n" \
  "v_mul_f32 v51, v51, v194\n" \
  "v_mul_f32 v56, v56, v194\n" \
  "v_mul_f32 v57, v57, v194\n" \
  "v_mul_f32 v58, v58, v194\n" \
  "v_mul_f32 v59, v59, v194\n" \
  "v_mul_f32 v64, v64, v194\n" \
  "v_mul_f32 v65, v65, v194\n" \
  "v_mul_f32 v66, v66, v194\n" \
  "v_mul_f32 v67, v67, v194\n" \
  "v_mul_f32 v72, v72, v194\n" \
  "v_mul_f32 v73, v73, v194\n" \
  "v_mul_f32 v74, v74, v194\n" \
  "v_mul_f32 v75, v75, v194\n" \
  "v_mul_f32 v80, v80, v194\n" \
  "v_mul_f32 v81, v81, v194\n" \
  "v_mul_f32 v82, v82, v194\n" \
  "v_mul_f32 v83, v83, v194\n" \
  "v_mul_f32 v88, v88, v194\n" \
  "v_mul_f32 v89, v89, v194\n" \
  "v_mul_f32 v90, v90, v194\n" \
  "v_mul_f32 v91, v91, v194\n" \
  "v_mul_f32 v96, v96, v194\n" \
  "v_mul_f32 v97, v97, v194\n" \
  "v_mul_f32 v98, v98, v194\n" \
  "v_mul_f32 v99, v99, v194\n" \
  "v_mul_f32 v104, v104, v194\n" \
  "v_mul_f32 v105, v105, v194\n" \
  "v_mul_f32 v106, v106, v194\n" \
  "v_mul_f32 v107, v107, v194\n" \
  "v_mul_f32 v112, v112, v194\n" \
  "v_mul_f32 v113, v113, v194\n" \
  "v_mul_f32 v114, v114, v194\n" \
  "v_mul_f32 v115, v115, v194\n" \
  "v_mul_f32 v120, v120, v194\n" \
  "v_mul_f32 v121, v121, v194\n" \
  "v_mul_f32 v122, v122, v194\n" \
  "v_mul_f32 v123, v123, v194\n" \
  "v_mul_f32 v200, v200, v194\n" \
  "v_mul_f32 v201, v201, v194\n" \
  "v_mul_f32 v202, v202, v194\n" \
  "v_mul_f32 v203, v203, v194\n" \
  "v_max3_f32 v192, v132, v133, v134\n" \
  "v_max3_f32 v192, v192, v135, v140\n" \
  "v_max3_f32 v192, v192, v141, v142\n" \
  "v_max_f32 v192, v192, v143\n" \
  "v_mov_b32 v193, v192\n" \
  "s_nop 1\n" \
  "v_permlane32_swap_b32 v192, v193\n" \
  "v_max_f32 v192, v192, v193\n" \
  "v_mov_b32 v193, v192\n" \
  "s_nop 1\n" \
  "v_permlane16_swap_b32 v192, v193\n" \
  "v_max_f32_e64 v192, v192, v193\n" \
  "v_add_f32 v192, 0x41a00000, v192\n" \
  "v_max_f32_e64 v192, 0, v192\n" \
  "v_exp_f32 v194, -v192\n" \
  "v_sub_f32 v164, v164, v192\n" \
  "v_sub_f32 v165, v165, v192\n" \
  "v_sub_f32 v166, v166, v192\n" \
  "v_sub_f32 v167, v167, v192\n" \
  "v_sub_f32 v148, v148, v192\n" \
  "v_sub_f32 v132, v132, v192\n" \
  "v_sub_f32 v149, v149, v192\n" \
  "v_sub_f32 v133, v133, v192\n" \
  "v_sub_f32 v150, v150, v192\n" \
  "v_sub_f32 v134, v134, v192\n" \
  "v_sub_f32 v151, v151, v192\n" \
  "v_sub_f32 v135, v135, v192\n" \
  "v_sub_f32 v156, v156, v192\n" \
  "v_sub_f32 v140, v140, v192\n" \
  "v_sub_f32 v157, v157, v192\n" \
  "v_sub_f32 v141, v141, v192\n" \
  "v_sub_f32 v158, v158, v192\n" \
  "v_sub_f32 v142, v142, v192\n" \
  "v_sub_f32 v159, v159, v192\n" \
  "v_sub_f32 v143, v143, v192\n" \
  "v_mul_f32 v4, v4, v194\n" \
  "v_mul_f32 v5, v5, v194\n" \
  "v_mul_f32 v6, v6, v194\n" \
  "v_mul_f32 v7, v7, v194\n" \
  "v_mul_f32 v12, v12, v194\n" \
  "v_mul_f32 v13, v13, v194\n" \
  "v_mul_f32 v14, v14, v194\n" \
  "v_mul_f32 v15, v15, v194\n" \
  "v_mul_f32 v20, v20, v194\n" \
  "v_mul_f32 v21, v21, v194\n" \
  "v_mul_f32 v22, v22, v194\n" \
  "v_mul_f32 v23, v23, v194\n" \
  "v_mul_f32 v28, v28, v194\n" \
  "v_mul_f32 v29, v29, v194\n" \
  "v_mul_f32 v30, v30, v194\n" \
  "v_mul_f32 v31, v31, v194\n" \
  "v_mul_f32 v36, v36, v194\n" \
  "v_mul_f32 v37, v37, v194\n" \
  "v_mul_f32 v38, v38, v194\n" \
  "v_mul_f32 v39, v39, v194\n" \
  "v_mul_f32 v44, v44, v194\n" \
  "v_mul_f32 v45, v45, v194\n" \
  "v_mul_f32 v46, v46, v194\n" \
  "v_mul_f32 v47, v47, v194\n" \
  "v_mul_f32 v52, v52, v194\n" \
  "v_mul_f32 v53, v53, v194\n" \
  "v_mul_f32 v54, v54, v194\n" \
  "v_mul_f32 v55, v55, v194\n" \
  "v_mul_f32 v60, v60, v194\n" \
  "v_mul_f32 v61, v61, v194\n" \
  "v_mul_f32 v62, v62, v194\n" \
  "v_mul_f32 v63, v63, v194\n" \
  "v_mul_f32 v68, v68, v194\n" \
  "v_mul_f32 v69, v69, v194\n" \
  "v_mul_f32 v70, v70, v194\n" \
  "v_mul_f32 v71, v71, v194\n" \
  "v_mul_f32 v76, v76, v194\n" \
  "v_mul_f32 v77, v77, v194\n" \
  "v_mul_f32 v78, v78, v194\n" \
  "v_mul_f32 v79, v79, v194\n" \
  "v_mul_f32 v84, v84, v194\n" \
  "v_mul_f32 v85, v85, v194\n" \
  "v_mul_f32 v86, v86, v194\n" \
  "v_mul_f32 v87, v87, v194\n" \
  "v_mul_f32 v92, v92, v194\n" \
  "v_mul_f32 v93, v93, v194\n" \
  "v_mul_f32 v94, v94, v194\n" \
  "v_mul_f32 v95, v95, v194\n" \
  "v_mul_f32 v100, v100, v194\n" \
  "v_mul_f32 v101, v101, v194\n" \
  "v_mul_f32 v102, v102, v194\n" \
  "v_mul_f32 v103, v103, v194\n" \
  "v_mul_f32 v108, v108, v194\n" \
  "v_mul_f32 v109, v109, v194\n" \
  "v_mul_f32 v110, v110, v194\n" \
  "v_mul_f32 v111, v111, v194\n" \
  "v_mul_f32 v116, v116, v194\n" \
  "v_mul_f32 v117, v117, v194\n" \
  "v_mul_f32 v118, v118, v194\n" \
  "v_mul_f32 v119, v119, v194\n" \
  "v_mul_f32 v124, v124, v194\n" \
  "v_mul_f32 v125, v125, v194\n" \
  "v_mul_f32 v126, v126, v194\n" \
  "v_mul_f32 v127, v127, v194\n" \
  "v_mul_f32 v204, v204, v194\n" \
  "v_mul_f32 v205, v205, v194\n" \
  "v_mul_f32 v206, v206, v194\n" \
  "v_mul_f32 v207, v207, v194\n" \
  "v_exp_f32 v192, v128\n" \
  "v_exp_f32 v193, v129\n" \
  "v_exp_f32 v194, v130\n" \
  "v_exp_f32 v195, v131\n" \
  "v_cvt_pk_bf16_f32 v176, v192, v193\n" \
  "v_exp_f32 v196, v132\n" \
  "v_exp_f32 v197, v133\n" \
  "v_cvt_pk_bf16_f32 v177, v194, v195\n" \
  "v_exp_f32 v198, v134\n" \
  "v_exp_f32 v199, v135\n" \
  "v_cvt_pk_bf16_f32 v180, v196, v197\n" \
  "v_cvt_pk_bf16_f32 v181, v198, v199\n" \
  "v_exp_f32 v192, v136\n" \
  "v_exp_f32 v193, v137\n" \
  "v_exp_f32 v194, v138\n" \
  "v_exp_f32 v195, v139\n" \
  "v_cvt_pk_bf16_f32 v178, v192, v193\n" \
  "v_exp_f32 v196, v140\n" \
  "v_exp_f32 v197, v141\n" \
  "v_cvt_pk_bf16_f32 v179, v194, v195\n" \
  "v_exp_f32 v198, v142\n" \
  "v_exp_f32 v199, v143\n" \
  "v_cvt_pk_bf16_f32 v182, v196, v197\n" \
  "v_cvt_pk_bf16_f32 v183, v198, v199\n" \
  "s_nop 1\n" \
  "s_branch L_back_p62_%=\n" \
  "L_rare_p63_%=:\n" \
  "s_nop 15\n" \
  "v_max3_f32 v192, v144, v145, v146\n" \
  "v_max3_f32 v192, v192, v147, v152\n" \
  "v_max3_f32 v192, v192, v153, v154\n" \
  "v_max_f32 v192, v192, v155\n" \
  "v_mov_b32 v193, v192\n" \
  "s_nop 1\n" \
  "v_permlane32_swap_b32 v192, v193\n" \
  "v_max_f32 v192, v192, v193\n" \
  "v_mov_b32 v193, v192\n" \
  "s_nop 1\n" \
  "v_permlane16_swap_b32 v192, v193\n" \
  "v_max_f32 v192, v192, v193\n" \
  "v_add_f32 v192, 0x41a00000, v192\n" \
  "v_max_f32_e64 v192, 0, v192\n" \
  "v_exp_f32 v194, -v192\n" \
  "v_sub_f32 v160, v160, v192\n" \
  "v_sub_f32 v161, v161, v192\n" \
  "v_sub_f32 v162, v162, v192\n" \
  "v_sub_f32 v163, v163, v192\n" \
  "v_sub_f32 v144, v144, v192\n" \
  "v_sub_f32 v145, v145, v192\n" \
  "v_sub_f32 v146, v146, v192\n" \
  "v_sub_f32 v147, v147, v192\n" \
  "v_sub_f32 v152, v152, v192\n" \
  "v_sub_f32 v153, v153, v192\n" \
  "v_sub_f32 v154, v154, v192\n" \
  "v_sub_f32 v155, v155, v192\n" \
  "v_mul_f32 v0, v0, v194\n" \
  "v_mul_f32 v1, v1, v194\n" \
  "v_mul_f32 v2, v2, v194\n" \
  "v_mul_f32 v3, v3, v194\n" \
  "v_mul_f32 v8, v8, v194\n" \
  "v_mul_f32 v9, v9, v194\n" \
  "v_mul_f32 v10, v10, v194\n" \
  "v_mul_f32 v11, v11, v194\n" \
  "v_mul_f32 v16, v16, v194\n" \
  "v_mul_f32 v17, v17, v194\n" \
  "v_mul_f32 v18, v18, v194\n" \
  "v_mul_f32 v19, v19, v194\n" \
  "v_mul_f32 v24, v24, v194\n" \
  "v_mul_f32 v25, v25, v194\n" \
  "v_mul_f32 v26, v26, v194\n" \
  "v_mul_f32 v27, v27, v194\n" \
  "v_mul_f32 v32, v32, v194\n" \
  "v_mul_f32 v33, v33, v194\n" \
  "v_mul_f32 v34, v34, v194\n" \
  "v_mul_f32 v35, v35, v194\n" \
  "v_mul_f32 v40, v40, v194\n" \
  "v_mul_f32 v41, v41, v194\n" \
  "v_mul_f32 v42, v42, v194\n" \
  "v_mul_f32 v43, v43, v194\n" \
  "v_mul_f32 v48, v48, v194\n" \
  "v_mul_f32 v49, v49, v194\n" \
  "v_mul_f32 v50, v50, v194\n" \
  "v_mul_f32 v51, v51, v194\n" \
  "v_mul_f32 v56, v56, v194\n" \
  "v_mul_f32 v57, v57, v194\n" \
  "v_mul_f32 v58, v58, v194\n" \
  "v_mul_f32 v59, v59, v194\n" \
  "v_mul_f32 v64, v64, v194\n" \
  "v_mul_f32 v65, v65, v194\n" \
  "v_mul_f32 v66, v66, v194\n" \
  "v_mul_f32 v67, v67, v194\n" \
  "v_mul_f32 v72, v72, v194\n" \
  "v_mul_f32 v73, v73, v194\n" \
  "v_mul_f32 v74, v74, v194\n" \
  "v_mul_f32 v75, v75, v194\n" \
  "v_mul_f32 v80, v80, v194\n" \
  "v_mul_f32 v81, v81, v194\n" \
  "v_mul_f32 v82, v82, v194\n" \
  "v_mul_f32 v83, v83, v194\n" \
  "v_mul_f32 v88, v88, v194\n" \
  "v_mul_f32 v89, v89, v194\n" \
  "v_mul_f32 v90, v90, v194\n" \
  "v_mul_f32 v91, v91, v194\n" \
  "v_mul_f32 v96, v96, v194\n" \
  "v_mul_f32 v97, v97, v194\n" \
  "v_mul_f32 v98, v98, v194\n" \
  "v_mul_f32 v99, v99, v194\n" \
  "v_mul_f32 v104, v104, v194\n" \
  "v_mul_f32 v105, v105, v194\n" \
  "v_mul_f32 v106, v106, v194\n" \
  "v_mul_f32 v107, v107, v194\n" \
  "v_mul_f32 v112, v112, v194\n" \
  "v_mul_f32 v113, v113, v194\n" \
  "v_mul_f32 v114, v114, v194\n" \
  "v_mul_f32 v115, v115, v194\n" \
  "v_mul_f32 v120, v120, v194\n" \
  "v_mul_f32 v121, v121, v194\n" \
  "v_mul_f32 v122, v122, v194\n" \
  "v_mul_f32 v123, v123, v194\n" \
  "v_mul_f32 v200, v200, v194\n" \
  "v_mul_f32 v201, v201, v194\n" \
  "v_mul_f32 v202, v202, v194\n" \
  "v_mul_f32 v203, v203, v194\n" \
  "v_max3_f32 v192, v148, v149, v150\n" \
  "v_max3_f32 v192, v192, v151, v156\n" \
  "v_max3_f32 v192, v192, v157, v158\n" \
  "v_max_f32 v192, v192, v159\n" \
  "v_mov_b32 v193, v192\n" \
  "s_nop 1\n" \
  "v_permlane32_swap_b32 v192, v193\n" \
  "v_max_f32 v192, v192, v193\n" \
  "v_mov_b32 v193, v192\n" \
  "s_nop 1\n" \
  "v_permlane16_swap_b32 v192, v193\n" \
  "v_max_f32_e64 v192, v192, v193\n" \
  "v_add_f32 v192, 0x41a00000, v192\n" \
  "v_max_f32_e64 v192, 0, v192\n" \
  "v_exp_f32 v194, -v192\n" \
  "v_sub_f32 v164, v164, v192\n" \
  "v_sub_f32 v165, v165, v192\n" \
  "v_sub_f32 v166, v166, v192\n" \
  "v_sub_f32 v167, v167, v192\n" \
  "v_sub_f32 v148, v148, v192\n" \
  "v_sub_f32 v149, v149, v192\n" \
  "v_sub_f32 v150, v150, v192\n" \
  "v_sub_f32 v151, v151, v192\n" \
  "v_sub_f32 v156, v156, v192\n" \
  "v_sub_f32 v157, v157, v192\n" \
  "v_sub_f32 v158, v158, v192\n" \
  "v_sub_f32 v159, v159, v192\n" \
  "v_mul_f32 v4, v4, v194\n" \
  "v_mul_f32 v5, v5, v194\n" \
  "v_mul_f32 v6, v6, v194\n" \
  "v_mul_f32 v7, v7, v194\n" \
  "v_mul_f32 v12, v12, v194\n" \
  "v_mul_f32 v13, v13, v194\n" \
  "v_mul_f32 v14, v14, v194\n" \
  "v_mul_f32 v15, v15, v194\n" \
  "v_mul_f32 v20, v20, v194\n" \
  "v_mul_f32 v21, v21, v194\n" \
  "v_mul_f32 v22, v22, v194\n" \
  "v_mul_f32 v23, v23, v194\n" \
  "v_mul_f32 v28, v28, v194\n" \
  "v_mul_f32 v29, v29, v194\n" \
  "v_mul_f32 v30, v30, v194\n" \
  "v_mul_f32 v31, v31, v194\n" \
  "v_mul_f32 v36, v36, v194\n" \
  "v_mul_f32 v37, v37, v194\n" \
  "v_mul_f32 v38, v38, v194\n" \
  "v_mul_f32 v39, v39, v194\n" \
  "v_mul_f32 v44, v44, v194\n" \
  "v_mul_f32 v45, v45, v194\n" \
  "v_mul_f32 v46, v46, v194\n" \
  "v_mul_f32 v47, v47, v194\n" \
  "v_mul_f32 v52, v52, v194\n" \
  "v_mul_f32 v53, v53, v194\n" \
  "v_mul_f32 v54, v54, v194\n" \
  "v_mul_f32 v55, v55, v194\n" \
  "v_mul_f32 v60, v60, v194\n" \
  "v_mul_f32 v61, v61, v194\n" \
  "v_mul_f32 v62, v62, v194\n" \
  "v_mul_f32 v63, v63, v194\n" \
  "v_mul_f32 v68, v68, v194\n" \
  "v_mul_f32 v69, v69, v194\n" \
  "v_mul_f32 v70, v70, v194\n" \
  "v_mul_f32 v71, v71, v194\n" \
  "v_mul_f32 v76, v76, v194\n" \
  "v_mul_f32 v77, v77, v194\n" \
  "v_mul_f32 v78, v78, v194\n" \
  "v_mul_f32 v79, v79, v194\n" \
  "v_mul_f32 v84, v84, v194\n" \
  "v_mul_f32 v85, v85, v194\n" \
  "v_mul_f32 v86, v86, v194\n" \
  "v_mul_f32 v87, v87, v194\n" \
  "v_mul_f32 v92, v92, v194\n" \
  "v_mul_f32 v93, v93, v194\n" \
  "v_mul_f32 v94, v94, v194\n" \
  "v_mul_f32 v95, v95, v194\n" \
  "v_mul_f32 v100, v100, v194\n" \
  "v_mul_f32 v101, v101, v194\n" \
  "v_mul_f32 v102, v102, v194\n" \
  "v_mul_f32 v103, v103, v194\n" \
  "v_mul_f32 v108, v108, v194\n" \
  "v_mul_f32 v109, v109, v194\n" \
  "v_mul_f32 v110, v110, v194\n" \
  "v_mul_f32 v111, v111, v194\n" \
  "v_mul_f32 v116, v116, v194\n" \
  "v_mul_f32 v117, v117, v194\n" \
  "v_mul_f32 v118, v118, v194\n" \
  "v_mul_f32 v119, v119, v194\n" \
  "v_mul_f32 v124, v124, v194\n" \
  "v_mul_f32 v125, v125, v194\n" \
  "v_mul_f32 v126, v126, v194\n" \
  "v_mul_f32 v127, v127, v194\n" \
  "v_mul_f32 v204, v204, v194\n" \
  "v_mul_f32 v205, v205, v194\n" \
  "v_mul_f32 v206, v206, v194\n" \
  "v_mul_f32 v207, v207, v194\n" \
  "v_exp_f32 v192, v144\n" \
  "v_exp_f32 v193, v145\n" \
  "v_exp_f32 v194, v146\n" \
  "v_exp_f32 v195, v147\n" \
  "v_cvt_pk_bf16_f32 v184, v192, v193\n" \
  "v_exp_f32 v196, v148\n" \
  "v_exp_f32 v197, v149\n" \
  "v_cvt_pk_bf16_f32 v185, v194, v195\n" \
  "v_exp_f32 v198, v150\n" \
  "v_exp_f32 v199, v151\n" \
  "v_cvt_pk_bf16_f32 v188, v196, v197\n" \
  "v_cvt_pk_bf16_f32 v189, v198, v199\n" \
  "v_exp_f32 v192, v152\n" \
  "v_exp_f32 v193, v153\n" \
  "v_exp_f32 v194, v154\n" \
  "v_exp_f32 v195, v155\n" \
  "v_cvt_pk_bf16_f32 v186, v192, v193\n" \
  "v_exp_f32 v196, v156\n" \
  "v_exp_f32 v197, v157\n" \
  "v_cvt_pk_bf16_f32 v187, v194, v195\n" \
  "v_exp_f32 v198, v158\n" \
  "v_exp_f32 v199, v159\n" \
  "v_cvt_pk_bf16_f32 v190, v196, v197\n" \
  "v_cvt_pk_bf16_f32 v191, v198, v199\n" \
  "s_nop 1\n" \
  "s_branch L_back_p63_%=\n" \
  "L_end_%=:\n"
#define KA_CLOB "v0","v1","v2","v3","v4","v5","v6","v7","v8","v9","v10","v11","v12","v13","v14","v15","v16","v17","v18","v19","v20","v21","v22","v23","v24","v25","v26","v27","v28","v29","v30","v31","v32","v33","v34","v35","v36","v37","v38","v39","v40","v41","v42","v43","v44","v45","v46","v47","v48","v49","v50","v51","v52","v53","v54","v55","v56","v57","v58","v59","v60","v61","v62","v63","v64","v65","v66","v67","v68","v69","v70","v71","v72","v73","v74","v75","v76","v77","v78","v79","v80","v81","v82","v83","v84","v85","v86","v87","v88","v89","v90","v91","v92","v93","v94","v95","v96","v97","v98","v99","v100","v101","v102","v103","v104","v105","v106","v107","v108","v109","v110","v111","v112","v113","v114","v115","v116","v117","v118","v119","v120","v121","v122","v123","v124","v125","v126","v127","v128","v129","v130","v131","v132","v133","v134","v135","v136","v137","v138","v139","v140","v141","v142","v143","v144","v145","v146","v147","v148","v149","v150","v151","v152","v153","v154","v155","v156","v157","v158","v159","v160","v161","v162","v163","v164","v165","v166","v167","v168","v169","v170","v171","v172","v173","v174","v175","v176","v177","v178","v179","v180","v181","v182","v183","v184","v185","v186","v187","v188","v189","v190","v191","v192","v193","v194","v195","v196","v197","v198","v199","v200","v201","v202","v203","v204","v205","v206","v207","v208","v209","v210","v211","v212","v213","v214","v215","v216","v217","v218","v219","v220","v221","v222","v223","v224","v225","v226","v227","v228","v229","v230","v231","v232","v233","v234","v235","v236","v237","v238","v239","v240","v241","v242","v243","v244","v245","v246","v247","v248","v249","v250","v251","v252","v253","v254","v255","a0","a1","a2","a3","a4","a5","a6","a7","a8","a9","a10","a11","a12","a13","a14","a15","a16","a17","a18","a19","a20","a21","a22","a23","a24","a25","a26","a27","a28","a29","a30","a31","a32","a33","a34","a35","a36","a37","a38","a39","a40","a41","a42","a43","a44","a45","a46","a47","a48","a49","a50","a51","a52","a53","a54","a55","a56","a57","a58","a59","a60","a61","a62","a63","a64","a65","a66","a67","a68","a69","a70","a71","a72","a73","a74","a75","a76","a77","a78","a79","a80","a81","a82","a83","a84","a85","a86","a87","a88","a89","a90","a91","a92","a93","a94","a95","a96","a97","a98","a99","a100","a101","a102","a103","a104","a105","a106","a107","a108","a109","a110","a111","a112","a113","a114","a115","a116","a117","a118","a119","a120","a121","a122","a123","a124","a125","a126","a127","a128","a129","a130","a131","a132","a133","a134","a135","a136","a137","a138","a139","a140","a141","a142","a143","a144","a145","a146","a147","a148","a149","a150","a151","a152","a153","a154","a155","a156","a157","a158","a159","a160","a161","a162","a163","a164","a165","a166","a167","a168","a169","a170","a171","a172","a173","a174","a175","a176","a177","a178","a179","a180","a181","a182","a183","a184","a185","a186","a187","a188","a189","a190","a191","a192","a193","a194","a195","a196","a197","a198","a199","a200","a201","a202","a203","a204","a205","a206","a207","a208","a209","a210","a211","a212","a213","a214","a215","a216","a217","a218","a219","a220","a221","a222","a223","a224","a225","a226","a227","a228","a229","a230","a231","a232","a233","a234","a235","a236","a237","a238","a239","a240","a241","a242","a243","a244","a245","a246","a247","a248","a249","a250","a251","a252","a253","a254","a255","s12","s13","s14","s15","s16","s17","s18","s19","s20","s21","s22","s23","s24","s25","s26","s27","s28","s29","s30","s31","s33","s34","s35","s36","s37","s38","s39","s40","s41","s42","s43","s44","s45","s46","s47","s48","s49","s50","s51","s52","s53","s54","s55","s56","s57","s58","s59","s60","s61","s62","s63","s64","s65","s66","s67","s68","s69","s70","s71","s72","s73","s74","s75","s76","s77","s78","s79","s80","s81","s82","s83","s84","s85","s86","s87","s88","s89","s90","s91","s92","s93","s94","s95","s96","s97","s98","s99","vcc","scc","memory"

#define GREV 0
#define HPX 2

__global__ __launch_bounds__(256, 1) void ka(const char* __restrict__ x, const char* __restrict__ K, const char* __restrict__ V,
                                             const char* __restrict__ wq, const char* __restrict__ wo, const char* __restrict__ w1,
                                             const char* __restrict__ w3, const char* __restrict__ w2, char* __restrict__ ws,
                                             char* __restrict__ out) {
  __shared__ __attribute__((aligned(1024))) char lds[163840];
  const unsigned b = blockIdx.x, xc = b & 7, i = b >> 3;
  const unsigned head = (xc % (16 / HPX)) * HPX + (i % HPX), qb = (xc / (16 / HPX)) * (32 / HPX) + i / HPX;
  const unsigned gq = (xc >> 1) * 8 + (i >> 2), gc = ((xc & 1) != GREV) ? ((i & 3) + ((i & 3) == 3 ? 5u : 0u)) : 3 + (i & 3);
  const unsigned w = __builtin_amdgcn_readfirstlane(threadIdx.x >> 6);
  const unsigned ldsb = (unsigned)(size_t)lds;
  asm volatile(KA_ASM :: "s"(__builtin_amdgcn_kernarg_segment_ptr()), "s"(w), "s"(ldsb), "s"(head), "s"(qb), "s"(gq), "s"(gc)
               : KA_CLOB);
}

extern "C" void kernel_launch(void* const* d_in, const int* in_sizes, int n_in, void* d_out, int out_size, void* d_ws, size_t ws_size,
                              hipStream_t stream) {
  ka<<<256, 256, 0, stream>>>((const char*)d_in[0], (const char*)d_in[1], (const char*)d_in[2], (const char*)d_in[3], (const char*)d_in[4],
                              (const char*)d_in[6], (const char*)d_in[7], (const char*)d_in[8], (char*)d_ws, (char*)d_out);
}
```
